```python
import torch

def _softmax(x, axis=-1):
    return torch.softmax(x, dim=axis)
def _gelu(x):
    return torch.nn.functional.gelu(x, approximate="tanh")
def _silu(x):
    return torch.nn.functional.silu(x)
def _sigmoid(x):
    return torch.sigmoid(x)
def _layernorm(x, axis=-1, eps=1e-5):
    m = x.mean(dim=axis, keepdim=True)
    v = x.var(dim=axis, keepdim=True, unbiased=False)
    return (x - m) / torch.sqrt(v + eps)
def _scatter_add(tgt, idx, src, axis=0):
    return tgt.clone().index_add_(axis, idx, src)
def _segment_sum(src, seg, n):
    out = torch.zeros((n,) + src.shape[1:], dtype=src.dtype)
    return out.index_add_(0, seg, src)
def _pad(x, pw):
    flat = tuple(v for pair in reversed(pw) for v in pair)
    return torch.nn.functional.pad(x, flat)

kernel_name = 'concat_heads__sliding_local_dilated__compressed_kv_cla__prompt'

def setup_inputs():

    _w = (lambda a: (lambda t: t.to(torch.bfloat16) if t.is_floating_point() else t)(torch.as_tensor(a)))
    def _A_setup():
        B, S, D = 1, 2048, 448
        return {
            "x": _w((lambda *s: torch.randn(*s, dtype=torch.bfloat16) * 0.4)(B, S, D)),
            "wqkv": _w((lambda *s: torch.randn(*s, dtype=torch.bfloat16) * 0.4)(D, 3 * D)),
            "wo": _w((lambda *s: torch.randn(*s, dtype=torch.bfloat16) * 0.05)(D, D)),
        }
    _A_in = _A_setup()

    def _B_setup():
        B, H, Sq, Sk, D, M = 1, 16, 2048, 2048, 256, 448
        Dc = 4 * D
        return {
            "x": _w((lambda *s: torch.randn(*s, dtype=torch.bfloat16) * 0.2)(B, Sq, M)),
            "c_kv": _w((lambda *s: torch.randn(*s, dtype=torch.bfloat16) * 1.6)(B, Sk, Dc)),
            "w_q": _w((lambda *s: torch.randn(*s, dtype=torch.bfloat16) * 3.2)(M, H, D) * (M ** -0.5)),
            "w_uk": _w((lambda *s: torch.randn(*s, dtype=torch.bfloat16) * 3.2)(Dc, H, D) * (Dc ** -0.5)),
            "w_uv": _w((lambda *s: torch.randn(*s, dtype=torch.bfloat16) * 1.6)(Dc, H, D) * (Dc ** -0.5)),
            "w_o": _w((lambda *s: torch.randn(*s, dtype=torch.bfloat16) * 1.6)(H, D, M) * ((H * D) ** -0.5)),
        }
    _B_in = _B_setup()

    return {
        "A_x": _A_in["x"],
        "A_wqkv": _A_in["wqkv"],
        "A_wo": _A_in["wo"],
        "B_x": _B_in["x"],
        "B_c_kv": _B_in["c_kv"],
        "B_w_q": _B_in["w_q"],
        "B_w_uk": _B_in["w_uk"],
        "B_w_uv": _B_in["w_uv"],
        "B_w_o": _B_in["w_o"],
    }

def reference(A_x, A_wqkv, A_wo, B_x, B_c_kv, B_w_q, B_w_uk, B_w_uv, B_w_o):
    def _A_ref(x, wqkv, wo):
        B, S, D = x.shape
        H = 16; Dh = D // H
        W, dil = 8, 2
        qkv = (lambda x, *d: x.permute(*d))((x @ wqkv).reshape(B, S, 3, H, Dh), 2, 0, 3, 1, 4)
        q, k, v = qkv[0], qkv[1], qkv[2]
        scale = Dh ** -0.5
        pos = torch.arange(S)
        off = torch.arange(W) * dil
        idx = pos[:, None] - off[None, :]
        mask = idx >= 0
        idx_c = torch.where(mask, idx, 0).reshape(-1)
        kg = k[:, :, idx_c, :].reshape(B, H, S, W, Dh)
        vg = v[:, :, idx_c, :].reshape(B, H, S, W, Dh)
        logits = torch.einsum("bhsd,bhswd->bhsw", q, kg) * scale
        logits = torch.where(mask[None, None, :, :], logits, -1e9)
        attn = _softmax(logits, axis=-1)
        o = torch.einsum("bhsw,bhswd->bhsd", attn, vg)
        return (lambda x, *d: x.permute(*d))(o, 0, 2, 1, 3).reshape(B, S, D) @ wo

    def _B_ref(x, c_kv, w_q, w_uk, w_uv, w_o):
        B, Sq, M = x.shape[0], x.shape[1], x.shape[2]
        Sk = c_kv.shape[1]
        H, D = w_q.shape[1], w_q.shape[2]
        q = torch.einsum("bqm,mhd->bhqd", x, w_q)
        k = torch.einsum("bkc,chd->bhkd", c_kv, w_uk)
        v = torch.einsum("bkc,chd->bhkd", c_kv, w_uv)
        scores = torch.einsum("bhqd,bhkd->bhqk", q, k) * (D ** -0.5)
        off = Sk - Sq
        qi = torch.arange(Sq)[:, None]
        kj = torch.arange(Sk)[None, :]
        cmask = kj <= (qi + off)
        scores = torch.where(cmask[None, None], scores, -1e9)
        p = _softmax(scores, axis=-1)
        ctx = torch.einsum("bhqk,bhkd->bhqd", p, v)
        return torch.einsum("bhqd,hdm->bqm", ctx, w_o)

    a = _A_ref(A_x, A_wqkv, A_wo)
    b = _B_ref(B_x, B_c_kv, B_w_q, B_w_uk, B_w_uv, B_w_o)
    return torch.concatenate([a, b], axis=-1)
```

```cpp
#include <hip/hip_runtime.h>
#include <hip/hip_bf16.h>
typedef __hip_bfloat16 bf16;
static __device__ __forceinline__ float b2f(bf16 x){return __bfloat162float(x);}
static __device__ __forceinline__ bf16 f2b(float x){return __float2bfloat16(x);}

__global__ void gemm_naive(const bf16* A, const bf16* B, bf16* C, int M, int N, int K, int ldc){
  __shared__ float As[16][17], Bs[16][17];
  int tx=threadIdx.x&15, ty=threadIdx.x>>4;
  int row=blockIdx.y*16+ty, col=blockIdx.x*16+tx;
  float acc=0;
  for(int k0=0;k0<K;k0+=16){
    As[ty][tx]=b2f(A[(long)row*K+k0+tx]);
    Bs[ty][tx]=b2f(B[(long)(k0+ty)*N+col]);
    __syncthreads();
    for(int k=0;k<16;++k) acc+=As[ty][k]*Bs[k][tx];
    __syncthreads();
  }
  C[(long)row*ldc+col]=f2b(acc);
}
__global__ void local_attn(const bf16* qkv, bf16* o){
  int idx=blockIdx.x*blockDim.x+threadIdx.x; if(idx>=2048*16) return;
  int s=idx>>4,h=idx&15;
  float lg[8]; float m=-1e30f;
  #pragma unroll
  for(int w=0;w<8;++w){int p=s-2*w; float a=0; if(p>=0){for(int d=0;d<28;++d)a+=b2f(qkv[s*1344+h*28+d])*b2f(qkv[p*1344+448+h*28+d]); a*=0.18898223650461362f;} else a=-1e9f; lg[w]=a; m=fmaxf(m,a);}
  float sum=0;
  #pragma unroll
  for(int w=0;w<8;++w){lg[w]=__expf(lg[w]-m); sum+=lg[w];}
  for(int d=0;d<28;++d){float a=0;
    #pragma unroll
    for(int w=0;w<8;++w){int p=s-2*w; if(p<0)p=0; a+=lg[w]*b2f(qkv[p*1344+896+h*28+d]);}
    o[s*448+h*28+d]=f2b(a/sum);}
}
__global__ void attn_naive(const bf16* q,const bf16* k,const bf16* v,bf16* ctx){
  int s=blockIdx.x,h=blockIdx.y,t=threadIdx.x;
  __shared__ float qs[256]; __shared__ float p[2048]; __shared__ float red[256];
  qs[t]=b2f(q[(long)s*4096+h*256+t]); __syncthreads();
  float m=-1e30f;
  for(int j=t;j<=s;j+=256){float a=0; const bf16* kr=k+(long)j*4096+h*256; for(int d=0;d<256;++d)a+=qs[d]*b2f(kr[d]); a*=0.0625f; p[j]=a; m=fmaxf(m,a);}
  red[t]=m; __syncthreads();
  for(int o=128;o>0;o>>=1){if(t<o)red[t]=fmaxf(red[t],red[t+o]); __syncthreads();}
  m=red[0]; __syncthreads();
  float sum=0; for(int j=t;j<=s;j+=256){float e=__expf(p[j]-m); p[j]=e; sum+=e;}
  red[t]=sum; __syncthreads();
  for(int o=128;o>0;o>>=1){if(t<o)red[t]+=red[t+o]; __syncthreads();}
  sum=red[0];
  float a=0; for(int j=0;j<=s;++j)a+=p[j]*b2f(v[(long)j*4096+h*256+t]);
  ctx[(long)s*4096+h*256+t]=f2b(a/sum);
}
extern "C" void kernel_launch(void* const* d_in, const int* in_sizes, int n_in, void* d_out, int out_size, void* d_ws, size_t ws_size, hipStream_t stream){
  const bf16* Ax=(const bf16*)d_in[0]; const bf16* Aw=(const bf16*)d_in[1]; const bf16* Awo=(const bf16*)d_in[2];
  const bf16* Bx=(const bf16*)d_in[3]; const bf16* ckv=(const bf16*)d_in[4]; const bf16* wq=(const bf16*)d_in[5];
  const bf16* wuk=(const bf16*)d_in[6]; const bf16* wuv=(const bf16*)d_in[7]; const bf16* wo=(const bf16*)d_in[8];
  bf16* out=(bf16*)d_out; bf16* ws=(bf16*)d_ws;
  bf16* qkv=ws; bf16* oa=qkv+2048*1344; bf16* q=oa+2048*448; bf16* k=q+2048*4096; bf16* v=k+2048*4096; bf16* ctx=v+2048*4096;
  gemm_naive<<<dim3(1344/16,128),256,0,stream>>>(Ax,Aw,qkv,2048,1344,448,1344);
  local_attn<<<128,256,0,stream>>>(qkv,oa);
  gemm_naive<<<dim3(448/16,128),256,0,stream>>>(oa,Awo,out,2048,448,448,896);
  gemm_naive<<<dim3(256,128),256,0,stream>>>(Bx,wq,q,2048,4096,448,4096);
  gemm_naive<<<dim3(256,128),256,0,stream>>>(ckv,wuk,k,2048,4096,1024,4096);
  gemm_naive<<<dim3(256,128),256,0,stream>>>(ckv,wuv,v,2048,4096,1024,4096);
  attn_naive<<<dim3(2048,16),256,0,stream>>>(q,k,v,ctx);
  gemm_naive<<<dim3(448/16,128),256,0,stream>>>(ctx,wo,out+448,2048,448,4096,896);
}
```

```cpp
#include <hip/hip_runtime.h>
#define ALL_REGS \
      "v0", "v1", "v2", "v3", "v4", "v5", "v6", "v7", "v8", "v9", "v10", "v11", "v12", "v13", "v14", "v15", "v16", "v17", "v18", "v19", "v20", "v21", "v22", "v23", "v24", "v25", "v26", "v27", "v28", "v29", "v30", "v31", \
      "v32", "v33", "v34", "v35", "v36", "v37", "v38", "v39", "v40", "v41", "v42", "v43", "v44", "v45", "v46", "v47", "v48", "v49", "v50", "v51", "v52", "v53", "v54", "v55", "v56", "v57", "v58", "v59", "v60", "v61", "v62", "v63", \
      "v64", "v65", "v66", "v67", "v68", "v69", "v70", "v71", "v72", "v73", "v74", "v75", "v76", "v77", "v78", "v79", "v80", "v81", "v82", "v83", "v84", "v85", "v86", "v87", "v88", "v89", "v90", "v91", "v92", "v93", "v94", "v95", \
      "v96", "v97", "v98", "v99", "v100", "v101", "v102", "v103", "v104", "v105", "v106", "v107", "v108", "v109", "v110", "v111", "v112", "v113", "v114", "v115", "v116", "v117", "v118", "v119", "v120", "v121", "v122", "v123", "v124", "v125", "v126", "v127", \
      "v128", "v129", "v130", "v131", "v132", "v133", "v134", "v135", "v136", "v137", "v138", "v139", "v140", "v141", "v142", "v143", "v144", "v145", "v146", "v147", "v148", "v149", "v150", "v151", "v152", "v153", "v154", "v155", "v156", "v157", "v158", "v159", \
      "v160", "v161", "v162", "v163", "v164", "v165", "v166", "v167", "v168", "v169", "v170", "v171", "v172", "v173", "v174", "v175", "v176", "v177", "v178", "v179", "v180", "v181", "v182", "v183", "v184", "v185", "v186", "v187", "v188", "v189", "v190", "v191", \
      "v192", "v193", "v194", "v195", "v196", "v197", "v198", "v199", "v200", "v201", "v202", "v203", "v204", "v205", "v206", "v207", "v208", "v209", "v210", "v211", "v212", "v213", "v214", "v215", "v216", "v217", "v218", "v219", "v220", "v221", "v222", "v223", \
      "v224", "v225", "v226", "v227", "v228", "v229", "v230", "v231", "v232", "v233", "v234", "v235", "v236", "v237", "v238", "v239", "v240", "v241", "v242", "v243", "v244", "v245", "v246", "v247", "v248", "v249", "v250", "v251", "v252", "v253", "v254", "v255", \
      "a0", "a1", "a2", "a3", "a4", "a5", "a6", "a7", "a8", "a9", "a10", "a11", "a12", "a13", "a14", "a15", "a16", "a17", "a18", "a19", "a20", "a21", "a22", "a23", "a24", "a25", "a26", "a27", "a28", "a29", "a30", "a31", \
      "a32", "a33", "a34", "a35", "a36", "a37", "a38", "a39", "a40", "a41", "a42", "a43", "a44", "a45", "a46", "a47", "a48", "a49", "a50", "a51", "a52", "a53", "a54", "a55", "a56", "a57", "a58", "a59", "a60", "a61", "a62", "a63", \
      "a64", "a65", "a66", "a67", "a68", "a69", "a70", "a71", "a72", "a73", "a74", "a75", "a76", "a77", "a78", "a79", "a80", "a81", "a82", "a83", "a84", "a85", "a86", "a87", "a88", "a89", "a90", "a91", "a92", "a93", "a94", "a95", \
      "a96", "a97", "a98", "a99", "a100", "a101", "a102", "a103", "a104", "a105", "a106", "a107", "a108", "a109", "a110", "a111", "a112", "a113", "a114", "a115", "a116", "a117", "a118", "a119", "a120", "a121", "a122", "a123", "a124", "a125", "a126", "a127", \
      "a128", "a129", "a130", "a131", "a132", "a133", "a134", "a135", "a136", "a137", "a138", "a139", "a140", "a141", "a142", "a143", "a144", "a145", "a146", "a147", "a148", "a149", "a150", "a151", "a152", "a153", "a154", "a155", "a156", "a157", "a158", "a159", \
      "a160", "a161", "a162", "a163", "a164", "a165", "a166", "a167", "a168", "a169", "a170", "a171", "a172", "a173", "a174", "a175", "a176", "a177", "a178", "a179", "a180", "a181", "a182", "a183", "a184", "a185", "a186", "a187", "a188", "a189", "a190", "a191", \
      "a192", "a193", "a194", "a195", "a196", "a197", "a198", "a199", "a200", "a201", "a202", "a203", "a204", "a205", "a206", "a207", "a208", "a209", "a210", "a211", "a212", "a213", "a214", "a215", "a216", "a217", "a218", "a219", "a220", "a221", "a222", "a223", \
      "a224", "a225", "a226", "a227", "a228", "a229", "a230", "a231", "a232", "a233", "a234", "a235", "a236", "a237", "a238", "a239", "a240", "a241", "a242", "a243", "a244", "a245", "a246", "a247", "a248", "a249", "a250", "a251", "a252", "a253", "a254", "a255", \
      "s4", "s5", "s6", "s7", "s8", "s9", "s10", "s11", "s12", "s13", "s14", "s15", "s16", "s17", "s18", "s19", "s20", "s21", "s22", "s23", "s24", "s25", "s26", "s27", "s28", "s33", "s34", "s35", "s36", "s37", "s38", "s39", \
      "s40", "s41", "s42", "s43", "s44", "s45", "s46", "s47", "s48", "s49", "s50", "s51", "s52", "s53", "s54", "s55", "s56", "s57", "s58", "s59", "s60", "s61", "s62", "s63", "s64", "s65", "s66", "s67", "s68", "s69", "s70", "s71", \
      "s72", "s73", "s74", "s75", "s76", "s77", "s78", "s79", "s80", "s81", "s82", "s83", "s84", "s85", "s86", "s87", "s88", "s89", "s90", "s91", "s92", "s93", "s94", "s95", "s96", "s97", "s98", "s99", "vcc", "scc", "memory"

extern "C" __global__ __launch_bounds__(256, 1) void fused_k(char* ws, const void* bx, const void* ckv, const void* wq, const void* wuk, const void* wuv, const void* ax, const void* aw,
                                                             const void* wo, const void* woA, void* out) {
  __shared__ __attribute__((aligned(16))) char lds[163840];
  asm volatile(
    "s_load_dwordx16 s[4:19], %0, 0x0\n"
    "s_load_dwordx4 s[20:23], %0, 0x40\n"
    "s_load_dwordx2 s[24:25], %0, 0x50\n"
    "s_mov_b32 s26, %1\n"
    "s_mov_b32 s27, %2\n"
    "s_mov_b32 s60, s27\n"
    "s_lshr_b32 s75, s26, 3\n"
    "s_and_b32 s84, s26, 7\n"
    "s_lshl_b32 s84, s84, 1\n"
    "s_and_b32 s85, s75, 1\n"
    "s_add_u32 s74, s84, s85\n"
    "s_lshr_b32 s86, s75, 2\n"
    "v_mbcnt_lo_u32_b32 v252, -1, 0\n"
    "v_mbcnt_hi_u32_b32 v252, -1, v252\n"
    "v_lshlrev_b32 v146, 4, v252\n"
    "v_lshrrev_b32 v163, 2, v252\n"
    "v_lshrrev_b32 v167, 4, v252\n"
    "v_and_b32 v166, 15, v252\n"
    "v_and_b32 v5, 31, v252\n"
    "v_lshrrev_b32 v170, 3, v252\n"
    "v_and_b32 v171, 7, v252\n"
    "v_xor_b32 v1, v171, v167\n"
    "v_lshlrev_b32 v1, 4, v1\n"
    "v_lshl_add_u32 v1, v170, 11, v1\n"
    "s_lshl_b32 s87, s60, 17\n"
    "v_add_u32 v128, s87, v1\n"
    "v_xor_b32 v1, 64, v128\n"
    "v_add_u32 v129, 0x3c00, v1\n"
    "v_add_u32 v130, 0x7800, v128\n"
    "v_add_u32 v131, 0x7800, v129\n"
    "s_mov_b32 s61, 0\n"
    "s_mov_b32 s62, 0\n"
    "s_lshl_b32 s63, s60, 13\n"
    "s_add_u32 s63, s63, 0x10000\n"
    "s_lshl_b32 s96, s60, 12\n"
    "s_mov_b32 s70, 0x10000\n"
    "s_mov_b32 s72, 0xffff0080\n"
    "s_mov_b32 s71, 0x40000\n"
    "v_lshrrev_b32 v4, 5, v252\n"
    "s_and_b32 s87, s60, 1\n"
    "s_lshl_b32 s87, s87, 2\n"
    "s_lshl_b32 s90, s60, 16\n"
    "v_add_u32 v6, 0, v4\n"
    "v_and_b32 v7, 3, v6\n"
    "v_or_b32 v7, s87, v7\n"
    "v_lshlrev_b32 v7, 1, v7\n"
    "v_xor_b32 v7, v7, v5\n"
    "v_lshlrev_b32 v7, 4, v7\n"
    "v_lshl_add_u32 v7, v6, 13, v7\n"
    "v_add_u32 v7, s90, v7\n"
    "v_subrev_u32 v132, 0, v7\n"
    "v_add_u32 v6, 2, v4\n"
    "v_and_b32 v7, 3, v6\n"
    "v_or_b32 v7, s87, v7\n"
    "v_lshlrev_b32 v7, 1, v7\n"
    "v_xor_b32 v7, v7, v5\n"
    "v_lshlrev_b32 v7, 4, v7\n"
    "v_lshl_add_u32 v7, v6, 13, v7\n"
    "v_add_u32 v7, s90, v7\n"
    "v_subrev_u32 v133, 1024, v7\n"
    "v_add_u32 v6, 4, v4\n"
    "v_and_b32 v7, 3, v6\n"
    "v_or_b32 v7, s87, v7\n"
    "v_lshlrev_b32 v7, 1, v7\n"
    "v_xor_b32 v7, v7, v5\n"
    "v_lshlrev_b32 v7, 4, v7\n"
    "v_lshl_add_u32 v7, v6, 13, v7\n"
    "v_add_u32 v7, s90, v7\n"
    "v_subrev_u32 v134, 2048, v7\n"
    "v_add_u32 v6, 6, v4\n"
    "v_and_b32 v7, 3, v6\n"
    "v_or_b32 v7, s87, v7\n"
    "v_lshlrev_b32 v7, 1, v7\n"
    "v_xor_b32 v7, v7, v5\n"
    "v_lshlrev_b32 v7, 4, v7\n"
    "v_lshl_add_u32 v7, v6, 13, v7\n"
    "v_add_u32 v7, s90, v7\n"
    "v_subrev_u32 v135, 3072, v7\n"
    "s_waitcnt lgkmcnt(0)\n"
    "s_lshl_b32 s87, s86, 19\n"
    "s_add_u32 s36, s8, s87\n"
    "s_addc_u32 s37, s9, 0\n"
    "s_and_b32 s37, s37, 0xffff\n"
    "s_mov_b32 s38, 524288\n"
    "s_mov_b32 s39, 0x20000\n"
    "s_bitcmp1_b32 s75, 1\n"
    "s_cselect_b32 s88, s14, s12\n"
    "s_cselect_b32 s89, s15, s13\n"
    "s_lshl_b32 s87, s74, 9\n"
    "s_add_u32 s40, s88, s87\n"
    "s_addc_u32 s41, s89, 0\n"
    "s_and_b32 s41, s41, 0xffff\n"
    "s_mov_b32 s42, 8380928\n"
    "s_mov_b32 s43, 0x20000\n"
    "s_mov_b32 m0, s63\n"
    "s_nop 0\n"
    "buffer_load_dwordx4 v128, s[36:39], s61 offen offset:0 lds\n"
    "buffer_load_dwordx4 v129, s[36:39], s61 offen offset:1024 lds\n"
    "buffer_load_dwordx4 v130, s[36:39], s61 offen offset:2048 lds\n"
    "buffer_load_dwordx4 v131, s[36:39], s61 offen offset:3072 lds\n"
    "s_add_u32 s61, s61, s70\n"
    "s_mov_b32 m0, s96\n"
    "s_nop 0\n"
    "buffer_load_dwordx4 v132, s[40:43], s62 offen offset:0 lds\n"
    "buffer_load_dwordx4 v133, s[40:43], s62 offen offset:1024 lds\n"
    "buffer_load_dwordx4 v134, s[40:43], s62 offen offset:2048 lds\n"
    "buffer_load_dwordx4 v135, s[40:43], s62 offen offset:3072 lds\n"
    "s_add_u32 s62, s62, s71\n"
    "s_add_u32 m0, s63, 0x1000\n"
    "s_nop 0\n"
    "buffer_load_dwordx4 v128, s[36:39], s61 offen offset:0 lds\n"
    "buffer_load_dwordx4 v129, s[36:39], s61 offen offset:1024 lds\n"
    "buffer_load_dwordx4 v130, s[36:39], s61 offen offset:2048 lds\n"
    "buffer_load_dwordx4 v131, s[36:39], s61 offen offset:3072 lds\n"
    "s_add_u32 s61, s61, s72\n"
    "s_add_u32 s63, s63, 0x8000\n"
    "s_cmp_ge_u32 s63, 0x28000\n"
    "s_cselect_b32 s78, 0x18000, 0\n"
    "s_sub_u32 s63, s63, s78\n"
    "s_add_u32 m0, s96, 0x4000\n"
    "s_nop 0\n"
    "buffer_load_dwordx4 v132, s[40:43], s62 offen offset:0 lds\n"
    "buffer_load_dwordx4 v133, s[40:43], s62 offen offset:1024 lds\n"
    "buffer_load_dwordx4 v134, s[40:43], s62 offen offset:2048 lds\n"
    "buffer_load_dwordx4 v135, s[40:43], s62 offen offset:3072 lds\n"
    "s_add_u32 s62, s62, s71\n"
    "s_xor_b32 s96, s96, 0x8000\n"
    "s_and_b32 s87, s75, 3\n"
    "s_lshl_b32 s87, s87, 1\n"
    "s_add_u32 s87, s87, s60\n"
    "s_lshl_b32 s87, s87, 16\n"
    "s_lshl_b32 s88, s86, 1\n"
    "s_add_u32 s88, s88, s60\n"
    "s_sub_u32 s88, s88, 2\n"
    "s_lshl_b32 s88, s88, 15\n"
    "v_lshlrev_b32 v3, 11, v5\n"
    "v_and_b32 v4, 3, v252\n"
    "v_lshlrev_b32 v4, 7, v4\n"
    "v_and_b32 v6, 3, v163\n"
    "v_lshl_add_u32 v4, v6, 13, v4\n"
    "s_movk_i32 s89, 128\n"
    "s_cmp_lt_u32 s60, 2\n"
    "s_cselect_b64 vcc, -1, 0\n"
    "s_cselect_b32 s97, s87, s88\n"
    "s_cselect_b32 s98, s89, 0x80000\n"
    "s_cselect_b32 s92, s36, s40\n"
    "s_cselect_b32 s93, s37, s41\n"
    "s_cselect_b32 s94, s38, s42\n"
    "s_mov_b32 s95, 0x20000\n"
    "v_cndmask_b32 v147, v4, v3, vcc\n"
    "s_mul_i32 s89, s98, 1\n"
    "s_add_u32 s97, s97, s89\n"
    "buffer_load_dword v254, v147, s[92:95], s97 offen offset:0\n"
    "s_add_u32 s97, s97, s98\n"
    "s_getpc_b64 s[48:49]\n"
    "CPF0_%=:\n"
    "s_add_u32 s48, s48, fused_k-CPF0_%=\n"
    "s_addc_u32 s49, s49, -1\n"
    "s_and_b32 s49, s49, 0xffff\n"
    "s_mov_b32 s50, END_%=-fused_k\n"
    "s_mov_b32 s51, 0x20000\n"
    "s_lshl_b32 vcc_lo, s75, 2\n"
    "s_add_u32 vcc_lo, vcc_lo, s60\n"
    "s_mul_i32 vcc_lo, vcc_lo, 640\n"
    "v_lshlrev_b32 v253, 3, v146\n"
    "v_and_b32 v253, 0x3ff, v253\n"
    "buffer_load_dword v254, v253, s[48:51], vcc_lo offen offset:0\n"
    "v_lshrrev_b32 v175, 1, v166\n"
    "v_xor_b32 v175, v175, v167\n"
    "v_lshlrev_b32 v175, 4, v175\n"
    "v_lshl_add_u32 v175, v166, 7, v175\n"
    "s_lshr_b32 s87, s60, 1\n"
    "s_lshl_b32 s87, s87, 14\n"
    "s_add_u32 s87, s87, 0x10000\n"
    "v_add_u32 v136, s87, v175\n"
    "v_xor_b32 v137, 64, v136\n"
    "v_bfe_u32 v11, v252, 2, 2\n"
    "v_and_b32 v12, 1, v167\n"
    "v_lshl_add_u32 v12, v12, 2, v11\n"
    "v_lshl_add_u32 v168, v167, 3, v11\n"
    "v_and_b32 v169, 3, v252\n"
    "v_lshlrev_b32 v169, 3, v169\n"
    "v_lshl_add_u32 v15, v168, 9, v169\n"
    "s_and_b32 s87, s60, 1\n"
    "s_lshl_b32 s87, s87, 8\n"
    "v_add_u32 v15, s87, v15\n"
    "v_xor_b32 v170, 0, v12\n"
    "v_lshl_add_u32 v138, v170, 5, v15\n"
    "v_xor_b32 v170, 1, v12\n"
    "v_lshl_add_u32 v139, v170, 5, v15\n"
    "v_xor_b32 v170, 2, v12\n"
    "v_lshl_add_u32 v140, v170, 5, v15\n"
    "v_xor_b32 v170, 3, v12\n"
    "v_lshl_add_u32 v141, v170, 5, v15\n"
    "v_xor_b32 v170, 4, v12\n"
    "v_lshl_add_u32 v142, v170, 5, v15\n"
    "v_xor_b32 v170, 5, v12\n"
    "v_lshl_add_u32 v143, v170, 5, v15\n"
    "v_xor_b32 v170, 6, v12\n"
    "v_lshl_add_u32 v144, v170, 5, v15\n"
    "v_xor_b32 v170, 7, v12\n"
    "v_lshl_add_u32 v145, v170, 5, v15\n"
    "s_mov_b32 s79, 0xffff0000\n"
    "s_mov_b32 s80, 0x8000\n"
    "s_mov_b32 s65, 0\n"
    "s_mov_b32 s66, 1\n"
    "s_mov_b32 s81, 0x6000\n"
    "s_waitcnt vmcnt(6)\n"
    "s_barrier\n"
    "ds_read_b128 v[176:179], v136 offset:0\n"
    "ds_read_b64_tr_b16 a[128:129], v138 offset:0\n"
    "ds_read_b64_tr_b16 a[130:131], v138 offset:2048\n"
    "ds_read_b64_tr_b16 a[132:133], v139 offset:0\n"
    "ds_read_b64_tr_b16 a[134:135], v139 offset:2048\n"
    "ds_read_b64_tr_b16 a[136:137], v140 offset:0\n"
    "ds_read_b64_tr_b16 a[138:139], v140 offset:2048\n"
    "ds_read_b64_tr_b16 a[140:141], v141 offset:0\n"
    "ds_read_b64_tr_b16 a[142:143], v141 offset:2048\n"
    "ds_read_b64_tr_b16 a[144:145], v142 offset:0\n"
    "ds_read_b64_tr_b16 a[146:147], v142 offset:2048\n"
    "ds_read_b64_tr_b16 a[148:149], v143 offset:0\n"
    "ds_read_b64_tr_b16 a[150:151], v143 offset:2048\n"
    "ds_read_b64_tr_b16 a[152:153], v144 offset:0\n"
    "ds_read_b64_tr_b16 a[154:155], v144 offset:2048\n"
    "ds_read_b64_tr_b16 a[156:157], v145 offset:0\n"
    "ds_read_b64_tr_b16 a[158:159], v145 offset:2048\n"
    "ds_read_b128 v[180:183], v136 offset:2048\n"
    "ds_read_b128 v[184:187], v136 offset:4096\n"
    "ds_read_b128 v[188:191], v136 offset:6144\n"
    "ds_read_b128 v[192:195], v136 offset:8192\n"
    "ds_read_b128 v[196:199], v136 offset:10240\n"
    "ds_read_b128 v[200:203], v136 offset:12288\n"
    "ds_read_b128 v[204:207], v136 offset:14336\n"
    "s_waitcnt lgkmcnt(2)\n"
    "s_bitcmp1_b32 s75, 1\n"
    "s_cbranch_scc1 PEELV_%=\n"
    "PEELK_%=:\n"
    "v_mfma_f32_16x16x32_bf16 v[0:3], a[128:131], v[176:179], 0\n"
    "s_add_u32 s65, s65, 1\n"
    "s_cmp_eq_u32 s65, 3\n"
    "v_mfma_f32_16x16x32_bf16 v[4:7], a[132:135], v[176:179], 0\n"
    "s_cselect_b32 s64, s79, s80\n"
    "s_cselect_b32 s65, 0, s65\n"
    "v_mfma_f32_16x16x32_bf16 v[8:11], a[136:139], v[176:179], 0\n"
    "s_waitcnt vmcnt(2)\n"
    "v_mfma_f32_16x16x32_bf16 v[12:15], a[140:143], v[176:179], 0\n"
    "s_barrier\n"
    "v_mfma_f32_16x16x32_bf16 v[16:19], a[144:147], v[176:179], 0\n"
    "ds_read_b64_tr_b16 a[160:161], v138 offset:16384\n"
    "s_mov_b32 m0, s96\n"
    "s_mul_i32 s87, s74, 0x200000\n"
    "s_add_u32 s87, s87, 0x0\n"
    "v_mfma_f32_16x16x32_bf16 v[20:23], a[148:151], v[176:179], 0\n"
    "buffer_load_dwordx4 v132, s[40:43], s62 offen offset:0 lds\n"
    "s_add_u32 s44, s4, s87\n"
    "s_addc_u32 s45, s5, 0\n"
    "v_mfma_f32_16x16x32_bf16 v[24:27], a[152:155], v[176:179], 0\n"
    "s_and_b32 s45, s45, 0xffff\n"
    "s_mov_b32 s46, 2097152\n"
    "s_mov_b32 s47, 0x20000\n"
    "v_mfma_f32_16x16x32_bf16 v[28:31], a[156:159], v[176:179], 0\n"
    "ds_read_b128 v[176:179], v137 offset:0\n"
    "s_lshr_b32 s87, s60, 1\n"
    "s_and_b32 s88, s60, 1\n"
    "v_mfma_f32_16x16x32_bf16 v[60:63], a[156:159], v[180:183], 0\n"
    "ds_read_b64_tr_b16 a[162:163], v138 offset:18432\n"
    "s_lshl_b32 s67, s86, 18\n"
    "s_lshl_b32 s89, s87, 17\n"
    "v_mfma_f32_16x16x32_bf16 v[56:59], a[152:155], v[180:183], 0\n"
    "buffer_load_dwordx4 v133, s[40:43], s62 offen offset:1024 lds\n"
    "s_add_u32 s67, s67, s89\n"
    "s_lshl_b32 s89, s88, 13\n"
    "v_mfma_f32_16x16x32_bf16 v[52:55], a[148:151], v[180:183], 0\n"
    "ds_read_b64_tr_b16 a[164:165], v139 offset:16384\n"
    "s_add_u32 s67, s67, s89\n"
    "s_movk_i32 s90, 0x800\n"
    "v_mfma_f32_16x16x32_bf16 v[48:51], a[144:147], v[180:183], 0\n"
    "buffer_load_dwordx4 v134, s[40:43], s62 offen offset:2048 lds\n"
    "s_bitcmp1_b32 s75, 1\n"
    "s_cselect_b32 s89, 0x4000, 0\n"
    "s_cselect_b32 s68, s90, 0x8000\n"
    "s_cselect_b32 s69, 0x8000, s90\n"
    "v_mfma_f32_16x16x32_bf16 v[44:47], a[140:143], v[180:183], 0\n"
    "ds_read_b64_tr_b16 a[166:167], v139 offset:18432\n"
    "v_mfma_f32_16x16x32_bf16 v[40:43], a[136:139], v[180:183], 0\n"
    "buffer_load_dwordx4 v135, s[40:43], s62 offen offset:3072 lds\n"
    "s_add_u32 s67, s67, s89\n"
    "s_lshl_b32 s76, s68, 1\n"
    "v_mfma_f32_16x16x32_bf16 v[36:39], a[132:135], v[180:183], 0\n"
    "s_waitcnt lgkmcnt(5)\n"
    "s_add_u32 s62, s62, s71\n"
    "s_add_u32 s76, s76, s67\n"
    "s_sub_u32 s52, s16, 0x1000\n"
    "s_subb_u32 s53, s17, 0\n"
    "v_mfma_f32_16x16x32_bf16 v[32:35], a[128:131], v[180:183], 0\n"
    "ds_read_b128 v[180:183], v137 offset:2048\n"
    "s_mov_b32 m0, s63\n"
    "s_sub_u32 s56, s18, 0x1000\n"
    "s_subb_u32 s57, s19, 0\n"
    "v_mfma_f32_16x16x32_bf16 v[64:67], a[128:131], v[184:187], 0\n"
    "ds_read_b64_tr_b16 a[168:169], v140 offset:16384\n"
    "s_and_b32 s53, s53, 0xffff\n"
    "s_and_b32 s57, s57, 0xffff\n"
    "v_mfma_f32_16x16x32_bf16 v[68:71], a[132:135], v[184:187], 0\n"
    "buffer_load_dwordx4 v128, s[36:39], s61 offen offset:0 lds\n"
    "s_mov_b32 s54, 1839104\n"
    "s_mov_b32 s58, 1208320\n"
    "v_mfma_f32_16x16x32_bf16 v[72:75], a[136:139], v[184:187], 0\n"
    "ds_read_b64_tr_b16 a[170:171], v140 offset:18432\n"
    "s_mov_b32 s55, 0x20000\n"
    "s_mov_b32 s59, 0x20000\n"
    "v_mfma_f32_16x16x32_bf16 v[76:79], a[140:143], v[184:187], 0\n"
    "buffer_load_dwordx4 v129, s[36:39], s61 offen offset:1024 lds\n"
    "v_lshrrev_b32 v170, 3, v252\n"
    "v_mov_b32 v233, 0x40000000\n"
    "v_mfma_f32_16x16x32_bf16 v[80:83], a[144:147], v[184:187], 0\n"
    "ds_read_b64_tr_b16 a[172:173], v141 offset:16384\n"
    "s_lshl_b32 s87, s74, 7\n"
    "s_sub_u32 s87, s87, 16\n"
    "v_mfma_f32_16x16x32_bf16 v[84:87], a[148:151], v[184:187], 0\n"
    "buffer_load_dwordx4 v130, s[36:39], s61 offen offset:2048 lds\n"
    "s_lshl_b32 s88, s60, 5\n"
    "s_add_u32 s88, s88, s87\n"
    "v_mfma_f32_16x16x32_bf16 v[88:91], a[152:155], v[184:187], 0\n"
    "buffer_load_dwordx4 v131, s[36:39], s61 offen offset:3072 lds\n"
    "v_add_u32 v174, 0, v170\n"
    "v_add_u32 v174, s88, v174\n"
    "v_mfma_f32_16x16x32_bf16 v[92:95], a[156:159], v[184:187], 0\n"
    "ds_read_b128 v[184:187], v137 offset:4096\n"
    "v_xor_b32 v231, 0, v167\n"
    "v_xor_b32 v231, v231, v171\n"
    "v_cmp_gt_i32 vcc, 0, v174\n"
    "v_mfma_f32_16x16x32_bf16 v[124:127], a[156:159], v[188:191], 0\n"
    "ds_read_b64_tr_b16 a[174:175], v141 offset:18432\n"
    "s_add_u32 s61, s61, s70\n"
    "v_lshlrev_b32 v231, 4, v231\n"
    "v_mul_i32_i24 v232, 896, v174\n"
    "v_mfma_f32_16x16x32_bf16 v[120:123], a[152:155], v[188:191], 0\n"
    "v_add_u32 v231, v231, v232\n"
    "v_add_u32 v231, 4096, v231\n"
    "v_mfma_f32_16x16x32_bf16 v[116:119], a[148:151], v[188:191], 0\n"
    "ds_read_b64_tr_b16 a[176:177], v142 offset:16384\n"
    "s_add_u32 m0, s63, 0x1000\n"
    "v_cndmask_b32 v148, v231, v233, vcc\n"
    "v_add_u32 v174, 8, v170\n"
    "v_mfma_f32_16x16x32_bf16 v[112:115], a[144:147], v[188:191], 0\n"
    "buffer_load_dwordx4 v128, s[36:39], s61 offen offset:0 lds\n"
    "v_add_u32 v174, s88, v174\n"
    "v_xor_b32 v231, 4, v167\n"
    "v_mfma_f32_16x16x32_bf16 v[108:111], a[140:143], v[188:191], 0\n"
    "ds_read_b64_tr_b16 a[178:179], v142 offset:18432\n"
    "v_xor_b32 v231, v231, v171\n"
    "v_cmp_gt_i32 vcc, 0, v174\n"
    "v_mfma_f32_16x16x32_bf16 v[104:107], a[136:139], v[188:191], 0\n"
    "buffer_load_dwordx4 v129, s[36:39], s61 offen offset:1024 lds\n"
    "v_lshlrev_b32 v231, 4, v231\n"
    "v_mul_i32_i24 v232, 896, v174\n"
    "v_mfma_f32_16x16x32_bf16 v[100:103], a[132:135], v[188:191], 0\n"
    "v_add_u32 v231, v231, v232\n"
    "v_add_u32 v231, 3072, v231\n"
    "v_mfma_f32_16x16x32_bf16 v[96:99], a[128:131], v[188:191], 0\n"
    "ds_read_b128 v[188:191], v137 offset:6144\n"
    "v_cndmask_b32 v149, v231, v233, vcc\n"
    "v_add_u32 v174, 16, v170\n"
    "v_mfma_f32_16x16x32_bf16 a[0:3], a[128:131], v[192:195], 0\n"
    "ds_read_b64_tr_b16 a[180:181], v143 offset:16384\n"
    "v_add_u32 v174, s88, v174\n"
    "v_xor_b32 v231, 0, v167\n"
    "v_xor_b32 v231, v231, v171\n"
    "v_mfma_f32_16x16x32_bf16 a[4:7], a[132:135], v[192:195], 0\n"
    "buffer_load_dwordx4 v130, s[36:39], s61 offen offset:2048 lds\n"
    "v_cmp_gt_i32 vcc, 0, v174\n"
    "v_lshlrev_b32 v231, 4, v231\n"
    "v_mfma_f32_16x16x32_bf16 a[8:11], a[136:139], v[192:195], 0\n"
    "ds_read_b64_tr_b16 a[182:183], v143 offset:18432\n"
    "v_mul_i32_i24 v232, 896, v174\n"
    "v_add_u32 v231, v231, v232\n"
    "v_mfma_f32_16x16x32_bf16 a[12:15], a[140:143], v[192:195], 0\n"
    "buffer_load_dwordx4 v131, s[36:39], s61 offen offset:3072 lds\n"
    "v_add_u32 v231, 2048, v231\n"
    "v_cndmask_b32 v150, v231, v233, vcc\n"
    "v_mfma_f32_16x16x32_bf16 a[16:19], a[144:147], v[192:195], 0\n"
    "ds_read_b64_tr_b16 a[184:185], v144 offset:16384\n"
    "s_add_u32 s61, s61, s72\n"
    "v_add_u32 v174, 24, v170\n"
    "v_add_u32 v174, s88, v174\n"
    "v_mfma_f32_16x16x32_bf16 a[20:23], a[148:151], v[192:195], 0\n"
    "s_add_u32 s63, s63, 0x8000\n"
    "v_xor_b32 v231, 4, v167\n"
    "v_xor_b32 v231, v231, v171\n"
    "v_mfma_f32_16x16x32_bf16 a[24:27], a[152:155], v[192:195], 0\n"
    "s_cmp_ge_u32 s63, 0x28000\n"
    "s_cselect_b32 s78, 0x18000, 0\n"
    "v_cmp_gt_i32 vcc, 0, v174\n"
    "v_lshlrev_b32 v231, 4, v231\n"
    "v_mfma_f32_16x16x32_bf16 a[28:31], a[156:159], v[192:195], 0\n"
    "ds_read_b128 v[192:195], v137 offset:8192\n"
    "v_mul_i32_i24 v232, 896, v174\n"
    "v_add_u32 v231, v231, v232\n"
    "v_mfma_f32_16x16x32_bf16 a[60:63], a[156:159], v[196:199], 0\n"
    "ds_read_b64_tr_b16 a[186:187], v144 offset:18432\n"
    "v_add_u32 v231, 1024, v231\n"
    "v_cndmask_b32 v151, v231, v233, vcc\n"
    "v_mfma_f32_16x16x32_bf16 a[56:59], a[152:155], v[196:199], 0\n"
    "v_add_u32 v174, 128, v170\n"
    "v_add_u32 v174, s87, v174\n"
    "v_mfma_f32_16x16x32_bf16 a[52:55], a[148:151], v[196:199], 0\n"
    "ds_read_b64_tr_b16 a[188:189], v145 offset:16384\n"
    "s_sub_u32 s63, s63, s78\n"
    "v_xor_b32 v231, 0, v167\n"
    "v_xor_b32 v231, v231, v171\n"
    "v_mfma_f32_16x16x32_bf16 a[48:51], a[144:147], v[196:199], 0\n"
    "v_cmp_gt_i32 vcc, 0, v174\n"
    "v_lshlrev_b32 v231, 4, v231\n"
    "v_mul_i32_i24 v232, 896, v174\n"
    "v_mfma_f32_16x16x32_bf16 a[44:47], a[140:143], v[196:199], 0\n"
    "ds_read_b64_tr_b16 a[190:191], v145 offset:18432\n"
    "v_add_u32 v231, v231, v232\n"
    "v_add_u32 v231, 4096, v231\n"
    "v_mfma_f32_16x16x32_bf16 a[40:43], a[136:139], v[196:199], 0\n"
    "buffer_load_dword v254, v147, s[92:95], s97 offen offset:0\n"
    "v_cndmask_b32 v234, v231, v233, vcc\n"
    "v_add_u32 v174, 136, v170\n"
    "v_mfma_f32_16x16x32_bf16 a[36:39], a[132:135], v[196:199], 0\n"
    "s_add_u32 s97, s97, s98\n"
    "v_add_u32 v174, s87, v174\n"
    "v_xor_b32 v231, 4, v167\n"
    "v_mfma_f32_16x16x32_bf16 a[32:35], a[128:131], v[196:199], 0\n"
    "ds_read_b128 v[196:199], v137 offset:10240\n"
    "v_xor_b32 v231, v231, v171\n"
    "v_cmp_gt_i32 vcc, 0, v174\n"
    "v_mfma_f32_16x16x32_bf16 a[64:67], a[128:131], v[200:203], 0\n"
    "s_add_u32 m0, s96, 0x4000\n"
    "v_lshlrev_b32 v231, 4, v231\n"
    "v_mul_i32_i24 v232, 896, v174\n"
    "v_mfma_f32_16x16x32_bf16 a[68:71], a[132:135], v[200:203], 0\n"
    "buffer_load_dwordx4 v132, s[40:43], s62 offen offset:0 lds\n"
    "v_add_u32 v231, v231, v232\n"
    "v_add_u32 v231, 3072, v231\n"
    "v_mfma_f32_16x16x32_bf16 a[72:75], a[136:139], v[200:203], 0\n"
    "v_cndmask_b32 v235, v231, v233, vcc\n"
    "v_bfe_u32 v172, v252, 1, 1\n"
    "v_mfma_f32_16x16x32_bf16 a[76:79], a[140:143], v[200:203], 0\n"
    "buffer_load_dwordx4 v133, s[40:43], s62 offen offset:1024 lds\n"
    "v_lshrrev_b32 v173, 5, v252\n"
    "v_xor_b32 v172, v172, v173\n"
    "v_mfma_f32_16x16x32_bf16 a[80:83], a[144:147], v[200:203], 0\n"
    "v_and_b32 v173, 1, v252\n"
    "v_lshl_add_u32 v172, v172, 1, v173\n"
    "v_lshlrev_b32 v172, 4, v172\n"
    "v_mfma_f32_16x16x32_bf16 a[84:87], a[148:151], v[200:203], 0\n"
    "buffer_load_dwordx4 v134, s[40:43], s62 offen offset:2048 lds\n"
    "v_mul_u32_u24 v173, 2688, v163\n"
    "v_add_u32 v172, v172, v173\n"
    "v_mfma_f32_16x16x32_bf16 a[88:91], a[152:155], v[200:203], 0\n"
    "s_lshr_b32 s89, s75, 1\n"
    "s_mul_i32 s89, s89, 56\n"
    "v_mfma_f32_16x16x32_bf16 a[92:95], a[156:159], v[200:203], 0\n"
    "ds_read_b128 v[200:203], v137 offset:12288\n"
    "s_mul_i32 s90, s60, 896\n"
    "s_add_u32 s89, s89, s90\n"
    "v_mfma_f32_16x16x32_bf16 a[124:127], a[156:159], v[204:207], 0\n"
    "buffer_load_dwordx4 v135, s[40:43], s62 offen offset:3072 lds\n"
    "v_add_u32 v172, s89, v172\n"
    "s_cmp_eq_u32 s60, 3\n"
    "s_cselect_b64 vcc, -1, 0\n"
    "v_mfma_f32_16x16x32_bf16 a[120:123], a[152:155], v[204:207], 0\n"
    "s_add_u32 s62, s62, s71\n"
    "v_add_u32 v173, 4096, v172\n"
    "v_mfma_f32_16x16x32_bf16 a[116:119], a[148:151], v[204:207], 0\n"
    "v_cndmask_b32 v152, v173, v234, vcc\n"
    "v_add_u32 v173, 46080, v172\n"
    "v_mfma_f32_16x16x32_bf16 a[112:115], a[144:147], v[204:207], 0\n"
    "s_xor_b32 s96, s96, 0x8000\n"
    "v_cndmask_b32 v153, v173, v235, vcc\n"
    "v_add_u32 v173, 88064, v172\n"
    "v_mfma_f32_16x16x32_bf16 a[108:111], a[140:143], v[204:207], 0\n"
    "v_cndmask_b32 v154, v173, v233, vcc\n"
    "v_add_u32 v173, 130048, v172\n"
    "v_mfma_f32_16x16x32_bf16 a[104:107], a[136:139], v[204:207], 0\n"
    "v_mfma_f32_16x16x32_bf16 a[100:103], a[132:135], v[204:207], 0\n"
    "v_mfma_f32_16x16x32_bf16 a[96:99], a[128:131], v[204:207], 0\n"
    "ds_read_b128 v[204:207], v137 offset:14336\n"
    "s_waitcnt lgkmcnt(2)\n"
    "v_mfma_f32_16x16x32_bf16 v[0:3], a[160:163], v[176:179], v[0:3]\n"
    "v_mfma_f32_16x16x32_bf16 v[4:7], a[164:167], v[176:179], v[4:7]\n"
    "v_mfma_f32_16x16x32_bf16 v[8:11], a[168:171], v[176:179], v[8:11]\n"
    "s_waitcnt vmcnt(13)\n"
    "v_mfma_f32_16x16x32_bf16 v[12:15], a[172:175], v[176:179], v[12:15]\n"
    "s_barrier\n"
    "v_mfma_f32_16x16x32_bf16 v[16:19], a[176:179], v[176:179], v[16:19]\n"
    "ds_read_b64_tr_b16 a[192:193], v138 offset:32768\n"
    "s_mov_b32 m0, s96\n"
    "v_cndmask_b32 v155, v173, v233, vcc\n"
    "s_lshl_b32 s87, s60, 13\n"
    "v_mfma_f32_16x16x32_bf16 v[20:23], a[180:183], v[176:179], v[20:23]\n"
    "v_add_u32 v136, s64, v136\n"
    "v_add_u32 v137, s64, v137\n"
    "v_add_u32 v156, s87, v175\n"
    "v_xor_b32 v157, 64, v156\n"
    "v_mfma_f32_16x16x32_bf16 v[24:27], a[184:187], v[176:179], v[24:27]\n"
    "ds_read_b64_tr_b16 a[194:195], v138 offset:34816\n"
    "buffer_load_dwordx4 v132, s[40:43], s62 offen offset:0 lds\n"
    "v_mov_b64 v[158:159], v[156:157]\n"
    "v_mfma_f32_16x16x32_bf16 v[28:31], a[188:191], v[176:179], v[28:31]\n"
    "buffer_load_dwordx4 v133, s[40:43], s62 offen offset:1024 lds\n"
    "v_and_b32 v171, 1, v167\n"
    "v_xor_b32 v171, 0, v171\n"
    "v_mfma_f32_16x16x32_bf16 v[60:63], a[188:191], v[180:183], v[60:63]\n"
    "ds_read_b64_tr_b16 a[196:197], v139 offset:32768\n"
    "v_lshl_add_u32 v171, v171, 5, v169\n"
    "v_lshl_add_u32 v160, v168, 6, v171\n"
    "v_and_b32 v171, 1, v167\n"
    "v_mfma_f32_16x16x32_bf16 v[56:59], a[184:187], v[180:183], v[56:59]\n"
    "buffer_load_dwordx4 v134, s[40:43], s62 offen offset:2048 lds\n"
    "v_xor_b32 v171, 1, v171\n"
    "v_lshl_add_u32 v171, v171, 5, v169\n"
    "v_mfma_f32_16x16x32_bf16 v[52:55], a[180:183], v[180:183], v[52:55]\n"
    "ds_read_b64_tr_b16 a[198:199], v139 offset:34816\n"
    "v_lshl_add_u32 v161, v168, 6, v171\n"
    "s_or_b32 s82, s74, s60\n"
    "s_lshl_b32 s83, s74, 17\n"
    "v_mfma_f32_16x16x32_bf16 v[48:51], a[176:179], v[180:183], v[48:51]\n"
    "buffer_load_dwordx4 v135, s[40:43], s62 offen offset:3072 lds\n"
    "s_lshl_b32 s87, s60, 15\n"
    "s_add_u32 s83, s83, s87\n"
    "v_mfma_f32_16x16x32_bf16 v[44:47], a[172:175], v[180:183], v[44:47]\n"
    "ds_read_b64_tr_b16 a[200:201], v140 offset:32768\n"
    "s_lshr_b32 s87, s75, 1\n"
    "s_lshl_b32 s87, s87, 10\n"
    "v_mfma_f32_16x16x32_bf16 v[40:43], a[168:171], v[180:183], v[40:43]\n"
    "s_add_u32 s62, s62, s71\n"
    "s_add_u32 s83, s83, s87\n"
    "s_mov_b32 s87, 0x3049800\n"
    "s_add_u32 s48, s4, s87\n"
    "s_addc_u32 s49, s5, 0\n"
    "v_mfma_f32_16x16x32_bf16 v[36:39], a[164:167], v[180:183], v[36:39]\n"
    "s_waitcnt lgkmcnt(5)\n"
    "ds_read_b64_tr_b16 a[202:203], v140 offset:34816\n"
    "s_and_b32 s49, s49, 0xffff\n"
    "v_mfma_f32_16x16x32_bf16 v[32:35], a[160:163], v[180:183], v[32:35]\n"
    "s_add_u32 m0, s96, 0x4000\n"
    "s_mov_b32 s50, 8\n"
    "s_mov_b32 s51, 0x20000\n"
    "v_mfma_f32_16x16x32_bf16 v[64:67], a[160:163], v[184:187], v[64:67]\n"
    "ds_read_b64_tr_b16 a[204:205], v141 offset:32768\n"
    "buffer_load_dwordx4 v132, s[40:43], s62 offen offset:0 lds\n"
    "v_mov_b32 v236, 0\n"
    "v_mfma_f32_16x16x32_bf16 v[68:71], a[164:167], v[184:187], v[68:71]\n"
    "buffer_load_dwordx2 v[164:165], v236, s[48:51], 0 offen sc1\n"
    "s_mov_b32 s84, s6\n"
    "s_mov_b32 s85, s7\n"
    "v_mfma_f32_16x16x32_bf16 v[72:75], a[168:171], v[184:187], v[72:75]\n"
    "ds_read_b64_tr_b16 a[206:207], v141 offset:34816\n"
    "buffer_load_dwordx4 v133, s[40:43], s62 offen offset:1024 lds\n"
    "s_mov_b32 s86, 1835008\n"
    "v_mfma_f32_16x16x32_bf16 v[76:79], a[172:175], v[184:187], v[76:79]\n"
    "s_mov_b32 s89, 5\n"
    "s_cmp_eq_u32 s60, 1\n"
    "s_cselect_b32 s84, s10, s84\n"
    "s_cselect_b32 s85, s11, s85\n"
    "s_cselect_b32 s86, 3670016, s86\n"
    "s_cselect_b32 s89, 10, s89\n"
    "v_mfma_f32_16x16x32_bf16 v[80:83], a[176:179], v[184:187], v[80:83]\n"
    "ds_read_b64_tr_b16 a[208:209], v142 offset:32768\n"
    "buffer_load_dwordx4 v134, s[40:43], s62 offen offset:2048 lds\n"
    "v_mfma_f32_16x16x32_bf16 v[84:87], a[180:183], v[184:187], v[84:87]\n"
    "s_cmp_eq_u32 s60, 2\n"
    "s_cselect_b32 s84, s20, s84\n"
    "s_cselect_b32 s85, s21, s85\n"
    "s_cselect_b32 s86, 3670016, s86\n"
    "s_cselect_b32 s89, 10, s89\n"
    "v_mfma_f32_16x16x32_bf16 v[88:91], a[184:187], v[184:187], v[88:91]\n"
    "ds_read_b64_tr_b16 a[210:211], v142 offset:34816\n"
    "buffer_load_dwordx4 v135, s[40:43], s62 offen offset:3072 lds\n"
    "v_mfma_f32_16x16x32_bf16 v[92:95], a[188:191], v[184:187], v[92:95]\n"
    "s_cmp_eq_u32 s60, 3\n"
    "s_cselect_b32 s84, s18, s84\n"
    "s_cselect_b32 s85, s19, s85\n"
    "s_cselect_b32 s86, 1204224, s86\n"
    "s_cselect_b32 s89, 4, s89\n"
    "v_mfma_f32_16x16x32_bf16 v[124:127], a[188:191], v[188:191], v[124:127]\n"
    "ds_read_b64_tr_b16 a[212:213], v143 offset:32768\n"
    "s_add_u32 s62, s62, s71\n"
    "v_mfma_f32_16x16x32_bf16 v[120:123], a[184:187], v[188:191], v[120:123]\n"
    "v_mfma_f32_16x16x32_bf16 v[116:119], a[180:183], v[188:191], v[116:119]\n"
    "ds_read_b64_tr_b16 a[214:215], v143 offset:34816\n"
    "s_xor_b32 s96, s96, 0x8000\n"
    "s_and_b32 s85, s85, 0xffff\n"
    "s_mov_b32 s87, 0x20000\n"
    "v_mfma_f32_16x16x32_bf16 v[112:115], a[176:179], v[188:191], v[112:115]\n"
    "buffer_load_dword v254, v147, s[92:95], s97 offen offset:0\n"
    "v_cmp_gt_u32 vcc, s89, v252\n"
    "s_lshl_b32 s89, s89, 7\n"
    "v_mfma_f32_16x16x32_bf16 v[108:111], a[172:175], v[188:191], v[108:111]\n"
    "ds_read_b64_tr_b16 a[216:217], v144 offset:32768\n"
    "s_mul_i32 s88, s89, s26\n"
    "s_lshl_b32 s89, s89, 8\n"
    "v_mfma_f32_16x16x32_bf16 v[104:107], a[168:171], v[188:191], v[104:107]\n"
    "s_add_u32 s97, s97, s98\n"
    "v_lshlrev_b32 v174, 3, v146\n"
    "v_cndmask_b32 v162, v233, v174, vcc\n"
    "v_mfma_f32_16x16x32_bf16 v[100:103], a[164:167], v[188:191], v[100:103]\n"
    "ds_read_b64_tr_b16 a[218:219], v144 offset:34816\n"
    "s_lshl_b32 s90, s75, 2\n"
    "s_add_u32 s90, s90, s60\n"
    "v_mfma_f32_16x16x32_bf16 v[96:99], a[160:163], v[188:191], v[96:99]\n"
    "s_mov_b32 m0, s63\n"
    "s_mul_i32 s90, s90, 1920\n"
    "s_lshr_b32 s91, s74, 1\n"
    "v_mfma_f32_16x16x32_bf16 a[0:3], a[160:163], v[192:195], a[0:3]\n"
    "ds_read_b64_tr_b16 a[220:221], v145 offset:32768\n"
    "s_mul_i32 s91, s91, 229376\n"
    "s_add_u32 s90, s90, s91\n"
    "s_add_u32 s90, s90, 0x1000\n"
    "v_mfma_f32_16x16x32_bf16 a[4:7], a[164:167], v[192:195], a[4:7]\n"
    "buffer_load_dwordx4 v128, s[36:39], s61 offen offset:0 lds\n"
    "s_mov_b32 s91, s88\n"
    "v_lshrrev_b32 v224, 4, v146\n"
    "v_mfma_f32_16x16x32_bf16 a[8:11], a[168:171], v[192:195], a[8:11]\n"
    "ds_read_b64_tr_b16 a[222:223], v145 offset:34816\n"
    "v_lshrrev_b32 v225, 4, v224\n"
    "v_lshrrev_b32 v226, 3, v224\n"
    "v_mfma_f32_16x16x32_bf16 a[12:15], a[172:175], v[192:195], a[12:15]\n"
    "buffer_load_dwordx4 v129, s[36:39], s61 offen offset:1024 lds\n"
    "v_and_b32 v227, 7, v224\n"
    "v_xor_b32 v227, v227, v225\n"
    "v_mfma_f32_16x16x32_bf16 a[16:19], a[176:179], v[192:195], a[16:19]\n"
    "s_waitcnt vmcnt(12)\n"
    "v_lshlrev_b32 v227, 4, v227\n"
    "v_mul_u32_u24 v228, 896, v226\n"
    "v_mfma_f32_16x16x32_bf16 a[20:23], a[180:183], v[192:195], a[20:23]\n"
    "s_barrier\n"
    "buffer_load_dwordx4 v130, s[36:39], s61 offen offset:2048 lds\n"
    "s_mul_i32 s99, s60, 28672\n"
    "v_mfma_f32_16x16x32_bf16 a[24:27], a[184:187], v[192:195], a[24:27]\n"
    "ds_read_b128 v[176:179], v136 offset:0\n"
    "buffer_load_dwordx4 v131, s[36:39], s61 offen offset:3072 lds\n"
    "v_add_u32 v225, s99, v228\n"
    "v_mfma_f32_16x16x32_bf16 a[28:31], a[188:191], v[192:195], a[28:31]\n"
    "ds_read_b128 v[180:183], v137 offset:0\n"
    "v_add_u32 v240, v225, v227\n"
    "v_xor_b32 v227, 64, v227\n"
    "v_add_u32 v227, v225, v227\n"
    "v_mfma_f32_16x16x32_bf16 a[60:63], a[188:191], v[196:199], a[60:63]\n"
    "ds_read_b128 v[184:187], v136 offset:2048\n"
    "s_add_u32 s61, s61, s70\n"
    "v_add_u32 v241, 6144, v227\n"
    "v_add_u32 v242, 12288, v240\n"
    "v_mfma_f32_16x16x32_bf16 a[56:59], a[184:187], v[196:199], a[56:59]\n"
    "ds_read_b128 v[188:191], v137 offset:2048\n"
    "v_add_u32 v243, 12288, v241\n"
    "v_mov_b64 v[244:245], v[132:133]\n"
    "v_mov_b64 v[246:247], v[134:135]\n"
    "v_mfma_f32_16x16x32_bf16 a[52:55], a[180:183], v[196:199], a[52:55]\n"
    "ds_read_b64_tr_b16 a[224:225], v138 offset:49152\n"
    "s_add_u32 m0, s63, 0x1000\n"
    "s_lshl_b32 s99, s60, 12\n"
    "v_add_u32 v250, s99, v146\n"
    "v_mfma_f32_16x16x32_bf16 a[48:51], a[176:179], v[196:199], a[48:51]\n"
    "ds_read_b64_tr_b16 a[226:227], v138 offset:51200\n"
    "v_add_u32 v251, 0x8000, v250\n"
    "s_add_u32 s99, s99, 0x4000\n"
    "v_add_u32 v248, s99, v175\n"
    "v_mfma_f32_16x16x32_bf16 a[44:47], a[172:175], v[196:199], a[44:47]\n"
    "ds_read_b64_tr_b16 a[228:229], v139 offset:49152\n"
    "buffer_load_dwordx4 v128, s[36:39], s61 offen offset:0 lds\n"
    "v_xor_b32 v249, 64, v248\n"
    "v_mfma_f32_16x16x32_bf16 a[40:43], a[168:171], v[196:199], a[40:43]\n"
    "ds_read_b64_tr_b16 a[230:231], v139 offset:51200\n"
    "v_and_b32 v208, 0xfffffeff, v138\n"
    "v_and_b32 v209, 0xfffffeff, v139\n"
    "v_and_b32 v210, 0xfffffeff, v140\n"
    "v_mfma_f32_16x16x32_bf16 a[36:39], a[164:167], v[196:199], a[36:39]\n"
    "ds_read_b64_tr_b16 a[232:233], v140 offset:49152\n"
    "buffer_load_dwordx4 v129, s[36:39], s61 offen offset:1024 lds\n"
    "v_and_b32 v211, 0xfffffeff, v141\n"
    "v_mfma_f32_16x16x32_bf16 a[32:35], a[160:163], v[196:199], a[32:35]\n"
    "ds_read_b64_tr_b16 a[234:235], v140 offset:51200\n"
    "v_and_b32 v212, 0xfffffeff, v142\n"
    "v_and_b32 v213, 0xfffffeff, v143\n"
    "v_and_b32 v214, 0xfffffeff, v144\n"
    "v_mfma_f32_16x16x32_bf16 a[64:67], a[160:163], v[200:203], a[64:67]\n"
    "ds_read_b64_tr_b16 a[236:237], v141 offset:49152\n"
    "buffer_load_dwordx4 v130, s[36:39], s61 offen offset:2048 lds\n"
    "v_and_b32 v215, 0xfffffeff, v145\n"
    "v_mfma_f32_16x16x32_bf16 a[68:71], a[164:167], v[200:203], a[68:71]\n"
    "ds_read_b64_tr_b16 a[238:239], v141 offset:51200\n"
    "buffer_load_dwordx4 v131, s[36:39], s61 offen offset:3072 lds\n"
    "v_lshrrev_b32 v225, 2, v224\n"
    "v_mfma_f32_16x16x32_bf16 a[72:75], a[168:171], v[200:203], a[72:75]\n"
    "ds_read_b64_tr_b16 a[240:241], v142 offset:49152\n"
    "v_bfe_u32 v229, v225, 1, 2\n"
    "v_and_b32 v230, 3, v224\n"
    "v_lshlrev_b32 v230, 3, v230\n"
    "v_mfma_f32_16x16x32_bf16 a[76:79], a[172:175], v[200:203], a[76:79]\n"
    "ds_read_b64_tr_b16 a[242:243], v142 offset:51200\n"
    "s_add_u32 s61, s61, s72\n"
    "v_lshl_add_u32 v230, v225, 7, v230\n"
    "v_add_u32 v230, 0x20000, v230\n"
    "v_mfma_f32_16x16x32_bf16 a[80:83], a[176:179], v[200:203], a[80:83]\n"
    "ds_read_b64_tr_b16 a[244:245], v143 offset:49152\n"
    "v_xor_b32 v227, 0, v229\n"
    "v_lshl_add_u32 v216, v227, 5, v230\n"
    "v_xor_b32 v227, 1, v229\n"
    "v_mfma_f32_16x16x32_bf16 a[84:87], a[180:183], v[200:203], a[84:87]\n"
    "ds_read_b64_tr_b16 a[246:247], v143 offset:51200\n"
    "s_add_u32 s63, s63, 0x8000\n"
    "v_lshl_add_u32 v217, v227, 5, v230\n"
    "v_xor_b32 v227, 2, v229\n"
    "v_mfma_f32_16x16x32_bf16 a[88:91], a[184:187], v[200:203], a[88:91]\n"
    "ds_read_b64_tr_b16 a[248:249], v144 offset:49152\n"
    "v_lshl_add_u32 v218, v227, 5, v230\n"
    "v_xor_b32 v227, 3, v229\n"
    "v_lshl_add_u32 v219, v227, 5, v230\n"
    "v_mfma_f32_16x16x32_bf16 a[92:95], a[188:191], v[200:203], a[92:95]\n"
    "ds_read_b64_tr_b16 a[250:251], v144 offset:51200\n"
    "s_cmp_ge_u32 s63, 0x28000\n"
    "s_cselect_b32 s78, 0x18000, 0\n"
    "v_bfe_u32 v229, v226, 1, 2\n"
    "v_mfma_f32_16x16x32_bf16 a[124:127], a[188:191], v[204:207], a[124:127]\n"
    "ds_read_b64_tr_b16 a[252:253], v145 offset:49152\n"
    "v_bfe_u32 v230, v224, 1, 2\n"
    "v_xor_b32 v229, v229, v230\n"
    "v_and_b32 v230, 1, v224\n"
    "v_mfma_f32_16x16x32_bf16 a[120:123], a[184:187], v[204:207], a[120:123]\n"
    "ds_read_b64_tr_b16 a[254:255], v145 offset:51200\n"
    "v_lshlrev_b32 v230, 4, v230\n"
    "v_lshl_add_u32 v230, v229, 5, v230\n"
    "v_mfma_f32_16x16x32_bf16 a[116:119], a[180:183], v[204:207], a[116:119]\n"
    "v_add_u32 v230, v230, v228\n"
    "s_mul_i32 s99, s60, 57344\n"
    "v_mfma_f32_16x16x32_bf16 a[112:115], a[176:179], v[204:207], a[112:115]\n"
    "s_sub_u32 s63, s63, s78\n"
    "v_add_u32 v220, s99, v230\n"
    "v_add_u32 v221, 6144, v220\n"
    "v_mfma_f32_16x16x32_bf16 a[108:111], a[172:175], v[204:207], a[108:111]\n"
    "v_add_u32 v222, 12288, v220\n"
    "v_add_u32 v223, 18432, v220\n"
    "v_mfma_f32_16x16x32_bf16 a[104:107], a[168:171], v[204:207], a[104:107]\n"
    "v_mfma_f32_16x16x32_bf16 a[100:103], a[164:167], v[204:207], a[100:103]\n"
    "v_mfma_f32_16x16x32_bf16 a[96:99], a[160:163], v[204:207], a[96:99]\n"
    "s_waitcnt lgkmcnt(0)\n"
    "s_branch MIDK_%=\n"
    "LOOPK_%=:\n"
    "s_bitcmp1_b32 s81, s66\n"
    "s_cbranch_scc1 SWLOOPK_%=\n"
    "SWBLOOPK_%=:\n"
    "v_mfma_f32_16x16x32_bf16 v[0:3], a[128:131], v[176:179], v[0:3]\n"
    "s_add_u32 s65, s65, 1\n"
    "s_cmp_eq_u32 s65, 3\n"
    "v_mfma_f32_16x16x32_bf16 v[0:3], a[160:163], v[180:183], v[0:3]\n"
    "s_cselect_b32 s64, s79, s80\n"
    "s_cselect_b32 s65, 0, s65\n"
    "v_mfma_f32_16x16x32_bf16 v[4:7], a[164:167], v[180:183], v[4:7]\n"
    "s_waitcnt vmcnt(8)\n"
    "v_mfma_f32_16x16x32_bf16 v[4:7], a[132:135], v[176:179], v[4:7]\n"
    "s_barrier\n"
    "v_mfma_f32_16x16x32_bf16 v[8:11], a[136:139], v[176:179], v[8:11]\n"
    "ds_read_b128 v[192:195], v136 offset:4096\n"
    "v_mfma_f32_16x16x32_bf16 v[8:11], a[168:171], v[180:183], v[8:11]\n"
    "buffer_load_dword v254, v147, s[92:95], s97 offen offset:0\n"
    "v_mfma_f32_16x16x32_bf16 v[12:15], a[172:175], v[180:183], v[12:15]\n"
    "ds_read_b64_tr_b16 a[192:193], v138 offset:32768\n"
    "s_add_u32 s97, s97, s98\n"
    "v_mfma_f32_16x16x32_bf16 v[12:15], a[140:143], v[176:179], v[12:15]\n"
    "buffer_load_dword v254, v162, s[84:87], s88 offen offset:0\n"
    "v_mfma_f32_16x16x32_bf16 v[16:19], a[144:147], v[176:179], v[16:19]\n"
    "ds_read_b64_tr_b16 a[194:195], v138 offset:34816\n"
    "s_add_u32 s88, s88, s89\n"
    "v_mfma_f32_16x16x32_bf16 v[16:19], a[176:179], v[180:183], v[16:19]\n"
    "s_mov_b32 m0, s96\n"
    "v_mfma_f32_16x16x32_bf16 v[20:23], a[180:183], v[180:183], v[20:23]\n"
    "ds_read_b128 v[196:199], v137 offset:4096\n"
    "v_mfma_f32_16x16x32_bf16 v[20:23], a[148:151], v[176:179], v[20:23]\n"
    "v_mfma_f32_16x16x32_bf16 v[24:27], a[152:155], v[176:179], v[24:27]\n"
    "ds_read_b64_tr_b16 a[224:225], v138 offset:49152\n"
    "v_mfma_f32_16x16x32_bf16 v[24:27], a[184:187], v[180:183], v[24:27]\n"
    "v_mfma_f32_16x16x32_bf16 v[28:31], a[188:191], v[180:183], v[28:31]\n"
    "ds_read_b64_tr_b16 a[226:227], v138 offset:51200\n"
    "v_mfma_f32_16x16x32_bf16 v[28:31], a[156:159], v[176:179], v[28:31]\n"
    "s_waitcnt lgkmcnt(6)\n"
    "v_mfma_f32_16x16x32_bf16 v[60:63], a[156:159], v[184:187], v[60:63]\n"
    "ds_read_b64_tr_b16 a[196:197], v139 offset:32768\n"
    "v_mfma_f32_16x16x32_bf16 v[60:63], a[188:191], v[188:191], v[60:63]\n"
    "buffer_load_dwordx4 v132, s[40:43], s62 offen offset:0 lds\n"
    "v_mfma_f32_16x16x32_bf16 v[56:59], a[184:187], v[188:191], v[56:59]\n"
    "ds_read_b64_tr_b16 a[198:199], v139 offset:34816\n"
    "v_mfma_f32_16x16x32_bf16 v[56:59], a[152:155], v[184:187], v[56:59]\n"
    "v_mfma_f32_16x16x32_bf16 v[52:55], a[148:151], v[184:187], v[52:55]\n"
    "ds_read_b128 v[200:203], v136 offset:6144\n"
    "v_mfma_f32_16x16x32_bf16 v[52:55], a[180:183], v[188:191], v[52:55]\n"
    "v_mfma_f32_16x16x32_bf16 v[48:51], a[176:179], v[188:191], v[48:51]\n"
    "ds_read_b64_tr_b16 a[228:229], v139 offset:49152\n"
    "v_mfma_f32_16x16x32_bf16 v[48:51], a[144:147], v[184:187], v[48:51]\n"
    "v_mfma_f32_16x16x32_bf16 v[44:47], a[140:143], v[184:187], v[44:47]\n"
    "ds_read_b64_tr_b16 a[230:231], v139 offset:51200\n"
    "v_mfma_f32_16x16x32_bf16 v[44:47], a[172:175], v[188:191], v[44:47]\n"
    "v_mfma_f32_16x16x32_bf16 v[40:43], a[168:171], v[188:191], v[40:43]\n"
    "ds_read_b128 v[204:207], v137 offset:6144\n"
    "v_mfma_f32_16x16x32_bf16 v[40:43], a[136:139], v[184:187], v[40:43]\n"
    "v_mfma_f32_16x16x32_bf16 v[36:39], a[132:135], v[184:187], v[36:39]\n"
    "ds_read_b64_tr_b16 a[200:201], v140 offset:32768\n"
    "v_mfma_f32_16x16x32_bf16 v[36:39], a[164:167], v[188:191], v[36:39]\n"
    "buffer_load_dwordx4 v133, s[40:43], s62 offen offset:1024 lds\n"
    "v_mfma_f32_16x16x32_bf16 v[32:35], a[160:163], v[188:191], v[32:35]\n"
    "ds_read_b64_tr_b16 a[202:203], v140 offset:34816\n"
    "v_mfma_f32_16x16x32_bf16 v[32:35], a[128:131], v[184:187], v[32:35]\n"
    "s_waitcnt lgkmcnt(10)\n"
    "v_mfma_f32_16x16x32_bf16 v[64:67], a[128:131], v[192:195], v[64:67]\n"
    "ds_read_b64_tr_b16 a[232:233], v140 offset:49152\n"
    "v_mfma_f32_16x16x32_bf16 v[64:67], a[160:163], v[196:199], v[64:67]\n"
    "v_mfma_f32_16x16x32_bf16 v[68:71], a[164:167], v[196:199], v[68:71]\n"
    "ds_read_b64_tr_b16 a[234:235], v140 offset:51200\n"
    "v_mfma_f32_16x16x32_bf16 v[68:71], a[132:135], v[192:195], v[68:71]\n"
    "v_mfma_f32_16x16x32_bf16 v[72:75], a[136:139], v[192:195], v[72:75]\n"
    "ds_read_b128 v[176:179], v136 offset:8192\n"
    "v_mfma_f32_16x16x32_bf16 v[72:75], a[168:171], v[196:199], v[72:75]\n"
    "v_mfma_f32_16x16x32_bf16 v[76:79], a[172:175], v[196:199], v[76:79]\n"
    "ds_read_b64_tr_b16 a[204:205], v141 offset:32768\n"
    "v_mfma_f32_16x16x32_bf16 v[76:79], a[140:143], v[192:195], v[76:79]\n"
    "v_mfma_f32_16x16x32_bf16 v[80:83], a[144:147], v[192:195], v[80:83]\n"
    "ds_read_b64_tr_b16 a[206:207], v141 offset:34816\n"
    "v_mfma_f32_16x16x32_bf16 v[80:83], a[176:179], v[196:199], v[80:83]\n"
    "buffer_load_dwordx4 v134, s[40:43], s62 offen offset:2048 lds\n"
    "v_mfma_f32_16x16x32_bf16 v[84:87], a[180:183], v[196:199], v[84:87]\n"
    "ds_read_b128 v[180:183], v137 offset:8192\n"
    "v_mfma_f32_16x16x32_bf16 v[84:87], a[148:151], v[192:195], v[84:87]\n"
    "v_mfma_f32_16x16x32_bf16 v[88:91], a[152:155], v[192:195], v[88:91]\n"
    "ds_read_b64_tr_b16 a[236:237], v141 offset:49152\n"
    "v_mfma_f32_16x16x32_bf16 v[88:91], a[184:187], v[196:199], v[88:91]\n"
    "v_mfma_f32_16x16x32_bf16 v[92:95], a[188:191], v[196:199], v[92:95]\n"
    "ds_read_b64_tr_b16 a[238:239], v141 offset:51200\n"
    "v_mfma_f32_16x16x32_bf16 v[92:95], a[156:159], v[192:195], v[92:95]\n"
    "s_waitcnt lgkmcnt(10)\n"
    "v_mfma_f32_16x16x32_bf16 v[124:127], a[156:159], v[200:203], v[124:127]\n"
    "ds_read_b64_tr_b16 a[208:209], v142 offset:32768\n"
    "v_mfma_f32_16x16x32_bf16 v[124:127], a[188:191], v[204:207], v[124:127]\n"
    "v_mfma_f32_16x16x32_bf16 v[120:123], a[184:187], v[204:207], v[120:123]\n"
    "ds_read_b64_tr_b16 a[210:211], v142 offset:34816\n"
    "v_mfma_f32_16x16x32_bf16 v[120:123], a[152:155], v[200:203], v[120:123]\n"
    "v_mfma_f32_16x16x32_bf16 v[116:119], a[148:151], v[200:203], v[116:119]\n"
    "ds_read_b128 v[184:187], v136 offset:10240\n"
    "v_mfma_f32_16x16x32_bf16 v[116:119], a[180:183], v[204:207], v[116:119]\n"
    "buffer_load_dwordx4 v135, s[40:43], s62 offen offset:3072 lds\n"
    "v_mfma_f32_16x16x32_bf16 v[112:115], a[176:179], v[204:207], v[112:115]\n"
    "ds_read_b64_tr_b16 a[240:241], v142 offset:49152\n"
    "s_add_u32 s62, s62, s71\n"
    "v_mfma_f32_16x16x32_bf16 v[112:115], a[144:147], v[200:203], v[112:115]\n"
    "s_add_u32 m0, s96, 0x4000\n"
    "v_mfma_f32_16x16x32_bf16 v[108:111], a[140:143], v[200:203], v[108:111]\n"
    "ds_read_b64_tr_b16 a[242:243], v142 offset:51200\n"
    "v_mfma_f32_16x16x32_bf16 v[108:111], a[172:175], v[204:207], v[108:111]\n"
    "v_mfma_f32_16x16x32_bf16 v[104:107], a[168:171], v[204:207], v[104:107]\n"
    "ds_read_b128 v[188:191], v137 offset:10240\n"
    "v_mfma_f32_16x16x32_bf16 v[104:107], a[136:139], v[200:203], v[104:107]\n"
    "v_mfma_f32_16x16x32_bf16 v[100:103], a[132:135], v[200:203], v[100:103]\n"
    "ds_read_b64_tr_b16 a[212:213], v143 offset:32768\n"
    "v_mfma_f32_16x16x32_bf16 v[100:103], a[164:167], v[204:207], v[100:103]\n"
    "v_mfma_f32_16x16x32_bf16 v[96:99], a[160:163], v[204:207], v[96:99]\n"
    "ds_read_b64_tr_b16 a[214:215], v143 offset:34816\n"
    "v_mfma_f32_16x16x32_bf16 v[96:99], a[128:131], v[200:203], v[96:99]\n"
    "s_waitcnt lgkmcnt(10)\n"
    "v_mfma_f32_16x16x32_bf16 a[0:3], a[128:131], v[176:179], a[0:3]\n"
    "ds_read_b64_tr_b16 a[244:245], v143 offset:49152\n"
    "v_mfma_f32_16x16x32_bf16 a[0:3], a[160:163], v[180:183], a[0:3]\n"
    "buffer_load_dwordx4 v132, s[40:43], s62 offen offset:0 lds\n"
    "v_mfma_f32_16x16x32_bf16 a[4:7], a[164:167], v[180:183], a[4:7]\n"
    "ds_read_b64_tr_b16 a[246:247], v143 offset:51200\n"
    "v_mfma_f32_16x16x32_bf16 a[4:7], a[132:135], v[176:179], a[4:7]\n"
    "v_mfma_f32_16x16x32_bf16 a[8:11], a[136:139], v[176:179], a[8:11]\n"
    "ds_read_b128 v[192:195], v136 offset:12288\n"
    "v_mfma_f32_16x16x32_bf16 a[8:11], a[168:171], v[180:183], a[8:11]\n"
    "v_mfma_f32_16x16x32_bf16 a[12:15], a[172:175], v[180:183], a[12:15]\n"
    "ds_read_b64_tr_b16 a[216:217], v144 offset:32768\n"
    "v_mfma_f32_16x16x32_bf16 a[12:15], a[140:143], v[176:179], a[12:15]\n"
    "v_mfma_f32_16x16x32_bf16 a[16:19], a[144:147], v[176:179], a[16:19]\n"
    "ds_read_b64_tr_b16 a[218:219], v144 offset:34816\n"
    "v_mfma_f32_16x16x32_bf16 a[16:19], a[176:179], v[180:183], a[16:19]\n"
    "buffer_load_dwordx4 v133, s[40:43], s62 offen offset:1024 lds\n"
    "v_mfma_f32_16x16x32_bf16 a[20:23], a[180:183], v[180:183], a[20:23]\n"
    "ds_read_b128 v[196:199], v137 offset:12288\n"
    "v_mfma_f32_16x16x32_bf16 a[20:23], a[148:151], v[176:179], a[20:23]\n"
    "v_mfma_f32_16x16x32_bf16 a[24:27], a[152:155], v[176:179], a[24:27]\n"
    "ds_read_b64_tr_b16 a[248:249], v144 offset:49152\n"
    "v_mfma_f32_16x16x32_bf16 a[24:27], a[184:187], v[180:183], a[24:27]\n"
    "v_mfma_f32_16x16x32_bf16 a[28:31], a[188:191], v[180:183], a[28:31]\n"
    "ds_read_b64_tr_b16 a[250:251], v144 offset:51200\n"
    "v_mfma_f32_16x16x32_bf16 a[28:31], a[156:159], v[176:179], a[28:31]\n"
    "s_waitcnt lgkmcnt(10)\n"
    "v_mfma_f32_16x16x32_bf16 a[60:63], a[156:159], v[184:187], a[60:63]\n"
    "ds_read_b64_tr_b16 a[220:221], v145 offset:32768\n"
    "v_mfma_f32_16x16x32_bf16 a[60:63], a[188:191], v[188:191], a[60:63]\n"
    "buffer_load_dwordx4 v134, s[40:43], s62 offen offset:2048 lds\n"
    "v_mfma_f32_16x16x32_bf16 a[56:59], a[184:187], v[188:191], a[56:59]\n"
    "ds_read_b64_tr_b16 a[222:223], v145 offset:34816\n"
    "v_mfma_f32_16x16x32_bf16 a[56:59], a[152:155], v[184:187], a[56:59]\n"
    "v_mfma_f32_16x16x32_bf16 a[52:55], a[148:151], v[184:187], a[52:55]\n"
    "ds_read_b128 v[200:203], v136 offset:14336\n"
    "v_mfma_f32_16x16x32_bf16 a[52:55], a[180:183], v[188:191], a[52:55]\n"
    "buffer_load_dwordx4 v135, s[40:43], s62 offen offset:3072 lds\n"
    "v_mfma_f32_16x16x32_bf16 a[48:51], a[176:179], v[188:191], a[48:51]\n"
    "ds_read_b64_tr_b16 a[252:253], v145 offset:49152\n"
    "s_add_u32 s62, s62, s71\n"
    "v_mfma_f32_16x16x32_bf16 a[48:51], a[144:147], v[184:187], a[48:51]\n"
    "s_xor_b32 s96, s96, 0x8000\n"
    "s_mov_b32 m0, s63\n"
    "v_mfma_f32_16x16x32_bf16 a[44:47], a[140:143], v[184:187], a[44:47]\n"
    "ds_read_b64_tr_b16 a[254:255], v145 offset:51200\n"
    "v_mfma_f32_16x16x32_bf16 a[44:47], a[172:175], v[188:191], a[44:47]\n"
    "v_mfma_f32_16x16x32_bf16 a[40:43], a[168:171], v[188:191], a[40:43]\n"
    "ds_read_b128 v[204:207], v137 offset:14336\n"
    "v_mfma_f32_16x16x32_bf16 a[40:43], a[136:139], v[184:187], a[40:43]\n"
    "buffer_load_dwordx4 v128, s[36:39], s61 offen offset:0 lds\n"
    "v_mfma_f32_16x16x32_bf16 a[36:39], a[132:135], v[184:187], a[36:39]\n"
    "v_add_u32 v136, s64, v136\n"
    "v_add_u32 v137, s64, v137\n"
    "v_mfma_f32_16x16x32_bf16 a[36:39], a[164:167], v[188:191], a[36:39]\n"
    "s_waitcnt vmcnt(11)\n"
    "v_mfma_f32_16x16x32_bf16 a[32:35], a[160:163], v[188:191], a[32:35]\n"
    "s_barrier\n"
    "v_mfma_f32_16x16x32_bf16 a[32:35], a[128:131], v[184:187], a[32:35]\n"
    "s_waitcnt lgkmcnt(8)\n"
    "v_mfma_f32_16x16x32_bf16 a[64:67], a[128:131], v[192:195], a[64:67]\n"
    "v_mfma_f32_16x16x32_bf16 a[64:67], a[160:163], v[196:199], a[64:67]\n"
    "buffer_load_dwordx4 v129, s[36:39], s61 offen offset:1024 lds\n"
    "v_mfma_f32_16x16x32_bf16 a[68:71], a[164:167], v[196:199], a[68:71]\n"
    "v_mfma_f32_16x16x32_bf16 a[68:71], a[132:135], v[192:195], a[68:71]\n"
    "v_mfma_f32_16x16x32_bf16 a[72:75], a[136:139], v[192:195], a[72:75]\n"
    "ds_read_b128 v[176:179], v136 offset:0\n"
    "v_mfma_f32_16x16x32_bf16 a[72:75], a[168:171], v[196:199], a[72:75]\n"
    "buffer_load_dwordx4 v130, s[36:39], s61 offen offset:2048 lds\n"
    "v_mfma_f32_16x16x32_bf16 a[76:79], a[172:175], v[196:199], a[76:79]\n"
    "v_mfma_f32_16x16x32_bf16 a[76:79], a[140:143], v[192:195], a[76:79]\n"
    "v_mfma_f32_16x16x32_bf16 a[80:83], a[144:147], v[192:195], a[80:83]\n"
    "v_mfma_f32_16x16x32_bf16 a[80:83], a[176:179], v[196:199], a[80:83]\n"
    "buffer_load_dwordx4 v131, s[36:39], s61 offen offset:3072 lds\n"
    "v_mfma_f32_16x16x32_bf16 a[84:87], a[180:183], v[196:199], a[84:87]\n"
    "ds_read_b128 v[180:183], v137 offset:0\n"
    "s_add_u32 s61, s61, s70\n"
    "v_mfma_f32_16x16x32_bf16 a[84:87], a[148:151], v[192:195], a[84:87]\n"
    "s_add_u32 m0, s63, 0x1000\n"
    "v_mfma_f32_16x16x32_bf16 a[88:91], a[152:155], v[192:195], a[88:91]\n"
    "v_mfma_f32_16x16x32_bf16 a[88:91], a[184:187], v[196:199], a[88:91]\n"
    "buffer_load_dwordx4 v128, s[36:39], s61 offen offset:0 lds\n"
    "v_mfma_f32_16x16x32_bf16 a[92:95], a[188:191], v[196:199], a[92:95]\n"
    "v_mfma_f32_16x16x32_bf16 a[92:95], a[156:159], v[192:195], a[92:95]\n"
    "s_waitcnt lgkmcnt(2)\n"
    "v_mfma_f32_16x16x32_bf16 a[124:127], a[156:159], v[200:203], a[124:127]\n"
    "v_mfma_f32_16x16x32_bf16 a[124:127], a[188:191], v[204:207], a[124:127]\n"
    "buffer_load_dwordx4 v129, s[36:39], s61 offen offset:1024 lds\n"
    "v_mfma_f32_16x16x32_bf16 a[120:123], a[184:187], v[204:207], a[120:123]\n"
    "v_mfma_f32_16x16x32_bf16 a[120:123], a[152:155], v[200:203], a[120:123]\n"
    "v_mfma_f32_16x16x32_bf16 a[116:119], a[148:151], v[200:203], a[116:119]\n"
    "ds_read_b128 v[184:187], v136 offset:2048\n"
    "v_mfma_f32_16x16x32_bf16 a[116:119], a[180:183], v[204:207], a[116:119]\n"
    "buffer_load_dwordx4 v130, s[36:39], s61 offen offset:2048 lds\n"
    "v_mfma_f32_16x16x32_bf16 a[112:115], a[176:179], v[204:207], a[112:115]\n"
    "v_mfma_f32_16x16x32_bf16 a[112:115], a[144:147], v[200:203], a[112:115]\n"
    "v_mfma_f32_16x16x32_bf16 a[108:111], a[140:143], v[200:203], a[108:111]\n"
    "v_mfma_f32_16x16x32_bf16 a[108:111], a[172:175], v[204:207], a[108:111]\n"
    "buffer_load_dwordx4 v131, s[36:39], s61 offen offset:3072 lds\n"
    "v_mfma_f32_16x16x32_bf16 a[104:107], a[168:171], v[204:207], a[104:107]\n"
    "ds_read_b128 v[188:191], v137 offset:2048\n"
    "s_add_u32 s61, s61, s72\n"
    "v_mfma_f32_16x16x32_bf16 a[104:107], a[136:139], v[200:203], a[104:107]\n"
    "s_add_u32 s63, s63, 0x8000\n"
    "s_cmp_ge_u32 s63, 0x28000\n"
    "s_cselect_b32 s78, 0x18000, 0\n"
    "v_mfma_f32_16x16x32_bf16 a[100:103], a[132:135], v[200:203], a[100:103]\n"
    "s_sub_u32 s63, s63, s78\n"
    "v_mfma_f32_16x16x32_bf16 a[100:103], a[164:167], v[204:207], a[100:103]\n"
    "s_add_u32 s66, s66, 1\n"
    "v_mfma_f32_16x16x32_bf16 a[96:99], a[160:163], v[204:207], a[96:99]\n"
    "s_waitcnt lgkmcnt(2)\n"
    "v_mfma_f32_16x16x32_bf16 a[96:99], a[128:131], v[200:203], a[96:99]\n"
    "MIDK_%=:\n"
    "s_bitcmp1_b32 s81, s66\n"
    "s_cbranch_scc1 SWMIDK_%=\n"
    "SWBMIDK_%=:\n"
    "v_mfma_f32_16x16x32_bf16 v[0:3], a[192:195], v[176:179], v[0:3]\n"
    "s_add_u32 s65, s65, 1\n"
    "s_cmp_eq_u32 s65, 3\n"
    "v_mfma_f32_16x16x32_bf16 v[0:3], a[224:227], v[180:183], v[0:3]\n"
    "s_cselect_b32 s64, s79, s80\n"
    "s_cselect_b32 s65, 0, s65\n"
    "v_mfma_f32_16x16x32_bf16 v[4:7], a[228:231], v[180:183], v[4:7]\n"
    "s_waitcnt vmcnt(8)\n"
    "v_mfma_f32_16x16x32_bf16 v[4:7], a[196:199], v[176:179], v[4:7]\n"
    "s_barrier\n"
    "v_mfma_f32_16x16x32_bf16 v[8:11], a[200:203], v[176:179], v[8:11]\n"
    "ds_read_b128 v[192:195], v136 offset:4096\n"
    "v_mfma_f32_16x16x32_bf16 v[8:11], a[232:235], v[180:183], v[8:11]\n"
    "buffer_load_dword v254, v147, s[92:95], s97 offen offset:0\n"
    "v_mfma_f32_16x16x32_bf16 v[12:15], a[236:239], v[180:183], v[12:15]\n"
    "ds_read_b64_tr_b16 a[128:129], v138 offset:0\n"
    "s_add_u32 s97, s97, s98\n"
    "v_mfma_f32_16x16x32_bf16 v[12:15], a[204:207], v[176:179], v[12:15]\n"
    "buffer_load_dword v254, v162, s[84:87], s88 offen offset:0\n"
    "v_mfma_f32_16x16x32_bf16 v[16:19], a[208:211], v[176:179], v[16:19]\n"
    "ds_read_b64_tr_b16 a[130:131], v138 offset:2048\n"
    "s_add_u32 s88, s88, s89\n"
    "v_mfma_f32_16x16x32_bf16 v[16:19], a[240:243], v[180:183], v[16:19]\n"
    "s_mov_b32 m0, s96\n"
    "v_mfma_f32_16x16x32_bf16 v[20:23], a[244:247], v[180:183], v[20:23]\n"
    "ds_read_b128 v[196:199], v137 offset:4096\n"
    "v_mfma_f32_16x16x32_bf16 v[20:23], a[212:215], v[176:179], v[20:23]\n"
    "v_mfma_f32_16x16x32_bf16 v[24:27], a[216:219], v[176:179], v[24:27]\n"
    "ds_read_b64_tr_b16 a[160:161], v138 offset:16384\n"
    "v_mfma_f32_16x16x32_bf16 v[24:27], a[248:251], v[180:183], v[24:27]\n"
    "v_mfma_f32_16x16x32_bf16 v[28:31], a[252:255], v[180:183], v[28:31]\n"
    "ds_read_b64_tr_b16 a[162:163], v138 offset:18432\n"
    "v_mfma_f32_16x16x32_bf16 v[28:31], a[220:223], v[176:179], v[28:31]\n"
    "s_waitcnt lgkmcnt(6)\n"
    "v_mfma_f32_16x16x32_bf16 v[60:63], a[220:223], v[184:187], v[60:63]\n"
    "ds_read_b64_tr_b16 a[132:133], v139 offset:0\n"
    "v_mfma_f32_16x16x32_bf16 v[60:63], a[252:255], v[188:191], v[60:63]\n"
    "buffer_load_dwordx4 v132, s[40:43], s62 offen offset:0 lds\n"
    "v_mfma_f32_16x16x32_bf16 v[56:59], a[248:251], v[188:191], v[56:59]\n"
    "ds_read_b64_tr_b16 a[134:135], v139 offset:2048\n"
    "v_mfma_f32_16x16x32_bf16 v[56:59], a[216:219], v[184:187], v[56:59]\n"
    "v_mfma_f32_16x16x32_bf16 v[52:55], a[212:215], v[184:187], v[52:55]\n"
    "ds_read_b128 v[200:203], v136 offset:6144\n"
    "v_mfma_f32_16x16x32_bf16 v[52:55], a[244:247], v[188:191], v[52:55]\n"
    "v_mfma_f32_16x16x32_bf16 v[48:51], a[240:243], v[188:191], v[48:51]\n"
    "ds_read_b64_tr_b16 a[164:165], v139 offset:16384\n"
    "v_mfma_f32_16x16x32_bf16 v[48:51], a[208:211], v[184:187], v[48:51]\n"
    "v_mfma_f32_16x16x32_bf16 v[44:47], a[204:207], v[184:187], v[44:47]\n"
    "ds_read_b64_tr_b16 a[166:167], v139 offset:18432\n"
    "v_mfma_f32_16x16x32_bf16 v[44:47], a[236:239], v[188:191], v[44:47]\n"
    "v_mfma_f32_16x16x32_bf16 v[40:43], a[232:235], v[188:191], v[40:43]\n"
    "ds_read_b128 v[204:207], v137 offset:6144\n"
    "v_mfma_f32_16x16x32_bf16 v[40:43], a[200:203], v[184:187], v[40:43]\n"
    "v_mfma_f32_16x16x32_bf16 v[36:39], a[196:199], v[184:187], v[36:39]\n"
    "ds_read_b64_tr_b16 a[136:137], v140 offset:0\n"
    "v_mfma_f32_16x16x32_bf16 v[36:39], a[228:231], v[188:191], v[36:39]\n"
    "buffer_load_dwordx4 v133, s[40:43], s62 offen offset:1024 lds\n"
    "v_mfma_f32_16x16x32_bf16 v[32:35], a[224:227], v[188:191], v[32:35]\n"
    "ds_read_b64_tr_b16 a[138:139], v140 offset:2048\n"
    "v_mfma_f32_16x16x32_bf16 v[32:35], a[192:195], v[184:187], v[32:35]\n"
    "s_waitcnt lgkmcnt(10)\n"
    "v_mfma_f32_16x16x32_bf16 v[64:67], a[192:195], v[192:195], v[64:67]\n"
    "ds_read_b64_tr_b16 a[168:169], v140 offset:16384\n"
    "v_mfma_f32_16x16x32_bf16 v[64:67], a[224:227], v[196:199], v[64:67]\n"
    "v_mfma_f32_16x16x32_bf16 v[68:71], a[228:231], v[196:199], v[68:71]\n"
    "ds_read_b64_tr_b16 a[170:171], v140 offset:18432\n"
    "v_mfma_f32_16x16x32_bf16 v[68:71], a[196:199], v[192:195], v[68:71]\n"
    "v_mfma_f32_16x16x32_bf16 v[72:75], a[200:203], v[192:195], v[72:75]\n"
    "ds_read_b128 v[176:179], v136 offset:8192\n"
    "v_mfma_f32_16x16x32_bf16 v[72:75], a[232:235], v[196:199], v[72:75]\n"
    "v_mfma_f32_16x16x32_bf16 v[76:79], a[236:239], v[196:199], v[76:79]\n"
    "ds_read_b64_tr_b16 a[140:141], v141 offset:0\n"
    "v_mfma_f32_16x16x32_bf16 v[76:79], a[204:207], v[192:195], v[76:79]\n"
    "v_mfma_f32_16x16x32_bf16 v[80:83], a[208:211], v[192:195], v[80:83]\n"
    "ds_read_b64_tr_b16 a[142:143], v141 offset:2048\n"
    "v_mfma_f32_16x16x32_bf16 v[80:83], a[240:243], v[196:199], v[80:83]\n"
    "buffer_load_dwordx4 v134, s[40:43], s62 offen offset:2048 lds\n"
    "v_mfma_f32_16x16x32_bf16 v[84:87], a[244:247], v[196:199], v[84:87]\n"
    "ds_read_b128 v[180:183], v137 offset:8192\n"
    "v_mfma_f32_16x16x32_bf16 v[84:87], a[212:215], v[192:195], v[84:87]\n"
    "v_mfma_f32_16x16x32_bf16 v[88:91], a[216:219], v[192:195], v[88:91]\n"
    "ds_read_b64_tr_b16 a[172:173], v141 offset:16384\n"
    "v_mfma_f32_16x16x32_bf16 v[88:91], a[248:251], v[196:199], v[88:91]\n"
    "v_mfma_f32_16x16x32_bf16 v[92:95], a[252:255], v[196:199], v[92:95]\n"
    "ds_read_b64_tr_b16 a[174:175], v141 offset:18432\n"
    "v_mfma_f32_16x16x32_bf16 v[92:95], a[220:223], v[192:195], v[92:95]\n"
    "s_waitcnt lgkmcnt(10)\n"
    "v_mfma_f32_16x16x32_bf16 v[124:127], a[220:223], v[200:203], v[124:127]\n"
    "ds_read_b64_tr_b16 a[144:145], v142 offset:0\n"
    "v_mfma_f32_16x16x32_bf16 v[124:127], a[252:255], v[204:207], v[124:127]\n"
    "v_mfma_f32_16x16x32_bf16 v[120:123], a[248:251], v[204:207], v[120:123]\n"
    "ds_read_b64_tr_b16 a[146:147], v142 offset:2048\n"
    "v_mfma_f32_16x16x32_bf16 v[120:123], a[216:219], v[200:203], v[120:123]\n"
    "v_mfma_f32_16x16x32_bf16 v[116:119], a[212:215], v[200:203], v[116:119]\n"
    "ds_read_b128 v[184:187], v136 offset:10240\n"
    "v_mfma_f32_16x16x32_bf16 v[116:119], a[244:247], v[204:207], v[116:119]\n"
    "buffer_load_dwordx4 v135, s[40:43], s62 offen offset:3072 lds\n"
    "v_mfma_f32_16x16x32_bf16 v[112:115], a[240:243], v[204:207], v[112:115]\n"
    "ds_read_b64_tr_b16 a[176:177], v142 offset:16384\n"
    "s_add_u32 s62, s62, s71\n"
    "v_mfma_f32_16x16x32_bf16 v[112:115], a[208:211], v[200:203], v[112:115]\n"
    "s_add_u32 m0, s96, 0x4000\n"
    "v_mfma_f32_16x16x32_bf16 v[108:111], a[204:207], v[200:203], v[108:111]\n"
    "ds_read_b64_tr_b16 a[178:179], v142 offset:18432\n"
    "v_mfma_f32_16x16x32_bf16 v[108:111], a[236:239], v[204:207], v[108:111]\n"
    "v_mfma_f32_16x16x32_bf16 v[104:107], a[232:235], v[204:207], v[104:107]\n"
    "ds_read_b128 v[188:191], v137 offset:10240\n"
    "v_mfma_f32_16x16x32_bf16 v[104:107], a[200:203], v[200:203], v[104:107]\n"
    "v_mfma_f32_16x16x32_bf16 v[100:103], a[196:199], v[200:203], v[100:103]\n"
    "ds_read_b64_tr_b16 a[148:149], v143 offset:0\n"
    "v_mfma_f32_16x16x32_bf16 v[100:103], a[228:231], v[204:207], v[100:103]\n"
    "v_mfma_f32_16x16x32_bf16 v[96:99], a[224:227], v[204:207], v[96:99]\n"
    "ds_read_b64_tr_b16 a[150:151], v143 offset:2048\n"
    "v_mfma_f32_16x16x32_bf16 v[96:99], a[192:195], v[200:203], v[96:99]\n"
    "s_waitcnt lgkmcnt(10)\n"
    "v_mfma_f32_16x16x32_bf16 a[0:3], a[192:195], v[176:179], a[0:3]\n"
    "ds_read_b64_tr_b16 a[180:181], v143 offset:16384\n"
    "v_mfma_f32_16x16x32_bf16 a[0:3], a[224:227], v[180:183], a[0:3]\n"
    "buffer_load_dwordx4 v132, s[40:43], s62 offen offset:0 lds\n"
    "v_mfma_f32_16x16x32_bf16 a[4:7], a[228:231], v[180:183], a[4:7]\n"
    "ds_read_b64_tr_b16 a[182:183], v143 offset:18432\n"
    "v_mfma_f32_16x16x32_bf16 a[4:7], a[196:199], v[176:179], a[4:7]\n"
    "v_mfma_f32_16x16x32_bf16 a[8:11], a[200:203], v[176:179], a[8:11]\n"
    "ds_read_b128 v[192:195], v136 offset:12288\n"
    "v_mfma_f32_16x16x32_bf16 a[8:11], a[232:235], v[180:183], a[8:11]\n"
    "v_mfma_f32_16x16x32_bf16 a[12:15], a[236:239], v[180:183], a[12:15]\n"
    "ds_read_b64_tr_b16 a[152:153], v144 offset:0\n"
    "v_mfma_f32_16x16x32_bf16 a[12:15], a[204:207], v[176:179], a[12:15]\n"
    "v_mfma_f32_16x16x32_bf16 a[16:19], a[208:211], v[176:179], a[16:19]\n"
    "ds_read_b64_tr_b16 a[154:155], v144 offset:2048\n"
    "v_mfma_f32_16x16x32_bf16 a[16:19], a[240:243], v[180:183], a[16:19]\n"
    "buffer_load_dwordx4 v133, s[40:43], s62 offen offset:1024 lds\n"
    "v_mfma_f32_16x16x32_bf16 a[20:23], a[244:247], v[180:183], a[20:23]\n"
    "ds_read_b128 v[196:199], v137 offset:12288\n"
    "v_mfma_f32_16x16x32_bf16 a[20:23], a[212:215], v[176:179], a[20:23]\n"
    "v_mfma_f32_16x16x32_bf16 a[24:27], a[216:219], v[176:179], a[24:27]\n"
    "ds_read_b64_tr_b16 a[184:185], v144 offset:16384\n"
    "v_mfma_f32_16x16x32_bf16 a[24:27], a[248:251], v[180:183], a[24:27]\n"
    "v_mfma_f32_16x16x32_bf16 a[28:31], a[252:255], v[180:183], a[28:31]\n"
    "ds_read_b64_tr_b16 a[186:187], v144 offset:18432\n"
    "v_mfma_f32_16x16x32_bf16 a[28:31], a[220:223], v[176:179], a[28:31]\n"
    "s_waitcnt lgkmcnt(10)\n"
    "v_mfma_f32_16x16x32_bf16 a[60:63], a[220:223], v[184:187], a[60:63]\n"
    "ds_read_b64_tr_b16 a[156:157], v145 offset:0\n"
    "v_mfma_f32_16x16x32_bf16 a[60:63], a[252:255], v[188:191], a[60:63]\n"
    "buffer_load_dwordx4 v134, s[40:43], s62 offen offset:2048 lds\n"
    "v_mfma_f32_16x16x32_bf16 a[56:59], a[248:251], v[188:191], a[56:59]\n"
    "ds_read_b64_tr_b16 a[158:159], v145 offset:2048\n"
    "v_mfma_f32_16x16x32_bf16 a[56:59], a[216:219], v[184:187], a[56:59]\n"
    "v_mfma_f32_16x16x32_bf16 a[52:55], a[212:215], v[184:187], a[52:55]\n"
    "ds_read_b128 v[200:203], v136 offset:14336\n"
    "v_mfma_f32_16x16x32_bf16 a[52:55], a[244:247], v[188:191], a[52:55]\n"
    "buffer_load_dwordx4 v135, s[40:43], s62 offen offset:3072 lds\n"
    "v_mfma_f32_16x16x32_bf16 a[48:51], a[240:243], v[188:191], a[48:51]\n"
    "ds_read_b64_tr_b16 a[188:189], v145 offset:16384\n"
    "s_add_u32 s62, s62, s71\n"
    "v_mfma_f32_16x16x32_bf16 a[48:51], a[208:211], v[184:187], a[48:51]\n"
    "s_xor_b32 s96, s96, 0x8000\n"
    "s_mov_b32 m0, s63\n"
    "v_mfma_f32_16x16x32_bf16 a[44:47], a[204:207], v[184:187], a[44:47]\n"
    "ds_read_b64_tr_b16 a[190:191], v145 offset:18432\n"
    "v_mfma_f32_16x16x32_bf16 a[44:47], a[236:239], v[188:191], a[44:47]\n"
    "v_mfma_f32_16x16x32_bf16 a[40:43], a[232:235], v[188:191], a[40:43]\n"
    "ds_read_b128 v[204:207], v137 offset:14336\n"
    "v_mfma_f32_16x16x32_bf16 a[40:43], a[200:203], v[184:187], a[40:43]\n"
    "buffer_load_dwordx4 v128, s[36:39], s61 offen offset:0 lds\n"
    "v_mfma_f32_16x16x32_bf16 a[36:39], a[196:199], v[184:187], a[36:39]\n"
    "v_add_u32 v136, s64, v136\n"
    "v_add_u32 v137, s64, v137\n"
    "v_mfma_f32_16x16x32_bf16 a[36:39], a[228:231], v[188:191], a[36:39]\n"
    "s_waitcnt vmcnt(11)\n"
    "v_mfma_f32_16x16x32_bf16 a[32:35], a[224:227], v[188:191], a[32:35]\n"
    "s_barrier\n"
    "v_mfma_f32_16x16x32_bf16 a[32:35], a[192:195], v[184:187], a[32:35]\n"
    "s_waitcnt lgkmcnt(8)\n"
    "v_mfma_f32_16x16x32_bf16 a[64:67], a[192:195], v[192:195], a[64:67]\n"
    "v_mfma_f32_16x16x32_bf16 a[64:67], a[224:227], v[196:199], a[64:67]\n"
    "buffer_load_dwordx4 v129, s[36:39], s61 offen offset:1024 lds\n"
    "v_mfma_f32_16x16x32_bf16 a[68:71], a[228:231], v[196:199], a[68:71]\n"
    "v_mfma_f32_16x16x32_bf16 a[68:71], a[196:199], v[192:195], a[68:71]\n"
    "v_mfma_f32_16x16x32_bf16 a[72:75], a[200:203], v[192:195], a[72:75]\n"
    "ds_read_b128 v[176:179], v136 offset:0\n"
    "v_mfma_f32_16x16x32_bf16 a[72:75], a[232:235], v[196:199], a[72:75]\n"
    "buffer_load_dwordx4 v130, s[36:39], s61 offen offset:2048 lds\n"
    "v_mfma_f32_16x16x32_bf16 a[76:79], a[236:239], v[196:199], a[76:79]\n"
    "v_mfma_f32_16x16x32_bf16 a[76:79], a[204:207], v[192:195], a[76:79]\n"
    "v_mfma_f32_16x16x32_bf16 a[80:83], a[208:211], v[192:195], a[80:83]\n"
    "v_mfma_f32_16x16x32_bf16 a[80:83], a[240:243], v[196:199], a[80:83]\n"
    "buffer_load_dwordx4 v131, s[36:39], s61 offen offset:3072 lds\n"
    "v_mfma_f32_16x16x32_bf16 a[84:87], a[244:247], v[196:199], a[84:87]\n"
    "ds_read_b128 v[180:183], v137 offset:0\n"
    "s_add_u32 s61, s61, s70\n"
    "v_mfma_f32_16x16x32_bf16 a[84:87], a[212:215], v[192:195], a[84:87]\n"
    "s_add_u32 m0, s63, 0x1000\n"
    "v_mfma_f32_16x16x32_bf16 a[88:91], a[216:219], v[192:195], a[88:91]\n"
    "v_mfma_f32_16x16x32_bf16 a[88:91], a[248:251], v[196:199], a[88:91]\n"
    "buffer_load_dwordx4 v128, s[36:39], s61 offen offset:0 lds\n"
    "v_mfma_f32_16x16x32_bf16 a[92:95], a[252:255], v[196:199], a[92:95]\n"
    "v_mfma_f32_16x16x32_bf16 a[92:95], a[220:223], v[192:195], a[92:95]\n"
    "s_waitcnt lgkmcnt(2)\n"
    "v_mfma_f32_16x16x32_bf16 a[124:127], a[220:223], v[200:203], a[124:127]\n"
    "v_mfma_f32_16x16x32_bf16 a[124:127], a[252:255], v[204:207], a[124:127]\n"
    "buffer_load_dwordx4 v129, s[36:39], s61 offen offset:1024 lds\n"
    "v_mfma_f32_16x16x32_bf16 a[120:123], a[248:251], v[204:207], a[120:123]\n"
    "v_mfma_f32_16x16x32_bf16 a[120:123], a[216:219], v[200:203], a[120:123]\n"
    "v_mfma_f32_16x16x32_bf16 a[116:119], a[212:215], v[200:203], a[116:119]\n"
    "ds_read_b128 v[184:187], v136 offset:2048\n"
    "v_mfma_f32_16x16x32_bf16 a[116:119], a[244:247], v[204:207], a[116:119]\n"
    "buffer_load_dwordx4 v130, s[36:39], s61 offen offset:2048 lds\n"
    "v_mfma_f32_16x16x32_bf16 a[112:115], a[240:243], v[204:207], a[112:115]\n"
    "v_mfma_f32_16x16x32_bf16 a[112:115], a[208:211], v[200:203], a[112:115]\n"
    "v_mfma_f32_16x16x32_bf16 a[108:111], a[204:207], v[200:203], a[108:111]\n"
    "v_mfma_f32_16x16x32_bf16 a[108:111], a[236:239], v[204:207], a[108:111]\n"
    "buffer_load_dwordx4 v131, s[36:39], s61 offen offset:3072 lds\n"
    "v_mfma_f32_16x16x32_bf16 a[104:107], a[232:235], v[204:207], a[104:107]\n"
    "ds_read_b128 v[188:191], v137 offset:2048\n"
    "s_add_u32 s61, s61, s72\n"
    "v_mfma_f32_16x16x32_bf16 a[104:107], a[200:203], v[200:203], a[104:107]\n"
    "s_add_u32 s63, s63, 0x8000\n"
    "s_cmp_ge_u32 s63, 0x28000\n"
    "s_cselect_b32 s78, 0x18000, 0\n"
    "v_mfma_f32_16x16x32_bf16 a[100:103], a[196:199], v[200:203], a[100:103]\n"
    "s_sub_u32 s63, s63, s78\n"
    "v_mfma_f32_16x16x32_bf16 a[100:103], a[228:231], v[204:207], a[100:103]\n"
    "s_add_u32 s66, s66, 1\n"
    "v_mfma_f32_16x16x32_bf16 a[96:99], a[224:227], v[204:207], a[96:99]\n"
    "s_waitcnt lgkmcnt(2)\n"
    "s_cmp_lt_u32 s66, 16\n"
    "v_mfma_f32_16x16x32_bf16 a[96:99], a[192:195], v[200:203], a[96:99]\n"
    "s_cbranch_scc1 LOOPK_%=\n"
    "s_branch APH_%=\n"
    "SWLOOPK_%=:\n"
    "s_cmp_eq_u32 s66, 1\n"
    "s_cbranch_scc0 SWNLOOPK_%=\n"
    "s_mov_b32 s86, s33\n"
    "s_mov_b32 s88, s91\n"
    "s_branch SWBLOOPK_%=\n"
    "SWNLOOPK_%=:\n"
    "s_cmp_eq_u32 s66, 14\n"
    "s_cbranch_scc1 SWALOOPK_%=\n"
    "s_mov_b64 s[92:93], s[52:53]\n"
    "s_mov_b32 s94, s54\n"
    "s_mov_b32 s97, s90\n"
    "s_mov_b32 s98, 0x10000000\n"
    "v_lshlrev_b32 v147, 3, v146\n"
    "v_and_b32 v252, 0x1ff, v147\n"
    "v_and_b32 v147, 0x7ff, v147\n"
    "s_mov_b32 s48, s22\n"
    "s_and_b32 s49, s23, 0xffff\n"
    "s_mov_b32 s50, 401408\n"
    "s_lshl_b32 vcc_lo, s26, 2\n"
    "s_add_u32 vcc_lo, vcc_lo, s60\n"
    "s_mul_i32 vcc_lo, vcc_lo, 392\n"
    "s_and_b32 vcc_lo, vcc_lo, 0xffffff80\n"
    "buffer_load_dword v254, v252, s[48:51], vcc_lo offen offset:0\n"
    "s_branch SWBLOOPK_%=\n"
    "SWALOOPK_%=:\n"
    "s_mov_b64 s[36:37], s[52:53]\n"
    "s_mov_b32 s38, s54\n"
    "s_cmp_lt_u32 s60, 3\n"
    "s_cselect_b32 s40, s56, s52\n"
    "s_cselect_b32 s41, s57, s53\n"
    "s_cselect_b32 s42, s58, s54\n"
    "s_movk_i32 s70, 128\n"
    "s_movk_i32 s72, 128\n"
    "s_cselect_b32 s71, 172032, s70\n"
    "s_mov_b32 s61, 0\n"
    "s_mov_b32 s62, 0\n"
    "v_mov_b64 v[128:129], v[148:149]\n"
    "v_mov_b64 v[130:131], v[150:151]\n"
    "v_mov_b64 v[132:133], v[152:153]\n"
    "v_mov_b64 v[134:135], v[154:155]\n"
    "s_branch SWBLOOPK_%=\n"
    "SWMIDK_%=:\n"
    "s_cmp_eq_u32 s66, 1\n"
    "s_cbranch_scc0 SWNMIDK_%=\n"
    "s_mov_b32 s86, s33\n"
    "s_mov_b32 s88, s91\n"
    "s_branch SWBMIDK_%=\n"
    "SWNMIDK_%=:\n"
    "s_cmp_eq_u32 s66, 14\n"
    "s_cbranch_scc1 SWAMIDK_%=\n"
    "s_mov_b64 s[92:93], s[52:53]\n"
    "s_mov_b32 s94, s54\n"
    "s_mov_b32 s97, s90\n"
    "s_mov_b32 s98, 0x10000000\n"
    "v_lshlrev_b32 v147, 3, v146\n"
    "v_and_b32 v252, 0x1ff, v147\n"
    "v_and_b32 v147, 0x7ff, v147\n"
    "s_mov_b32 s48, s22\n"
    "s_and_b32 s49, s23, 0xffff\n"
    "s_mov_b32 s50, 401408\n"
    "s_lshl_b32 vcc_lo, s26, 2\n"
    "s_add_u32 vcc_lo, vcc_lo, s60\n"
    "s_mul_i32 vcc_lo, vcc_lo, 392\n"
    "s_and_b32 vcc_lo, vcc_lo, 0xffffff80\n"
    "buffer_load_dword v254, v252, s[48:51], vcc_lo offen offset:0\n"
    "s_branch SWBMIDK_%=\n"
    "SWAMIDK_%=:\n"
    "s_mov_b64 s[36:37], s[52:53]\n"
    "s_mov_b32 s38, s54\n"
    "s_cmp_lt_u32 s60, 3\n"
    "s_cselect_b32 s40, s56, s52\n"
    "s_cselect_b32 s41, s57, s53\n"
    "s_cselect_b32 s42, s58, s54\n"
    "s_movk_i32 s70, 128\n"
    "s_movk_i32 s72, 128\n"
    "s_cselect_b32 s71, 172032, s70\n"
    "s_mov_b32 s61, 0\n"
    "s_mov_b32 s62, 0\n"
    "v_mov_b64 v[128:129], v[148:149]\n"
    "v_mov_b64 v[130:131], v[150:151]\n"
    "v_mov_b64 v[132:133], v[152:153]\n"
    "v_mov_b64 v[134:135], v[154:155]\n"
    "s_branch SWBMIDK_%=\n"
    "SWLOOPV_%=:\n"
    "s_cmp_eq_u32 s66, 1\n"
    "s_cbranch_scc0 SWNLOOPV_%=\n"
    "s_mov_b32 s86, s33\n"
    "s_mov_b32 s88, s91\n"
    "s_branch SWBLOOPV_%=\n"
    "SWNLOOPV_%=:\n"
    "s_cmp_eq_u32 s66, 14\n"
    "s_cbranch_scc1 SWALOOPV_%=\n"
    "s_mov_b64 s[92:93], s[52:53]\n"
    "s_mov_b32 s94, s54\n"
    "s_mov_b32 s97, s90\n"
    "s_mov_b32 s98, 0x10000000\n"
    "v_lshlrev_b32 v147, 3, v146\n"
    "v_and_b32 v252, 0x1ff, v147\n"
    "v_and_b32 v147, 0x7ff, v147\n"
    "s_mov_b32 s48, s22\n"
    "s_and_b32 s49, s23, 0xffff\n"
    "s_mov_b32 s50, 401408\n"
    "s_lshl_b32 vcc_lo, s26, 2\n"
    "s_add_u32 vcc_lo, vcc_lo, s60\n"
    "s_mul_i32 vcc_lo, vcc_lo, 392\n"
    "s_and_b32 vcc_lo, vcc_lo, 0xffffff80\n"
    "buffer_load_dword v254, v252, s[48:51], vcc_lo offen offset:0\n"
    "s_branch SWBLOOPV_%=\n"
    "SWALOOPV_%=:\n"
    "s_mov_b64 s[36:37], s[52:53]\n"
    "s_mov_b32 s38, s54\n"
    "s_cmp_lt_u32 s60, 3\n"
    "s_cselect_b32 s40, s56, s52\n"
    "s_cselect_b32 s41, s57, s53\n"
    "s_cselect_b32 s42, s58, s54\n"
    "s_movk_i32 s70, 128\n"
    "s_movk_i32 s72, 128\n"
    "s_cselect_b32 s71, 172032, s70\n"
    "s_mov_b32 s61, 0\n"
    "s_mov_b32 s62, 0\n"
    "v_mov_b64 v[128:129], v[148:149]\n"
    "v_mov_b64 v[130:131], v[150:151]\n"
    "v_mov_b64 v[132:133], v[152:153]\n"
    "v_mov_b64 v[134:135], v[154:155]\n"
    "s_branch SWBLOOPV_%=\n"
    "SWMIDV_%=:\n"
    "s_cmp_eq_u32 s66, 1\n"
    "s_cbranch_scc0 SWNMIDV_%=\n"
    "s_mov_b32 s86, s33\n"
    "s_mov_b32 s88, s91\n"
    "s_branch SWBMIDV_%=\n"
    "SWNMIDV_%=:\n"
    "s_cmp_eq_u32 s66, 14\n"
    "s_cbranch_scc1 SWAMIDV_%=\n"
    "s_mov_b64 s[92:93], s[52:53]\n"
    "s_mov_b32 s94, s54\n"
    "s_mov_b32 s97, s90\n"
    "s_mov_b32 s98, 0x10000000\n"
    "v_lshlrev_b32 v147, 3, v146\n"
    "v_and_b32 v252, 0x1ff, v147\n"
    "v_and_b32 v147, 0x7ff, v147\n"
    "s_mov_b32 s48, s22\n"
    "s_and_b32 s49, s23, 0xffff\n"
    "s_mov_b32 s50, 401408\n"
    "s_lshl_b32 vcc_lo, s26, 2\n"
    "s_add_u32 vcc_lo, vcc_lo, s60\n"
    "s_mul_i32 vcc_lo, vcc_lo, 392\n"
    "s_and_b32 vcc_lo, vcc_lo, 0xffffff80\n"
    "buffer_load_dword v254, v252, s[48:51], vcc_lo offen offset:0\n"
    "s_branch SWBMIDV_%=\n"
    "SWAMIDV_%=:\n"
    "s_mov_b64 s[36:37], s[52:53]\n"
    "s_mov_b32 s38, s54\n"
    "s_cmp_lt_u32 s60, 3\n"
    "s_cselect_b32 s40, s56, s52\n"
    "s_cselect_b32 s41, s57, s53\n"
    "s_cselect_b32 s42, s58, s54\n"
    "s_movk_i32 s70, 128\n"
    "s_movk_i32 s72, 128\n"
    "s_cselect_b32 s71, 172032, s70\n"
    "s_mov_b32 s61, 0\n"
    "s_mov_b32 s62, 0\n"
    "v_mov_b64 v[128:129], v[148:149]\n"
    "v_mov_b64 v[130:131], v[150:151]\n"
    "v_mov_b64 v[132:133], v[152:153]\n"
    "v_mov_b64 v[134:135], v[154:155]\n"
    "s_branch SWBMIDV_%=\n"
    "PEELV_%=:\n"
    "v_mfma_f32_16x16x32_bf16 v[0:3], v[176:179], a[128:131], 0\n"
    "s_add_u32 s65, s65, 1\n"
    "s_cmp_eq_u32 s65, 3\n"
    "v_mfma_f32_16x16x32_bf16 v[32:35], v[176:179], a[132:135], 0\n"
    "s_cselect_b32 s64, s79, s80\n"
    "s_cselect_b32 s65, 0, s65\n"
    "v_mfma_f32_16x16x32_bf16 v[64:67], v[176:179], a[136:139], 0\n"
    "s_waitcnt vmcnt(2)\n"
    "v_mfma_f32_16x16x32_bf16 v[96:99], v[176:179], a[140:143], 0\n"
    "s_barrier\n"
    "v_mfma_f32_16x16x32_bf16 a[0:3], v[176:179], a[144:147], 0\n"
    "ds_read_b64_tr_b16 a[160:161], v138 offset:16384\n"
    "s_mov_b32 m0, s96\n"
    "s_mul_i32 s87, s74, 0x200000\n"
    "s_add_u32 s87, s87, 0x0\n"
    "v_mfma_f32_16x16x32_bf16 a[32:35], v[176:179], a[148:151], 0\n"
    "buffer_load_dwordx4 v132, s[40:43], s62 offen offset:0 lds\n"
    "s_add_u32 s44, s4, s87\n"
    "s_addc_u32 s45, s5, 0\n"
    "v_mfma_f32_16x16x32_bf16 a[64:67], v[176:179], a[152:155], 0\n"
    "s_and_b32 s45, s45, 0xffff\n"
    "s_mov_b32 s46, 2097152\n"
    "s_mov_b32 s47, 0x20000\n"
    "v_mfma_f32_16x16x32_bf16 a[96:99], v[176:179], a[156:159], 0\n"
    "ds_read_b128 v[176:179], v137 offset:0\n"
    "s_lshr_b32 s87, s60, 1\n"
    "s_and_b32 s88, s60, 1\n"
    "v_mfma_f32_16x16x32_bf16 a[100:103], v[180:183], a[156:159], 0\n"
    "ds_read_b64_tr_b16 a[162:163], v138 offset:18432\n"
    "s_lshl_b32 s67, s86, 18\n"
    "s_lshl_b32 s89, s87, 17\n"
    "v_mfma_f32_16x16x32_bf16 a[68:71], v[180:183], a[152:155], 0\n"
    "buffer_load_dwordx4 v133, s[40:43], s62 offen offset:1024 lds\n"
    "s_add_u32 s67, s67, s89\n"
    "s_lshl_b32 s89, s88, 13\n"
    "v_mfma_f32_16x16x32_bf16 a[36:39], v[180:183], a[148:151], 0\n"
    "ds_read_b64_tr_b16 a[164:165], v139 offset:16384\n"
    "s_add_u32 s67, s67, s89\n"
    "s_movk_i32 s90, 0x800\n"
    "v_mfma_f32_16x16x32_bf16 a[4:7], v[180:183], a[144:147], 0\n"
    "buffer_load_dwordx4 v134, s[40:43], s62 offen offset:2048 lds\n"
    "s_bitcmp1_b32 s75, 1\n"
    "s_cselect_b32 s89, 0x4000, 0\n"
    "s_cselect_b32 s68, s90, 0x8000\n"
    "s_cselect_b32 s69, 0x8000, s90\n"
    "v_mfma_f32_16x16x32_bf16 v[100:103], v[180:183], a[140:143], 0\n"
    "ds_read_b64_tr_b16 a[166:167], v139 offset:18432\n"
    "v_mfma_f32_16x16x32_bf16 v[68:71], v[180:183], a[136:139], 0\n"
    "buffer_load_dwordx4 v135, s[40:43], s62 offen offset:3072 lds\n"
    "s_add_u32 s67, s67, s89\n"
    "s_lshl_b32 s76, s68, 1\n"
    "v_mfma_f32_16x16x32_bf16 v[36:39], v[180:183], a[132:135], 0\n"
    "s_waitcnt lgkmcnt(5)\n"
    "s_add_u32 s62, s62, s71\n"
    "s_add_u32 s76, s76, s67\n"
    "s_sub_u32 s52, s16, 0x1000\n"
    "s_subb_u32 s53, s17, 0\n"
    "v_mfma_f32_16x16x32_bf16 v[4:7], v[180:183], a[128:131], 0\n"
    "ds_read_b128 v[180:183], v137 offset:2048\n"
    "s_mov_b32 m0, s63\n"
    "s_sub_u32 s56, s18, 0x1000\n"
    "s_subb_u32 s57, s19, 0\n"
    "v_mfma_f32_16x16x32_bf16 v[8:11], v[184:187], a[128:131], 0\n"
    "ds_read_b64_tr_b16 a[168:169], v140 offset:16384\n"
    "s_and_b32 s53, s53, 0xffff\n"
    "s_and_b32 s57, s57, 0xffff\n"
    "v_mfma_f32_16x16x32_bf16 v[40:43], v[184:187], a[132:135], 0\n"
    "buffer_load_dwordx4 v128, s[36:39], s61 offen offset:0 lds\n"
    "s_mov_b32 s54, 1839104\n"
    "s_mov_b32 s58, 1208320\n"
    "v_mfma_f32_16x16x32_bf16 v[72:75], v[184:187], a[136:139], 0\n"
    "ds_read_b64_tr_b16 a[170:171], v140 offset:18432\n"
    "s_mov_b32 s55, 0x20000\n"
    "s_mov_b32 s59, 0x20000\n"
    "v_mfma_f32_16x16x32_bf16 v[104:107], v[184:187], a[140:143], 0\n"
    "buffer_load_dwordx4 v129, s[36:39], s61 offen offset:1024 lds\n"
    "v_lshrrev_b32 v170, 3, v252\n"
    "v_mov_b32 v233, 0x40000000\n"
    "v_mfma_f32_16x16x32_bf16 a[8:11], v[184:187], a[144:147], 0\n"
    "ds_read_b64_tr_b16 a[172:173], v141 offset:16384\n"
    "s_lshl_b32 s87, s74, 7\n"
    "s_sub_u32 s87, s87, 16\n"
    "v_mfma_f32_16x16x32_bf16 a[40:43], v[184:187], a[148:151], 0\n"
    "buffer_load_dwordx4 v130, s[36:39], s61 offen offset:2048 lds\n"
    "s_lshl_b32 s88, s60, 5\n"
    "s_add_u32 s88, s88, s87\n"
    "v_mfma_f32_16x16x32_bf16 a[72:75], v[184:187], a[152:155], 0\n"
    "buffer_load_dwordx4 v131, s[36:39], s61 offen offset:3072 lds\n"
    "v_add_u32 v174, 0, v170\n"
    "v_add_u32 v174, s88, v174\n"
    "v_mfma_f32_16x16x32_bf16 a[104:107], v[184:187], a[156:159], 0\n"
    "ds_read_b128 v[184:187], v137 offset:4096\n"
    "v_xor_b32 v231, 0, v167\n"
    "v_xor_b32 v231, v231, v171\n"
    "v_cmp_gt_i32 vcc, 0, v174\n"
    "v_mfma_f32_16x16x32_bf16 a[108:111], v[188:191], a[156:159], 0\n"
    "ds_read_b64_tr_b16 a[174:175], v141 offset:18432\n"
    "s_add_u32 s61, s61, s70\n"
    "v_lshlrev_b32 v231, 4, v231\n"
    "v_mul_i32_i24 v232, 896, v174\n"
    "v_mfma_f32_16x16x32_bf16 a[76:79], v[188:191], a[152:155], 0\n"
    "v_add_u32 v231, v231, v232\n"
    "v_add_u32 v231, 4096, v231\n"
    "v_mfma_f32_16x16x32_bf16 a[44:47], v[188:191], a[148:151], 0\n"
    "ds_read_b64_tr_b16 a[176:177], v142 offset:16384\n"
    "s_add_u32 m0, s63, 0x1000\n"
    "v_cndmask_b32 v148, v231, v233, vcc\n"
    "v_add_u32 v174, 8, v170\n"
    "v_mfma_f32_16x16x32_bf16 a[12:15], v[188:191], a[144:147], 0\n"
    "buffer_load_dwordx4 v128, s[36:39], s61 offen offset:0 lds\n"
    "v_add_u32 v174, s88, v174\n"
    "v_xor_b32 v231, 4, v167\n"
    "v_mfma_f32_16x16x32_bf16 v[108:111], v[188:191], a[140:143], 0\n"
    "ds_read_b64_tr_b16 a[178:179], v142 offset:18432\n"
    "v_xor_b32 v231, v231, v171\n"
    "v_cmp_gt_i32 vcc, 0, v174\n"
    "v_mfma_f32_16x16x32_bf16 v[76:79], v[188:191], a[136:139], 0\n"
    "buffer_load_dwordx4 v129, s[36:39], s61 offen offset:1024 lds\n"
    "v_lshlrev_b32 v231, 4, v231\n"
    "v_mul_i32_i24 v232, 896, v174\n"
    "v_mfma_f32_16x16x32_bf16 v[44:47], v[188:191], a[132:135], 0\n"
    "v_add_u32 v231, v231, v232\n"
    "v_add_u32 v231, 3072, v231\n"
    "v_mfma_f32_16x16x32_bf16 v[12:15], v[188:191], a[128:131], 0\n"
    "ds_read_b128 v[188:191], v137 offset:6144\n"
    "v_cndmask_b32 v149, v231, v233, vcc\n"
    "v_add_u32 v174, 16, v170\n"
    "v_mfma_f32_16x16x32_bf16 v[16:19], v[192:195], a[128:131], 0\n"
    "ds_read_b64_tr_b16 a[180:181], v143 offset:16384\n"
    "v_add_u32 v174, s88, v174\n"
    "v_xor_b32 v231, 0, v167\n"
    "v_xor_b32 v231, v231, v171\n"
    "v_mfma_f32_16x16x32_bf16 v[48:51], v[192:195], a[132:135], 0\n"
    "buffer_load_dwordx4 v130, s[36:39], s61 offen offset:2048 lds\n"
    "v_cmp_gt_i32 vcc, 0, v174\n"
    "v_lshlrev_b32 v231, 4, v231\n"
    "v_mfma_f32_16x16x32_bf16 v[80:83], v[192:195], a[136:139], 0\n"
    "ds_read_b64_tr_b16 a[182:183], v143 offset:18432\n"
    "v_mul_i32_i24 v232, 896, v174\n"
    "v_add_u32 v231, v231, v232\n"
    "v_mfma_f32_16x16x32_bf16 v[112:115], v[192:195], a[140:143], 0\n"
    "buffer_load_dwordx4 v131, s[36:39], s61 offen offset:3072 lds\n"
    "v_add_u32 v231, 2048, v231\n"
    "v_cndmask_b32 v150, v231, v233, vcc\n"
    "v_mfma_f32_16x16x32_bf16 a[16:19], v[192:195], a[144:147], 0\n"
    "ds_read_b64_tr_b16 a[184:185], v144 offset:16384\n"
    "s_add_u32 s61, s61, s72\n"
    "v_add_u32 v174, 24, v170\n"
    "v_add_u32 v174, s88, v174\n"
    "v_mfma_f32_16x16x32_bf16 a[48:51], v[192:195], a[148:151], 0\n"
    "s_add_u32 s63, s63, 0x8000\n"
    "v_xor_b32 v231, 4, v167\n"
    "v_xor_b32 v231, v231, v171\n"
    "v_mfma_f32_16x16x32_bf16 a[80:83], v[192:195], a[152:155], 0\n"
    "s_cmp_ge_u32 s63, 0x28000\n"
    "s_cselect_b32 s78, 0x18000, 0\n"
    "v_cmp_gt_i32 vcc, 0, v174\n"
    "v_lshlrev_b32 v231, 4, v231\n"
    "v_mfma_f32_16x16x32_bf16 a[112:115], v[192:195], a[156:159], 0\n"
    "ds_read_b128 v[192:195], v137 offset:8192\n"
    "v_mul_i32_i24 v232, 896, v174\n"
    "v_add_u32 v231, v231, v232\n"
    "v_mfma_f32_16x16x32_bf16 a[116:119], v[196:199], a[156:159], 0\n"
    "ds_read_b64_tr_b16 a[186:187], v144 offset:18432\n"
    "v_add_u32 v231, 1024, v231\n"
    "v_cndmask_b32 v151, v231, v233, vcc\n"
    "v_mfma_f32_16x16x32_bf16 a[84:87], v[196:199], a[152:155], 0\n"
    "v_add_u32 v174, 128, v170\n"
    "v_add_u32 v174, s87, v174\n"
    "v_mfma_f32_16x16x32_bf16 a[52:55], v[196:199], a[148:151], 0\n"
    "ds_read_b64_tr_b16 a[188:189], v145 offset:16384\n"
    "s_sub_u32 s63, s63, s78\n"
    "v_xor_b32 v231, 0, v167\n"
    "v_xor_b32 v231, v231, v171\n"
    "v_mfma_f32_16x16x32_bf16 a[20:23], v[196:199], a[144:147], 0\n"
    "v_cmp_gt_i32 vcc, 0, v174\n"
    "v_lshlrev_b32 v231, 4, v231\n"
    "v_mul_i32_i24 v232, 896, v174\n"
    "v_mfma_f32_16x16x32_bf16 v[116:119], v[196:199], a[140:143], 0\n"
    "ds_read_b64_tr_b16 a[190:191], v145 offset:18432\n"
    "v_add_u32 v231, v231, v232\n"
    "v_add_u32 v231, 4096, v231\n"
    "v_mfma_f32_16x16x32_bf16 v[84:87], v[196:199], a[136:139], 0\n"
    "buffer_load_dword v254, v147, s[92:95], s97 offen offset:0\n"
    "v_cndmask_b32 v234, v231, v233, vcc\n"
    "v_add_u32 v174, 136, v170\n"
    "v_mfma_f32_16x16x32_bf16 v[52:55], v[196:199], a[132:135], 0\n"
    "s_add_u32 s97, s97, s98\n"
    "v_add_u32 v174, s87, v174\n"
    "v_xor_b32 v231, 4, v167\n"
    "v_mfma_f32_16x16x32_bf16 v[20:23], v[196:199], a[128:131], 0\n"
    "ds_read_b128 v[196:199], v137 offset:10240\n"
    "v_xor_b32 v231, v231, v171\n"
    "v_cmp_gt_i32 vcc, 0, v174\n"
    "v_mfma_f32_16x16x32_bf16 v[24:27], v[200:203], a[128:131], 0\n"
    "s_add_u32 m0, s96, 0x4000\n"
    "v_lshlrev_b32 v231, 4, v231\n"
    "v_mul_i32_i24 v232, 896, v174\n"
    "v_mfma_f32_16x16x32_bf16 v[56:59], v[200:203], a[132:135], 0\n"
    "buffer_load_dwordx4 v132, s[40:43], s62 offen offset:0 lds\n"
    "v_add_u32 v231, v231, v232\n"
    "v_add_u32 v231, 3072, v231\n"
    "v_mfma_f32_16x16x32_bf16 v[88:91], v[200:203], a[136:139], 0\n"
    "v_cndmask_b32 v235, v231, v233, vcc\n"
    "v_bfe_u32 v172, v252, 1, 1\n"
    "v_mfma_f32_16x16x32_bf16 v[120:123], v[200:203], a[140:143], 0\n"
    "buffer_load_dwordx4 v133, s[40:43], s62 offen offset:1024 lds\n"
    "v_lshrrev_b32 v173, 5, v252\n"
    "v_xor_b32 v172, v172, v173\n"
    "v_mfma_f32_16x16x32_bf16 a[24:27], v[200:203], a[144:147], 0\n"
    "v_and_b32 v173, 1, v252\n"
    "v_lshl_add_u32 v172, v172, 1, v173\n"
    "v_lshlrev_b32 v172, 4, v172\n"
    "v_mfma_f32_16x16x32_bf16 a[56:59], v[200:203], a[148:151], 0\n"
    "buffer_load_dwordx4 v134, s[40:43], s62 offen offset:2048 lds\n"
    "v_mul_u32_u24 v173, 2688, v163\n"
    "v_add_u32 v172, v172, v173\n"
    "v_mfma_f32_16x16x32_bf16 a[88:91], v[200:203], a[152:155], 0\n"
    "s_lshr_b32 s89, s75, 1\n"
    "s_mul_i32 s89, s89, 56\n"
    "v_mfma_f32_16x16x32_bf16 a[120:123], v[200:203], a[156:159], 0\n"
    "ds_read_b128 v[200:203], v137 offset:12288\n"
    "s_mul_i32 s90, s60, 896\n"
    "s_add_u32 s89, s89, s90\n"
    "v_mfma_f32_16x16x32_bf16 a[124:127], v[204:207], a[156:159], 0\n"
    "buffer_load_dwordx4 v135, s[40:43], s62 offen offset:3072 lds\n"
    "v_add_u32 v172, s89, v172\n"
    "s_cmp_eq_u32 s60, 3\n"
    "s_cselect_b64 vcc, -1, 0\n"
    "v_mfma_f32_16x16x32_bf16 a[92:95], v[204:207], a[152:155], 0\n"
    "s_add_u32 s62, s62, s71\n"
    "v_add_u32 v173, 4096, v172\n"
    "v_mfma_f32_16x16x32_bf16 a[60:63], v[204:207], a[148:151], 0\n"
    "v_cndmask_b32 v152, v173, v234, vcc\n"
    "v_add_u32 v173, 46080, v172\n"
    "v_mfma_f32_16x16x32_bf16 a[28:31], v[204:207], a[144:147], 0\n"
    "s_xor_b32 s96, s96, 0x8000\n"
    "v_cndmask_b32 v153, v173, v235, vcc\n"
    "v_add_u32 v173, 88064, v172\n"
    "v_mfma_f32_16x16x32_bf16 v[124:127], v[204:207], a[140:143], 0\n"
    "v_cndmask_b32 v154, v173, v233, vcc\n"
    "v_add_u32 v173, 130048, v172\n"
    "v_mfma_f32_16x16x32_bf16 v[92:95], v[204:207], a[136:139], 0\n"
    "v_mfma_f32_16x16x32_bf16 v[60:63], v[204:207], a[132:135], 0\n"
    "v_mfma_f32_16x16x32_bf16 v[28:31], v[204:207], a[128:131], 0\n"
    "ds_read_b128 v[204:207], v137 offset:14336\n"
    "s_waitcnt lgkmcnt(2)\n"
    "v_mfma_f32_16x16x32_bf16 v[0:3], v[176:179], a[160:163], v[0:3]\n"
    "v_mfma_f32_16x16x32_bf16 v[32:35], v[176:179], a[164:167], v[32:35]\n"
    "v_mfma_f32_16x16x32_bf16 v[64:67], v[176:179], a[168:171], v[64:67]\n"
    "s_waitcnt vmcnt(13)\n"
    "v_mfma_f32_16x16x32_bf16 v[96:99], v[176:179], a[172:175], v[96:99]\n"
    "s_barrier\n"
    "v_mfma_f32_16x16x32_bf16 a[0:3], v[176:179], a[176:179], a[0:3]\n"
    "ds_read_b64_tr_b16 a[192:193], v138 offset:32768\n"
    "s_mov_b32 m0, s96\n"
    "v_cndmask_b32 v155, v173, v233, vcc\n"
    "s_lshl_b32 s87, s60, 13\n"
    "v_mfma_f32_16x16x32_bf16 a[32:35], v[176:179], a[180:183], a[32:35]\n"
    "v_add_u32 v136, s64, v136\n"
    "v_add_u32 v137, s64, v137\n"
    "v_add_u32 v156, s87, v175\n"
    "v_xor_b32 v157, 64, v156\n"
    "v_mfma_f32_16x16x32_bf16 a[64:67], v[176:179], a[184:187], a[64:67]\n"
    "ds_read_b64_tr_b16 a[194:195], v138 offset:34816\n"
    "buffer_load_dwordx4 v132, s[40:43], s62 offen offset:0 lds\n"
    "v_mov_b64 v[158:159], v[156:157]\n"
    "v_mfma_f32_16x16x32_bf16 a[96:99], v[176:179], a[188:191], a[96:99]\n"
    "buffer_load_dwordx4 v133, s[40:43], s62 offen offset:1024 lds\n"
    "v_and_b32 v171, 1, v167\n"
    "v_xor_b32 v171, 0, v171\n"
    "v_mfma_f32_16x16x32_bf16 a[100:103], v[180:183], a[188:191], a[100:103]\n"
    "ds_read_b64_tr_b16 a[196:197], v139 offset:32768\n"
    "v_lshl_add_u32 v171, v171, 5, v169\n"
    "v_lshl_add_u32 v160, v168, 6, v171\n"
    "v_and_b32 v171, 1, v167\n"
    "v_mfma_f32_16x16x32_bf16 a[68:71], v[180:183], a[184:187], a[68:71]\n"
    "buffer_load_dwordx4 v134, s[40:43], s62 offen offset:2048 lds\n"
    "v_xor_b32 v171, 1, v171\n"
    "v_lshl_add_u32 v171, v171, 5, v169\n"
    "v_mfma_f32_16x16x32_bf16 a[36:39], v[180:183], a[180:183], a[36:39]\n"
    "ds_read_b64_tr_b16 a[198:199], v139 offset:34816\n"
    "v_lshl_add_u32 v161, v168, 6, v171\n"
    "s_or_b32 s82, s74, s60\n"
    "s_lshl_b32 s83, s74, 17\n"
    "v_mfma_f32_16x16x32_bf16 a[4:7], v[180:183], a[176:179], a[4:7]\n"
    "buffer_load_dwordx4 v135, s[40:43], s62 offen offset:3072 lds\n"
    "s_lshl_b32 s87, s60, 15\n"
    "s_add_u32 s83, s83, s87\n"
    "v_mfma_f32_16x16x32_bf16 v[100:103], v[180:183], a[172:175], v[100:103]\n"
    "ds_read_b64_tr_b16 a[200:201], v140 offset:32768\n"
    "s_lshr_b32 s87, s75, 1\n"
    "s_lshl_b32 s87, s87, 10\n"
    "v_mfma_f32_16x16x32_bf16 v[68:71], v[180:183], a[168:171], v[68:71]\n"
    "s_add_u32 s62, s62, s71\n"
    "s_add_u32 s83, s83, s87\n"
    "s_mov_b32 s87, 0x3049800\n"
    "s_add_u32 s48, s4, s87\n"
    "s_addc_u32 s49, s5, 0\n"
    "v_mfma_f32_16x16x32_bf16 v[36:39], v[180:183], a[164:167], v[36:39]\n"
    "s_waitcnt lgkmcnt(5)\n"
    "ds_read_b64_tr_b16 a[202:203], v140 offset:34816\n"
    "s_and_b32 s49, s49, 0xffff\n"
    "v_mfma_f32_16x16x32_bf16 v[4:7], v[180:183], a[160:163], v[4:7]\n"
    "s_add_u32 m0, s96, 0x4000\n"
    "s_mov_b32 s50, 8\n"
    "s_mov_b32 s51, 0x20000\n"
    "v_mfma_f32_16x16x32_bf16 v[8:11], v[184:187], a[160:163], v[8:11]\n"
    "ds_read_b64_tr_b16 a[204:205], v141 offset:32768\n"
    "buffer_load_dwordx4 v132, s[40:43], s62 offen offset:0 lds\n"
    "v_mov_b32 v236, 0\n"
    "v_mfma_f32_16x16x32_bf16 v[40:43], v[184:187], a[164:167], v[40:43]\n"
    "buffer_load_dwordx2 v[164:165], v236, s[48:51], 0 offen sc1\n"
    "s_mov_b32 s84, s6\n"
    "s_mov_b32 s85, s7\n"
    "v_mfma_f32_16x16x32_bf16 v[72:75], v[184:187], a[168:171], v[72:75]\n"
    "ds_read_b64_tr_b16 a[206:207], v141 offset:34816\n"
    "buffer_load_dwordx4 v133, s[40:43], s62 offen offset:1024 lds\n"
    "s_mov_b32 s86, 1835008\n"
    "v_mfma_f32_16x16x32_bf16 v[104:107], v[184:187], a[172:175], v[104:107]\n"
    "s_mov_b32 s89, 5\n"
    "s_cmp_eq_u32 s60, 1\n"
    "s_cselect_b32 s84, s10, s84\n"
    "s_cselect_b32 s85, s11, s85\n"
    "s_cselect_b32 s86, 3670016, s86\n"
    "s_cselect_b32 s89, 10, s89\n"
    "v_mfma_f32_16x16x32_bf16 a[8:11], v[184:187], a[176:179], a[8:11]\n"
    "ds_read_b64_tr_b16 a[208:209], v142 offset:32768\n"
    "buffer_load_dwordx4 v134, s[40:43], s62 offen offset:2048 lds\n"
    "v_mfma_f32_16x16x32_bf16 a[40:43], v[184:187], a[180:183], a[40:43]\n"
    "s_cmp_eq_u32 s60, 2\n"
    "s_cselect_b32 s84, s20, s84\n"
    "s_cselect_b32 s85, s21, s85\n"
    "s_cselect_b32 s86, 3670016, s86\n"
    "s_cselect_b32 s89, 10, s89\n"
    "v_mfma_f32_16x16x32_bf16 a[72:75], v[184:187], a[184:187], a[72:75]\n"
    "ds_read_b64_tr_b16 a[210:211], v142 offset:34816\n"
    "buffer_load_dwordx4 v135, s[40:43], s62 offen offset:3072 lds\n"
    "v_mfma_f32_16x16x32_bf16 a[104:107], v[184:187], a[188:191], a[104:107]\n"
    "s_cmp_eq_u32 s60, 3\n"
    "s_cselect_b32 s84, s18, s84\n"
    "s_cselect_b32 s85, s19, s85\n"
    "s_cselect_b32 s86, 1204224, s86\n"
    "s_cselect_b32 s89, 4, s89\n"
    "v_mfma_f32_16x16x32_bf16 a[108:111], v[188:191], a[188:191], a[108:111]\n"
    "ds_read_b64_tr_b16 a[212:213], v143 offset:32768\n"
    "s_add_u32 s62, s62, s71\n"
    "v_mfma_f32_16x16x32_bf16 a[76:79], v[188:191], a[184:187], a[76:79]\n"
    "v_mfma_f32_16x16x32_bf16 a[44:47], v[188:191], a[180:183], a[44:47]\n"
    "ds_read_b64_tr_b16 a[214:215], v143 offset:34816\n"
    "s_xor_b32 s96, s96, 0x8000\n"
    "s_and_b32 s85, s85, 0xffff\n"
    "s_mov_b32 s87, 0x20000\n"
    "v_mfma_f32_16x16x32_bf16 a[12:15], v[188:191], a[176:179], a[12:15]\n"
    "buffer_load_dword v254, v147, s[92:95], s97 offen offset:0\n"
    "v_cmp_gt_u32 vcc, s89, v252\n"
    "s_lshl_b32 s89, s89, 7\n"
    "v_mfma_f32_16x16x32_bf16 v[108:111], v[188:191], a[172:175], v[108:111]\n"
    "ds_read_b64_tr_b16 a[216:217], v144 offset:32768\n"
    "s_mul_i32 s88, s89, s26\n"
    "s_lshl_b32 s89, s89, 8\n"
    "v_mfma_f32_16x16x32_bf16 v[76:79], v[188:191], a[168:171], v[76:79]\n"
    "s_add_u32 s97, s97, s98\n"
    "v_lshlrev_b32 v174, 3, v146\n"
    "v_cndmask_b32 v162, v233, v174, vcc\n"
    "v_mfma_f32_16x16x32_bf16 v[44:47], v[188:191], a[164:167], v[44:47]\n"
    "ds_read_b64_tr_b16 a[218:219], v144 offset:34816\n"
    "s_lshl_b32 s90, s75, 2\n"
    "s_add_u32 s90, s90, s60\n"
    "v_mfma_f32_16x16x32_bf16 v[12:15], v[188:191], a[160:163], v[12:15]\n"
    "s_mov_b32 m0, s63\n"
    "s_mul_i32 s90, s90, 1920\n"
    "s_lshr_b32 s91, s74, 1\n"
    "v_mfma_f32_16x16x32_bf16 v[16:19], v[192:195], a[160:163], v[16:19]\n"
    "ds_read_b64_tr_b16 a[220:221], v145 offset:32768\n"
    "s_mul_i32 s91, s91, 229376\n"
    "s_add_u32 s90, s90, s91\n"
    "s_add_u32 s90, s90, 0x1000\n"
    "v_mfma_f32_16x16x32_bf16 v[48:51], v[192:195], a[164:167], v[48:51]\n"
    "buffer_load_dwordx4 v128, s[36:39], s61 offen offset:0 lds\n"
    "s_mov_b32 s91, s88\n"
    "v_lshrrev_b32 v224, 4, v146\n"
    "v_mfma_f32_16x16x32_bf16 v[80:83], v[192:195], a[168:171], v[80:83]\n"
    "ds_read_b64_tr_b16 a[222:223], v145 offset:34816\n"
    "v_lshrrev_b32 v225, 4, v224\n"
    "v_lshrrev_b32 v226, 3, v224\n"
    "v_mfma_f32_16x16x32_bf16 v[112:115], v[192:195], a[172:175], v[112:115]\n"
    "buffer_load_dwordx4 v129, s[36:39], s61 offen offset:1024 lds\n"
    "v_and_b32 v227, 7, v224\n"
    "v_xor_b32 v227, v227, v225\n"
    "v_mfma_f32_16x16x32_bf16 a[16:19], v[192:195], a[176:179], a[16:19]\n"
    "s_waitcnt vmcnt(12)\n"
    "v_lshlrev_b32 v227, 4, v227\n"
    "v_mul_u32_u24 v228, 896, v226\n"
    "v_mfma_f32_16x16x32_bf16 a[48:51], v[192:195], a[180:183], a[48:51]\n"
    "s_barrier\n"
    "buffer_load_dwordx4 v130, s[36:39], s61 offen offset:2048 lds\n"
    "s_mul_i32 s99, s60, 28672\n"
    "v_mfma_f32_16x16x32_bf16 a[80:83], v[192:195], a[184:187], a[80:83]\n"
    "ds_read_b128 v[176:179], v136 offset:0\n"
    "buffer_load_dwordx4 v131, s[36:39], s61 offen offset:3072 lds\n"
    "v_add_u32 v225, s99, v228\n"
    "v_mfma_f32_16x16x32_bf16 a[112:115], v[192:195], a[188:191], a[112:115]\n"
    "ds_read_b128 v[180:183], v137 offset:0\n"
    "v_add_u32 v240, v225, v227\n"
    "v_xor_b32 v227, 64, v227\n"
    "v_add_u32 v227, v225, v227\n"
    "v_mfma_f32_16x16x32_bf16 a[116:119], v[196:199], a[188:191], a[116:119]\n"
    "ds_read_b128 v[184:187], v136 offset:2048\n"
    "s_add_u32 s61, s61, s70\n"
    "v_add_u32 v241, 6144, v227\n"
    "v_add_u32 v242, 12288, v240\n"
    "v_mfma_f32_16x16x32_bf16 a[84:87], v[196:199], a[184:187], a[84:87]\n"
    "ds_read_b128 v[188:191], v137 offset:2048\n"
    "v_add_u32 v243, 12288, v241\n"
    "v_mov_b64 v[244:245], v[132:133]\n"
    "v_mov_b64 v[246:247], v[134:135]\n"
    "v_mfma_f32_16x16x32_bf16 a[52:55], v[196:199], a[180:183], a[52:55]\n"
    "ds_read_b64_tr_b16 a[224:225], v138 offset:49152\n"
    "s_add_u32 m0, s63, 0x1000\n"
    "s_lshl_b32 s99, s60, 12\n"
    "v_add_u32 v250, s99, v146\n"
    "v_mfma_f32_16x16x32_bf16 a[20:23], v[196:199], a[176:179], a[20:23]\n"
    "ds_read_b64_tr_b16 a[226:227], v138 offset:51200\n"
    "v_add_u32 v251, 0x8000, v250\n"
    "s_add_u32 s99, s99, 0x4000\n"
    "v_add_u32 v248, s99, v175\n"
    "v_mfma_f32_16x16x32_bf16 v[116:119], v[196:199], a[172:175], v[116:119]\n"
    "ds_read_b64_tr_b16 a[228:229], v139 offset:49152\n"
    "buffer_load_dwordx4 v128, s[36:39], s61 offen offset:0 lds\n"
    "v_xor_b32 v249, 64, v248\n"
    "v_mfma_f32_16x16x32_bf16 v[84:87], v[196:199], a[168:171], v[84:87]\n"
    "ds_read_b64_tr_b16 a[230:231], v139 offset:51200\n"
    "v_and_b32 v208, 0xfffffeff, v138\n"
    "v_and_b32 v209, 0xfffffeff, v139\n"
    "v_and_b32 v210, 0xfffffeff, v140\n"
    "v_mfma_f32_16x16x32_bf16 v[52:55], v[196:199], a[164:167], v[52:55]\n"
    "ds_read_b64_tr_b16 a[232:233], v140 offset:49152\n"
    "buffer_load_dwordx4 v129, s[36:39], s61 offen offset:1024 lds\n"
    "v_and_b32 v211, 0xfffffeff, v141\n"
    "v_mfma_f32_16x16x32_bf16 v[20:23], v[196:199], a[160:163], v[20:23]\n"
    "ds_read_b64_tr_b16 a[234:235], v140 offset:51200\n"
    "v_and_b32 v212, 0xfffffeff, v142\n"
    "v_and_b32 v213, 0xfffffeff, v143\n"
    "v_and_b32 v214, 0xfffffeff, v144\n"
    "v_mfma_f32_16x16x32_bf16 v[24:27], v[200:203], a[160:163], v[24:27]\n"
    "ds_read_b64_tr_b16 a[236:237], v141 offset:49152\n"
    "buffer_load_dwordx4 v130, s[36:39], s61 offen offset:2048 lds\n"
    "v_and_b32 v215, 0xfffffeff, v145\n"
    "v_mfma_f32_16x16x32_bf16 v[56:59], v[200:203], a[164:167], v[56:59]\n"
    "ds_read_b64_tr_b16 a[238:239], v141 offset:51200\n"
    "buffer_load_dwordx4 v131, s[36:39], s61 offen offset:3072 lds\n"
    "v_lshrrev_b32 v225, 2, v224\n"
    "v_mfma_f32_16x16x32_bf16 v[88:91], v[200:203], a[168:171], v[88:91]\n"
    "ds_read_b64_tr_b16 a[240:241], v142 offset:49152\n"
    "v_bfe_u32 v229, v225, 1, 2\n"
    "v_and_b32 v230, 3, v224\n"
    "v_lshlrev_b32 v230, 3, v230\n"
    "v_mfma_f32_16x16x32_bf16 v[120:123], v[200:203], a[172:175], v[120:123]\n"
    "ds_read_b64_tr_b16 a[242:243], v142 offset:51200\n"
    "s_add_u32 s61, s61, s72\n"
    "v_lshl_add_u32 v230, v225, 7, v230\n"
    "v_add_u32 v230, 0x20000, v230\n"
    "v_mfma_f32_16x16x32_bf16 a[24:27], v[200:203], a[176:179], a[24:27]\n"
    "ds_read_b64_tr_b16 a[244:245], v143 offset:49152\n"
    "v_xor_b32 v227, 0, v229\n"
    "v_lshl_add_u32 v216, v227, 5, v230\n"
    "v_xor_b32 v227, 1, v229\n"
    "v_mfma_f32_16x16x32_bf16 a[56:59], v[200:203], a[180:183], a[56:59]\n"
    "ds_read_b64_tr_b16 a[246:247], v143 offset:51200\n"
    "s_add_u32 s63, s63, 0x8000\n"
    "v_lshl_add_u32 v217, v227, 5, v230\n"
    "v_xor_b32 v227, 2, v229\n"
    "v_mfma_f32_16x16x32_bf16 a[88:91], v[200:203], a[184:187], a[88:91]\n"
    "ds_read_b64_tr_b16 a[248:249], v144 offset:49152\n"
    "v_lshl_add_u32 v218, v227, 5, v230\n"
    "v_xor_b32 v227, 3, v229\n"
    "v_lshl_add_u32 v219, v227, 5, v230\n"
    "v_mfma_f32_16x16x32_bf16 a[120:123], v[200:203], a[188:191], a[120:123]\n"
    "ds_read_b64_tr_b16 a[250:251], v144 offset:51200\n"
    "s_cmp_ge_u32 s63, 0x28000\n"
    "s_cselect_b32 s78, 0x18000, 0\n"
    "v_bfe_u32 v229, v226, 1, 2\n"
    "v_mfma_f32_16x16x32_bf16 a[124:127], v[204:207], a[188:191], a[124:127]\n"
    "ds_read_b64_tr_b16 a[252:253], v145 offset:49152\n"
    "v_bfe_u32 v230, v224, 1, 2\n"
    "v_xor_b32 v229, v229, v230\n"
    "v_and_b32 v230, 1, v224\n"
    "v_mfma_f32_16x16x32_bf16 a[92:95], v[204:207], a[184:187], a[92:95]\n"
    "ds_read_b64_tr_b16 a[254:255], v145 offset:51200\n"
    "v_lshlrev_b32 v230, 4, v230\n"
    "v_lshl_add_u32 v230, v229, 5, v230\n"
    "v_mfma_f32_16x16x32_bf16 a[60:63], v[204:207], a[180:183], a[60:63]\n"
    "v_add_u32 v230, v230, v228\n"
    "s_mul_i32 s99, s60, 57344\n"
    "v_mfma_f32_16x16x32_bf16 a[28:31], v[204:207], a[176:179], a[28:31]\n"
    "s_sub_u32 s63, s63, s78\n"
    "v_add_u32 v220, s99, v230\n"
    "v_add_u32 v221, 6144, v220\n"
    "v_mfma_f32_16x16x32_bf16 v[124:127], v[204:207], a[172:175], v[124:127]\n"
    "v_add_u32 v222, 12288, v220\n"
    "v_add_u32 v223, 18432, v220\n"
    "v_mfma_f32_16x16x32_bf16 v[92:95], v[204:207], a[168:171], v[92:95]\n"
    "v_mfma_f32_16x16x32_bf16 v[60:63], v[204:207], a[164:167], v[60:63]\n"
    "v_mfma_f32_16x16x32_bf16 v[28:31], v[204:207], a[160:163], v[28:31]\n"
    "s_waitcnt lgkmcnt(0)\n"
    "s_branch MIDV_%=\n"
    "LOOPV_%=:\n"
    "s_bitcmp1_b32 s81, s66\n"
    "s_cbranch_scc1 SWLOOPV_%=\n"
    "SWBLOOPV_%=:\n"
    "v_mfma_f32_16x16x32_bf16 v[0:3], v[176:179], a[128:131], v[0:3]\n"
    "s_add_u32 s65, s65, 1\n"
    "s_cmp_eq_u32 s65, 3\n"
    "v_mfma_f32_16x16x32_bf16 v[0:3], v[180:183], a[160:163], v[0:3]\n"
    "s_cselect_b32 s64, s79, s80\n"
    "s_cselect_b32 s65, 0, s65\n"
    "v_mfma_f32_16x16x32_bf16 v[32:35], v[180:183], a[164:167], v[32:35]\n"
    "s_waitcnt vmcnt(8)\n"
    "v_mfma_f32_16x16x32_bf16 v[32:35], v[176:179], a[132:135], v[32:35]\n"
    "s_barrier\n"
    "v_mfma_f32_16x16x32_bf16 v[64:67], v[176:179], a[136:139], v[64:67]\n"
    "ds_read_b128 v[192:195], v136 offset:4096\n"
    "v_mfma_f32_16x16x32_bf16 v[64:67], v[180:183], a[168:171], v[64:67]\n"
    "buffer_load_dword v254, v147, s[92:95], s97 offen offset:0\n"
    "v_mfma_f32_16x16x32_bf16 v[96:99], v[180:183], a[172:175], v[96:99]\n"
    "ds_read_b64_tr_b16 a[192:193], v138 offset:32768\n"
    "s_add_u32 s97, s97, s98\n"
    "v_mfma_f32_16x16x32_bf16 v[96:99], v[176:179], a[140:143], v[96:99]\n"
    "buffer_load_dword v254, v162, s[84:87], s88 offen offset:0\n"
    "v_mfma_f32_16x16x32_bf16 a[0:3], v[176:179], a[144:147], a[0:3]\n"
    "ds_read_b64_tr_b16 a[194:195], v138 offset:34816\n"
    "s_add_u32 s88, s88, s89\n"
    "v_mfma_f32_16x16x32_bf16 a[0:3], v[180:183], a[176:179], a[0:3]\n"
    "s_mov_b32 m0, s96\n"
    "v_mfma_f32_16x16x32_bf16 a[32:35], v[180:183], a[180:183], a[32:35]\n"
    "ds_read_b128 v[196:199], v137 offset:4096\n"
    "v_mfma_f32_16x16x32_bf16 a[32:35], v[176:179], a[148:151], a[32:35]\n"
    "v_mfma_f32_16x16x32_bf16 a[64:67], v[176:179], a[152:155], a[64:67]\n"
    "ds_read_b64_tr_b16 a[224:225], v138 offset:49152\n"
    "v_mfma_f32_16x16x32_bf16 a[64:67], v[180:183], a[184:187], a[64:67]\n"
    "v_mfma_f32_16x16x32_bf16 a[96:99], v[180:183], a[188:191], a[96:99]\n"
    "ds_read_b64_tr_b16 a[226:227], v138 offset:51200\n"
    "v_mfma_f32_16x16x32_bf16 a[96:99], v[176:179], a[156:159], a[96:99]\n"
    "s_waitcnt lgkmcnt(6)\n"
    "v_mfma_f32_16x16x32_bf16 a[100:103], v[184:187], a[156:159], a[100:103]\n"
    "ds_read_b64_tr_b16 a[196:197], v139 offset:32768\n"
    "v_mfma_f32_16x16x32_bf16 a[100:103], v[188:191], a[188:191], a[100:103]\n"
    "buffer_load_dwordx4 v132, s[40:43], s62 offen offset:0 lds\n"
    "v_mfma_f32_16x16x32_bf16 a[68:71], v[188:191], a[184:187], a[68:71]\n"
    "ds_read_b64_tr_b16 a[198:199], v139 offset:34816\n"
    "v_mfma_f32_16x16x32_bf16 a[68:71], v[184:187], a[152:155], a[68:71]\n"
    "v_mfma_f32_16x16x32_bf16 a[36:39], v[184:187], a[148:151], a[36:39]\n"
    "ds_read_b128 v[200:203], v136 offset:6144\n"
    "v_mfma_f32_16x16x32_bf16 a[36:39], v[188:191], a[180:183], a[36:39]\n"
    "v_mfma_f32_16x16x32_bf16 a[4:7], v[188:191], a[176:179], a[4:7]\n"
    "ds_read_b64_tr_b16 a[228:229], v139 offset:49152\n"
    "v_mfma_f32_16x16x32_bf16 a[4:7], v[184:187], a[144:147], a[4:7]\n"
    "v_mfma_f32_16x16x32_bf16 v[100:103], v[184:187], a[140:143], v[100:103]\n"
    "ds_read_b64_tr_b16 a[230:231], v139 offset:51200\n"
    "v_mfma_f32_16x16x32_bf16 v[100:103], v[188:191], a[172:175], v[100:103]\n"
    "v_mfma_f32_16x16x32_bf16 v[68:71], v[188:191], a[168:171], v[68:71]\n"
    "ds_read_b128 v[204:207], v137 offset:6144\n"
    "v_mfma_f32_16x16x32_bf16 v[68:71], v[184:187], a[136:139], v[68:71]\n"
    "v_mfma_f32_16x16x32_bf16 v[36:39], v[184:187], a[132:135], v[36:39]\n"
    "ds_read_b64_tr_b16 a[200:201], v140 offset:32768\n"
    "v_mfma_f32_16x16x32_bf16 v[36:39], v[188:191], a[164:167], v[36:39]\n"
    "buffer_load_dwordx4 v133, s[40:43], s62 offen offset:1024 lds\n"
    "v_mfma_f32_16x16x32_bf16 v[4:7], v[188:191], a[160:163], v[4:7]\n"
    "ds_read_b64_tr_b16 a[202:203], v140 offset:34816\n"
    "v_mfma_f32_16x16x32_bf16 v[4:7], v[184:187], a[128:131], v[4:7]\n"
    "s_waitcnt lgkmcnt(10)\n"
    "v_mfma_f32_16x16x32_bf16 v[8:11], v[192:195], a[128:131], v[8:11]\n"
    "ds_read_b64_tr_b16 a[232:233], v140 offset:49152\n"
    "v_mfma_f32_16x16x32_bf16 v[8:11], v[196:199], a[160:163], v[8:11]\n"
    "v_mfma_f32_16x16x32_bf16 v[40:43], v[196:199], a[164:167], v[40:43]\n"
    "ds_read_b64_tr_b16 a[234:235], v140 offset:51200\n"
    "v_mfma_f32_16x16x32_bf16 v[40:43], v[192:195], a[132:135], v[40:43]\n"
    "v_mfma_f32_16x16x32_bf16 v[72:75], v[192:195], a[136:139], v[72:75]\n"
    "ds_read_b128 v[176:179], v136 offset:8192\n"
    "v_mfma_f32_16x16x32_bf16 v[72:75], v[196:199], a[168:171], v[72:75]\n"
    "v_mfma_f32_16x16x32_bf16 v[104:107], v[196:199], a[172:175], v[104:107]\n"
    "ds_read_b64_tr_b16 a[204:205], v141 offset:32768\n"
    "v_mfma_f32_16x16x32_bf16 v[104:107], v[192:195], a[140:143], v[104:107]\n"
    "v_mfma_f32_16x16x32_bf16 a[8:11], v[192:195], a[144:147], a[8:11]\n"
    "ds_read_b64_tr_b16 a[206:207], v141 offset:34816\n"
    "v_mfma_f32_16x16x32_bf16 a[8:11], v[196:199], a[176:179], a[8:11]\n"
    "buffer_load_dwordx4 v134, s[40:43], s62 offen offset:2048 lds\n"
    "v_mfma_f32_16x16x32_bf16 a[40:43], v[196:199], a[180:183], a[40:43]\n"
    "ds_read_b128 v[180:183], v137 offset:8192\n"
    "v_mfma_f32_16x16x32_bf16 a[40:43], v[192:195], a[148:151], a[40:43]\n"
    "v_mfma_f32_16x16x32_bf16 a[72:75], v[192:195], a[152:155], a[72:75]\n"
    "ds_read_b64_tr_b16 a[236:237], v141 offset:49152\n"
    "v_mfma_f32_16x16x32_bf16 a[72:75], v[196:199], a[184:187], a[72:75]\n"
    "v_mfma_f32_16x16x32_bf16 a[104:107], v[196:199], a[188:191], a[104:107]\n"
    "ds_read_b64_tr_b16 a[238:239], v141 offset:51200\n"
    "v_mfma_f32_16x16x32_bf16 a[104:107], v[192:195], a[156:159], a[104:107]\n"
    "s_waitcnt lgkmcnt(10)\n"
    "v_mfma_f32_16x16x32_bf16 a[108:111], v[200:203], a[156:159], a[108:111]\n"
    "ds_read_b64_tr_b16 a[208:209], v142 offset:32768\n"
    "v_mfma_f32_16x16x32_bf16 a[108:111], v[204:207], a[188:191], a[108:111]\n"
    "v_mfma_f32_16x16x32_bf16 a[76:79], v[204:207], a[184:187], a[76:79]\n"
    "ds_read_b64_tr_b16 a[210:211], v142 offset:34816\n"
    "v_mfma_f32_16x16x32_bf16 a[76:79], v[200:203], a[152:155], a[76:79]\n"
    "v_mfma_f32_16x16x32_bf16 a[44:47], v[200:203], a[148:151], a[44:47]\n"
    "ds_read_b128 v[184:187], v136 offset:10240\n"
    "v_mfma_f32_16x16x32_bf16 a[44:47], v[204:207], a[180:183], a[44:47]\n"
    "buffer_load_dwordx4 v135, s[40:43], s62 offen offset:3072 lds\n"
    "v_mfma_f32_16x16x32_bf16 a[12:15], v[204:207], a[176:179], a[12:15]\n"
    "ds_read_b64_tr_b16 a[240:241], v142 offset:49152\n"
    "s_add_u32 s62, s62, s71\n"
    "v_mfma_f32_16x16x32_bf16 a[12:15], v[200:203], a[144:147], a[12:15]\n"
    "s_add_u32 m0, s96, 0x4000\n"
    "v_mfma_f32_16x16x32_bf16 v[108:111], v[200:203], a[140:143], v[108:111]\n"
    "ds_read_b64_tr_b16 a[242:243], v142 offset:51200\n"
    "v_mfma_f32_16x16x32_bf16 v[108:111], v[204:207], a[172:175], v[108:111]\n"
    "v_mfma_f32_16x16x32_bf16 v[76:79], v[204:207], a[168:171], v[76:79]\n"
    "ds_read_b128 v[188:191], v137 offset:10240\n"
    "v_mfma_f32_16x16x32_bf16 v[76:79], v[200:203], a[136:139], v[76:79]\n"
    "v_mfma_f32_16x16x32_bf16 v[44:47], v[200:203], a[132:135], v[44:47]\n"
    "ds_read_b64_tr_b16 a[212:213], v143 offset:32768\n"
    "v_mfma_f32_16x16x32_bf16 v[44:47], v[204:207], a[164:167], v[44:47]\n"
    "v_mfma_f32_16x16x32_bf16 v[12:15], v[204:207], a[160:163], v[12:15]\n"
    "ds_read_b64_tr_b16 a[214:215], v143 offset:34816\n"
    "v_mfma_f32_16x16x32_bf16 v[12:15], v[200:203], a[128:131], v[12:15]\n"
    "s_waitcnt lgkmcnt(10)\n"
    "v_mfma_f32_16x16x32_bf16 v[16:19], v[176:179], a[128:131], v[16:19]\n"
    "ds_read_b64_tr_b16 a[244:245], v143 offset:49152\n"
    "v_mfma_f32_16x16x32_bf16 v[16:19], v[180:183], a[160:163], v[16:19]\n"
    "buffer_load_dwordx4 v132, s[40:43], s62 offen offset:0 lds\n"
    "v_mfma_f32_16x16x32_bf16 v[48:51], v[180:183], a[164:167], v[48:51]\n"
    "ds_read_b64_tr_b16 a[246:247], v143 offset:51200\n"
    "v_mfma_f32_16x16x32_bf16 v[48:51], v[176:179], a[132:135], v[48:51]\n"
    "v_mfma_f32_16x16x32_bf16 v[80:83], v[176:179], a[136:139], v[80:83]\n"
    "ds_read_b128 v[192:195], v136 offset:12288\n"
    "v_mfma_f32_16x16x32_bf16 v[80:83], v[180:183], a[168:171], v[80:83]\n"
    "v_mfma_f32_16x16x32_bf16 v[112:115], v[180:183], a[172:175], v[112:115]\n"
    "ds_read_b64_tr_b16 a[216:217], v144 offset:32768\n"
    "v_mfma_f32_16x16x32_bf16 v[112:115], v[176:179], a[140:143], v[112:115]\n"
    "v_mfma_f32_16x16x32_bf16 a[16:19], v[176:179], a[144:147], a[16:19]\n"
    "ds_read_b64_tr_b16 a[218:219], v144 offset:34816\n"
    "v_mfma_f32_16x16x32_bf16 a[16:19], v[180:183], a[176:179], a[16:19]\n"
    "buffer_load_dwordx4 v133, s[40:43], s62 offen offset:1024 lds\n"
    "v_mfma_f32_16x16x32_bf16 a[48:51], v[180:183], a[180:183], a[48:51]\n"
    "ds_read_b128 v[196:199], v137 offset:12288\n"
    "v_mfma_f32_16x16x32_bf16 a[48:51], v[176:179], a[148:151], a[48:51]\n"
    "v_mfma_f32_16x16x32_bf16 a[80:83], v[176:179], a[152:155], a[80:83]\n"
    "ds_read_b64_tr_b16 a[248:249], v144 offset:49152\n"
    "v_mfma_f32_16x16x32_bf16 a[80:83], v[180:183], a[184:187], a[80:83]\n"
    "v_mfma_f32_16x16x32_bf16 a[112:115], v[180:183], a[188:191], a[112:115]\n"
    "ds_read_b64_tr_b16 a[250:251], v144 offset:51200\n"
    "v_mfma_f32_16x16x32_bf16 a[112:115], v[176:179], a[156:159], a[112:115]\n"
    "s_waitcnt lgkmcnt(10)\n"
    "v_mfma_f32_16x16x32_bf16 a[116:119], v[184:187], a[156:159], a[116:119]\n"
    "ds_read_b64_tr_b16 a[220:221], v145 offset:32768\n"
    "v_mfma_f32_16x16x32_bf16 a[116:119], v[188:191], a[188:191], a[116:119]\n"
    "buffer_load_dwordx4 v134, s[40:43], s62 offen offset:2048 lds\n"
    "v_mfma_f32_16x16x32_bf16 a[84:87], v[188:191], a[184:187], a[84:87]\n"
    "ds_read_b64_tr_b16 a[222:223], v145 offset:34816\n"
    "v_mfma_f32_16x16x32_bf16 a[84:87], v[184:187], a[152:155], a[84:87]\n"
    "v_mfma_f32_16x16x32_bf16 a[52:55], v[184:187], a[148:151], a[52:55]\n"
    "ds_read_b128 v[200:203], v136 offset:14336\n"
    "v_mfma_f32_16x16x32_bf16 a[52:55], v[188:191], a[180:183], a[52:55]\n"
    "buffer_load_dwordx4 v135, s[40:43], s62 offen offset:3072 lds\n"
    "v_mfma_f32_16x16x32_bf16 a[20:23], v[188:191], a[176:179], a[20:23]\n"
    "ds_read_b64_tr_b16 a[252:253], v145 offset:49152\n"
    "s_add_u32 s62, s62, s71\n"
    "v_mfma_f32_16x16x32_bf16 a[20:23], v[184:187], a[144:147], a[20:23]\n"
    "s_xor_b32 s96, s96, 0x8000\n"
    "s_mov_b32 m0, s63\n"
    "v_mfma_f32_16x16x32_bf16 v[116:119], v[184:187], a[140:143], v[116:119]\n"
    "ds_read_b64_tr_b16 a[254:255], v145 offset:51200\n"
    "v_mfma_f32_16x16x32_bf16 v[116:119], v[188:191], a[172:175], v[116:119]\n"
    "v_mfma_f32_16x16x32_bf16 v[84:87], v[188:191], a[168:171], v[84:87]\n"
    "ds_read_b128 v[204:207], v137 offset:14336\n"
    "v_mfma_f32_16x16x32_bf16 v[84:87], v[184:187], a[136:139], v[84:87]\n"
    "buffer_load_dwordx4 v128, s[36:39], s61 offen offset:0 lds\n"
    "v_mfma_f32_16x16x32_bf16 v[52:55], v[184:187], a[132:135], v[52:55]\n"
    "v_add_u32 v136, s64, v136\n"
    "v_add_u32 v137, s64, v137\n"
    "v_mfma_f32_16x16x32_bf16 v[52:55], v[188:191], a[164:167], v[52:55]\n"
    "s_waitcnt vmcnt(11)\n"
    "v_mfma_f32_16x16x32_bf16 v[20:23], v[188:191], a[160:163], v[20:23]\n"
    "s_barrier\n"
    "v_mfma_f32_16x16x32_bf16 v[20:23], v[184:187], a[128:131], v[20:23]\n"
    "s_waitcnt lgkmcnt(8)\n"
    "v_mfma_f32_16x16x32_bf16 v[24:27], v[192:195], a[128:131], v[24:27]\n"
    "v_mfma_f32_16x16x32_bf16 v[24:27], v[196:199], a[160:163], v[24:27]\n"
    "buffer_load_dwordx4 v129, s[36:39], s61 offen offset:1024 lds\n"
    "v_mfma_f32_16x16x32_bf16 v[56:59], v[196:199], a[164:167], v[56:59]\n"
    "v_mfma_f32_16x16x32_bf16 v[56:59], v[192:195], a[132:135], v[56:59]\n"
    "v_mfma_f32_16x16x32_bf16 v[88:91], v[192:195], a[136:139], v[88:91]\n"
    "ds_read_b128 v[176:179], v136 offset:0\n"
    "v_mfma_f32_16x16x32_bf16 v[88:91], v[196:199], a[168:171], v[88:91]\n"
    "buffer_load_dwordx4 v130, s[36:39], s61 offen offset:2048 lds\n"
    "v_mfma_f32_16x16x32_bf16 v[120:123], v[196:199], a[172:175], v[120:123]\n"
    "v_mfma_f32_16x16x32_bf16 v[120:123], v[192:195], a[140:143], v[120:123]\n"
    "v_mfma_f32_16x16x32_bf16 a[24:27], v[192:195], a[144:147], a[24:27]\n"
    "v_mfma_f32_16x16x32_bf16 a[24:27], v[196:199], a[176:179], a[24:27]\n"
    "buffer_load_dwordx4 v131, s[36:39], s61 offen offset:3072 lds\n"
    "v_mfma_f32_16x16x32_bf16 a[56:59], v[196:199], a[180:183], a[56:59]\n"
    "ds_read_b128 v[180:183], v137 offset:0\n"
    "s_add_u32 s61, s61, s70\n"
    "v_mfma_f32_16x16x32_bf16 a[56:59], v[192:195], a[148:151], a[56:59]\n"
    "s_add_u32 m0, s63, 0x1000\n"
    "v_mfma_f32_16x16x32_bf16 a[88:91], v[192:195], a[152:155], a[88:91]\n"
    "v_mfma_f32_16x16x32_bf16 a[88:91], v[196:199], a[184:187], a[88:91]\n"
    "buffer_load_dwordx4 v128, s[36:39], s61 offen offset:0 lds\n"
    "v_mfma_f32_16x16x32_bf16 a[120:123], v[196:199], a[188:191], a[120:123]\n"
    "v_mfma_f32_16x16x32_bf16 a[120:123], v[192:195], a[156:159], a[120:123]\n"
    "s_waitcnt lgkmcnt(2)\n"
    "v_mfma_f32_16x16x32_bf16 a[124:127], v[200:203], a[156:159], a[124:127]\n"
    "v_mfma_f32_16x16x32_bf16 a[124:127], v[204:207], a[188:191], a[124:127]\n"
    "buffer_load_dwordx4 v129, s[36:39], s61 offen offset:1024 lds\n"
    "v_mfma_f32_16x16x32_bf16 a[92:95], v[204:207], a[184:187], a[92:95]\n"
    "v_mfma_f32_16x16x32_bf16 a[92:95], v[200:203], a[152:155], a[92:95]\n"
    "v_mfma_f32_16x16x32_bf16 a[60:63], v[200:203], a[148:151], a[60:63]\n"
    "ds_read_b128 v[184:187], v136 offset:2048\n"
    "v_mfma_f32_16x16x32_bf16 a[60:63], v[204:207], a[180:183], a[60:63]\n"
    "buffer_load_dwordx4 v130, s[36:39], s61 offen offset:2048 lds\n"
    "v_mfma_f32_16x16x32_bf16 a[28:31], v[204:207], a[176:179], a[28:31]\n"
    "v_mfma_f32_16x16x32_bf16 a[28:31], v[200:203], a[144:147], a[28:31]\n"
    "v_mfma_f32_16x16x32_bf16 v[124:127], v[200:203], a[140:143], v[124:127]\n"
    "v_mfma_f32_16x16x32_bf16 v[124:127], v[204:207], a[172:175], v[124:127]\n"
    "buffer_load_dwordx4 v131, s[36:39], s61 offen offset:3072 lds\n"
    "v_mfma_f32_16x16x32_bf16 v[92:95], v[204:207], a[168:171], v[92:95]\n"
    "ds_read_b128 v[188:191], v137 offset:2048\n"
    "s_add_u32 s61, s61, s72\n"
    "v_mfma_f32_16x16x32_bf16 v[92:95], v[200:203], a[136:139], v[92:95]\n"
    "s_add_u32 s63, s63, 0x8000\n"
    "s_cmp_ge_u32 s63, 0x28000\n"
    "s_cselect_b32 s78, 0x18000, 0\n"
    "v_mfma_f32_16x16x32_bf16 v[60:63], v[200:203], a[132:135], v[60:63]\n"
    "s_sub_u32 s63, s63, s78\n"
    "v_mfma_f32_16x16x32_bf16 v[60:63], v[204:207], a[164:167], v[60:63]\n"
    "s_add_u32 s66, s66, 1\n"
    "v_mfma_f32_16x16x32_bf16 v[28:31], v[204:207], a[160:163], v[28:31]\n"
    "s_waitcnt lgkmcnt(2)\n"
    "v_mfma_f32_16x16x32_bf16 v[28:31], v[200:203], a[128:131], v[28:31]\n"
    "MIDV_%=:\n"
    "s_bitcmp1_b32 s81, s66\n"
    "s_cbranch_scc1 SWMIDV_%=\n"
    "SWBMIDV_%=:\n"
    "v_mfma_f32_16x16x32_bf16 v[0:3], v[176:179], a[192:195], v[0:3]\n"
    "s_add_u32 s65, s65, 1\n"
    "s_cmp_eq_u32 s65, 3\n"
    "v_mfma_f32_16x16x32_bf16 v[0:3], v[180:183], a[224:227], v[0:3]\n"
    "s_cselect_b32 s64, s79, s80\n"
    "s_cselect_b32 s65, 0, s65\n"
    "v_mfma_f32_16x16x32_bf16 v[32:35], v[180:183], a[228:231], v[32:35]\n"
    "s_waitcnt vmcnt(8)\n"
    "v_mfma_f32_16x16x32_bf16 v[32:35], v[176:179], a[196:199], v[32:35]\n"
    "s_barrier\n"
    "v_mfma_f32_16x16x32_bf16 v[64:67], v[176:179], a[200:203], v[64:67]\n"
    "ds_read_b128 v[192:195], v136 offset:4096\n"
    "v_mfma_f32_16x16x32_bf16 v[64:67], v[180:183], a[232:235], v[64:67]\n"
    "buffer_load_dword v254, v147, s[92:95], s97 offen offset:0\n"
    "v_mfma_f32_16x16x32_bf16 v[96:99], v[180:183], a[236:239], v[96:99]\n"
    "ds_read_b64_tr_b16 a[128:129], v138 offset:0\n"
    "s_add_u32 s97, s97, s98\n"
    "v_mfma_f32_16x16x32_bf16 v[96:99], v[176:179], a[204:207], v[96:99]\n"
    "buffer_load_dword v254, v162, s[84:87], s88 offen offset:0\n"
    "v_mfma_f32_16x16x32_bf16 a[0:3], v[176:179], a[208:211], a[0:3]\n"
    "ds_read_b64_tr_b16 a[130:131], v138 offset:2048\n"
    "s_add_u32 s88, s88, s89\n"
    "v_mfma_f32_16x16x32_bf16 a[0:3], v[180:183], a[240:243], a[0:3]\n"
    "s_mov_b32 m0, s96\n"
    "v_mfma_f32_16x16x32_bf16 a[32:35], v[180:183], a[244:247], a[32:35]\n"
    "ds_read_b128 v[196:199], v137 offset:4096\n"
    "v_mfma_f32_16x16x32_bf16 a[32:35], v[176:179], a[212:215], a[32:35]\n"
    "v_mfma_f32_16x16x32_bf16 a[64:67], v[176:179], a[216:219], a[64:67]\n"
    "ds_read_b64_tr_b16 a[160:161], v138 offset:16384\n"
    "v_mfma_f32_16x16x32_bf16 a[64:67], v[180:183], a[248:251], a[64:67]\n"
    "v_mfma_f32_16x16x32_bf16 a[96:99], v[180:183], a[252:255], a[96:99]\n"
    "ds_read_b64_tr_b16 a[162:163], v138 offset:18432\n"
    "v_mfma_f32_16x16x32_bf16 a[96:99], v[176:179], a[220:223], a[96:99]\n"
    "s_waitcnt lgkmcnt(6)\n"
    "v_mfma_f32_16x16x32_bf16 a[100:103], v[184:187], a[220:223], a[100:103]\n"
    "ds_read_b64_tr_b16 a[132:133], v139 offset:0\n"
    "v_mfma_f32_16x16x32_bf16 a[100:103], v[188:191], a[252:255], a[100:103]\n"
    "buffer_load_dwordx4 v132, s[40:43], s62 offen offset:0 lds\n"
    "v_mfma_f32_16x16x32_bf16 a[68:71], v[188:191], a[248:251], a[68:71]\n"
    "ds_read_b64_tr_b16 a[134:135], v139 offset:2048\n"
    "v_mfma_f32_16x16x32_bf16 a[68:71], v[184:187], a[216:219], a[68:71]\n"
    "v_mfma_f32_16x16x32_bf16 a[36:39], v[184:187], a[212:215], a[36:39]\n"
    "ds_read_b128 v[200:203], v136 offset:6144\n"
    "v_mfma_f32_16x16x32_bf16 a[36:39], v[188:191], a[244:247], a[36:39]\n"
    "v_mfma_f32_16x16x32_bf16 a[4:7], v[188:191], a[240:243], a[4:7]\n"
    "ds_read_b64_tr_b16 a[164:165], v139 offset:16384\n"
    "v_mfma_f32_16x16x32_bf16 a[4:7], v[184:187], a[208:211], a[4:7]\n"
    "v_mfma_f32_16x16x32_bf16 v[100:103], v[184:187], a[204:207], v[100:103]\n"
    "ds_read_b64_tr_b16 a[166:167], v139 offset:18432\n"
    "v_mfma_f32_16x16x32_bf16 v[100:103], v[188:191], a[236:239], v[100:103]\n"
    "v_mfma_f32_16x16x32_bf16 v[68:71], v[188:191], a[232:235], v[68:71]\n"
    "ds_read_b128 v[204:207], v137 offset:6144\n"
    "v_mfma_f32_16x16x32_bf16 v[68:71], v[184:187], a[200:203], v[68:71]\n"
    "v_mfma_f32_16x16x32_bf16 v[36:39], v[184:187], a[196:199], v[36:39]\n"
    "ds_read_b64_tr_b16 a[136:137], v140 offset:0\n"
    "v_mfma_f32_16x16x32_bf16 v[36:39], v[188:191], a[228:231], v[36:39]\n"
    "buffer_load_dwordx4 v133, s[40:43], s62 offen offset:1024 lds\n"
    "v_mfma_f32_16x16x32_bf16 v[4:7], v[188:191], a[224:227], v[4:7]\n"
    "ds_read_b64_tr_b16 a[138:139], v140 offset:2048\n"
    "v_mfma_f32_16x16x32_bf16 v[4:7], v[184:187], a[192:195], v[4:7]\n"
    "s_waitcnt lgkmcnt(10)\n"
    "v_mfma_f32_16x16x32_bf16 v[8:11], v[192:195], a[192:195], v[8:11]\n"
    "ds_read_b64_tr_b16 a[168:169], v140 offset:16384\n"
    "v_mfma_f32_16x16x32_bf16 v[8:11], v[196:199], a[224:227], v[8:11]\n"
    "v_mfma_f32_16x16x32_bf16 v[40:43], v[196:199], a[228:231], v[40:43]\n"
    "ds_read_b64_tr_b16 a[170:171], v140 offset:18432\n"
    "v_mfma_f32_16x16x32_bf16 v[40:43], v[192:195], a[196:199], v[40:43]\n"
    "v_mfma_f32_16x16x32_bf16 v[72:75], v[192:195], a[200:203], v[72:75]\n"
    "ds_read_b128 v[176:179], v136 offset:8192\n"
    "v_mfma_f32_16x16x32_bf16 v[72:75], v[196:199], a[232:235], v[72:75]\n"
    "v_mfma_f32_16x16x32_bf16 v[104:107], v[196:199], a[236:239], v[104:107]\n"
    "ds_read_b64_tr_b16 a[140:141], v141 offset:0\n"
    "v_mfma_f32_16x16x32_bf16 v[104:107], v[192:195], a[204:207], v[104:107]\n"
    "v_mfma_f32_16x16x32_bf16 a[8:11], v[192:195], a[208:211], a[8:11]\n"
    "ds_read_b64_tr_b16 a[142:143], v141 offset:2048\n"
    "v_mfma_f32_16x16x32_bf16 a[8:11], v[196:199], a[240:243], a[8:11]\n"
    "buffer_load_dwordx4 v134, s[40:43], s62 offen offset:2048 lds\n"
    "v_mfma_f32_16x16x32_bf16 a[40:43], v[196:199], a[244:247], a[40:43]\n"
    "ds_read_b128 v[180:183], v137 offset:8192\n"
    "v_mfma_f32_16x16x32_bf16 a[40:43], v[192:195], a[212:215], a[40:43]\n"
    "v_mfma_f32_16x16x32_bf16 a[72:75], v[192:195], a[216:219], a[72:75]\n"
    "ds_read_b64_tr_b16 a[172:173], v141 offset:16384\n"
    "v_mfma_f32_16x16x32_bf16 a[72:75], v[196:199], a[248:251], a[72:75]\n"
    "v_mfma_f32_16x16x32_bf16 a[104:107], v[196:199], a[252:255], a[104:107]\n"
    "ds_read_b64_tr_b16 a[174:175], v141 offset:18432\n"
    "v_mfma_f32_16x16x32_bf16 a[104:107], v[192:195], a[220:223], a[104:107]\n"
    "s_waitcnt lgkmcnt(10)\n"
    "v_mfma_f32_16x16x32_bf16 a[108:111], v[200:203], a[220:223], a[108:111]\n"
    "ds_read_b64_tr_b16 a[144:145], v142 offset:0\n"
    "v_mfma_f32_16x16x32_bf16 a[108:111], v[204:207], a[252:255], a[108:111]\n"
    "v_mfma_f32_16x16x32_bf16 a[76:79], v[204:207], a[248:251], a[76:79]\n"
    "ds_read_b64_tr_b16 a[146:147], v142 offset:2048\n"
    "v_mfma_f32_16x16x32_bf16 a[76:79], v[200:203], a[216:219], a[76:79]\n"
    "v_mfma_f32_16x16x32_bf16 a[44:47], v[200:203], a[212:215], a[44:47]\n"
    "ds_read_b128 v[184:187], v136 offset:10240\n"
    "v_mfma_f32_16x16x32_bf16 a[44:47], v[204:207], a[244:247], a[44:47]\n"
    "buffer_load_dwordx4 v135, s[40:43], s62 offen offset:3072 lds\n"
    "v_mfma_f32_16x16x32_bf16 a[12:15], v[204:207], a[240:243], a[12:15]\n"
    "ds_read_b64_tr_b16 a[176:177], v142 offset:16384\n"
    "s_add_u32 s62, s62, s71\n"
    "v_mfma_f32_16x16x32_bf16 a[12:15], v[200:203], a[208:211], a[12:15]\n"
    "s_add_u32 m0, s96, 0x4000\n"
    "v_mfma_f32_16x16x32_bf16 v[108:111], v[200:203], a[204:207], v[108:111]\n"
    "ds_read_b64_tr_b16 a[178:179], v142 offset:18432\n"
    "v_mfma_f32_16x16x32_bf16 v[108:111], v[204:207], a[236:239], v[108:111]\n"
    "v_mfma_f32_16x16x32_bf16 v[76:79], v[204:207], a[232:235], v[76:79]\n"
    "ds_read_b128 v[188:191], v137 offset:10240\n"
    "v_mfma_f32_16x16x32_bf16 v[76:79], v[200:203], a[200:203], v[76:79]\n"
    "v_mfma_f32_16x16x32_bf16 v[44:47], v[200:203], a[196:199], v[44:47]\n"
    "ds_read_b64_tr_b16 a[148:149], v143 offset:0\n"
    "v_mfma_f32_16x16x32_bf16 v[44:47], v[204:207], a[228:231], v[44:47]\n"
    "v_mfma_f32_16x16x32_bf16 v[12:15], v[204:207], a[224:227], v[12:15]\n"
    "ds_read_b64_tr_b16 a[150:151], v143 offset:2048\n"
    "v_mfma_f32_16x16x32_bf16 v[12:15], v[200:203], a[192:195], v[12:15]\n"
    "s_waitcnt lgkmcnt(10)\n"
    "v_mfma_f32_16x16x32_bf16 v[16:19], v[176:179], a[192:195], v[16:19]\n"
    "ds_read_b64_tr_b16 a[180:181], v143 offset:16384\n"
    "v_mfma_f32_16x16x32_bf16 v[16:19], v[180:183], a[224:227], v[16:19]\n"
    "buffer_load_dwordx4 v132, s[40:43], s62 offen offset:0 lds\n"
    "v_mfma_f32_16x16x32_bf16 v[48:51], v[180:183], a[228:231], v[48:51]\n"
    "ds_read_b64_tr_b16 a[182:183], v143 offset:18432\n"
    "v_mfma_f32_16x16x32_bf16 v[48:51], v[176:179], a[196:199], v[48:51]\n"
    "v_mfma_f32_16x16x32_bf16 v[80:83], v[176:179], a[200:203], v[80:83]\n"
    "ds_read_b128 v[192:195], v136 offset:12288\n"
    "v_mfma_f32_16x16x32_bf16 v[80:83], v[180:183], a[232:235], v[80:83]\n"
    "v_mfma_f32_16x16x32_bf16 v[112:115], v[180:183], a[236:239], v[112:115]\n"
    "ds_read_b64_tr_b16 a[152:153], v144 offset:0\n"
    "v_mfma_f32_16x16x32_bf16 v[112:115], v[176:179], a[204:207], v[112:115]\n"
    "v_mfma_f32_16x16x32_bf16 a[16:19], v[176:179], a[208:211], a[16:19]\n"
    "ds_read_b64_tr_b16 a[154:155], v144 offset:2048\n"
    "v_mfma_f32_16x16x32_bf16 a[16:19], v[180:183], a[240:243], a[16:19]\n"
    "buffer_load_dwordx4 v133, s[40:43], s62 offen offset:1024 lds\n"
    "v_mfma_f32_16x16x32_bf16 a[48:51], v[180:183], a[244:247], a[48:51]\n"
    "ds_read_b128 v[196:199], v137 offset:12288\n"
    "v_mfma_f32_16x16x32_bf16 a[48:51], v[176:179], a[212:215], a[48:51]\n"
    "v_mfma_f32_16x16x32_bf16 a[80:83], v[176:179], a[216:219], a[80:83]\n"
    "ds_read_b64_tr_b16 a[184:185], v144 offset:16384\n"
    "v_mfma_f32_16x16x32_bf16 a[80:83], v[180:183], a[248:251], a[80:83]\n"
    "v_mfma_f32_16x16x32_bf16 a[112:115], v[180:183], a[252:255], a[112:115]\n"
    "ds_read_b64_tr_b16 a[186:187], v144 offset:18432\n"
    "v_mfma_f32_16x16x32_bf16 a[112:115], v[176:179], a[220:223], a[112:115]\n"
    "s_waitcnt lgkmcnt(10)\n"
    "v_mfma_f32_16x16x32_bf16 a[116:119], v[184:187], a[220:223], a[116:119]\n"
    "ds_read_b64_tr_b16 a[156:157], v145 offset:0\n"
    "v_mfma_f32_16x16x32_bf16 a[116:119], v[188:191], a[252:255], a[116:119]\n"
    "buffer_load_dwordx4 v134, s[40:43], s62 offen offset:2048 lds\n"
    "v_mfma_f32_16x16x32_bf16 a[84:87], v[188:191], a[248:251], a[84:87]\n"
    "ds_read_b64_tr_b16 a[158:159], v145 offset:2048\n"
    "v_mfma_f32_16x16x32_bf16 a[84:87], v[184:187], a[216:219], a[84:87]\n"
    "v_mfma_f32_16x16x32_bf16 a[52:55], v[184:187], a[212:215], a[52:55]\n"
    "ds_read_b128 v[200:203], v136 offset:14336\n"
    "v_mfma_f32_16x16x32_bf16 a[52:55], v[188:191], a[244:247], a[52:55]\n"
    "buffer_load_dwordx4 v135, s[40:43], s62 offen offset:3072 lds\n"
    "v_mfma_f32_16x16x32_bf16 a[20:23], v[188:191], a[240:243], a[20:23]\n"
    "ds_read_b64_tr_b16 a[188:189], v145 offset:16384\n"
    "s_add_u32 s62, s62, s71\n"
    "v_mfma_f32_16x16x32_bf16 a[20:23], v[184:187], a[208:211], a[20:23]\n"
    "s_xor_b32 s96, s96, 0x8000\n"
    "s_mov_b32 m0, s63\n"
    "v_mfma_f32_16x16x32_bf16 v[116:119], v[184:187], a[204:207], v[116:119]\n"
    "ds_read_b64_tr_b16 a[190:191], v145 offset:18432\n"
    "v_mfma_f32_16x16x32_bf16 v[116:119], v[188:191], a[236:239], v[116:119]\n"
    "v_mfma_f32_16x16x32_bf16 v[84:87], v[188:191], a[232:235], v[84:87]\n"
    "ds_read_b128 v[204:207], v137 offset:14336\n"
    "v_mfma_f32_16x16x32_bf16 v[84:87], v[184:187], a[200:203], v[84:87]\n"
    "buffer_load_dwordx4 v128, s[36:39], s61 offen offset:0 lds\n"
    "v_mfma_f32_16x16x32_bf16 v[52:55], v[184:187], a[196:199], v[52:55]\n"
    "v_add_u32 v136, s64, v136\n"
    "v_add_u32 v137, s64, v137\n"
    "v_mfma_f32_16x16x32_bf16 v[52:55], v[188:191], a[228:231], v[52:55]\n"
    "s_waitcnt vmcnt(11)\n"
    "v_mfma_f32_16x16x32_bf16 v[20:23], v[188:191], a[224:227], v[20:23]\n"
    "s_barrier\n"
    "v_mfma_f32_16x16x32_bf16 v[20:23], v[184:187], a[192:195], v[20:23]\n"
    "s_waitcnt lgkmcnt(8)\n"
    "v_mfma_f32_16x16x32_bf16 v[24:27], v[192:195], a[192:195], v[24:27]\n"
    "v_mfma_f32_16x16x32_bf16 v[24:27], v[196:199], a[224:227], v[24:27]\n"
    "buffer_load_dwordx4 v129, s[36:39], s61 offen offset:1024 lds\n"
    "v_mfma_f32_16x16x32_bf16 v[56:59], v[196:199], a[228:231], v[56:59]\n"
    "v_mfma_f32_16x16x32_bf16 v[56:59], v[192:195], a[196:199], v[56:59]\n"
    "v_mfma_f32_16x16x32_bf16 v[88:91], v[192:195], a[200:203], v[88:91]\n"
    "ds_read_b128 v[176:179], v136 offset:0\n"
    "v_mfma_f32_16x16x32_bf16 v[88:91], v[196:199], a[232:235], v[88:91]\n"
    "buffer_load_dwordx4 v130, s[36:39], s61 offen offset:2048 lds\n"
    "v_mfma_f32_16x16x32_bf16 v[120:123], v[196:199], a[236:239], v[120:123]\n"
    "v_mfma_f32_16x16x32_bf16 v[120:123], v[192:195], a[204:207], v[120:123]\n"
    "v_mfma_f32_16x16x32_bf16 a[24:27], v[192:195], a[208:211], a[24:27]\n"
    "v_mfma_f32_16x16x32_bf16 a[24:27], v[196:199], a[240:243], a[24:27]\n"
    "buffer_load_dwordx4 v131, s[36:39], s61 offen offset:3072 lds\n"
    "v_mfma_f32_16x16x32_bf16 a[56:59], v[196:199], a[244:247], a[56:59]\n"
    "ds_read_b128 v[180:183], v137 offset:0\n"
    "s_add_u32 s61, s61, s70\n"
    "v_mfma_f32_16x16x32_bf16 a[56:59], v[192:195], a[212:215], a[56:59]\n"
    "s_add_u32 m0, s63, 0x1000\n"
    "v_mfma_f32_16x16x32_bf16 a[88:91], v[192:195], a[216:219], a[88:91]\n"
    "v_mfma_f32_16x16x32_bf16 a[88:91], v[196:199], a[248:251], a[88:91]\n"
    "buffer_load_dwordx4 v128, s[36:39], s61 offen offset:0 lds\n"
    "v_mfma_f32_16x16x32_bf16 a[120:123], v[196:199], a[252:255], a[120:123]\n"
    "v_mfma_f32_16x16x32_bf16 a[120:123], v[192:195], a[220:223], a[120:123]\n"
    "s_waitcnt lgkmcnt(2)\n"
    "v_mfma_f32_16x16x32_bf16 a[124:127], v[200:203], a[220:223], a[124:127]\n"
    "v_mfma_f32_16x16x32_bf16 a[124:127], v[204:207], a[252:255], a[124:127]\n"
    "buffer_load_dwordx4 v129, s[36:39], s61 offen offset:1024 lds\n"
    "v_mfma_f32_16x16x32_bf16 a[92:95], v[204:207], a[248:251], a[92:95]\n"
    "v_mfma_f32_16x16x32_bf16 a[92:95], v[200:203], a[216:219], a[92:95]\n"
    "v_mfma_f32_16x16x32_bf16 a[60:63], v[200:203], a[212:215], a[60:63]\n"
    "ds_read_b128 v[184:187], v136 offset:2048\n"
    "v_mfma_f32_16x16x32_bf16 a[60:63], v[204:207], a[244:247], a[60:63]\n"
    "buffer_load_dwordx4 v130, s[36:39], s61 offen offset:2048 lds\n"
    "v_mfma_f32_16x16x32_bf16 a[28:31], v[204:207], a[240:243], a[28:31]\n"
    "v_mfma_f32_16x16x32_bf16 a[28:31], v[200:203], a[208:211], a[28:31]\n"
    "v_mfma_f32_16x16x32_bf16 v[124:127], v[200:203], a[204:207], v[124:127]\n"
    "v_mfma_f32_16x16x32_bf16 v[124:127], v[204:207], a[236:239], v[124:127]\n"
    "buffer_load_dwordx4 v131, s[36:39], s61 offen offset:3072 lds\n"
    "v_mfma_f32_16x16x32_bf16 v[92:95], v[204:207], a[232:235], v[92:95]\n"
    "ds_read_b128 v[188:191], v137 offset:2048\n"
    "s_add_u32 s61, s61, s72\n"
    "v_mfma_f32_16x16x32_bf16 v[92:95], v[200:203], a[200:203], v[92:95]\n"
    "s_add_u32 s63, s63, 0x8000\n"
    "s_cmp_ge_u32 s63, 0x28000\n"
    "s_cselect_b32 s78, 0x18000, 0\n"
    "v_mfma_f32_16x16x32_bf16 v[60:63], v[200:203], a[196:199], v[60:63]\n"
    "s_sub_u32 s63, s63, s78\n"
    "v_mfma_f32_16x16x32_bf16 v[60:63], v[204:207], a[228:231], v[60:63]\n"
    "s_add_u32 s66, s66, 1\n"
    "v_mfma_f32_16x16x32_bf16 v[28:31], v[204:207], a[224:227], v[28:31]\n"
    "s_waitcnt lgkmcnt(2)\n"
    "s_cmp_lt_u32 s66, 16\n"
    "v_mfma_f32_16x16x32_bf16 v[28:31], v[200:203], a[192:195], v[28:31]\n"
    "s_cbranch_scc1 LOOPV_%=\n"
    "APH_%=:\n"
    "v_readfirstlane_b32 s34, v164\n"
    "v_readfirstlane_b32 s35, v165\n"
    "s_nop 0\n"
    "s_add_u32 s34, s34, 1\n"
    "s_addc_u32 s35, s35, 0\n"
    "s_lshl_b32 s63, s60, 13\n"
    "s_lshl_b32 s96, s60, 12\n"
    "s_cmp_eq_u32 s60, 3\n"
    "s_cselect_b32 s65, 1, 0\n"
    "v_add_u32 v156, 0x18000, v156\n"
    "ds_read_b128 a[152:155], v156 offset:0\n"
    "ds_read_b128 a[156:159], v156 offset:2048\n"
    "s_mul_i32 s78, s65, 0xfffe3000\n"
    "s_add_u32 s78, s78, 0x1a000\n"
    "v_add_u32 v252, s78, v158\n"
    "ds_read_b128 a[224:227], v252\n"
    "ds_read_b64_tr_b16 a[128:129], v160 offset:0\n"
    "ds_read_b64_tr_b16 a[130:131], v160 offset:256\n"
    "ds_read_b64_tr_b16 a[136:137], v160 offset:4096\n"
    "ds_read_b64_tr_b16 a[138:139], v160 offset:4352\n"
    "ds_read_b64_tr_b16 a[144:145], v160 offset:8192\n"
    "ds_read_b64_tr_b16 a[146:147], v160 offset:8448\n"
    "ds_read_b64_tr_b16 a[132:133], v161 offset:0\n"
    "ds_read_b64_tr_b16 a[134:135], v161 offset:256\n"
    "ds_read_b64_tr_b16 a[140:141], v161 offset:4096\n"
    "ds_read_b64_tr_b16 a[142:143], v161 offset:4352\n"
    "ds_read_b64_tr_b16 a[148:149], v161 offset:8192\n"
    "ds_read_b64_tr_b16 a[150:151], v161 offset:8448\n"
    "v_accvgpr_write_b32 a232, v208\n"
    "v_accvgpr_write_b32 a233, v209\n"
    "v_accvgpr_write_b32 a234, v210\n"
    "v_accvgpr_write_b32 a235, v211\n"
    "v_accvgpr_write_b32 a236, v212\n"
    "v_accvgpr_write_b32 a237, v213\n"
    "v_accvgpr_write_b32 a238, v214\n"
    "v_accvgpr_write_b32 a239, v215\n"
    "v_accvgpr_write_b32 a240, v216\n"
    "v_accvgpr_write_b32 a241, v217\n"
    "v_accvgpr_write_b32 a242, v218\n"
    "v_accvgpr_write_b32 a243, v219\n"
    "v_accvgpr_write_b32 a244, v220\n"
    "v_accvgpr_write_b32 a245, v221\n"
    "v_accvgpr_write_b32 a246, v222\n"
    "v_accvgpr_write_b32 a247, v223\n"
    "s_waitcnt lgkmcnt(0)\n"
    "v_mfma_f32_16x16x32_bf16 v[192:195], a[136:139], a[152:155], 0\n"
    "v_add_u32 v157, 0x18000, v157\n"
    "s_bitcmp1_b32 s26, 4\n"
    "s_cselect_b32 s66, 1.0, 0x3db8aa3b\n"
    "s_add_u32 s77, s76, s68\n"
    "v_mfma_f32_16x16x32_bf16 v[196:199], a[140:143], a[152:155], 0\n"
    "ds_read_b128 a[184:187], v157 offset:0\n"
    "ds_read_b128 a[188:191], v157 offset:2048\n"
    "s_mul_i32 s73, s69, 3\n"
    "s_sub_u32 s73, s68, s73\n"
    "v_mfma_f32_16x16x32_bf16 v[216:219], a[152:155], a[144:147], 0\n"
    "s_mul_i32 s78, s65, 0xfffe3000\n"
    "s_add_u32 s78, s78, 0x1a000\n"
    "s_mov_b32 s68, s67\n"
    "v_mul_f32 v0, s66, v0\n"
    "v_mfma_f32_16x16x32_bf16 v[228:231], a[152:155], a[148:151], 0\n"
    "v_add_u32 v252, s78, v159\n"
    "v_mul_f32 v1, s66, v1\n"
    "v_mul_f32 v2, s66, v2\n"
    "v_mul_f32 v3, s66, v3\n"
    "v_mfma_f32_16x16x32_bf16 v[200:203], a[136:139], a[156:159], 0\n"
    "ds_read_b128 a[228:231], v252\n"
    "ds_read_b64_tr_b16 a[160:161], v160 offset:2048\n"
    "v_mul_f32 v4, s66, v4\n"
    "v_mul_f32 v5, s66, v5\n"
    "v_mfma_f32_16x16x32_bf16 v[204:207], a[140:143], a[156:159], 0\n"
    "ds_read_b64_tr_b16 a[162:163], v160 offset:2304\n"
    "v_mul_f32 v6, s66, v6\n"
    "v_mul_f32 v7, s66, v7\n"
    "v_mfma_f32_16x16x32_bf16 v[220:223], a[156:159], a[144:147], 0\n"
    "ds_read_b64_tr_b16 a[168:169], v160 offset:6144\n"
    "ds_read_b64_tr_b16 a[170:171], v160 offset:6400\n"
    "v_cvt_pk_bf16_f32 v0, v0, v1\n"
    "v_cvt_pk_bf16_f32 v1, v2, v3\n"
    "v_mfma_f32_16x16x32_bf16 v[232:235], a[156:159], a[148:151], 0\n"
    "ds_read_b64_tr_b16 a[176:177], v160 offset:10240\n"
    "v_cvt_pk_bf16_f32 v2, v4, v5\n"
    "v_cvt_pk_bf16_f32 v3, v6, v7\n"
    "v_mfma_f32_16x16x32_bf16 v[176:179], a[128:131], a[156:159], 0\n"
    "ds_read_b64_tr_b16 a[178:179], v160 offset:10496\n"
    "ds_read_b64_tr_b16 a[164:165], v161 offset:2048\n"
    "buffer_store_dwordx4 v[0:3], v146, s[44:47], s68 offen offset:0 sc1\n"
    "v_mfma_f32_16x16x32_bf16 v[180:183], a[132:135], a[156:159], 0\n"
    "ds_read_b64_tr_b16 a[166:167], v161 offset:2304\n"
    "v_mul_f32 v32, s66, v32\n"
    "v_mul_f32 v33, s66, v33\n"
    "v_mul_f32 v34, s66, v34\n"
    "v_mul_f32 v35, s66, v35\n"
    "v_mfma_f32_16x16x32_bf16 v[208:211], a[136:139], a[224:227], 0\n"
    "ds_read_b64_tr_b16 a[172:173], v161 offset:6144\n"
    "ds_read_b64_tr_b16 a[174:175], v161 offset:6400\n"
    "v_mul_f32 v36, s66, v36\n"
    "v_mul_f32 v37, s66, v37\n"
    "v_mfma_f32_16x16x32_bf16 v[212:215], a[140:143], a[224:227], 0\n"
    "ds_read_b64_tr_b16 a[180:181], v161 offset:10240\n"
    "v_mul_f32 v38, s66, v38\n"
    "v_mul_f32 v39, s66, v39\n"
    "v_cvt_pk_bf16_f32 v32, v32, v33\n"
    "v_mfma_f32_16x16x32_bf16 v[224:227], a[224:227], a[144:147], 0\n"
    "ds_read_b64_tr_b16 a[182:183], v161 offset:10496\n"
    "v_cvt_pk_bf16_f32 v33, v34, v35\n"
    "v_cvt_pk_bf16_f32 v34, v36, v37\n"
    "v_mfma_f32_16x16x32_bf16 v[236:239], a[224:227], a[148:151], 0\n"
    "v_cvt_pk_bf16_f32 v35, v38, v39\n"
    "v_mfma_f32_16x16x32_bf16 v[184:187], a[128:131], a[224:227], 0\n"
    "buffer_store_dwordx4 v[32:35], v146, s[44:47], s68 offen offset:1024 sc1\n"
    "v_mfma_f32_16x16x32_bf16 v[188:191], a[132:135], a[224:227], 0\n"
    "s_waitcnt lgkmcnt(0)\n"
    "v_mfma_f32_16x16x32_bf16 v[192:195], a[168:171], a[184:187], v[192:195]\n"
    "s_waitcnt vmcnt(28)\n"
    "s_barrier\n"
    "v_mul_f32 v8, s66, v8\n"
    "v_mfma_f32_16x16x32_bf16 v[196:199], a[172:175], a[184:187], v[196:199]\n"
    "ds_read_b128 a[152:155], v156 offset:4096\n"
    "ds_read_b128 a[156:159], v156 offset:6144\n"
    "v_mul_f32 v9, s66, v9\n"
    "v_mfma_f32_16x16x32_bf16 v[216:219], a[184:187], a[176:179], v[216:219]\n"
    "s_mul_i32 s78, s65, 0xfffe6000\n"
    "s_add_u32 m0, s63, 0x10000\n"
    "v_mul_f32 v10, s66, v10\n"
    "v_mfma_f32_16x16x32_bf16 v[228:231], a[184:187], a[180:183], v[228:231]\n"
    "s_add_u32 s78, s78, 0x1b000\n"
    "v_add_u32 v252, s78, v158\n"
    "v_mul_f32 v11, s66, v11\n"
    "v_mfma_f32_16x16x32_bf16 v[200:203], a[168:171], a[188:191], v[200:203]\n"
    "ds_read_b128 a[224:227], v252\n"
    "buffer_load_dwordx4 v128, s[36:39], s61 offen offset:0 lds\n"
    "v_mul_f32 v12, s66, v12\n"
    "v_mfma_f32_16x16x32_bf16 v[204:207], a[172:175], a[188:191], v[204:207]\n"
    "ds_read_b64_tr_b16 a[128:129], v160 offset:16384\n"
    "ds_read_b64_tr_b16 a[130:131], v160 offset:16640\n"
    "v_mul_f32 v13, s66, v13\n"
    "v_mfma_f32_16x16x32_bf16 v[220:223], a[188:191], a[176:179], v[220:223]\n"
    "ds_read_b64_tr_b16 a[136:137], v160 offset:20480\n"
    "buffer_load_dwordx4 v129, s[36:39], s61 offen offset:1024 lds\n"
    "v_mul_f32 v14, s66, v14\n"
    "v_mfma_f32_16x16x32_bf16 v[232:235], a[188:191], a[180:183], v[232:235]\n"
    "ds_read_b64_tr_b16 a[138:139], v160 offset:20736\n"
    "ds_read_b64_tr_b16 a[144:145], v160 offset:24576\n"
    "v_mul_f32 v15, s66, v15\n"
    "v_mfma_f32_16x16x32_bf16 v[176:179], a[160:163], a[188:191], v[176:179]\n"
    "ds_read_b64_tr_b16 a[146:147], v160 offset:24832\n"
    "buffer_load_dwordx4 v130, s[36:39], s61 offen offset:2048 lds\n"
    "v_cvt_pk_bf16_f32 v8, v8, v9\n"
    "v_mfma_f32_16x16x32_bf16 v[180:183], a[164:167], a[188:191], v[180:183]\n"
    "ds_read_b64_tr_b16 a[132:133], v161 offset:16384\n"
    "ds_read_b64_tr_b16 a[134:135], v161 offset:16640\n"
    "v_cvt_pk_bf16_f32 v9, v10, v11\n"
    "v_mfma_f32_16x16x32_bf16 v[208:211], a[168:171], a[228:231], v[208:211]\n"
    "ds_read_b64_tr_b16 a[140:141], v161 offset:20480\n"
    "buffer_load_dwordx4 v131, s[36:39], s61 offen offset:3072 lds\n"
    "v_cvt_pk_bf16_f32 v10, v12, v13\n"
    "v_mfma_f32_16x16x32_bf16 v[212:215], a[172:175], a[228:231], v[212:215]\n"
    "ds_read_b64_tr_b16 a[142:143], v161 offset:20736\n"
    "ds_read_b64_tr_b16 a[148:149], v161 offset:24576\n"
    "v_cvt_pk_bf16_f32 v11, v14, v15\n"
    "v_mfma_f32_16x16x32_bf16 v[224:227], a[228:231], a[176:179], v[224:227]\n"
    "ds_read_b64_tr_b16 a[150:151], v161 offset:24832\n"
    "s_add_u32 s61, s61, s70\n"
    "s_add_u32 s68, s68, s69\n"
    "v_mfma_f32_16x16x32_bf16 v[236:239], a[228:231], a[180:183], v[236:239]\n"
    "buffer_store_dwordx4 v[8:11], v146, s[44:47], s68 offen offset:0 sc1\n"
    "v_mfma_f32_16x16x32_bf16 v[184:187], a[160:163], a[228:231], v[184:187]\n"
    "v_mfma_f32_16x16x32_bf16 v[188:191], a[164:167], a[228:231], v[188:191]\n"
    "s_waitcnt lgkmcnt(0)\n"
    "v_mfma_f32_16x16x32_bf16 v[192:195], a[136:139], a[152:155], v[192:195]\n"
    "ds_read_b128 a[184:187], v157 offset:4096\n"
    "ds_read_b128 a[188:191], v157 offset:6144\n"
    "v_mul_f32 v40, s66, v40\n"
    "v_mfma_f32_16x16x32_bf16 v[196:199], a[140:143], a[152:155], v[196:199]\n"
    "s_mul_i32 s78, s65, 0xfffe6000\n"
    "s_add_u32 m0, s96, 0x0\n"
    "v_mul_f32 v41, s66, v41\n"
    "v_mfma_f32_16x16x32_bf16 v[216:219], a[152:155], a[144:147], v[216:219]\n"
    "s_add_u32 s78, s78, 0x1b000\n"
    "v_add_u32 v252, s78, v159\n"
    "v_mul_f32 v42, s66, v42\n"
    "v_mfma_f32_16x16x32_bf16 v[228:231], a[152:155], a[148:151], v[228:231]\n"
    "ds_read_b128 a[228:231], v252\n"
    "buffer_load_dwordx4 v132, s[40:43], s62 offen offset:0 lds\n"
    "v_mul_f32 v43, s66, v43\n"
    "v_mfma_f32_16x16x32_bf16 v[200:203], a[136:139], a[156:159], v[200:203]\n"
    "ds_read_b64_tr_b16 a[160:161], v160 offset:18432\n"
    "buffer_load_dwordx4 v133, s[40:43], s62 offen offset:1024 lds\n"
    "v_mul_f32 v44, s66, v44\n"
    "v_mfma_f32_16x16x32_bf16 v[204:207], a[140:143], a[156:159], v[204:207]\n"
    "ds_read_b64_tr_b16 a[162:163], v160 offset:18688\n"
    "ds_read_b64_tr_b16 a[168:169], v160 offset:22528\n"
    "v_mul_f32 v45, s66, v45\n"
    "v_mfma_f32_16x16x32_bf16 v[220:223], a[156:159], a[144:147], v[220:223]\n"
    "ds_read_b64_tr_b16 a[170:171], v160 offset:22784\n"
    "v_mul_f32 v46, s66, v46\n"
    "v_mfma_f32_16x16x32_bf16 v[232:235], a[156:159], a[148:151], v[232:235]\n"
    "ds_read_b64_tr_b16 a[176:177], v160 offset:26624\n"
    "ds_read_b64_tr_b16 a[178:179], v160 offset:26880\n"
    "v_mul_f32 v47, s66, v47\n"
    "v_mfma_f32_16x16x32_bf16 v[176:179], a[128:131], a[156:159], v[176:179]\n"
    "ds_read_b64_tr_b16 a[164:165], v161 offset:18432\n"
    "buffer_load_dwordx4 v134, s[40:43], s62 offen offset:2048 lds\n"
    "v_cvt_pk_bf16_f32 v40, v40, v41\n"
    "v_mfma_f32_16x16x32_bf16 v[180:183], a[132:135], a[156:159], v[180:183]\n"
    "ds_read_b64_tr_b16 a[166:167], v161 offset:18688\n"
    "buffer_load_dwordx4 v135, s[40:43], s62 offen offset:3072 lds\n"
    "v_cvt_pk_bf16_f32 v41, v42, v43\n"
    "v_mfma_f32_16x16x32_bf16 v[208:211], a[136:139], a[224:227], v[208:211]\n"
    "ds_read_b64_tr_b16 a[172:173], v161 offset:22528\n"
    "ds_read_b64_tr_b16 a[174:175], v161 offset:22784\n"
    "v_cvt_pk_bf16_f32 v42, v44, v45\n"
    "v_mfma_f32_16x16x32_bf16 v[212:215], a[140:143], a[224:227], v[212:215]\n"
    "ds_read_b64_tr_b16 a[180:181], v161 offset:26624\n"
    "s_add_u32 s62, s62, s71\n"
    "v_cvt_pk_bf16_f32 v43, v46, v47\n"
    "v_mfma_f32_16x16x32_bf16 v[224:227], a[224:227], a[144:147], v[224:227]\n"
    "ds_read_b64_tr_b16 a[182:183], v161 offset:26880\n"
    "buffer_store_dwordx4 v[40:43], v146, s[44:47], s68 offen offset:1024 sc1\n"
    "v_mfma_f32_16x16x32_bf16 v[236:239], a[224:227], a[148:151], v[236:239]\n"
    "v_mfma_f32_16x16x32_bf16 v[184:187], a[128:131], a[224:227], v[184:187]\n"
    "v_mfma_f32_16x16x32_bf16 v[188:191], a[132:135], a[224:227], v[188:191]\n"
    "s_waitcnt lgkmcnt(0)\n"
    "v_mfma_f32_16x16x32_bf16 v[192:195], a[168:171], a[184:187], v[192:195]\n"
    "s_waitcnt vmcnt(16)\n"
    "s_barrier\n"
    "v_mul_f32 v16, s66, v16\n"
    "v_mfma_f32_16x16x32_bf16 v[196:199], a[172:175], a[184:187], v[196:199]\n"
    "ds_read_b128 a[152:155], v156 offset:32768\n"
    "ds_read_b128 a[156:159], v156 offset:34816\n"
    "v_mul_f32 v17, s66, v17\n"
    "v_mfma_f32_16x16x32_bf16 v[216:219], a[184:187], a[176:179], v[216:219]\n"
    "s_mul_i32 s78, s65, 0xfffe3000\n"
    "s_add_u32 m0, s63, 0x11000\n"
    "v_mul_f32 v18, s66, v18\n"
    "v_mfma_f32_16x16x32_bf16 v[228:231], a[184:187], a[180:183], v[228:231]\n"
    "s_add_u32 s78, s78, 0x22000\n"
    "v_add_u32 v252, s78, v158\n"
    "v_mul_f32 v19, s66, v19\n"
    "v_mfma_f32_16x16x32_bf16 v[200:203], a[168:171], a[188:191], v[200:203]\n"
    "ds_read_b128 a[224:227], v252\n"
    "buffer_load_dwordx4 v128, s[36:39], s61 offen offset:0 lds\n"
    "v_mul_f32 v20, s66, v20\n"
    "v_mfma_f32_16x16x32_bf16 v[204:207], a[172:175], a[188:191], v[204:207]\n"
    "ds_read_b64_tr_b16 a[128:129], v160 offset:32768\n"
    "ds_read_b64_tr_b16 a[130:131], v160 offset:33024\n"
    "v_mul_f32 v21, s66, v21\n"
    "v_mfma_f32_16x16x32_bf16 v[220:223], a[188:191], a[176:179], v[220:223]\n"
    "ds_read_b64_tr_b16 a[136:137], v160 offset:36864\n"
    "buffer_load_dwordx4 v129, s[36:39], s61 offen offset:1024 lds\n"
    "v_mul_f32 v22, s66, v22\n"
    "v_mfma_f32_16x16x32_bf16 v[232:235], a[188:191], a[180:183], v[232:235]\n"
    "ds_read_b64_tr_b16 a[138:139], v160 offset:37120\n"
    "ds_read_b64_tr_b16 a[144:145], v160 offset:40960\n"
    "v_mul_f32 v23, s66, v23\n"
    "v_mfma_f32_16x16x32_bf16 v[176:179], a[160:163], a[188:191], v[176:179]\n"
    "ds_read_b64_tr_b16 a[146:147], v160 offset:41216\n"
    "buffer_load_dwordx4 v130, s[36:39], s61 offen offset:2048 lds\n"
    "v_cvt_pk_bf16_f32 v16, v16, v17\n"
    "v_mfma_f32_16x16x32_bf16 v[180:183], a[164:167], a[188:191], v[180:183]\n"
    "ds_read_b64_tr_b16 a[132:133], v161 offset:32768\n"
    "ds_read_b64_tr_b16 a[134:135], v161 offset:33024\n"
    "v_cvt_pk_bf16_f32 v17, v18, v19\n"
    "v_mfma_f32_16x16x32_bf16 v[208:211], a[168:171], a[228:231], v[208:211]\n"
    "ds_read_b64_tr_b16 a[140:141], v161 offset:36864\n"
    "buffer_load_dwordx4 v131, s[36:39], s61 offen offset:3072 lds\n"
    "v_cvt_pk_bf16_f32 v18, v20, v21\n"
    "v_mfma_f32_16x16x32_bf16 v[212:215], a[172:175], a[228:231], v[212:215]\n"
    "ds_read_b64_tr_b16 a[142:143], v161 offset:37120\n"
    "ds_read_b64_tr_b16 a[148:149], v161 offset:40960\n"
    "v_cvt_pk_bf16_f32 v19, v22, v23\n"
    "v_mfma_f32_16x16x32_bf16 v[224:227], a[228:231], a[176:179], v[224:227]\n"
    "ds_read_b64_tr_b16 a[150:151], v161 offset:41216\n"
    "s_add_u32 s61, s61, s72\n"
    "s_add_u32 s68, s68, s69\n"
    "v_mfma_f32_16x16x32_bf16 v[236:239], a[228:231], a[180:183], v[236:239]\n"
    "buffer_store_dwordx4 v[16:19], v146, s[44:47], s68 offen offset:0 sc1\n"
    "v_mfma_f32_16x16x32_bf16 v[184:187], a[160:163], a[228:231], v[184:187]\n"
    "v_mfma_f32_16x16x32_bf16 v[188:191], a[164:167], a[228:231], v[188:191]\n"
    "s_waitcnt lgkmcnt(0)\n"
    "v_mfma_f32_16x16x32_bf16 v[192:195], a[136:139], a[152:155], v[192:195]\n"
    "ds_read_b128 a[184:187], v157 offset:32768\n"
    "ds_read_b128 a[188:191], v157 offset:34816\n"
    "v_mul_f32 v48, s66, v48\n"
    "v_mfma_f32_16x16x32_bf16 v[196:199], a[140:143], a[152:155], v[196:199]\n"
    "s_mul_i32 s78, s65, 0xfffe3000\n"
    "s_add_u32 m0, s96, 0x4000\n"
    "v_mul_f32 v49, s66, v49\n"
    "v_mfma_f32_16x16x32_bf16 v[216:219], a[152:155], a[144:147], v[216:219]\n"
    "s_add_u32 s78, s78, 0x22000\n"
    "v_add_u32 v252, s78, v159\n"
    "v_mul_f32 v50, s66, v50\n"
    "v_mfma_f32_16x16x32_bf16 v[228:231], a[152:155], a[148:151], v[228:231]\n"
    "ds_read_b128 a[228:231], v252\n"
    "buffer_load_dwordx4 v132, s[40:43], s62 offen offset:0 lds\n"
    "v_mul_f32 v51, s66, v51\n"
    "v_mfma_f32_16x16x32_bf16 v[200:203], a[136:139], a[156:159], v[200:203]\n"
    "ds_read_b64_tr_b16 a[160:161], v160 offset:34816\n"
    "buffer_load_dwordx4 v133, s[40:43], s62 offen offset:1024 lds\n"
    "v_mul_f32 v52, s66, v52\n"
    "v_mfma_f32_16x16x32_bf16 v[204:207], a[140:143], a[156:159], v[204:207]\n"
    "ds_read_b64_tr_b16 a[162:163], v160 offset:35072\n"
    "ds_read_b64_tr_b16 a[168:169], v160 offset:38912\n"
    "v_mul_f32 v53, s66, v53\n"
    "v_mfma_f32_16x16x32_bf16 v[220:223], a[156:159], a[144:147], v[220:223]\n"
    "ds_read_b64_tr_b16 a[170:171], v160 offset:39168\n"
    "v_mul_f32 v54, s66, v54\n"
    "v_mfma_f32_16x16x32_bf16 v[232:235], a[156:159], a[148:151], v[232:235]\n"
    "ds_read_b64_tr_b16 a[176:177], v160 offset:43008\n"
    "ds_read_b64_tr_b16 a[178:179], v160 offset:43264\n"
    "v_mul_f32 v55, s66, v55\n"
    "v_mfma_f32_16x16x32_bf16 v[176:179], a[128:131], a[156:159], v[176:179]\n"
    "ds_read_b64_tr_b16 a[164:165], v161 offset:34816\n"
    "buffer_load_dwordx4 v134, s[40:43], s62 offen offset:2048 lds\n"
    "v_cvt_pk_bf16_f32 v48, v48, v49\n"
    "v_mfma_f32_16x16x32_bf16 v[180:183], a[132:135], a[156:159], v[180:183]\n"
    "ds_read_b64_tr_b16 a[166:167], v161 offset:35072\n"
    "buffer_load_dwordx4 v135, s[40:43], s62 offen offset:3072 lds\n"
    "v_cvt_pk_bf16_f32 v49, v50, v51\n"
    "v_mfma_f32_16x16x32_bf16 v[208:211], a[136:139], a[224:227], v[208:211]\n"
    "ds_read_b64_tr_b16 a[172:173], v161 offset:38912\n"
    "ds_read_b64_tr_b16 a[174:175], v161 offset:39168\n"
    "v_cvt_pk_bf16_f32 v50, v52, v53\n"
    "v_mfma_f32_16x16x32_bf16 v[212:215], a[140:143], a[224:227], v[212:215]\n"
    "ds_read_b64_tr_b16 a[180:181], v161 offset:43008\n"
    "s_add_u32 s62, s62, s71\n"
    "v_cvt_pk_bf16_f32 v51, v54, v55\n"
    "v_mfma_f32_16x16x32_bf16 v[224:227], a[224:227], a[144:147], v[224:227]\n"
    "ds_read_b64_tr_b16 a[182:183], v161 offset:43264\n"
    "buffer_store_dwordx4 v[48:51], v146, s[44:47], s68 offen offset:1024 sc1\n"
    "v_mfma_f32_16x16x32_bf16 v[236:239], a[224:227], a[148:151], v[236:239]\n"
    "v_mfma_f32_16x16x32_bf16 v[184:187], a[128:131], a[224:227], v[184:187]\n"
    "v_mfma_f32_16x16x32_bf16 v[188:191], a[132:135], a[224:227], v[188:191]\n"
    "s_waitcnt lgkmcnt(0)\n"
    "v_mfma_f32_16x16x32_bf16 v[192:195], a[168:171], a[184:187], v[192:195]\n"
    "s_waitcnt vmcnt(22)\n"
    "s_barrier\n"
    "v_mul_f32 v24, s66, v24\n"
    "v_mfma_f32_16x16x32_bf16 v[196:199], a[172:175], a[184:187], v[196:199]\n"
    "ds_read_b128 a[152:155], v156 offset:36864\n"
    "ds_read_b128 a[156:159], v156 offset:38912\n"
    "v_mul_f32 v25, s66, v25\n"
    "v_mfma_f32_16x16x32_bf16 v[216:219], a[184:187], a[176:179], v[216:219]\n"
    "s_mul_i32 s78, s65, 0xfffe6000\n"
    "s_add_u32 m0, s63, 0x18000\n"
    "v_mul_f32 v26, s66, v26\n"
    "v_mfma_f32_16x16x32_bf16 v[228:231], a[184:187], a[180:183], v[228:231]\n"
    "s_add_u32 s78, s78, 0x23000\n"
    "v_add_u32 v252, s78, v158\n"
    "v_mul_f32 v27, s66, v27\n"
    "v_mfma_f32_16x16x32_bf16 v[200:203], a[168:171], a[188:191], v[200:203]\n"
    "ds_read_b128 a[224:227], v252\n"
    "buffer_load_dwordx4 v128, s[36:39], s61 offen offset:0 lds\n"
    "v_mul_f32 v28, s66, v28\n"
    "v_mfma_f32_16x16x32_bf16 v[204:207], a[172:175], a[188:191], v[204:207]\n"
    "ds_read_b64_tr_b16 a[128:129], v160 offset:49152\n"
    "ds_read_b64_tr_b16 a[130:131], v160 offset:49408\n"
    "v_mul_f32 v29, s66, v29\n"
    "v_mfma_f32_16x16x32_bf16 v[220:223], a[188:191], a[176:179], v[220:223]\n"
    "ds_read_b64_tr_b16 a[136:137], v160 offset:53248\n"
    "buffer_load_dwordx4 v129, s[36:39], s61 offen offset:1024 lds\n"
    "v_mul_f32 v30, s66, v30\n"
    "v_mfma_f32_16x16x32_bf16 v[232:235], a[188:191], a[180:183], v[232:235]\n"
    "ds_read_b64_tr_b16 a[138:139], v160 offset:53504\n"
    "ds_read_b64_tr_b16 a[144:145], v160 offset:57344\n"
    "v_mul_f32 v31, s66, v31\n"
    "v_mfma_f32_16x16x32_bf16 v[176:179], a[160:163], a[188:191], v[176:179]\n"
    "ds_read_b64_tr_b16 a[146:147], v160 offset:57600\n"
    "buffer_load_dwordx4 v130, s[36:39], s61 offen offset:2048 lds\n"
    "v_cvt_pk_bf16_f32 v24, v24, v25\n"
    "v_mfma_f32_16x16x32_bf16 v[180:183], a[164:167], a[188:191], v[180:183]\n"
    "ds_read_b64_tr_b16 a[132:133], v161 offset:49152\n"
    "ds_read_b64_tr_b16 a[134:135], v161 offset:49408\n"
    "v_cvt_pk_bf16_f32 v25, v26, v27\n"
    "v_mfma_f32_16x16x32_bf16 v[208:211], a[168:171], a[228:231], v[208:211]\n"
    "ds_read_b64_tr_b16 a[140:141], v161 offset:53248\n"
    "buffer_load_dwordx4 v131, s[36:39], s61 offen offset:3072 lds\n"
    "v_cvt_pk_bf16_f32 v26, v28, v29\n"
    "v_mfma_f32_16x16x32_bf16 v[212:215], a[172:175], a[228:231], v[212:215]\n"
    "ds_read_b64_tr_b16 a[142:143], v161 offset:53504\n"
    "ds_read_b64_tr_b16 a[148:149], v161 offset:57344\n"
    "v_cvt_pk_bf16_f32 v27, v30, v31\n"
    "v_mfma_f32_16x16x32_bf16 v[224:227], a[228:231], a[176:179], v[224:227]\n"
    "ds_read_b64_tr_b16 a[150:151], v161 offset:57600\n"
    "s_add_u32 s61, s61, s70\n"
    "s_add_u32 s68, s68, s69\n"
    "v_mfma_f32_16x16x32_bf16 v[236:239], a[228:231], a[180:183], v[236:239]\n"
    "buffer_store_dwordx4 v[24:27], v146, s[44:47], s68 offen offset:0 sc1\n"
    "v_mfma_f32_16x16x32_bf16 v[184:187], a[160:163], a[228:231], v[184:187]\n"
    "v_mfma_f32_16x16x32_bf16 v[188:191], a[164:167], a[228:231], v[188:191]\n"
    "s_waitcnt lgkmcnt(0)\n"
    "v_mfma_f32_16x16x32_bf16 v[192:195], a[136:139], a[152:155], v[192:195]\n"
    "ds_read_b128 a[184:187], v157 offset:36864\n"
    "ds_read_b128 a[188:191], v157 offset:38912\n"
    "v_mul_f32 v56, s66, v56\n"
    "v_mfma_f32_16x16x32_bf16 v[196:199], a[140:143], a[152:155], v[196:199]\n"
    "s_mul_i32 s78, s65, 0xfffe6000\n"
    "s_add_u32 m0, s96, 0x8000\n"
    "v_mul_f32 v57, s66, v57\n"
    "v_mfma_f32_16x16x32_bf16 v[216:219], a[152:155], a[144:147], v[216:219]\n"
    "s_add_u32 s78, s78, 0x23000\n"
    "v_add_u32 v252, s78, v159\n"
    "v_mul_f32 v58, s66, v58\n"
    "v_mfma_f32_16x16x32_bf16 v[228:231], a[152:155], a[148:151], v[228:231]\n"
    "ds_read_b128 a[228:231], v252\n"
    "buffer_load_dwordx4 v132, s[40:43], s62 offen offset:0 lds\n"
    "v_mul_f32 v59, s66, v59\n"
    "v_mfma_f32_16x16x32_bf16 v[200:203], a[136:139], a[156:159], v[200:203]\n"
    "ds_read_b64_tr_b16 a[160:161], v160 offset:51200\n"
    "buffer_load_dwordx4 v133, s[40:43], s62 offen offset:1024 lds\n"
    "v_mul_f32 v60, s66, v60\n"
    "v_mfma_f32_16x16x32_bf16 v[204:207], a[140:143], a[156:159], v[204:207]\n"
    "ds_read_b64_tr_b16 a[162:163], v160 offset:51456\n"
    "ds_read_b64_tr_b16 a[168:169], v160 offset:55296\n"
    "v_mul_f32 v61, s66, v61\n"
    "v_mfma_f32_16x16x32_bf16 v[220:223], a[156:159], a[144:147], v[220:223]\n"
    "ds_read_b64_tr_b16 a[170:171], v160 offset:55552\n"
    "v_mul_f32 v62, s66, v62\n"
    "v_mfma_f32_16x16x32_bf16 v[232:235], a[156:159], a[148:151], v[232:235]\n"
    "ds_read_b64_tr_b16 a[176:177], v160 offset:59392\n"
    "ds_read_b64_tr_b16 a[178:179], v160 offset:59648\n"
    "v_mul_f32 v63, s66, v63\n"
    "v_mfma_f32_16x16x32_bf16 v[176:179], a[128:131], a[156:159], v[176:179]\n"
    "ds_read_b64_tr_b16 a[164:165], v161 offset:51200\n"
    "buffer_load_dwordx4 v134, s[40:43], s62 offen offset:2048 lds\n"
    "v_cvt_pk_bf16_f32 v56, v56, v57\n"
    "v_mfma_f32_16x16x32_bf16 v[180:183], a[132:135], a[156:159], v[180:183]\n"
    "ds_read_b64_tr_b16 a[166:167], v161 offset:51456\n"
    "buffer_load_dwordx4 v135, s[40:43], s62 offen offset:3072 lds\n"
    "v_cvt_pk_bf16_f32 v57, v58, v59\n"
    "v_mfma_f32_16x16x32_bf16 v[208:211], a[136:139], a[224:227], v[208:211]\n"
    "ds_read_b64_tr_b16 a[172:173], v161 offset:55296\n"
    "ds_read_b64_tr_b16 a[174:175], v161 offset:55552\n"
    "v_cvt_pk_bf16_f32 v58, v60, v61\n"
    "v_mfma_f32_16x16x32_bf16 v[212:215], a[140:143], a[224:227], v[212:215]\n"
    "ds_read_b64_tr_b16 a[180:181], v161 offset:59392\n"
    "s_add_u32 s62, s62, s71\n"
    "v_cvt_pk_bf16_f32 v59, v62, v63\n"
    "v_mfma_f32_16x16x32_bf16 v[224:227], a[224:227], a[144:147], v[224:227]\n"
    "ds_read_b64_tr_b16 a[182:183], v161 offset:59648\n"
    "buffer_store_dwordx4 v[56:59], v146, s[44:47], s68 offen offset:1024 sc1\n"
    "v_mfma_f32_16x16x32_bf16 v[236:239], a[224:227], a[148:151], v[236:239]\n"
    "v_mfma_f32_16x16x32_bf16 v[184:187], a[128:131], a[224:227], v[184:187]\n"
    "v_mfma_f32_16x16x32_bf16 v[188:191], a[132:135], a[224:227], v[188:191]\n"
    "s_waitcnt lgkmcnt(0)\n"
    "v_mfma_f32_16x16x32_bf16 v[192:195], a[168:171], a[184:187], v[192:195]\n"
    "s_waitcnt vmcnt(21)\n"
    "s_barrier\n"
    "v_mul_f32 v64, s66, v64\n"
    "v_mul_f32 v65, s66, v65\n"
    "v_mfma_f32_16x16x32_bf16 v[196:199], a[172:175], a[184:187], v[196:199]\n"
    "v_add_u32 v156, 0xffff8000, v156\n"
    "v_mul_f32 v66, s66, v66\n"
    "v_mul_f32 v67, s66, v67\n"
    "v_mfma_f32_16x16x32_bf16 v[216:219], a[184:187], a[176:179], v[216:219]\n"
    "ds_read_b128 a[152:155], v156 offset:0\n"
    "ds_read_b128 a[156:159], v156 offset:2048\n"
    "v_mul_f32 v68, s66, v68\n"
    "v_mfma_f32_16x16x32_bf16 v[228:231], a[184:187], a[180:183], v[228:231]\n"
    "s_mul_i32 s78, s65, 0xfffeb000\n"
    "s_add_u32 s78, s78, 0x12000\n"
    "v_mul_f32 v69, s66, v69\n"
    "v_mul_f32 v70, s66, v70\n"
    "v_mfma_f32_16x16x32_bf16 v[200:203], a[168:171], a[188:191], v[200:203]\n"
    "v_add_u32 v252, s78, v158\n"
    "v_mul_f32 v71, s66, v71\n"
    "v_cvt_pk_bf16_f32 v64, v64, v65\n"
    "v_cvt_pk_bf16_f32 v65, v66, v67\n"
    "v_mfma_f32_16x16x32_bf16 v[204:207], a[172:175], a[188:191], v[204:207]\n"
    "ds_read_b128 a[224:227], v252\n"
    "ds_read_b64_tr_b16 a[128:129], v160 offset:0\n"
    "v_cvt_pk_bf16_f32 v66, v68, v69\n"
    "v_mfma_f32_16x16x32_bf16 v[220:223], a[188:191], a[176:179], v[220:223]\n"
    "ds_read_b64_tr_b16 a[130:131], v160 offset:256\n"
    "ds_read_b64_tr_b16 a[136:137], v160 offset:4096\n"
    "v_cvt_pk_bf16_f32 v67, v70, v71\n"
    "v_mfma_f32_16x16x32_bf16 v[232:235], a[188:191], a[180:183], v[232:235]\n"
    "ds_read_b64_tr_b16 a[138:139], v160 offset:4352\n"
    "ds_read_b64_tr_b16 a[144:145], v160 offset:8192\n"
    "s_add_u32 s68, s68, s73\n"
    "v_mfma_f32_16x16x32_bf16 v[176:179], a[160:163], a[188:191], v[176:179]\n"
    "ds_read_b64_tr_b16 a[146:147], v160 offset:8448\n"
    "buffer_store_dwordx4 v[64:67], v146, s[44:47], s68 offen offset:0 sc1\n"
    "v_mfma_f32_16x16x32_bf16 v[180:183], a[164:167], a[188:191], v[180:183]\n"
    "ds_read_b64_tr_b16 a[132:133], v161 offset:0\n"
    "ds_read_b64_tr_b16 a[134:135], v161 offset:256\n"
    "v_mul_f32 v96, s66, v96\n"
    "v_mfma_f32_16x16x32_bf16 v[208:211], a[168:171], a[228:231], v[208:211]\n"
    "ds_read_b64_tr_b16 a[140:141], v161 offset:4096\n"
    "v_mul_f32 v97, s66, v97\n"
    "v_mul_f32 v98, s66, v98\n"
    "v_mul_f32 v99, s66, v99\n"
    "v_mfma_f32_16x16x32_bf16 v[212:215], a[172:175], a[228:231], v[212:215]\n"
    "ds_read_b64_tr_b16 a[142:143], v161 offset:4352\n"
    "ds_read_b64_tr_b16 a[148:149], v161 offset:8192\n"
    "v_mul_f32 v100, s66, v100\n"
    "v_mfma_f32_16x16x32_bf16 v[224:227], a[228:231], a[176:179], v[224:227]\n"
    "ds_read_b64_tr_b16 a[150:151], v161 offset:8448\n"
    "v_mul_f32 v101, s66, v101\n"
    "v_mul_f32 v102, s66, v102\n"
    "v_mul_f32 v103, s66, v103\n"
    "v_mfma_f32_16x16x32_bf16 v[236:239], a[228:231], a[180:183], v[236:239]\n"
    "v_cvt_pk_bf16_f32 v96, v96, v97\n"
    "v_cvt_pk_bf16_f32 v97, v98, v99\n"
    "v_cvt_pk_bf16_f32 v98, v100, v101\n"
    "v_cvt_pk_bf16_f32 v99, v102, v103\n"
    "v_mfma_f32_16x16x32_bf16 v[184:187], a[160:163], a[228:231], v[184:187]\n"
    "buffer_store_dwordx4 v[96:99], v146, s[44:47], s68 offen offset:1024 sc1\n"
    "v_mfma_f32_16x16x32_bf16 v[188:191], a[164:167], a[228:231], v[188:191]\n"
    "s_waitcnt lgkmcnt(0)\n"
    "v_mfma_f32_16x16x32_bf16 v[192:195], a[136:139], a[152:155], v[192:195]\n"
    "v_add_u32 v157, 0xffff8000, v157\n"
    "v_mul_f32 v72, s66, v72\n"
    "s_bfe_u32 s52, s26, 0x30004\n"
    "v_mfma_f32_16x16x32_bf16 v[196:199], a[140:143], a[152:155], v[196:199]\n"
    "ds_read_b128 a[184:187], v157 offset:0\n"
    "ds_read_b128 a[188:191], v157 offset:2048\n"
    "v_mfma_f32_16x16x32_bf16 v[216:219], a[152:155], a[144:147], v[216:219]\n"
    "s_mul_i32 s78, s65, 0xfffeb000\n"
    "s_add_u32 s78, s78, 0x12000\n"
    "v_mul_f32 v73, s66, v73\n"
    "v_mfma_f32_16x16x32_bf16 v[228:231], a[152:155], a[148:151], v[228:231]\n"
    "v_add_u32 v252, s78, v159\n"
    "v_mul_f32 v74, s66, v74\n"
    "v_mul_f32 v75, s66, v75\n"
    "v_mfma_f32_16x16x32_bf16 v[200:203], a[136:139], a[156:159], v[200:203]\n"
    "ds_read_b128 a[228:231], v252\n"
    "ds_read_b64_tr_b16 a[160:161], v160 offset:2048\n"
    "v_mfma_f32_16x16x32_bf16 v[204:207], a[140:143], a[156:159], v[204:207]\n"
    "ds_read_b64_tr_b16 a[162:163], v160 offset:2304\n"
    "v_mul_f32 v76, s66, v76\n"
    "v_mul_f32 v77, s66, v77\n"
    "v_mfma_f32_16x16x32_bf16 v[220:223], a[156:159], a[144:147], v[220:223]\n"
    "ds_read_b64_tr_b16 a[168:169], v160 offset:6144\n"
    "ds_read_b64_tr_b16 a[170:171], v160 offset:6400\n"
    "v_mfma_f32_16x16x32_bf16 v[232:235], a[156:159], a[148:151], v[232:235]\n"
    "ds_read_b64_tr_b16 a[176:177], v160 offset:10240\n"
    "v_mul_f32 v78, s66, v78\n"
    "v_mul_f32 v79, s66, v79\n"
    "v_mfma_f32_16x16x32_bf16 v[176:179], a[128:131], a[156:159], v[176:179]\n"
    "ds_read_b64_tr_b16 a[178:179], v160 offset:10496\n"
    "ds_read_b64_tr_b16 a[164:165], v161 offset:2048\n"
    "v_mfma_f32_16x16x32_bf16 v[180:183], a[132:135], a[156:159], v[180:183]\n"
    "ds_read_b64_tr_b16 a[166:167], v161 offset:2304\n"
    "v_cvt_pk_bf16_f32 v72, v72, v73\n"
    "v_cvt_pk_bf16_f32 v73, v74, v75\n"
    "v_mfma_f32_16x16x32_bf16 v[208:211], a[136:139], a[224:227], v[208:211]\n"
    "ds_read_b64_tr_b16 a[172:173], v161 offset:6144\n"
    "ds_read_b64_tr_b16 a[174:175], v161 offset:6400\n"
    "v_mfma_f32_16x16x32_bf16 v[212:215], a[140:143], a[224:227], v[212:215]\n"
    "ds_read_b64_tr_b16 a[180:181], v161 offset:10240\n"
    "v_cvt_pk_bf16_f32 v74, v76, v77\n"
    "v_cvt_pk_bf16_f32 v75, v78, v79\n"
    "v_mfma_f32_16x16x32_bf16 v[224:227], a[224:227], a[144:147], v[224:227]\n"
    "ds_read_b64_tr_b16 a[182:183], v161 offset:10496\n"
    "s_add_u32 s68, s68, s69\n"
    "s_sub_u32 s53, 15, s52\n"
    "v_mfma_f32_16x16x32_bf16 v[236:239], a[224:227], a[148:151], v[236:239]\n"
    "buffer_store_dwordx4 v[72:75], v146, s[44:47], s68 offen offset:0 sc1\n"
    "v_mfma_f32_16x16x32_bf16 v[184:187], a[128:131], a[224:227], v[184:187]\n"
    "s_bitcmp1_b32 s26, 7\n"
    "s_cselect_b32 s52, s53, s52\n"
    "v_mfma_f32_16x16x32_bf16 v[188:191], a[132:135], a[224:227], v[188:191]\n"
    "s_waitcnt lgkmcnt(0)\n"
    "v_mfma_f32_16x16x32_bf16 v[192:195], a[168:171], a[184:187], v[192:195]\n"
    "s_waitcnt vmcnt(14)\n"
    "s_barrier\n"
    "v_mul_f32 v104, s66, v104\n"
    "v_mfma_f32_16x16x32_bf16 v[196:199], a[172:175], a[184:187], v[196:199]\n"
    "ds_read_b128 a[152:155], v156 offset:4096\n"
    "ds_read_b128 a[156:159], v156 offset:6144\n"
    "v_mfma_f32_16x16x32_bf16 v[216:219], a[184:187], a[176:179], v[216:219]\n"
    "s_mul_i32 s78, s65, 0xfffee000\n"
    "v_mul_f32 v105, s66, v105\n"
    "v_mul_f32 v106, s66, v106\n"
    "v_mfma_f32_16x16x32_bf16 v[228:231], a[184:187], a[180:183], v[228:231]\n"
    "s_add_u32 s78, s78, 0x13000\n"
    "v_add_u32 v252, s78, v158\n"
    "v_mul_f32 v107, s66, v107\n"
    "v_mfma_f32_16x16x32_bf16 v[200:203], a[168:171], a[188:191], v[200:203]\n"
    "ds_read_b128 a[224:227], v252\n"
    "v_mul_f32 v108, s66, v108\n"
    "v_mul_f32 v109, s66, v109\n"
    "v_mfma_f32_16x16x32_bf16 v[204:207], a[172:175], a[188:191], v[204:207]\n"
    "ds_read_b64_tr_b16 a[128:129], v160 offset:16384\n"
    "ds_read_b64_tr_b16 a[130:131], v160 offset:16640\n"
    "v_mfma_f32_16x16x32_bf16 v[220:223], a[188:191], a[176:179], v[220:223]\n"
    "ds_read_b64_tr_b16 a[136:137], v160 offset:20480\n"
    "v_mul_f32 v110, s66, v110\n"
    "v_mul_f32 v111, s66, v111\n"
    "v_mfma_f32_16x16x32_bf16 v[232:235], a[188:191], a[180:183], v[232:235]\n"
    "ds_read_b64_tr_b16 a[138:139], v160 offset:20736\n"
    "ds_read_b64_tr_b16 a[144:145], v160 offset:24576\n"
    "v_mfma_f32_16x16x32_bf16 v[176:179], a[160:163], a[188:191], v[176:179]\n"
    "ds_read_b64_tr_b16 a[146:147], v160 offset:24832\n"
    "v_cvt_pk_bf16_f32 v104, v104, v105\n"
    "v_cvt_pk_bf16_f32 v105, v106, v107\n"
    "v_mfma_f32_16x16x32_bf16 v[180:183], a[164:167], a[188:191], v[180:183]\n"
    "ds_read_b64_tr_b16 a[132:133], v161 offset:16384\n"
    "ds_read_b64_tr_b16 a[134:135], v161 offset:16640\n"
    "v_mfma_f32_16x16x32_bf16 v[208:211], a[168:171], a[228:231], v[208:211]\n"
    "ds_read_b64_tr_b16 a[140:141], v161 offset:20480\n"
    "v_cvt_pk_bf16_f32 v106, v108, v109\n"
    "v_cvt_pk_bf16_f32 v107, v110, v111\n"
    "v_mfma_f32_16x16x32_bf16 v[212:215], a[172:175], a[228:231], v[212:215]\n"
    "ds_read_b64_tr_b16 a[142:143], v161 offset:20736\n"
    "ds_read_b64_tr_b16 a[148:149], v161 offset:24576\n"
    "v_mfma_f32_16x16x32_bf16 v[224:227], a[228:231], a[176:179], v[224:227]\n"
    "ds_read_b64_tr_b16 a[150:151], v161 offset:24832\n"
    "buffer_store_dwordx4 v[104:107], v146, s[44:47], s68 offen offset:1024 sc1\n"
    "v_mfma_f32_16x16x32_bf16 v[236:239], a[228:231], a[180:183], v[236:239]\n"
    "s_mov_b32 s70, s77\n"
    "s_mov_b32 s71, s76\n"
    "s_mul_i32 s52, s52, 114688\n"
    "v_mfma_f32_16x16x32_bf16 v[184:187], a[160:163], a[228:231], v[184:187]\n"
    "s_add_u32 s40, s6, s52\n"
    "s_addc_u32 s41, s7, 0\n"
    "s_and_b32 s41, s41, 0xffff\n"
    "v_mfma_f32_16x16x32_bf16 v[188:191], a[164:167], a[228:231], v[188:191]\n"
    "s_waitcnt lgkmcnt(0)\n"
    "v_mfma_f32_16x16x32_bf16 v[192:195], a[136:139], a[152:155], v[192:195]\n"
    "ds_read_b128 a[184:187], v157 offset:4096\n"
    "ds_read_b128 a[188:191], v157 offset:6144\n"
    "v_mfma_f32_16x16x32_bf16 v[196:199], a[140:143], a[152:155], v[196:199]\n"
    "s_mul_i32 s78, s65, 0xfffee000\n"
    "v_mul_f32 v80, s66, v80\n"
    "v_mul_f32 v81, s66, v81\n"
    "v_mfma_f32_16x16x32_bf16 v[216:219], a[152:155], a[144:147], v[216:219]\n"
    "s_add_u32 s78, s78, 0x13000\n"
    "v_add_u32 v252, s78, v159\n"
    "v_mul_f32 v82, s66, v82\n"
    "v_mfma_f32_16x16x32_bf16 v[228:231], a[152:155], a[148:151], v[228:231]\n"
    "ds_read_b128 a[228:231], v252\n"
    "v_mul_f32 v83, s66, v83\n"
    "s_mov_b32 s42, 0x1c000\n"
    "v_mfma_f32_16x16x32_bf16 v[200:203], a[136:139], a[156:159], v[200:203]\n"
    "ds_read_b64_tr_b16 a[160:161], v160 offset:18432\n"
    "v_mul_f32 v84, s66, v84\n"
    "s_mov_b32 s43, 0x20000\n"
    "v_mfma_f32_16x16x32_bf16 v[204:207], a[140:143], a[156:159], v[204:207]\n"
    "ds_read_b64_tr_b16 a[162:163], v160 offset:18688\n"
    "ds_read_b64_tr_b16 a[168:169], v160 offset:22528\n"
    "v_mfma_f32_16x16x32_bf16 v[220:223], a[156:159], a[144:147], v[220:223]\n"
    "ds_read_b64_tr_b16 a[170:171], v160 offset:22784\n"
    "v_mul_f32 v85, s66, v85\n"
    "v_mul_f32 v86, s66, v86\n"
    "v_mfma_f32_16x16x32_bf16 v[232:235], a[156:159], a[148:151], v[232:235]\n"
    "ds_read_b64_tr_b16 a[176:177], v160 offset:26624\n"
    "ds_read_b64_tr_b16 a[178:179], v160 offset:26880\n"
    "v_mfma_f32_16x16x32_bf16 v[176:179], a[128:131], a[156:159], v[176:179]\n"
    "ds_read_b64_tr_b16 a[164:165], v161 offset:18432\n"
    "v_mul_f32 v87, s66, v87\n"
    "v_cvt_pk_bf16_f32 v80, v80, v81\n"
    "v_mfma_f32_16x16x32_bf16 v[180:183], a[132:135], a[156:159], v[180:183]\n"
    "ds_read_b64_tr_b16 a[166:167], v161 offset:18688\n"
    "v_cvt_pk_bf16_f32 v81, v82, v83\n"
    "s_lshl_b32 s52, s74, 9\n"
    "v_mfma_f32_16x16x32_bf16 v[208:211], a[136:139], a[224:227], v[208:211]\n"
    "ds_read_b64_tr_b16 a[172:173], v161 offset:22528\n"
    "ds_read_b64_tr_b16 a[174:175], v161 offset:22784\n"
    "v_mfma_f32_16x16x32_bf16 v[212:215], a[140:143], a[224:227], v[212:215]\n"
    "ds_read_b64_tr_b16 a[180:181], v161 offset:26624\n"
    "v_cvt_pk_bf16_f32 v82, v84, v85\n"
    "v_cvt_pk_bf16_f32 v83, v86, v87\n"
    "v_mfma_f32_16x16x32_bf16 v[224:227], a[224:227], a[144:147], v[224:227]\n"
    "ds_read_b64_tr_b16 a[182:183], v161 offset:26880\n"
    "s_add_u32 s68, s68, s69\n"
    "s_add_u32 s84, s10, s52\n"
    "s_addc_u32 s85, s11, 0\n"
    "v_mfma_f32_16x16x32_bf16 v[236:239], a[224:227], a[148:151], v[236:239]\n"
    "buffer_store_dwordx4 v[80:83], v146, s[44:47], s68 offen offset:0 sc1\n"
    "v_mfma_f32_16x16x32_bf16 v[184:187], a[128:131], a[224:227], v[184:187]\n"
    "v_mfma_f32_16x16x32_bf16 v[188:191], a[132:135], a[224:227], v[188:191]\n"
    "s_waitcnt lgkmcnt(0)\n"
    "v_mfma_f32_16x16x32_bf16 v[192:195], a[168:171], a[184:187], v[192:195]\n"
    "s_waitcnt vmcnt(6)\n"
    "s_barrier\n"
    "v_mul_f32 v112, s66, v112\n"
    "v_mfma_f32_16x16x32_bf16 v[196:199], a[172:175], a[184:187], v[196:199]\n"
    "ds_read_b128 a[152:155], v156 offset:32768\n"
    "ds_read_b128 a[156:159], v156 offset:34816\n"
    "v_mfma_f32_16x16x32_bf16 v[216:219], a[184:187], a[176:179], v[216:219]\n"
    "s_mul_i32 s78, s65, 0xfffeb000\n"
    "v_mul_f32 v113, s66, v113\n"
    "v_mul_f32 v114, s66, v114\n"
    "v_mfma_f32_16x16x32_bf16 v[228:231], a[184:187], a[180:183], v[228:231]\n"
    "s_add_u32 s78, s78, 0x1a000\n"
    "v_add_u32 v252, s78, v158\n"
    "v_mul_f32 v115, s66, v115\n"
    "v_mfma_f32_16x16x32_bf16 v[200:203], a[168:171], a[188:191], v[200:203]\n"
    "ds_read_b128 a[224:227], v252\n"
    "v_mul_f32 v116, s66, v116\n"
    "s_and_b32 s85, s85, 0xffff\n"
    "v_mfma_f32_16x16x32_bf16 v[204:207], a[172:175], a[188:191], v[204:207]\n"
    "ds_read_b64_tr_b16 a[128:129], v160 offset:32768\n"
    "ds_read_b64_tr_b16 a[130:131], v160 offset:33024\n"
    "v_mfma_f32_16x16x32_bf16 v[220:223], a[188:191], a[176:179], v[220:223]\n"
    "ds_read_b64_tr_b16 a[136:137], v160 offset:36864\n"
    "v_mul_f32 v117, s66, v117\n"
    "v_mul_f32 v118, s66, v118\n"
    "v_mfma_f32_16x16x32_bf16 v[232:235], a[188:191], a[180:183], v[232:235]\n"
    "ds_read_b64_tr_b16 a[138:139], v160 offset:37120\n"
    "ds_read_b64_tr_b16 a[144:145], v160 offset:40960\n"
    "v_mfma_f32_16x16x32_bf16 v[176:179], a[160:163], a[188:191], v[176:179]\n"
    "ds_read_b64_tr_b16 a[146:147], v160 offset:41216\n"
    "v_mul_f32 v119, s66, v119\n"
    "v_cvt_pk_bf16_f32 v112, v112, v113\n"
    "v_mfma_f32_16x16x32_bf16 v[180:183], a[164:167], a[188:191], v[180:183]\n"
    "ds_read_b64_tr_b16 a[132:133], v161 offset:32768\n"
    "ds_read_b64_tr_b16 a[134:135], v161 offset:33024\n"
    "v_mfma_f32_16x16x32_bf16 v[208:211], a[168:171], a[228:231], v[208:211]\n"
    "ds_read_b64_tr_b16 a[140:141], v161 offset:36864\n"
    "v_cvt_pk_bf16_f32 v113, v114, v115\n"
    "v_cvt_pk_bf16_f32 v114, v116, v117\n"
    "v_mfma_f32_16x16x32_bf16 v[212:215], a[172:175], a[228:231], v[212:215]\n"
    "ds_read_b64_tr_b16 a[142:143], v161 offset:37120\n"
    "ds_read_b64_tr_b16 a[148:149], v161 offset:40960\n"
    "v_mfma_f32_16x16x32_bf16 v[224:227], a[228:231], a[176:179], v[224:227]\n"
    "ds_read_b64_tr_b16 a[150:151], v161 offset:41216\n"
    "v_cvt_pk_bf16_f32 v115, v118, v119\n"
    "s_mov_b32 s86, 0x37e200\n"
    "v_mfma_f32_16x16x32_bf16 v[236:239], a[228:231], a[180:183], v[236:239]\n"
    "buffer_store_dwordx4 v[112:115], v146, s[44:47], s68 offen offset:1024 sc1\n"
    "v_mfma_f32_16x16x32_bf16 v[184:187], a[160:163], a[228:231], v[184:187]\n"
    "s_mov_b32 s87, 0x20000\n"
    "v_mov_b64 v[128:129], v[240:241]\n"
    "v_mfma_f32_16x16x32_bf16 v[188:191], a[164:167], a[228:231], v[188:191]\n"
    "s_waitcnt lgkmcnt(0)\n"
    "v_mfma_f32_16x16x32_bf16 v[192:195], a[136:139], a[152:155], v[192:195]\n"
    "ds_read_b128 a[184:187], v157 offset:32768\n"
    "ds_read_b128 a[188:191], v157 offset:34816\n"
    "v_mfma_f32_16x16x32_bf16 v[196:199], a[140:143], a[152:155], v[196:199]\n"
    "s_mul_i32 s78, s65, 0xfffeb000\n"
    "v_mul_f32 v88, s66, v88\n"
    "v_mul_f32 v89, s66, v89\n"
    "v_mfma_f32_16x16x32_bf16 v[216:219], a[152:155], a[144:147], v[216:219]\n"
    "s_add_u32 s78, s78, 0x1a000\n"
    "v_add_u32 v252, s78, v159\n"
    "v_mul_f32 v90, s66, v90\n"
    "v_mfma_f32_16x16x32_bf16 v[228:231], a[152:155], a[148:151], v[228:231]\n"
    "ds_read_b128 a[228:231], v252\n"
    "v_mul_f32 v91, s66, v91\n"
    "v_mov_b64 v[130:131], v[242:243]\n"
    "v_mfma_f32_16x16x32_bf16 v[200:203], a[136:139], a[156:159], v[200:203]\n"
    "ds_read_b64_tr_b16 a[160:161], v160 offset:34816\n"
    "v_mul_f32 v92, s66, v92\n"
    "v_mov_b64 v[132:133], v[244:245]\n"
    "v_mfma_f32_16x16x32_bf16 v[204:207], a[140:143], a[156:159], v[204:207]\n"
    "ds_read_b64_tr_b16 a[162:163], v160 offset:35072\n"
    "ds_read_b64_tr_b16 a[168:169], v160 offset:38912\n"
    "v_mfma_f32_16x16x32_bf16 v[220:223], a[156:159], a[144:147], v[220:223]\n"
    "ds_read_b64_tr_b16 a[170:171], v160 offset:39168\n"
    "v_mul_f32 v93, s66, v93\n"
    "v_mul_f32 v94, s66, v94\n"
    "v_mfma_f32_16x16x32_bf16 v[232:235], a[156:159], a[148:151], v[232:235]\n"
    "ds_read_b64_tr_b16 a[176:177], v160 offset:43008\n"
    "ds_read_b64_tr_b16 a[178:179], v160 offset:43264\n"
    "v_mfma_f32_16x16x32_bf16 v[176:179], a[128:131], a[156:159], v[176:179]\n"
    "ds_read_b64_tr_b16 a[164:165], v161 offset:34816\n"
    "v_mul_f32 v95, s66, v95\n"
    "v_cvt_pk_bf16_f32 v88, v88, v89\n"
    "v_mfma_f32_16x16x32_bf16 v[180:183], a[132:135], a[156:159], v[180:183]\n"
    "ds_read_b64_tr_b16 a[166:167], v161 offset:35072\n"
    "v_cvt_pk_bf16_f32 v89, v90, v91\n"
    "v_mov_b64 v[134:135], v[246:247]\n"
    "v_mfma_f32_16x16x32_bf16 v[208:211], a[136:139], a[224:227], v[208:211]\n"
    "ds_read_b64_tr_b16 a[172:173], v161 offset:38912\n"
    "ds_read_b64_tr_b16 a[174:175], v161 offset:39168\n"
    "v_mfma_f32_16x16x32_bf16 v[212:215], a[140:143], a[224:227], v[212:215]\n"
    "ds_read_b64_tr_b16 a[180:181], v161 offset:43008\n"
    "v_cvt_pk_bf16_f32 v90, v92, v93\n"
    "v_cvt_pk_bf16_f32 v91, v94, v95\n"
    "v_mfma_f32_16x16x32_bf16 v[224:227], a[224:227], a[144:147], v[224:227]\n"
    "ds_read_b64_tr_b16 a[182:183], v161 offset:43264\n"
    "s_add_u32 s68, s68, s69\n"
    "s_lshl_b32 s79, s60, 12\n"
    "v_mfma_f32_16x16x32_bf16 v[236:239], a[224:227], a[148:151], v[236:239]\n"
    "buffer_store_dwordx4 v[88:91], v146, s[44:47], s68 offen offset:0 sc1\n"
    "v_mfma_f32_16x16x32_bf16 v[184:187], a[128:131], a[224:227], v[184:187]\n"
    "s_mov_b32 s63, 0\n"
    "v_mfma_f32_16x16x32_bf16 v[188:191], a[132:135], a[224:227], v[188:191]\n"
    "s_waitcnt lgkmcnt(0)\n"
    "v_mfma_f32_16x16x32_bf16 v[192:195], a[168:171], a[184:187], v[192:195]\n"
    "s_barrier\n"
    "v_mul_f32 v120, s66, v120\n"
    "v_mfma_f32_16x16x32_bf16 v[196:199], a[172:175], a[184:187], v[196:199]\n"
    "s_mov_b32 s78, 0\n"
    "v_mul_f32 v121, s66, v121\n"
    "v_mfma_f32_16x16x32_bf16 v[216:219], a[184:187], a[176:179], v[216:219]\n"
    "v_mul_f32 v122, s66, v122\n"
    "v_mfma_f32_16x16x32_bf16 v[228:231], a[184:187], a[180:183], v[228:231]\n"
    "s_add_u32 m0, s79, 0xc000\n"
    "v_mul_f32 v123, s66, v123\n"
    "v_mfma_f32_16x16x32_bf16 v[200:203], a[168:171], a[188:191], v[200:203]\n"
    "v_mul_f32 v124, s66, v124\n"
    "v_mfma_f32_16x16x32_bf16 v[204:207], a[172:175], a[188:191], v[204:207]\n"
    "buffer_load_dwordx4 v128, s[40:43], s63 offen offset:0 lds\n"
    "v_mfma_f32_16x16x32_bf16 v[220:223], a[188:191], a[176:179], v[220:223]\n"
    "v_mul_f32 v125, s66, v125\n"
    "v_mul_f32 v126, s66, v126\n"
    "v_mfma_f32_16x16x32_bf16 v[232:235], a[188:191], a[180:183], v[232:235]\n"
    "v_mul_f32 v127, s66, v127\n"
    "v_mfma_f32_16x16x32_bf16 v[176:179], a[160:163], a[188:191], v[176:179]\n"
    "buffer_load_dwordx4 v129, s[40:43], s63 offen offset:1024 lds\n"
    "v_mfma_f32_16x16x32_bf16 v[180:183], a[164:167], a[188:191], v[180:183]\n"
    "v_cvt_pk_bf16_f32 v120, v120, v121\n"
    "v_cvt_pk_bf16_f32 v121, v122, v123\n"
    "v_mfma_f32_16x16x32_bf16 v[208:211], a[168:171], a[228:231], v[208:211]\n"
    "buffer_load_dwordx4 v130, s[40:43], s63 offen offset:2048 lds\n"
    "v_mfma_f32_16x16x32_bf16 v[212:215], a[172:175], a[228:231], v[212:215]\n"
    "v_cvt_pk_bf16_f32 v122, v124, v125\n"
    "v_cvt_pk_bf16_f32 v123, v126, v127\n"
    "v_mfma_f32_16x16x32_bf16 v[224:227], a[228:231], a[176:179], v[224:227]\n"
    "buffer_load_dwordx4 v131, s[40:43], s63 offen offset:3072 lds\n"
    "v_mfma_f32_16x16x32_bf16 v[236:239], a[228:231], a[180:183], v[236:239]\n"
    "buffer_store_dwordx4 v[120:123], v146, s[44:47], s68 offen offset:1024 sc1\n"
    "v_mfma_f32_16x16x32_bf16 v[184:187], a[160:163], a[228:231], v[184:187]\n"
    "v_mfma_f32_16x16x32_bf16 v[188:191], a[164:167], a[228:231], v[188:191]\n"
    "v_lshrrev_b32 v84, 4, v146\n"
    "v_and_b32 v85, 15, v84\n"
    "v_lshrrev_b32 v88, 4, v84\n"
    "v_cmp_gt_u32 s[64:65], 3, v88\n"
    "v_lshlrev_b32 v88, 2, v88\n"
    "v_sub_u32 v85, v85, v88\n"
    "v_add_u32 v88, 16, v85\n"
    "v_and_b32 v88, 0xfffffff1, v88\n"
    "v_cmp_eq_u32 s[88:89], 0, v88\n"
    "v_add_u32 v88, 15, v85\n"
    "v_and_b32 v88, 0xfffffff1, v88\n"
    "v_cmp_eq_u32 s[90:91], 0, v88\n"
    "v_add_u32 v88, 14, v85\n"
    "v_and_b32 v88, 0xfffffff1, v88\n"
    "v_cmp_eq_u32 s[92:93], 0, v88\n"
    "v_add_u32 v88, 13, v85\n"
    "s_add_u32 s63, s63, 128\n"
    "v_and_b32 v88, 0xfffffff1, v88\n"
    "v_cmp_eq_u32 s[94:95], 0, v88\n"
    "v_add_u32 v88, 0, v85\n"
    "v_and_b32 v88, 0xfffffff1, v88\n"
    "v_cmp_eq_u32 s[96:97], 0, v88\n"
    "v_add_u32 v88, -1, v85\n"
    "v_and_b32 v88, 0xfffffff1, v88\n"
    "v_cmp_eq_u32 s[98:99], 0, v88\n"
    "v_add_u32 v88, -2, v85\n"
    "v_and_b32 v88, 0xfffffff1, v88\n"
    "v_cmp_eq_u32 s[52:53], 0, v88\n"
    "v_add_u32 v88, -3, v85\n"
    "v_and_b32 v88, 0xfffffff1, v88\n"
    "v_cmp_eq_u32 s[54:55], 0, v88\n"
    "v_mov_b32 v90, 0xf149f2ca\n"
    "s_mov_b32 s56, 0x3e41848f\n"
    "s_mov_b32 s57, 0x3fb8aa3b\n"
    "v_cvt_pk_bf16_f32 v8, v192, v193\n"
    "v_cvt_pk_bf16_f32 v9, v194, v195\n"
    "v_cvt_pk_bf16_f32 v10, v196, v197\n"
    "v_cvt_pk_bf16_f32 v11, v198, v199\n"
    "v_cvt_pk_bf16_f32 v12, v200, v201\n"
    "v_cvt_pk_bf16_f32 v13, v202, v203\n"
    "v_cvt_pk_bf16_f32 v14, v204, v205\n"
    "v_cvt_pk_bf16_f32 v15, v206, v207\n"
    "v_cvt_pk_bf16_f32 v16, v208, v209\n"
    "v_cvt_pk_bf16_f32 v17, v210, v211\n"
    "v_cvt_pk_bf16_f32 v18, v212, v213\n"
    "v_cvt_pk_bf16_f32 v19, v214, v215\n"
    "v_cvt_pk_bf16_f32 v20, v216, v217\n"
    "v_cvt_pk_bf16_f32 v21, v218, v219\n"
    "v_cvt_pk_bf16_f32 v22, v220, v221\n"
    "v_cvt_pk_bf16_f32 v23, v222, v223\n"
    "v_cvt_pk_bf16_f32 v24, v228, v229\n"
    "v_cvt_pk_bf16_f32 v25, v230, v231\n"
    "s_add_u32 m0, s79, 0x8000\n"
    "v_cvt_pk_bf16_f32 v26, v232, v233\n"
    "v_cvt_pk_bf16_f32 v27, v234, v235\n"
    "v_cvt_pk_bf16_f32 v28, v220, v221\n"
    "v_cvt_pk_bf16_f32 v29, v222, v223\n"
    "v_cvt_pk_bf16_f32 v30, v224, v225\n"
    "v_cvt_pk_bf16_f32 v31, v226, v227\n"
    "v_cvt_pk_bf16_f32 v32, v232, v233\n"
    "v_cvt_pk_bf16_f32 v33, v234, v235\n"
    "v_cvt_pk_bf16_f32 v34, v236, v237\n"
    "v_cvt_pk_bf16_f32 v35, v238, v239\n"
    "v_cvt_pk_bf16_f32 v0, v176, v177\n"
    "v_cvt_pk_bf16_f32 v1, v178, v179\n"
    "v_cvt_pk_bf16_f32 v2, v180, v181\n"
    "v_cvt_pk_bf16_f32 v3, v182, v183\n"
    "v_cndmask_b32 v2, 0, v2, s[64:65]\n"
    "v_cndmask_b32 v3, 0, v3, s[64:65]\n"
    "v_cvt_pk_bf16_f32 v4, v184, v185\n"
    "v_cvt_pk_bf16_f32 v5, v186, v187\n"
    "v_cvt_pk_bf16_f32 v6, v188, v189\n"
    "v_cvt_pk_bf16_f32 v7, v190, v191\n"
    "v_cndmask_b32 v6, 0, v6, s[64:65]\n"
    "v_cndmask_b32 v7, 0, v7, s[64:65]\n"
    "v_accvgpr_read_b32 v168, a64\n"
    "v_accvgpr_read_b32 v169, a65\n"
    "v_accvgpr_read_b32 v170, a66\n"
    "v_accvgpr_read_b32 v171, a67\n"
    "v_accvgpr_read_b32 v172, a68\n"
    "v_accvgpr_read_b32 v173, a69\n"
    "v_accvgpr_read_b32 v174, a70\n"
    "v_accvgpr_read_b32 v175, a71\n"
    "v_mul_f32 v168, s66, v168\n"
    "v_mul_f32 v169, s66, v169\n"
    "buffer_load_dwordx4 v132, s[84:87], s78 offen offset:0 lds\n"
    "v_mul_f32 v170, s66, v170\n"
    "v_mul_f32 v171, s66, v171\n"
    "v_mul_f32 v172, s66, v172\n"
    "v_mul_f32 v173, s66, v173\n"
    "v_mul_f32 v174, s66, v174\n"
    "v_mul_f32 v175, s66, v175\n"
    "v_cvt_pk_bf16_f32 v176, v168, v169\n"
    "v_cvt_pk_bf16_f32 v177, v170, v171\n"
    "v_cvt_pk_bf16_f32 v178, v172, v173\n"
    "v_cvt_pk_bf16_f32 v179, v174, v175\n"
    "buffer_store_dwordx4 v[176:179], v146, s[44:47], s70 offen offset:0 sc1\n"
    "v_mfma_f32_16x16x32_bf16 v[36:39], v[8:11], v[0:3], 0\n"
    "v_mfma_f32_16x16x32_bf16 v[40:43], v[12:15], v[0:3], 0\n"
    "v_mfma_f32_16x16x32_bf16 v[44:47], v[12:15], v[4:7], 0\n"
    "v_mfma_f32_16x16x32_bf16 v[48:51], v[16:19], v[4:7], 0\n"
    "s_nop 7\n"
    "v_accvgpr_read_b32 v168, a96\n"
    "v_accvgpr_read_b32 v169, a97\n"
    "v_accvgpr_read_b32 v170, a98\n"
    "v_accvgpr_read_b32 v171, a99\n"
    "v_accvgpr_read_b32 v172, a100\n"
    "v_accvgpr_read_b32 v173, a101\n"
    "v_accvgpr_read_b32 v174, a102\n"
    "v_accvgpr_read_b32 v175, a103\n"
    "v_mul_f32 v168, s66, v168\n"
    "v_mul_f32 v169, s66, v169\n"
    "v_mul_f32 v170, s66, v170\n"
    "v_mul_f32 v171, s66, v171\n"
    "v_mul_f32 v172, s66, v172\n"
    "v_mul_f32 v173, s66, v173\n"
    "v_mul_f32 v174, s66, v174\n"
    "v_mul_f32 v175, s66, v175\n"
    "v_cvt_pk_bf16_f32 v180, v168, v169\n"
    "v_cvt_pk_bf16_f32 v181, v170, v171\n"
    "v_cvt_pk_bf16_f32 v182, v172, v173\n"
    "v_cvt_pk_bf16_f32 v183, v174, v175\n"
    "buffer_store_dwordx4 v[180:183], v146, s[44:47], s70 offen offset:1024 sc1\n"
    "v_cvt_pk_bf16_f32 v88, v36, v37\n"
    "v_lshlrev_b32 v36, 16, v88\n"
    "v_and_b32 v37, 0xffff0000, v88\n"
    "v_mul_f32 v36, s56, v36\n"
    "buffer_load_dwordx4 v133, s[84:87], s78 offen offset:1024 lds\n"
    "v_mul_f32 v37, s56, v37\n"
    "v_cvt_pk_bf16_f32 v88, v36, v37\n"
    "v_lshlrev_b32 v36, 16, v88\n"
    "v_and_b32 v37, 0xffff0000, v88\n"
    "v_mul_f32 v36, s57, v36\n"
    "v_mul_f32 v37, s57, v37\n"
    "v_cvt_pk_bf16_f32 v88, v38, v39\n"
    "v_lshlrev_b32 v38, 16, v88\n"
    "v_and_b32 v39, 0xffff0000, v88\n"
    "v_mul_f32 v38, s56, v38\n"
    "v_mul_f32 v39, s56, v39\n"
    "v_cvt_pk_bf16_f32 v88, v38, v39\n"
    "v_lshlrev_b32 v38, 16, v88\n"
    "v_and_b32 v39, 0xffff0000, v88\n"
    "v_mul_f32 v38, s57, v38\n"
    "v_mul_f32 v39, s57, v39\n"
    "v_cndmask_b32 v36, v90, v36, s[88:89]\n"
    "v_cndmask_b32 v37, v90, v37, s[90:91]\n"
    "v_cndmask_b32 v38, v90, v38, s[92:93]\n"
    "v_cndmask_b32 v39, v90, v39, s[94:95]\n"
    "v_cvt_pk_bf16_f32 v88, v40, v41\n"
    "v_lshlrev_b32 v40, 16, v88\n"
    "v_and_b32 v41, 0xffff0000, v88\n"
    "v_mul_f32 v40, s56, v40\n"
    "v_mul_f32 v41, s56, v41\n"
    "v_cvt_pk_bf16_f32 v88, v40, v41\n"
    "v_lshlrev_b32 v40, 16, v88\n"
    "v_and_b32 v41, 0xffff0000, v88\n"
    "v_mul_f32 v40, s57, v40\n"
    "v_mul_f32 v41, s57, v41\n"
    "v_cvt_pk_bf16_f32 v88, v42, v43\n"
    "v_lshlrev_b32 v42, 16, v88\n"
    "buffer_load_dwordx4 v134, s[84:87], s78 offen offset:2048 lds\n"
    "v_and_b32 v43, 0xffff0000, v88\n"
    "v_mul_f32 v42, s56, v42\n"
    "v_mul_f32 v43, s56, v43\n"
    "v_cvt_pk_bf16_f32 v88, v42, v43\n"
    "v_lshlrev_b32 v42, 16, v88\n"
    "v_and_b32 v43, 0xffff0000, v88\n"
    "v_mul_f32 v42, s57, v42\n"
    "v_mul_f32 v43, s57, v43\n"
    "v_cndmask_b32 v40, v90, v40, s[96:97]\n"
    "v_cndmask_b32 v41, v90, v41, s[98:99]\n"
    "v_cndmask_b32 v42, v90, v42, s[52:53]\n"
    "v_cndmask_b32 v43, v90, v43, s[54:55]\n"
    "v_accvgpr_read_b32 v168, a72\n"
    "v_accvgpr_read_b32 v169, a73\n"
    "v_accvgpr_read_b32 v170, a74\n"
    "v_accvgpr_read_b32 v171, a75\n"
    "v_accvgpr_read_b32 v172, a76\n"
    "v_accvgpr_read_b32 v173, a77\n"
    "v_accvgpr_read_b32 v174, a78\n"
    "v_accvgpr_read_b32 v175, a79\n"
    "v_mul_f32 v168, s66, v168\n"
    "v_mul_f32 v169, s66, v169\n"
    "v_mul_f32 v170, s66, v170\n"
    "v_mul_f32 v171, s66, v171\n"
    "v_mul_f32 v172, s66, v172\n"
    "v_mul_f32 v173, s66, v173\n"
    "v_mul_f32 v174, s66, v174\n"
    "v_mul_f32 v175, s66, v175\n"
    "v_cvt_pk_bf16_f32 v184, v168, v169\n"
    "v_cvt_pk_bf16_f32 v185, v170, v171\n"
    "v_cvt_pk_bf16_f32 v186, v172, v173\n"
    "v_cvt_pk_bf16_f32 v187, v174, v175\n"
    "s_add_u32 s70, s70, s69\n"
    "buffer_store_dwordx4 v[184:187], v146, s[44:47], s70 offen offset:0 sc1\n"
    "v_cvt_pk_bf16_f32 v88, v44, v45\n"
    "buffer_load_dwordx4 v135, s[84:87], s78 offen offset:3072 lds\n"
    "v_lshlrev_b32 v44, 16, v88\n"
    "v_and_b32 v45, 0xffff0000, v88\n"
    "v_mul_f32 v44, s56, v44\n"
    "v_mul_f32 v45, s56, v45\n"
    "v_cvt_pk_bf16_f32 v88, v44, v45\n"
    "v_lshlrev_b32 v44, 16, v88\n"
    "v_and_b32 v45, 0xffff0000, v88\n"
    "v_mul_f32 v44, s57, v44\n"
    "v_mul_f32 v45, s57, v45\n"
    "v_cvt_pk_bf16_f32 v88, v46, v47\n"
    "v_lshlrev_b32 v46, 16, v88\n"
    "v_and_b32 v47, 0xffff0000, v88\n"
    "v_mul_f32 v46, s56, v46\n"
    "v_mul_f32 v47, s56, v47\n"
    "v_cvt_pk_bf16_f32 v88, v46, v47\n"
    "v_lshlrev_b32 v46, 16, v88\n"
    "v_and_b32 v47, 0xffff0000, v88\n"
    "v_mul_f32 v46, s57, v46\n"
    "v_mul_f32 v47, s57, v47\n"
    "v_cndmask_b32 v44, v90, v44, s[88:89]\n"
    "v_cndmask_b32 v45, v90, v45, s[90:91]\n"
    "v_cndmask_b32 v46, v90, v46, s[92:93]\n"
    "v_cndmask_b32 v47, v90, v47, s[94:95]\n"
    "v_cvt_pk_bf16_f32 v88, v48, v49\n"
    "v_lshlrev_b32 v48, 16, v88\n"
    "v_and_b32 v49, 0xffff0000, v88\n"
    "v_mul_f32 v48, s56, v48\n"
    "v_mul_f32 v49, s56, v49\n"
    "v_cvt_pk_bf16_f32 v88, v48, v49\n"
    "v_lshlrev_b32 v48, 16, v88\n"
    "v_and_b32 v49, 0xffff0000, v88\n"
    "v_mul_f32 v48, s57, v48\n"
    "s_add_u32 s78, s78, 0x40000\n"
    "v_mul_f32 v49, s57, v49\n"
    "v_cvt_pk_bf16_f32 v88, v50, v51\n"
    "v_lshlrev_b32 v50, 16, v88\n"
    "v_and_b32 v51, 0xffff0000, v88\n"
    "v_mul_f32 v50, s56, v50\n"
    "v_mul_f32 v51, s56, v51\n"
    "v_cvt_pk_bf16_f32 v88, v50, v51\n"
    "v_lshlrev_b32 v50, 16, v88\n"
    "v_and_b32 v51, 0xffff0000, v88\n"
    "v_mul_f32 v50, s57, v50\n"
    "v_mul_f32 v51, s57, v51\n"
    "v_cndmask_b32 v48, v90, v48, s[96:97]\n"
    "v_cndmask_b32 v49, v90, v49, s[98:99]\n"
    "v_cndmask_b32 v50, v90, v50, s[52:53]\n"
    "v_cndmask_b32 v51, v90, v51, s[54:55]\n"
    "s_cmp_eq_u32 s82, 0\n"
    "s_cbranch_scc0 NOTFIRST_%=\n"
    "v_mov_b32 v36, v90\n"
    "v_mov_b32 v37, v90\n"
    "v_mov_b32 v38, v90\n"
    "v_mov_b32 v39, v90\n"
    "NOTFIRST_%=:\n"
    "v_accvgpr_read_b32 v168, a104\n"
    "v_accvgpr_read_b32 v169, a105\n"
    "v_accvgpr_read_b32 v170, a106\n"
    "v_accvgpr_read_b32 v171, a107\n"
    "v_accvgpr_read_b32 v172, a108\n"
    "v_accvgpr_read_b32 v173, a109\n"
    "v_accvgpr_read_b32 v174, a110\n"
    "v_accvgpr_read_b32 v175, a111\n"
    "v_mul_f32 v168, s66, v168\n"
    "v_mul_f32 v169, s66, v169\n"
    "v_mul_f32 v170, s66, v170\n"
    "v_mul_f32 v171, s66, v171\n"
    "v_mul_f32 v172, s66, v172\n"
    "v_mul_f32 v173, s66, v173\n"
    "v_mul_f32 v174, s66, v174\n"
    "v_mul_f32 v175, s66, v175\n"
    "v_cvt_pk_bf16_f32 v188, v168, v169\n"
    "s_add_u32 m0, s79, 0x10000\n"
    "v_cvt_pk_bf16_f32 v189, v170, v171\n"
    "v_cvt_pk_bf16_f32 v190, v172, v173\n"
    "v_cvt_pk_bf16_f32 v191, v174, v175\n"
    "buffer_store_dwordx4 v[188:191], v146, s[44:47], s70 offen offset:1024 sc1\n"
    "v_max3_f32 v86, v36, v37, v38\n"
    "v_max3_f32 v86, v86, v39, v40\n"
    "v_max3_f32 v86, v86, v41, v42\n"
    "v_max_f32 v86, v86, v43\n"
    "v_max3_f32 v87, v44, v45, v46\n"
    "v_max3_f32 v87, v87, v47, v48\n"
    "v_max3_f32 v87, v87, v49, v50\n"
    "v_max_f32 v87, v87, v51\n"
    "v_mov_b32 v88, v86\n"
    "s_nop 1\n"
    "v_permlane16_swap_b32 v86, v88\n"
    "v_max_f32 v86, v86, v88\n"
    "v_mov_b32 v88, v86\n"
    "s_nop 1\n"
    "v_permlane32_swap_b32 v86, v88\n"
    "v_max_f32 v86, v86, v88\n"
    "v_mov_b32 v88, v87\n"
    "s_nop 1\n"
    "v_permlane16_swap_b32 v87, v88\n"
    "v_max_f32 v87, v87, v88\n"
    "v_mov_b32 v88, v87\n"
    "s_nop 1\n"
    "v_permlane32_swap_b32 v87, v88\n"
    "v_max_f32 v87, v87, v88\n"
    "v_sub_f32 v36, v36, v86\n"
    "v_sub_f32 v37, v37, v86\n"
    "v_sub_f32 v38, v38, v86\n"
    "v_sub_f32 v39, v39, v86\n"
    "v_sub_f32 v40, v40, v86\n"
    "v_sub_f32 v41, v41, v86\n"
    "v_sub_f32 v42, v42, v86\n"
    "v_sub_f32 v43, v43, v86\n"
    "v_exp_f32 v36, v36\n"
    "v_exp_f32 v37, v37\n"
    "v_exp_f32 v38, v38\n"
    "v_exp_f32 v39, v39\n"
    "v_exp_f32 v40, v40\n"
    "v_exp_f32 v41, v41\n"
    "buffer_load_dwordx4 v132, s[84:87], s78 offen offset:0 lds\n"
    "v_exp_f32 v42, v42\n"
    "v_exp_f32 v43, v43\n"
    "v_sub_f32 v44, v44, v87\n"
    "v_sub_f32 v45, v45, v87\n"
    "v_sub_f32 v46, v46, v87\n"
    "v_sub_f32 v47, v47, v87\n"
    "v_sub_f32 v48, v48, v87\n"
    "v_sub_f32 v49, v49, v87\n"
    "v_sub_f32 v50, v50, v87\n"
    "v_sub_f32 v51, v51, v87\n"
    "v_exp_f32 v44, v44\n"
    "v_exp_f32 v45, v45\n"
    "v_exp_f32 v46, v46\n"
    "v_exp_f32 v47, v47\n"
    "v_exp_f32 v48, v48\n"
    "v_exp_f32 v49, v49\n"
    "v_exp_f32 v50, v50\n"
    "v_exp_f32 v51, v51\n"
    "v_add_f32 v86, v36, v37\n"
    "v_add_f32 v86, v86, v38\n"
    "v_add_f32 v86, v86, v39\n"
    "v_add_f32 v86, v86, v40\n"
    "v_add_f32 v86, v86, v41\n"
    "v_add_f32 v86, v86, v42\n"
    "v_add_f32 v86, v86, v43\n"
    "v_add_f32 v87, v44, v45\n"
    "v_add_f32 v87, v87, v46\n"
    "v_add_f32 v87, v87, v47\n"
    "v_add_f32 v87, v87, v48\n"
    "v_add_f32 v87, v87, v49\n"
    "v_add_f32 v87, v87, v50\n"
    "v_add_f32 v87, v87, v51\n"
    "buffer_load_dwordx4 v133, s[84:87], s78 offen offset:1024 lds\n"
    "v_accvgpr_read_b32 v168, a80\n"
    "v_accvgpr_read_b32 v169, a81\n"
    "v_accvgpr_read_b32 v170, a82\n"
    "v_accvgpr_read_b32 v171, a83\n"
    "v_accvgpr_read_b32 v172, a84\n"
    "v_accvgpr_read_b32 v173, a85\n"
    "v_accvgpr_read_b32 v174, a86\n"
    "v_accvgpr_read_b32 v175, a87\n"
    "v_mul_f32 v168, s66, v168\n"
    "v_mul_f32 v169, s66, v169\n"
    "v_mul_f32 v170, s66, v170\n"
    "v_mul_f32 v171, s66, v171\n"
    "v_mul_f32 v172, s66, v172\n"
    "v_mul_f32 v173, s66, v173\n"
    "v_mul_f32 v174, s66, v174\n"
    "v_mul_f32 v175, s66, v175\n"
    "v_cvt_pk_bf16_f32 v192, v168, v169\n"
    "v_cvt_pk_bf16_f32 v193, v170, v171\n"
    "v_cvt_pk_bf16_f32 v194, v172, v173\n"
    "v_cvt_pk_bf16_f32 v195, v174, v175\n"
    "s_add_u32 s70, s70, s69\n"
    "buffer_store_dwordx4 v[192:195], v146, s[44:47], s70 offen offset:0 sc1\n"
    "v_mov_b32 v88, v86\n"
    "s_nop 1\n"
    "v_permlane16_swap_b32 v86, v88\n"
    "v_add_f32 v86, v86, v88\n"
    "v_mov_b32 v88, v86\n"
    "s_nop 1\n"
    "v_permlane32_swap_b32 v86, v88\n"
    "v_add_f32 v86, v86, v88\n"
    "v_mov_b32 v88, v87\n"
    "s_nop 1\n"
    "v_permlane16_swap_b32 v87, v88\n"
    "v_add_f32 v87, v87, v88\n"
    "v_mov_b32 v88, v87\n"
    "s_nop 1\n"
    "v_permlane32_swap_b32 v87, v88\n"
    "v_add_f32 v87, v87, v88\n"
    "v_rcp_f32 v86, v86\n"
    "v_rcp_f32 v87, v87\n"
    "s_nop 0\n"
    "v_mul_f32 v36, v36, v86\n"
    "v_mul_f32 v37, v37, v86\n"
    "v_mul_f32 v38, v38, v86\n"
    "v_mul_f32 v39, v39, v86\n"
    "buffer_load_dwordx4 v134, s[84:87], s78 offen offset:2048 lds\n"
    "v_mul_f32 v40, v40, v86\n"
    "v_mul_f32 v41, v41, v86\n"
    "v_mul_f32 v42, v42, v86\n"
    "v_mul_f32 v43, v43, v86\n"
    "v_cvt_pk_bf16_f32 v52, v36, v37\n"
    "v_cvt_pk_bf16_f32 v53, v38, v39\n"
    "v_cvt_pk_bf16_f32 v54, v40, v41\n"
    "v_cvt_pk_bf16_f32 v55, v42, v43\n"
    "v_mul_f32 v44, v44, v87\n"
    "v_mul_f32 v45, v45, v87\n"
    "v_mul_f32 v46, v46, v87\n"
    "v_mul_f32 v47, v47, v87\n"
    "v_mul_f32 v48, v48, v87\n"
    "v_mul_f32 v49, v49, v87\n"
    "v_mul_f32 v50, v50, v87\n"
    "v_mul_f32 v51, v51, v87\n"
    "v_cvt_pk_bf16_f32 v56, v44, v45\n"
    "v_cvt_pk_bf16_f32 v57, v46, v47\n"
    "v_cvt_pk_bf16_f32 v58, v48, v49\n"
    "v_cvt_pk_bf16_f32 v59, v50, v51\n"
    "s_nop 1\n"
    "v_mfma_f32_16x16x32_bf16 v[60:63], v[20:23], v[52:55], 0\n"
    "v_mfma_f32_16x16x32_bf16 v[64:67], v[24:27], v[52:55], 0\n"
    "v_mfma_f32_16x16x32_bf16 v[68:71], v[28:31], v[56:59], 0\n"
    "v_mfma_f32_16x16x32_bf16 v[72:75], v[32:35], v[56:59], 0\n"
    "s_mov_b32 s81, 0x4e00000\n"
    "s_add_u32 s48, s4, s81\n"
    "s_addc_u32 s49, s5, 0\n"
    "s_and_b32 s49, s49, 0xffff\n"
    "s_mov_b32 s50, 2097152\n"
    "s_mov_b32 s51, 0x20000\n"
    "s_nop 3\n"
    "v_accvgpr_read_b32 v168, a112\n"
    "v_accvgpr_read_b32 v169, a113\n"
    "v_accvgpr_read_b32 v170, a114\n"
    "v_accvgpr_read_b32 v171, a115\n"
    "v_accvgpr_read_b32 v172, a116\n"
    "v_accvgpr_read_b32 v173, a117\n"
    "v_accvgpr_read_b32 v174, a118\n"
    "v_accvgpr_read_b32 v175, a119\n"
    "v_mul_f32 v168, s66, v168\n"
    "v_mul_f32 v169, s66, v169\n"
    "v_mul_f32 v170, s66, v170\n"
    "v_mul_f32 v171, s66, v171\n"
    "v_mul_f32 v172, s66, v172\n"
    "buffer_load_dwordx4 v135, s[84:87], s78 offen offset:3072 lds\n"
    "v_mul_f32 v173, s66, v173\n"
    "v_mul_f32 v174, s66, v174\n"
    "v_mul_f32 v175, s66, v175\n"
    "v_cvt_pk_bf16_f32 v196, v168, v169\n"
    "v_cvt_pk_bf16_f32 v197, v170, v171\n"
    "v_cvt_pk_bf16_f32 v198, v172, v173\n"
    "v_cvt_pk_bf16_f32 v199, v174, v175\n"
    "buffer_store_dwordx4 v[196:199], v146, s[44:47], s70 offen offset:1024 sc1\n"
    "v_accvgpr_read_b32 v168, a88\n"
    "v_accvgpr_read_b32 v169, a89\n"
    "v_accvgpr_read_b32 v170, a90\n"
    "v_accvgpr_read_b32 v171, a91\n"
    "v_accvgpr_read_b32 v172, a92\n"
    "v_accvgpr_read_b32 v173, a93\n"
    "v_accvgpr_read_b32 v174, a94\n"
    "v_accvgpr_read_b32 v175, a95\n"
    "v_mul_f32 v168, s66, v168\n"
    "v_mul_f32 v169, s66, v169\n"
    "v_mul_f32 v170, s66, v170\n"
    "v_mul_f32 v171, s66, v171\n"
    "v_mul_f32 v172, s66, v172\n"
    "v_mul_f32 v173, s66, v173\n"
    "v_mul_f32 v174, s66, v174\n"
    "v_mul_f32 v175, s66, v175\n"
    "v_cvt_pk_bf16_f32 v200, v168, v169\n"
    "v_cvt_pk_bf16_f32 v201, v170, v171\n"
    "v_cvt_pk_bf16_f32 v202, v172, v173\n"
    "v_cvt_pk_bf16_f32 v203, v174, v175\n"
    "s_add_u32 s70, s70, s69\n"
    "buffer_store_dwordx4 v[200:203], v146, s[44:47], s70 offen offset:0 sc1\n"
    "v_accvgpr_read_b32 v168, a120\n"
    "v_accvgpr_read_b32 v169, a121\n"
    "v_accvgpr_read_b32 v170, a122\n"
    "v_accvgpr_read_b32 v171, a123\n"
    "v_accvgpr_read_b32 v172, a124\n"
    "v_accvgpr_read_b32 v173, a125\n"
    "v_accvgpr_read_b32 v174, a126\n"
    "s_add_u32 s78, s78, 0x40000\n"
    "v_accvgpr_read_b32 v175, a127\n"
    "v_mul_f32 v168, s66, v168\n"
    "v_mul_f32 v169, s66, v169\n"
    "v_mul_f32 v170, s66, v170\n"
    "v_mul_f32 v171, s66, v171\n"
    "v_mul_f32 v172, s66, v172\n"
    "v_mul_f32 v173, s66, v173\n"
    "v_mul_f32 v174, s66, v174\n"
    "v_mul_f32 v175, s66, v175\n"
    "v_cvt_pk_bf16_f32 v148, v168, v169\n"
    "v_cvt_pk_bf16_f32 v149, v170, v171\n"
    "v_cvt_pk_bf16_f32 v150, v172, v173\n"
    "v_cvt_pk_bf16_f32 v151, v174, v175\n"
    "buffer_store_dwordx4 v[148:151], v146, s[44:47], s70 offen offset:1024 sc1\n"
    "v_cvt_pk_bf16_f32 v76, v60, v61\n"
    "v_cvt_pk_bf16_f32 v77, v62, v63\n"
    "v_cvt_pk_bf16_f32 v78, v64, v65\n"
    "v_cvt_pk_bf16_f32 v79, v66, v67\n"
    "v_cndmask_b32 v78, 0, v78, s[64:65]\n"
    "v_cndmask_b32 v79, 0, v79, s[64:65]\n"
    "buffer_store_dwordx4 v[76:79], v146, s[48:51], s83 offen sc1\n"
    "s_add_u32 s83, s83, 0x4000\n"
    "v_cvt_pk_bf16_f32 v80, v68, v69\n"
    "v_cvt_pk_bf16_f32 v81, v70, v71\n"
    "v_cvt_pk_bf16_f32 v82, v72, v73\n"
    "v_cvt_pk_bf16_f32 v83, v74, v75\n"
    "v_cndmask_b32 v82, 0, v82, s[64:65]\n"
    "v_cndmask_b32 v83, 0, v83, s[64:65]\n"
    "buffer_store_dwordx4 v[80:83], v146, s[48:51], s83 offen sc1\n"
    "s_add_u32 s83, s83, 0x4000\n"
    "s_mov_b32 s56, s27\n"
    "s_and_b32 s92, s26, 7\n"
    "s_lshr_b32 s93, s26, 3\n"
    "s_and_b32 s94, s93, 1\n"
    "s_lshl_b32 s92, s92, 1\n"
    "s_add_u32 s76, s92, s94\n"
    "s_lshr_b32 s93, s93, 1\n"
    "s_and_b32 s77, s93, 7\n"
    "s_lshr_b32 s95, s93, 3\n"
    "s_lshl_b32 s92, s77, 2\n"
    "s_mov_b32 s73, 0\n"
    "s_cmp_eq_u32 s95, 1\n"
    "s_cbranch_scc0 TYPEB_%=\n"
    "s_sub_u32 s72, 15, s77\n"
    "s_cmp_lt_u32 s77, 7\n"
    "s_cselect_b32 s74, 38, 36\n"
    "s_cselect_b32 s75, 2, 0\n"
    "s_branch JDONE_%=\n"
    "TYPEB_%=:\n"
    "s_mov_b32 s72, s77\n"
    "s_add_u32 s74, s92, 4\n"
    "s_cmp_lt_u32 s77, 7\n"
    "s_cselect_b32 s75, 3, 0\n"
    "JDONE_%=:\n"
    "s_lshl_b32 s80, s76, 7\n"
    "s_add_u32 s80, s80, 0x1049000\n"
    "s_mul_i32 s92, s76, 0x200000\n"
    "s_add_u32 s92, s92, 0x0\n"
    "s_add_u32 s36, s4, s92\n"
    "s_addc_u32 s37, s5, 0\n"
    "s_and_b32 s37, s37, 0xffff\n"
    "s_mov_b32 s38, 0x0\n"
    "s_mov_b32 s39, 0x20000\n"
    "s_lshl_b32 s92, s76, 10\n"
    "s_add_u32 s92, s92, 0x3200000\n"
    "s_add_u32 s48, s4, s92\n"
    "s_addc_u32 s49, s5, 0\n"
    "s_and_b32 s49, s49, 0xffff\n"
    "s_mov_b32 s50, 0x1bfc400\n"
    "s_mov_b32 s51, 0x20000\n"
    "s_mov_b32 s92, 0x2000000\n"
    "s_add_u32 s52, s4, s92\n"
    "s_addc_u32 s53, s5, 0\n"
    "s_and_b32 s53, s53, 0xffff\n"
    "s_mov_b32 s54, 0x1049880\n"
    "s_mov_b32 s55, 0x20000\n"
    "s_lshl_b32 s92, s76, 3\n"
    "s_add_u32 s92, s92, s77\n"
    "s_lshl_b32 s83, s92, 7\n"
    "s_add_u32 s83, s83, 0x1040000\n"
    "s_lshl_b32 s92, s92, 2\n"
    "s_add_u32 s92, s92, s56\n"
    "s_lshl_b32 s81, s92, 14\n"
    "s_lshl_b32 s82, s92, 9\n"
    "s_add_u32 s82, s82, 0x1000000\n"
    "s_mov_b64 s[88:89], s[52:53]\n"
    "s_mov_b32 s91, 0x20000\n"
    "s_cmp_eq_u32 s75, 2\n"
    "s_cselect_b32 s90, s54, 0\n"
    "v_accvgpr_read_b32 v168, a0\n"
    "v_accvgpr_read_b32 v169, a1\n"
    "v_accvgpr_read_b32 v170, a2\n"
    "v_accvgpr_read_b32 v171, a3\n"
    "v_accvgpr_read_b32 v172, a4\n"
    "v_accvgpr_read_b32 v173, a5\n"
    "v_accvgpr_read_b32 v174, a6\n"
    "v_accvgpr_read_b32 v175, a7\n"
    "s_add_u32 m0, s79, 0x1c000\n"
    "v_mul_f32 v168, s66, v168\n"
    "v_mul_f32 v169, s66, v169\n"
    "v_mul_f32 v170, s66, v170\n"
    "v_mul_f32 v171, s66, v171\n"
    "v_mul_f32 v172, s66, v172\n"
    "v_mul_f32 v173, s66, v173\n"
    "v_mul_f32 v174, s66, v174\n"
    "buffer_load_dwordx4 v128, s[40:43], s63 offen offset:0 lds\n"
    "v_mul_f32 v175, s66, v175\n"
    "v_cvt_pk_bf16_f32 v152, v168, v169\n"
    "v_cvt_pk_bf16_f32 v153, v170, v171\n"
    "v_cvt_pk_bf16_f32 v154, v172, v173\n"
    "v_cvt_pk_bf16_f32 v155, v174, v175\n"
    "buffer_store_dwordx4 v[152:155], v146, s[44:47], s71 offen offset:0 sc1\n"
    "v_mov_b32 v210, v146\n"
    "buffer_load_dwordx4 v129, s[40:43], s63 offen offset:1024 lds\n"
    "v_lshrrev_b32 v217, 2, v146\n"
    "v_mov_b32 v222, 0\n"
    "v_mov_b32 v211, 0xf149f2ca\n"
    "v_mov_b32 v213, 0x4e800000\n"
    "v_mov_b64 v[144:145], v[248:249]\n"
    "v_mov_b64 v[208:209], v[250:251]\n"
    "v_accvgpr_read_b32 v136, a232\n"
    "v_accvgpr_read_b32 v137, a233\n"
    "buffer_load_dwordx4 v130, s[40:43], s63 offen offset:2048 lds\n"
    "v_accvgpr_read_b32 v138, a234\n"
    "v_accvgpr_read_b32 v139, a235\n"
    "v_accvgpr_read_b32 v140, a236\n"
    "v_accvgpr_read_b32 v141, a237\n"
    "v_accvgpr_read_b32 v142, a238\n"
    "v_accvgpr_read_b32 v143, a239\n"
    "v_accvgpr_read_b32 v224, a240\n"
    "buffer_load_dwordx4 v131, s[40:43], s63 offen offset:3072 lds\n"
    "v_accvgpr_read_b32 v225, a241\n"
    "v_accvgpr_read_b32 v226, a242\n"
    "v_accvgpr_read_b32 v227, a243\n"
    "v_accvgpr_read_b32 v228, a244\n"
    "v_accvgpr_read_b32 v229, a245\n"
    "v_accvgpr_read_b32 v230, a246\n"
    "v_accvgpr_read_b32 v231, a247\n"
    "s_waitcnt vmcnt(16)\n"
    "s_barrier\n"
    "ds_read_b128 a[128:131], v144 offset:32768\n"
    "ds_read_b128 a[132:135], v144 offset:34816\n"
    "ds_read_b64_tr_b16 a[64:65], v136 offset:32768\n"
    "ds_read_b64_tr_b16 a[66:67], v136 offset:34816\n"
    "ds_read_b64_tr_b16 a[68:69], v137 offset:32768\n"
    "ds_read_b64_tr_b16 a[70:71], v137 offset:34816\n"
    "ds_read_b64_tr_b16 a[72:73], v138 offset:32768\n"
    "ds_read_b64_tr_b16 a[74:75], v138 offset:34816\n"
    "ds_read_b64_tr_b16 a[76:77], v139 offset:32768\n"
    "ds_read_b64_tr_b16 a[78:79], v139 offset:34816\n"
    "ds_read_b64_tr_b16 a[80:81], v140 offset:32768\n"
    "ds_read_b64_tr_b16 a[82:83], v140 offset:34816\n"
    "ds_read_b64_tr_b16 a[84:85], v141 offset:32768\n"
    "ds_read_b64_tr_b16 a[86:87], v141 offset:34816\n"
    "s_waitcnt lgkmcnt(8)\n"
    "v_mfma_f32_16x16x32_bf16 v[0:3], a[64:67], a[128:131], 0\n"
    "ds_read_b64_tr_b16 a[88:89], v142 offset:32768\n"
    "s_add_u32 s63, s63, 128\n"
    "v_accvgpr_read_b32 v168, a32\n"
    "v_mfma_f32_16x16x32_bf16 v[4:7], a[64:67], a[132:135], 0\n"
    "ds_read_b64_tr_b16 a[90:91], v142 offset:34816\n"
    "s_add_u32 m0, s79, 0x18000\n"
    "v_accvgpr_read_b32 v169, a33\n"
    "v_mfma_f32_16x16x32_bf16 v[12:15], a[68:71], a[132:135], 0\n"
    "ds_read_b64_tr_b16 a[92:93], v143 offset:32768\n"
    "buffer_load_dwordx4 v132, s[84:87], s78 offen offset:0 lds\n"
    "v_mfma_f32_16x16x32_bf16 v[8:11], a[68:71], a[128:131], 0\n"
    "ds_read_b64_tr_b16 a[94:95], v143 offset:34816\n"
    "s_waitcnt lgkmcnt(8)\n"
    "buffer_load_dwordx4 v133, s[84:87], s78 offen offset:1024 lds\n"
    "v_mfma_f32_16x16x32_bf16 v[16:19], a[72:75], a[128:131], 0\n"
    "ds_read_b64_tr_b16 a[96:97], v136 offset:33024\n"
    "v_accvgpr_read_b32 v170, a34\n"
    "v_accvgpr_read_b32 v171, a35\n"
    "v_mfma_f32_16x16x32_bf16 v[20:23], a[72:75], a[132:135], 0\n"
    "ds_read_b64_tr_b16 a[98:99], v136 offset:35072\n"
    "buffer_load_dwordx4 v134, s[84:87], s78 offen offset:2048 lds\n"
    "v_mfma_f32_16x16x32_bf16 v[28:31], a[76:79], a[132:135], 0\n"
    "ds_read_b64_tr_b16 a[100:101], v137 offset:33024\n"
    "buffer_load_dwordx4 v135, s[84:87], s78 offen offset:3072 lds\n"
    "v_add_u32 v136, 0x10000, v136\n"
    "v_mfma_f32_16x16x32_bf16 v[24:27], a[76:79], a[128:131], 0\n"
    "ds_read_b64_tr_b16 a[102:103], v137 offset:35072\n"
    "s_waitcnt lgkmcnt(8)\n"
    "s_add_u32 s78, s78, 0x40000\n"
    "v_mfma_f32_16x16x32_bf16 v[32:35], a[80:83], a[128:131], 0\n"
    "ds_read_b64_tr_b16 a[104:105], v138 offset:33024\n"
    "v_add_u32 v137, 0x10000, v137\n"
    "v_accvgpr_read_b32 v172, a36\n"
    "v_mfma_f32_16x16x32_bf16 v[36:39], a[80:83], a[132:135], 0\n"
    "ds_read_b64_tr_b16 a[106:107], v138 offset:35072\n"
    "s_add_u32 m0, s79, 0x20000\n"
    "v_accvgpr_read_b32 v173, a37\n"
    "v_mfma_f32_16x16x32_bf16 v[44:47], a[84:87], a[132:135], 0\n"
    "ds_read_b64_tr_b16 a[108:109], v139 offset:33024\n"
    "buffer_load_dwordx4 v132, s[84:87], s78 offen offset:0 lds\n"
    "v_add_u32 v138, 0x10000, v138\n"
    "v_mfma_f32_16x16x32_bf16 v[40:43], a[84:87], a[128:131], 0\n"
    "ds_read_b64_tr_b16 a[110:111], v139 offset:35072\n"
    "s_waitcnt lgkmcnt(8)\n"
    "buffer_load_dwordx4 v133, s[84:87], s78 offen offset:1024 lds\n"
    "v_mfma_f32_16x16x32_bf16 v[48:51], a[88:91], a[128:131], 0\n"
    "ds_read_b64_tr_b16 a[112:113], v140 offset:33024\n"
    "v_add_u32 v139, 0x10000, v139\n"
    "v_accvgpr_read_b32 v174, a38\n"
    "v_mfma_f32_16x16x32_bf16 v[52:55], a[88:91], a[132:135], 0\n"
    "ds_read_b64_tr_b16 a[114:115], v140 offset:35072\n"
    "buffer_load_dwordx4 v134, s[84:87], s78 offen offset:2048 lds\n"
    "v_mfma_f32_16x16x32_bf16 v[60:63], a[92:95], a[132:135], 0\n"
    "ds_read_b64_tr_b16 a[116:117], v141 offset:33024\n"
    "buffer_load_dwordx4 v135, s[84:87], s78 offen offset:3072 lds\n"
    "v_add_u32 v140, 0x10000, v140\n"
    "v_mfma_f32_16x16x32_bf16 v[56:59], a[92:95], a[128:131], 0\n"
    "ds_read_b64_tr_b16 a[118:119], v141 offset:35072\n"
    "s_waitcnt lgkmcnt(8)\n"
    "s_add_u32 s78, s78, 0x40000\n"
    "v_mfma_f32_16x16x32_bf16 v[64:67], a[96:99], a[128:131], 0\n"
    "ds_read_b64_tr_b16 a[120:121], v142 offset:33024\n"
    "v_add_u32 v141, 0x10000, v141\n"
    "v_accvgpr_read_b32 v175, a39\n"
    "v_mfma_f32_16x16x32_bf16 v[68:71], a[96:99], a[132:135], 0\n"
    "ds_read_b64_tr_b16 a[122:123], v142 offset:35072\n"
    "v_mul_f32 v168, s66, v168\n"
    "v_mul_f32 v169, s66, v169\n"
    "v_mfma_f32_16x16x32_bf16 v[76:79], a[100:103], a[132:135], 0\n"
    "ds_read_b64_tr_b16 a[124:125], v143 offset:33024\n"
    "s_waitcnt vmcnt(18) lgkmcnt(7)\n"
    "v_mul_f32 v170, s66, v170\n"
    "v_mfma_f32_16x16x32_bf16 v[72:75], a[100:103], a[128:131], 0\n"
    "ds_read_b64_tr_b16 a[126:127], v143 offset:35072\n"
    "s_barrier\n"
    "v_mul_f32 v171, s66, v171\n"
    "v_mfma_f32_16x16x32_bf16 v[80:83], a[104:107], a[128:131], 0\n"
    "ds_read_b64_tr_b16 a[64:65], v136 offset:0\n"
    "ds_read_b128 a[136:139], v145 offset:32768\n"
    "v_mfma_f32_16x16x32_bf16 v[84:87], a[104:107], a[132:135], 0\n"
    "ds_read_b64_tr_b16 a[66:67], v136 offset:2048\n"
    "ds_read_b128 a[140:143], v145 offset:34816\n"
    "s_add_u32 m0, s79, 0x4000\n"
    "v_mfma_f32_16x16x32_bf16 v[92:95], a[108:111], a[132:135], 0\n"
    "ds_read_b64_tr_b16 a[68:69], v137 offset:0\n"
    "v_add_u32 v142, 0x10000, v142\n"
    "v_mul_f32 v172, s66, v172\n"
    "v_mfma_f32_16x16x32_bf16 v[88:91], a[108:111], a[128:131], 0\n"
    "ds_read_b64_tr_b16 a[70:71], v137 offset:2048\n"
    "s_waitcnt lgkmcnt(10)\n"
    "v_mul_f32 v173, s66, v173\n"
    "v_mfma_f32_16x16x32_bf16 v[96:99], a[112:115], a[128:131], 0\n"
    "ds_read_b64_tr_b16 a[72:73], v138 offset:0\n"
    "buffer_load_dwordx4 v128, s[40:43], s63 offen offset:0 lds\n"
    "v_add_u32 v143, 0x10000, v143\n"
    "v_mul_f32 v174, s66, v174\n"
    "v_mfma_f32_16x16x32_bf16 v[100:103], a[112:115], a[132:135], 0\n"
    "ds_read_b64_tr_b16 a[74:75], v138 offset:2048\n"
    "buffer_load_dwordx4 v129, s[40:43], s63 offen offset:1024 lds\n"
    "v_mul_f32 v175, s66, v175\n"
    "v_cvt_pk_bf16_f32 v156, v168, v169\n"
    "v_mfma_f32_16x16x32_bf16 v[108:111], a[116:119], a[132:135], 0\n"
    "ds_read_b64_tr_b16 a[76:77], v139 offset:0\n"
    "v_cvt_pk_bf16_f32 v157, v170, v171\n"
    "v_cvt_pk_bf16_f32 v158, v172, v173\n"
    "v_mfma_f32_16x16x32_bf16 v[104:107], a[116:119], a[128:131], 0\n"
    "ds_read_b64_tr_b16 a[78:79], v139 offset:2048\n"
    "s_waitcnt lgkmcnt(10)\n"
    "v_cvt_pk_bf16_f32 v159, v174, v175\n"
    "v_mfma_f32_16x16x32_bf16 v[112:115], a[120:123], a[128:131], 0\n"
    "ds_read_b64_tr_b16 a[80:81], v140 offset:0\n"
    "buffer_store_dwordx4 v[156:159], v146, s[44:47], s71 offen offset:1024 sc1\n"
    "v_mfma_f32_16x16x32_bf16 v[116:119], a[120:123], a[132:135], 0\n"
    "ds_read_b64_tr_b16 a[82:83], v140 offset:2048\n"
    "buffer_load_dwordx4 v130, s[40:43], s63 offen offset:2048 lds\n"
    "v_mfma_f32_16x16x32_bf16 v[124:127], a[124:127], a[132:135], 0\n"
    "ds_read_b64_tr_b16 a[84:85], v141 offset:0\n"
    "v_mfma_f32_16x16x32_bf16 v[120:123], a[124:127], a[128:131], 0\n"
    "ds_read_b64_tr_b16 a[86:87], v141 offset:2048\n"
    "s_waitcnt lgkmcnt(8)\n"
    "v_mfma_f32_16x16x32_bf16 v[0:3], a[64:67], a[136:139], v[0:3]\n"
    "ds_read_b64_tr_b16 a[88:89], v142 offset:0\n"
    "buffer_load_dwordx4 v131, s[40:43], s63 offen offset:3072 lds\n"
    "v_accvgpr_read_b32 v168, a8\n"
    "v_mfma_f32_16x16x32_bf16 v[4:7], a[64:67], a[140:143], v[4:7]\n"
    "ds_read_b64_tr_b16 a[90:91], v142 offset:2048\n"
    "v_add_u32 v144, 0x18000, v144\n"
    "v_accvgpr_read_b32 v169, a9\n"
    "v_accvgpr_read_b32 v170, a10\n"
    "v_mfma_f32_16x16x32_bf16 v[12:15], a[68:71], a[140:143], v[12:15]\n"
    "ds_read_b64_tr_b16 a[92:93], v143 offset:0\n"
    "s_add_u32 s63, s63, 128\n"
    "v_accvgpr_read_b32 v171, a11\n"
    "v_accvgpr_read_b32 v172, a12\n"
    "v_mfma_f32_16x16x32_bf16 v[8:11], a[68:71], a[136:139], v[8:11]\n"
    "ds_read_b64_tr_b16 a[94:95], v143 offset:2048\n"
    "s_waitcnt lgkmcnt(8)\n"
    "v_accvgpr_read_b32 v173, a13\n"
    "v_mfma_f32_16x16x32_bf16 v[16:19], a[72:75], a[136:139], v[16:19]\n"
    "ds_read_b64_tr_b16 a[96:97], v136 offset:256\n"
    "s_add_u32 m0, s79, 0x0\n"
    "v_accvgpr_read_b32 v174, a14\n"
    "v_accvgpr_read_b32 v175, a15\n"
    "v_mfma_f32_16x16x32_bf16 v[20:23], a[72:75], a[140:143], v[20:23]\n"
    "ds_read_b64_tr_b16 a[98:99], v136 offset:2304\n"
    "v_mul_f32 v168, s66, v168\n"
    "v_mfma_f32_16x16x32_bf16 v[28:31], a[76:79], a[140:143], v[28:31]\n"
    "ds_read_b64_tr_b16 a[100:101], v137 offset:256\n"
    "buffer_load_dwordx4 v132, s[84:87], s78 offen offset:0 lds\n"
    "v_mul_f32 v169, s66, v169\n"
    "v_mfma_f32_16x16x32_bf16 v[24:27], a[76:79], a[136:139], v[24:27]\n"
    "ds_read_b64_tr_b16 a[102:103], v137 offset:2304\n"
    "s_waitcnt lgkmcnt(8)\n"
    "v_mul_f32 v170, s66, v170\n"
    "v_mul_f32 v171, s66, v171\n"
    "v_mfma_f32_16x16x32_bf16 v[32:35], a[80:83], a[136:139], v[32:35]\n"
    "ds_read_b64_tr_b16 a[104:105], v138 offset:256\n"
    "buffer_load_dwordx4 v133, s[84:87], s78 offen offset:1024 lds\n"
    "v_mul_f32 v172, s66, v172\n"
    "v_mfma_f32_16x16x32_bf16 v[36:39], a[80:83], a[140:143], v[36:39]\n"
    "ds_read_b64_tr_b16 a[106:107], v138 offset:2304\n"
    "v_mul_f32 v173, s66, v173\n"
    "v_mul_f32 v174, s66, v174\n"
    "v_mfma_f32_16x16x32_bf16 v[44:47], a[84:87], a[140:143], v[44:47]\n"
    "ds_read_b64_tr_b16 a[108:109], v139 offset:256\n"
    "buffer_load_dwordx4 v134, s[84:87], s78 offen offset:2048 lds\n"
    "v_mul_f32 v175, s66, v175\n"
    "v_mfma_f32_16x16x32_bf16 v[40:43], a[84:87], a[136:139], v[40:43]\n"
    "ds_read_b64_tr_b16 a[110:111], v139 offset:2304\n"
    "s_waitcnt lgkmcnt(8)\n"
    "v_cvt_pk_bf16_f32 v160, v168, v169\n"
    "v_cvt_pk_bf16_f32 v161, v170, v171\n"
    "v_mfma_f32_16x16x32_bf16 v[48:51], a[88:91], a[136:139], v[48:51]\n"
    "ds_read_b64_tr_b16 a[112:113], v140 offset:256\n"
    "buffer_load_dwordx4 v135, s[84:87], s78 offen offset:3072 lds\n"
    "v_cvt_pk_bf16_f32 v162, v172, v173\n"
    "v_mfma_f32_16x16x32_bf16 v[52:55], a[88:91], a[140:143], v[52:55]\n"
    "ds_read_b64_tr_b16 a[114:115], v140 offset:2304\n"
    "v_cvt_pk_bf16_f32 v163, v174, v175\n"
    "s_add_u32 s71, s71, s69\n"
    "v_mfma_f32_16x16x32_bf16 v[60:63], a[92:95], a[140:143], v[60:63]\n"
    "ds_read_b64_tr_b16 a[116:117], v141 offset:256\n"
    "buffer_store_dwordx4 v[160:163], v146, s[44:47], s71 offen offset:0 sc1\n"
    "s_add_u32 s78, s78, 0x40000\n"
    "v_mfma_f32_16x16x32_bf16 v[56:59], a[92:95], a[136:139], v[56:59]\n"
    "ds_read_b64_tr_b16 a[118:119], v141 offset:2304\n"
    "s_waitcnt lgkmcnt(8)\n"
    "v_accvgpr_read_b32 v168, a40\n"
    "v_mfma_f32_16x16x32_bf16 v[64:67], a[96:99], a[136:139], v[64:67]\n"
    "ds_read_b64_tr_b16 a[120:121], v142 offset:256\n"
    "v_accvgpr_read_b32 v169, a41\n"
    "v_accvgpr_read_b32 v170, a42\n"
    "v_mfma_f32_16x16x32_bf16 v[68:71], a[96:99], a[140:143], v[68:71]\n"
    "ds_read_b64_tr_b16 a[122:123], v142 offset:2304\n"
    "v_accvgpr_read_b32 v171, a43\n"
    "v_mfma_f32_16x16x32_bf16 v[76:79], a[100:103], a[140:143], v[76:79]\n"
    "ds_read_b64_tr_b16 a[124:125], v143 offset:256\n"
    "s_waitcnt vmcnt(14) lgkmcnt(7)\n"
    "v_accvgpr_read_b32 v172, a44\n"
    "v_accvgpr_read_b32 v173, a45\n"
    "v_mfma_f32_16x16x32_bf16 v[72:75], a[100:103], a[136:139], v[72:75]\n"
    "ds_read_b64_tr_b16 a[126:127], v143 offset:2304\n"
    "s_barrier\n"
    "v_accvgpr_read_b32 v174, a46\n"
    "v_mfma_f32_16x16x32_bf16 v[80:83], a[104:107], a[136:139], v[80:83]\n"
    "ds_read_b64_tr_b16 a[64:65], v136 offset:32768\n"
    "ds_read_b128 a[128:131], v144 offset:0\n"
    "v_accvgpr_read_b32 v175, a47\n"
    "v_mfma_f32_16x16x32_bf16 v[84:87], a[104:107], a[140:143], v[84:87]\n"
    "ds_read_b64_tr_b16 a[66:67], v136 offset:34816\n"
    "ds_read_b128 a[132:135], v144 offset:2048\n"
    "v_mul_f32 v168, s66, v168\n"
    "v_mfma_f32_16x16x32_bf16 v[92:95], a[108:111], a[140:143], v[92:95]\n"
    "ds_read_b64_tr_b16 a[68:69], v137 offset:32768\n"
    "v_mul_f32 v169, s66, v169\n"
    "v_mul_f32 v170, s66, v170\n"
    "v_mfma_f32_16x16x32_bf16 v[88:91], a[108:111], a[136:139], v[88:91]\n"
    "ds_read_b64_tr_b16 a[70:71], v137 offset:34816\n"
    "s_waitcnt lgkmcnt(10)\n"
    "v_mul_f32 v171, s66, v171\n"
    "v_mul_f32 v172, s66, v172\n"
    "v_mfma_f32_16x16x32_bf16 v[96:99], a[112:115], a[136:139], v[96:99]\n"
    "ds_read_b64_tr_b16 a[72:73], v138 offset:32768\n"
    "v_mul_f32 v173, s66, v173\n"
    "s_add_u32 m0, s79, 0x8000\n"
    "v_mfma_f32_16x16x32_bf16 v[100:103], a[112:115], a[140:143], v[100:103]\n"
    "ds_read_b64_tr_b16 a[74:75], v138 offset:34816\n"
    "v_mul_f32 v174, s66, v174\n"
    "v_mul_f32 v175, s66, v175\n"
    "v_mfma_f32_16x16x32_bf16 v[108:111], a[116:119], a[140:143], v[108:111]\n"
    "ds_read_b64_tr_b16 a[76:77], v139 offset:32768\n"
    "v_cvt_pk_bf16_f32 v164, v168, v169\n"
    "v_mfma_f32_16x16x32_bf16 v[104:107], a[116:119], a[136:139], v[104:107]\n"
    "ds_read_b64_tr_b16 a[78:79], v139 offset:34816\n"
    "s_waitcnt lgkmcnt(10)\n"
    "v_cvt_pk_bf16_f32 v165, v170, v171\n"
    "v_cvt_pk_bf16_f32 v166, v172, v173\n"
    "v_mfma_f32_16x16x32_bf16 v[112:115], a[120:123], a[136:139], v[112:115]\n"
    "ds_read_b64_tr_b16 a[80:81], v140 offset:32768\n"
    "v_cvt_pk_bf16_f32 v167, v174, v175\n"
    "v_mfma_f32_16x16x32_bf16 v[116:119], a[120:123], a[140:143], v[116:119]\n"
    "ds_read_b64_tr_b16 a[82:83], v140 offset:34816\n"
    "buffer_store_dwordx4 v[164:167], v146, s[44:47], s71 offen offset:1024 sc1\n"
    "v_mfma_f32_16x16x32_bf16 v[124:127], a[124:127], a[140:143], v[124:127]\n"
    "ds_read_b64_tr_b16 a[84:85], v141 offset:32768\n"
    "buffer_load_dwordx4 v132, s[84:87], s78 offen offset:0 lds\n"
    "v_mfma_f32_16x16x32_bf16 v[120:123], a[124:127], a[136:139], v[120:123]\n"
    "ds_read_b64_tr_b16 a[86:87], v141 offset:34816\n"
    "s_waitcnt lgkmcnt(8)\n"
    "v_mfma_f32_16x16x32_bf16 v[0:3], a[64:67], a[128:131], v[0:3]\n"
    "ds_read_b64_tr_b16 a[88:89], v142 offset:32768\n"
    "buffer_load_dwordx4 v133, s[84:87], s78 offen offset:1024 lds\n"
    "v_accvgpr_read_b32 v168, a16\n"
    "v_mfma_f32_16x16x32_bf16 v[4:7], a[64:67], a[132:135], v[4:7]\n"
    "ds_read_b64_tr_b16 a[90:91], v142 offset:34816\n"
    "v_add_u32 v145, 0x18000, v145\n"
    "v_accvgpr_read_b32 v169, a17\n"
    "v_accvgpr_read_b32 v170, a18\n"
    "v_mfma_f32_16x16x32_bf16 v[12:15], a[68:71], a[132:135], v[12:15]\n"
    "ds_read_b64_tr_b16 a[92:93], v143 offset:32768\n"
    "v_accvgpr_read_b32 v171, a19\n"
    "v_accvgpr_read_b32 v172, a20\n"
    "v_mfma_f32_16x16x32_bf16 v[8:11], a[68:71], a[128:131], v[8:11]\n"
    "ds_read_b64_tr_b16 a[94:95], v143 offset:34816\n"
    "s_waitcnt lgkmcnt(8)\n"
    "v_accvgpr_read_b32 v173, a21\n"
    "v_mfma_f32_16x16x32_bf16 v[16:19], a[72:75], a[128:131], v[16:19]\n"
    "ds_read_b64_tr_b16 a[96:97], v136 offset:33024\n"
    "buffer_load_dwordx4 v134, s[84:87], s78 offen offset:2048 lds\n"
    "v_accvgpr_read_b32 v174, a22\n"
    "v_mfma_f32_16x16x32_bf16 v[20:23], a[72:75], a[132:135], v[20:23]\n"
    "ds_read_b64_tr_b16 a[98:99], v136 offset:35072\n"
    "v_accvgpr_read_b32 v175, a23\n"
    "v_mul_f32 v168, s66, v168\n"
    "v_mfma_f32_16x16x32_bf16 v[28:31], a[76:79], a[132:135], v[28:31]\n"
    "ds_read_b64_tr_b16 a[100:101], v137 offset:33024\n"
    "v_add_u32 v136, 0x10000, v136\n"
    "v_mul_f32 v169, s66, v169\n"
    "v_mul_f32 v170, s66, v170\n"
    "v_mfma_f32_16x16x32_bf16 v[24:27], a[76:79], a[128:131], v[24:27]\n"
    "ds_read_b64_tr_b16 a[102:103], v137 offset:35072\n"
    "s_waitcnt lgkmcnt(8)\n"
    "v_mul_f32 v171, s66, v171\n"
    "v_mfma_f32_16x16x32_bf16 v[32:35], a[80:83], a[128:131], v[32:35]\n"
    "ds_read_b64_tr_b16 a[104:105], v138 offset:33024\n"
    "v_add_u32 v137, 0x10000, v137\n"
    "v_mul_f32 v172, s66, v172\n"
    "v_mfma_f32_16x16x32_bf16 v[36:39], a[80:83], a[132:135], v[36:39]\n"
    "ds_read_b64_tr_b16 a[106:107], v138 offset:35072\n"
    "buffer_load_dwordx4 v135, s[84:87], s78 offen offset:3072 lds\n"
    "v_mul_f32 v173, s66, v173\n"
    "v_mfma_f32_16x16x32_bf16 v[44:47], a[84:87], a[132:135], v[44:47]\n"
    "ds_read_b64_tr_b16 a[108:109], v139 offset:33024\n"
    "v_add_u32 v138, 0x10000, v138\n"
    "v_mul_f32 v174, s66, v174\n"
    "v_mul_f32 v175, s66, v175\n"
    "v_mfma_f32_16x16x32_bf16 v[40:43], a[84:87], a[128:131], v[40:43]\n"
    "ds_read_b64_tr_b16 a[110:111], v139 offset:35072\n"
    "s_waitcnt lgkmcnt(8)\n"
    "v_cvt_pk_bf16_f32 v152, v168, v169\n"
    "v_cvt_pk_bf16_f32 v153, v170, v171\n"
    "v_mfma_f32_16x16x32_bf16 v[48:51], a[88:91], a[128:131], v[48:51]\n"
    "ds_read_b64_tr_b16 a[112:113], v140 offset:33024\n"
    "v_add_u32 v139, 0x10000, v139\n"
    "s_add_u32 s78, s78, 0x40000\n"
    "v_cvt_pk_bf16_f32 v154, v172, v173\n"
    "v_mfma_f32_16x16x32_bf16 v[52:55], a[88:91], a[132:135], v[52:55]\n"
    "ds_read_b64_tr_b16 a[114:115], v140 offset:35072\n"
    "v_cvt_pk_bf16_f32 v155, v174, v175\n"
    "s_add_u32 s71, s71, s69\n"
    "v_mfma_f32_16x16x32_bf16 v[60:63], a[92:95], a[132:135], v[60:63]\n"
    "ds_read_b64_tr_b16 a[116:117], v141 offset:33024\n"
    "buffer_store_dwordx4 v[152:155], v146, s[44:47], s71 offen offset:0 sc1\n"
    "v_add_u32 v140, 0x10000, v140\n"
    "v_mfma_f32_16x16x32_bf16 v[56:59], a[92:95], a[128:131], v[56:59]\n"
    "ds_read_b64_tr_b16 a[118:119], v141 offset:35072\n"
    "s_waitcnt lgkmcnt(8)\n"
    "v_accvgpr_read_b32 v168, a48\n"
    "v_mfma_f32_16x16x32_bf16 v[64:67], a[96:99], a[128:131], v[64:67]\n"
    "ds_read_b64_tr_b16 a[120:121], v142 offset:33024\n"
    "v_add_u32 v141, 0x10000, v141\n"
    "v_accvgpr_read_b32 v169, a49\n"
    "v_accvgpr_read_b32 v170, a50\n"
    "v_mfma_f32_16x16x32_bf16 v[68:71], a[96:99], a[132:135], v[68:71]\n"
    "ds_read_b64_tr_b16 a[122:123], v142 offset:35072\n"
    "v_accvgpr_read_b32 v171, a51\n"
    "v_mfma_f32_16x16x32_bf16 v[76:79], a[100:103], a[132:135], v[76:79]\n"
    "ds_read_b64_tr_b16 a[124:125], v143 offset:33024\n"
    "s_waitcnt vmcnt(16) lgkmcnt(7)\n"
    "v_accvgpr_read_b32 v172, a52\n"
    "v_accvgpr_read_b32 v173, a53\n"
    "v_mfma_f32_16x16x32_bf16 v[72:75], a[100:103], a[128:131], v[72:75]\n"
    "ds_read_b64_tr_b16 a[126:127], v143 offset:35072\n"
    "s_barrier\n"
    "v_accvgpr_read_b32 v174, a54\n"
    "v_mfma_f32_16x16x32_bf16 v[80:83], a[104:107], a[128:131], v[80:83]\n"
    "ds_read_b64_tr_b16 a[64:65], v136 offset:0\n"
    "ds_read_b128 a[136:139], v145 offset:0\n"
    "v_accvgpr_read_b32 v175, a55\n"
    "v_mfma_f32_16x16x32_bf16 v[84:87], a[104:107], a[132:135], v[84:87]\n"
    "ds_read_b64_tr_b16 a[66:67], v136 offset:2048\n"
    "ds_read_b128 a[140:143], v145 offset:2048\n"
    "v_mul_f32 v168, s66, v168\n"
    "v_mfma_f32_16x16x32_bf16 v[92:95], a[108:111], a[132:135], v[92:95]\n"
    "ds_read_b64_tr_b16 a[68:69], v137 offset:0\n"
    "v_add_u32 v142, 0x10000, v142\n"
    "v_mul_f32 v169, s66, v169\n"
    "v_mul_f32 v170, s66, v170\n"
    "v_mfma_f32_16x16x32_bf16 v[88:91], a[108:111], a[128:131], v[88:91]\n"
    "ds_read_b64_tr_b16 a[70:71], v137 offset:2048\n"
    "s_waitcnt lgkmcnt(10)\n"
    "v_mul_f32 v171, s66, v171\n"
    "v_mul_f32 v172, s66, v172\n"
    "v_mfma_f32_16x16x32_bf16 v[96:99], a[112:115], a[128:131], v[96:99]\n"
    "ds_read_b64_tr_b16 a[72:73], v138 offset:0\n"
    "v_add_u32 v143, 0x10000, v143\n"
    "v_mul_f32 v173, s66, v173\n"
    "s_add_u32 m0, s79, 0x14000\n"
    "v_mfma_f32_16x16x32_bf16 v[100:103], a[112:115], a[132:135], v[100:103]\n"
    "ds_read_b64_tr_b16 a[74:75], v138 offset:2048\n"
    "v_mul_f32 v174, s66, v174\n"
    "v_mul_f32 v175, s66, v175\n"
    "v_mfma_f32_16x16x32_bf16 v[108:111], a[116:119], a[132:135], v[108:111]\n"
    "ds_read_b64_tr_b16 a[76:77], v139 offset:0\n"
    "buffer_load_dwordx4 v128, s[40:43], s63 offen offset:0 lds\n"
    "v_cvt_pk_bf16_f32 v156, v168, v169\n"
    "v_mfma_f32_16x16x32_bf16 v[104:107], a[116:119], a[128:131], v[104:107]\n"
    "ds_read_b64_tr_b16 a[78:79], v139 offset:2048\n"
    "s_waitcnt lgkmcnt(10)\n"
    "v_cvt_pk_bf16_f32 v157, v170, v171\n"
    "v_cvt_pk_bf16_f32 v158, v172, v173\n"
    "v_mfma_f32_16x16x32_bf16 v[112:115], a[120:123], a[128:131], v[112:115]\n"
    "ds_read_b64_tr_b16 a[80:81], v140 offset:0\n"
    "buffer_load_dwordx4 v129, s[40:43], s63 offen offset:1024 lds\n"
    "v_cvt_pk_bf16_f32 v159, v174, v175\n"
    "v_mfma_f32_16x16x32_bf16 v[116:119], a[120:123], a[132:135], v[116:119]\n"
    "ds_read_b64_tr_b16 a[82:83], v140 offset:2048\n"
    "buffer_store_dwordx4 v[156:159], v146, s[44:47], s71 offen offset:1024 sc1\n"
    "v_mfma_f32_16x16x32_bf16 v[124:127], a[124:127], a[132:135], v[124:127]\n"
    "ds_read_b64_tr_b16 a[84:85], v141 offset:0\n"
    "buffer_load_dwordx4 v130, s[40:43], s63 offen offset:2048 lds\n"
    "v_mfma_f32_16x16x32_bf16 v[120:123], a[124:127], a[128:131], v[120:123]\n"
    "ds_read_b64_tr_b16 a[86:87], v141 offset:2048\n"
    "s_waitcnt lgkmcnt(8)\n"
    "v_mfma_f32_16x16x32_bf16 v[0:3], a[64:67], a[136:139], v[0:3]\n"
    "ds_read_b64_tr_b16 a[88:89], v142 offset:0\n"
    "buffer_load_dwordx4 v131, s[40:43], s63 offen offset:3072 lds\n"
    "v_accvgpr_read_b32 v168, a24\n"
    "v_mfma_f32_16x16x32_bf16 v[4:7], a[64:67], a[140:143], v[4:7]\n"
    "ds_read_b64_tr_b16 a[90:91], v142 offset:2048\n"
    "v_add_u32 v144, 0xfffe8000, v144\n"
    "v_accvgpr_read_b32 v169, a25\n"
    "v_accvgpr_read_b32 v170, a26\n"
    "v_mfma_f32_16x16x32_bf16 v[12:15], a[68:71], a[140:143], v[12:15]\n"
    "ds_read_b64_tr_b16 a[92:93], v143 offset:0\n"
    "s_add_u32 s63, s63, 128\n"
    "v_accvgpr_read_b32 v171, a27\n"
    "v_accvgpr_read_b32 v172, a28\n"
    "v_mfma_f32_16x16x32_bf16 v[8:11], a[68:71], a[136:139], v[8:11]\n"
    "ds_read_b64_tr_b16 a[94:95], v143 offset:2048\n"
    "s_waitcnt lgkmcnt(8)\n"
    "v_accvgpr_read_b32 v173, a29\n"
    "v_mfma_f32_16x16x32_bf16 v[16:19], a[72:75], a[136:139], v[16:19]\n"
    "ds_read_b64_tr_b16 a[96:97], v136 offset:256\n"
    "s_add_u32 m0, s79, 0x10000\n"
    "v_accvgpr_read_b32 v174, a30\n"
    "v_accvgpr_read_b32 v175, a31\n"
    "v_mfma_f32_16x16x32_bf16 v[20:23], a[72:75], a[140:143], v[20:23]\n"
    "ds_read_b64_tr_b16 a[98:99], v136 offset:2304\n"
    "v_mul_f32 v168, s66, v168\n"
    "v_mfma_f32_16x16x32_bf16 v[28:31], a[76:79], a[140:143], v[28:31]\n"
    "ds_read_b64_tr_b16 a[100:101], v137 offset:256\n"
    "v_add_u32 v136, 0xfffe0000, v136\n"
    "v_mul_f32 v169, s66, v169\n"
    "v_mul_f32 v170, s66, v170\n"
    "v_mfma_f32_16x16x32_bf16 v[24:27], a[76:79], a[136:139], v[24:27]\n"
    "ds_read_b64_tr_b16 a[102:103], v137 offset:2304\n"
    "s_waitcnt lgkmcnt(8)\n"
    "buffer_load_dwordx4 v132, s[84:87], s78 offen offset:0 lds\n"
    "v_mul_f32 v171, s66, v171\n"
    "v_mfma_f32_16x16x32_bf16 v[32:35], a[80:83], a[136:139], v[32:35]\n"
    "ds_read_b64_tr_b16 a[104:105], v138 offset:256\n"
    "v_add_u32 v137, 0xfffe0000, v137\n"
    "v_mul_f32 v172, s66, v172\n"
    "v_mfma_f32_16x16x32_bf16 v[36:39], a[80:83], a[140:143], v[36:39]\n"
    "ds_read_b64_tr_b16 a[106:107], v138 offset:2304\n"
    "buffer_load_dwordx4 v133, s[84:87], s78 offen offset:1024 lds\n"
    "v_mul_f32 v173, s66, v173\n"
    "v_mfma_f32_16x16x32_bf16 v[44:47], a[84:87], a[140:143], v[44:47]\n"
    "ds_read_b64_tr_b16 a[108:109], v139 offset:256\n"
    "v_add_u32 v138, 0xfffe0000, v138\n"
    "v_mul_f32 v174, s66, v174\n"
    "v_mul_f32 v175, s66, v175\n"
    "v_mfma_f32_16x16x32_bf16 v[40:43], a[84:87], a[136:139], v[40:43]\n"
    "ds_read_b64_tr_b16 a[110:111], v139 offset:2304\n"
    "s_waitcnt lgkmcnt(8)\n"
    "buffer_load_dwordx4 v134, s[84:87], s78 offen offset:2048 lds\n"
    "v_cvt_pk_bf16_f32 v160, v168, v169\n"
    "v_mfma_f32_16x16x32_bf16 v[48:51], a[88:91], a[136:139], v[48:51]\n"
    "ds_read_b64_tr_b16 a[112:113], v140 offset:256\n"
    "v_add_u32 v139, 0xfffe0000, v139\n"
    "v_cvt_pk_bf16_f32 v161, v170, v171\n"
    "v_cvt_pk_bf16_f32 v162, v172, v173\n"
    "v_mfma_f32_16x16x32_bf16 v[52:55], a[88:91], a[140:143], v[52:55]\n"
    "ds_read_b64_tr_b16 a[114:115], v140 offset:2304\n"
    "buffer_load_dwordx4 v135, s[84:87], s78 offen offset:3072 lds\n"
    "v_cvt_pk_bf16_f32 v163, v174, v175\n"
    "v_mfma_f32_16x16x32_bf16 v[60:63], a[92:95], a[140:143], v[60:63]\n"
    "ds_read_b64_tr_b16 a[116:117], v141 offset:256\n"
    "v_add_u32 v140, 0xfffe0000, v140\n"
    "s_add_u32 s78, s78, 0x40000\n"
    "s_add_u32 s71, s71, s69\n"
    "v_mfma_f32_16x16x32_bf16 v[56:59], a[92:95], a[136:139], v[56:59]\n"
    "ds_read_b64_tr_b16 a[118:119], v141 offset:2304\n"
    "s_waitcnt lgkmcnt(8)\n"
    "buffer_store_dwordx4 v[160:163], v146, s[44:47], s71 offen offset:0 sc1\n"
    "v_mfma_f32_16x16x32_bf16 v[64:67], a[96:99], a[136:139], v[64:67]\n"
    "ds_read_b64_tr_b16 a[120:121], v142 offset:256\n"
    "v_add_u32 v141, 0xfffe0000, v141\n"
    "v_accvgpr_read_b32 v168, a56\n"
    "v_accvgpr_read_b32 v169, a57\n"
    "v_mfma_f32_16x16x32_bf16 v[68:71], a[96:99], a[140:143], v[68:71]\n"
    "ds_read_b64_tr_b16 a[122:123], v142 offset:2304\n"
    "v_accvgpr_read_b32 v170, a58\n"
    "v_accvgpr_read_b32 v171, a59\n"
    "v_mfma_f32_16x16x32_bf16 v[76:79], a[100:103], a[140:143], v[76:79]\n"
    "ds_read_b64_tr_b16 a[124:125], v143 offset:256\n"
    "s_waitcnt vmcnt(17) lgkmcnt(7)\n"
    "v_accvgpr_read_b32 v172, a60\n"
    "v_accvgpr_read_b32 v173, a61\n"
    "v_mfma_f32_16x16x32_bf16 v[72:75], a[100:103], a[136:139], v[72:75]\n"
    "ds_read_b64_tr_b16 a[126:127], v143 offset:2304\n"
    "s_barrier\n"
    "v_accvgpr_read_b32 v174, a62\n"
    "v_mfma_f32_16x16x32_bf16 v[80:83], a[104:107], a[136:139], v[80:83]\n"
    "ds_read_b64_tr_b16 a[64:65], v136 offset:0\n"
    "ds_read_b128 a[128:131], v144 offset:0\n"
    "v_accvgpr_read_b32 v175, a63\n"
    "v_mfma_f32_16x16x32_bf16 v[84:87], a[104:107], a[140:143], v[84:87]\n"
    "ds_read_b64_tr_b16 a[66:67], v136 offset:2048\n"
    "ds_read_b128 a[132:135], v144 offset:2048\n"
    "v_mul_f32 v168, s66, v168\n"
    "v_mfma_f32_16x16x32_bf16 v[92:95], a[108:111], a[140:143], v[92:95]\n"
    "ds_read_b64_tr_b16 a[68:69], v137 offset:0\n"
    "v_add_u32 v142, 0xfffe0000, v142\n"
    "v_mul_f32 v169, s66, v169\n"
    "v_mul_f32 v170, s66, v170\n"
    "v_mfma_f32_16x16x32_bf16 v[88:91], a[108:111], a[136:139], v[88:91]\n"
    "ds_read_b64_tr_b16 a[70:71], v137 offset:2048\n"
    "s_waitcnt lgkmcnt(10)\n"
    "v_mul_f32 v171, s66, v171\n"
    "v_mul_f32 v172, s66, v172\n"
    "v_mfma_f32_16x16x32_bf16 v[96:99], a[112:115], a[136:139], v[96:99]\n"
    "ds_read_b64_tr_b16 a[72:73], v138 offset:0\n"
    "v_add_u32 v143, 0xfffe0000, v143\n"
    "v_mul_f32 v173, s66, v173\n"
    "v_mfma_f32_16x16x32_bf16 v[100:103], a[112:115], a[140:143], v[100:103]\n"
    "ds_read_b64_tr_b16 a[74:75], v138 offset:2048\n"
    "v_mul_f32 v174, s66, v174\n"
    "v_mul_f32 v175, s66, v175\n"
    "v_mfma_f32_16x16x32_bf16 v[108:111], a[116:119], a[140:143], v[108:111]\n"
    "ds_read_b64_tr_b16 a[76:77], v139 offset:0\n"
    "v_cvt_pk_bf16_f32 v164, v168, v169\n"
    "s_add_u32 m0, s79, 0x18000\n"
    "v_mfma_f32_16x16x32_bf16 v[104:107], a[116:119], a[136:139], v[104:107]\n"
    "ds_read_b64_tr_b16 a[78:79], v139 offset:2048\n"
    "s_waitcnt lgkmcnt(10)\n"
    "v_cvt_pk_bf16_f32 v165, v170, v171\n"
    "v_cvt_pk_bf16_f32 v166, v172, v173\n"
    "v_mfma_f32_16x16x32_bf16 v[112:115], a[120:123], a[136:139], v[112:115]\n"
    "ds_read_b64_tr_b16 a[80:81], v140 offset:0\n"
    "v_cvt_pk_bf16_f32 v167, v174, v175\n"
    "v_mfma_f32_16x16x32_bf16 v[116:119], a[120:123], a[140:143], v[116:119]\n"
    "ds_read_b64_tr_b16 a[82:83], v140 offset:2048\n"
    "buffer_store_dwordx4 v[164:167], v146, s[44:47], s71 offen offset:1024 sc1\n"
    "v_mfma_f32_16x16x32_bf16 v[124:127], a[124:127], a[140:143], v[124:127]\n"
    "ds_read_b64_tr_b16 a[84:85], v141 offset:0\n"
    "buffer_load_dwordx4 v132, s[84:87], s78 offen offset:0 lds\n"
    "v_mfma_f32_16x16x32_bf16 v[120:123], a[124:127], a[136:139], v[120:123]\n"
    "ds_read_b64_tr_b16 a[86:87], v141 offset:2048\n"
    "s_waitcnt lgkmcnt(8)\n"
    "v_mfma_f32_16x16x32_bf16 v[0:3], a[64:67], a[128:131], v[0:3]\n"
    "ds_read_b64_tr_b16 a[88:89], v142 offset:0\n"
    "buffer_load_dwordx4 v133, s[84:87], s78 offen offset:1024 lds\n"
    "v_mfma_f32_16x16x32_bf16 v[4:7], a[64:67], a[132:135], v[4:7]\n"
    "ds_read_b64_tr_b16 a[90:91], v142 offset:2048\n"
    "v_add_u32 v145, 0xfffe8000, v145\n"
    "v_mfma_f32_16x16x32_bf16 v[12:15], a[68:71], a[132:135], v[12:15]\n"
    "ds_read_b64_tr_b16 a[92:93], v143 offset:0\n"
    "s_mul_i32 s97, s76, 229376\n"
    "v_mfma_f32_16x16x32_bf16 v[8:11], a[68:71], a[128:131], v[8:11]\n"
    "ds_read_b64_tr_b16 a[94:95], v143 offset:2048\n"
    "s_waitcnt lgkmcnt(8)\n"
    "v_mfma_f32_16x16x32_bf16 v[16:19], a[72:75], a[128:131], v[16:19]\n"
    "ds_read_b64_tr_b16 a[96:97], v136 offset:256\n"
    "buffer_load_dwordx4 v134, s[84:87], s78 offen offset:2048 lds\n"
    "v_mfma_f32_16x16x32_bf16 v[20:23], a[72:75], a[132:135], v[20:23]\n"
    "ds_read_b64_tr_b16 a[98:99], v136 offset:2304\n"
    "s_add_u32 s44, s20, s97\n"
    "s_addc_u32 s45, s21, 0\n"
    "v_mfma_f32_16x16x32_bf16 v[28:31], a[76:79], a[132:135], v[28:31]\n"
    "ds_read_b64_tr_b16 a[100:101], v137 offset:256\n"
    "v_mfma_f32_16x16x32_bf16 v[24:27], a[76:79], a[128:131], v[24:27]\n"
    "ds_read_b64_tr_b16 a[102:103], v137 offset:2304\n"
    "s_waitcnt lgkmcnt(8)\n"
    "v_mfma_f32_16x16x32_bf16 v[32:35], a[80:83], a[128:131], v[32:35]\n"
    "ds_read_b64_tr_b16 a[104:105], v138 offset:256\n"
    "buffer_load_dwordx4 v135, s[84:87], s78 offen offset:3072 lds\n"
    "v_mfma_f32_16x16x32_bf16 v[36:39], a[80:83], a[132:135], v[36:39]\n"
    "ds_read_b64_tr_b16 a[106:107], v138 offset:2304\n"
    "v_mfma_f32_16x16x32_bf16 v[44:47], a[84:87], a[132:135], v[44:47]\n"
    "ds_read_b64_tr_b16 a[108:109], v139 offset:256\n"
    "v_mfma_f32_16x16x32_bf16 v[40:43], a[84:87], a[128:131], v[40:43]\n"
    "ds_read_b64_tr_b16 a[110:111], v139 offset:2304\n"
    "s_waitcnt lgkmcnt(8)\n"
    "v_mfma_f32_16x16x32_bf16 v[48:51], a[88:91], a[128:131], v[48:51]\n"
    "ds_read_b64_tr_b16 a[112:113], v140 offset:256\n"
    "s_add_u32 s78, s78, 0x40000\n"
    "v_mfma_f32_16x16x32_bf16 v[52:55], a[88:91], a[132:135], v[52:55]\n"
    "ds_read_b64_tr_b16 a[114:115], v140 offset:2304\n"
    "v_mfma_f32_16x16x32_bf16 v[60:63], a[92:95], a[132:135], v[60:63]\n"
    "ds_read_b64_tr_b16 a[116:117], v141 offset:256\n"
    "v_mfma_f32_16x16x32_bf16 v[56:59], a[92:95], a[128:131], v[56:59]\n"
    "ds_read_b64_tr_b16 a[118:119], v141 offset:2304\n"
    "s_waitcnt lgkmcnt(8)\n"
    "v_mfma_f32_16x16x32_bf16 v[64:67], a[96:99], a[128:131], v[64:67]\n"
    "ds_read_b64_tr_b16 a[120:121], v142 offset:256\n"
    "s_and_b32 s45, s45, 0xffff\n"
    "v_mfma_f32_16x16x32_bf16 v[68:71], a[96:99], a[132:135], v[68:71]\n"
    "ds_read_b64_tr_b16 a[122:123], v142 offset:2304\n"
    "v_mfma_f32_16x16x32_bf16 v[76:79], a[100:103], a[132:135], v[76:79]\n"
    "ds_read_b64_tr_b16 a[124:125], v143 offset:256\n"
    "s_waitcnt vmcnt(16) lgkmcnt(7)\n"
    "v_mfma_f32_16x16x32_bf16 v[72:75], a[100:103], a[128:131], v[72:75]\n"
    "ds_read_b64_tr_b16 a[126:127], v143 offset:2304\n"
    "s_barrier\n"
    "v_mfma_f32_16x16x32_bf16 v[80:83], a[104:107], a[128:131], v[80:83]\n"
    "ds_read_b64_tr_b16 a[64:65], v136 offset:32768\n"
    "ds_read_b128 a[136:139], v145 offset:0\n"
    "v_mfma_f32_16x16x32_bf16 v[84:87], a[104:107], a[132:135], v[84:87]\n"
    "ds_read_b64_tr_b16 a[66:67], v136 offset:34816\n"
    "ds_read_b128 a[140:143], v145 offset:2048\n"
    "v_mfma_f32_16x16x32_bf16 v[92:95], a[108:111], a[132:135], v[92:95]\n"
    "ds_read_b64_tr_b16 a[68:69], v137 offset:32768\n"
    "s_mov_b32 s46, 0x38000\n"
    "s_add_u32 m0, s79, 0x24000\n"
    "v_mfma_f32_16x16x32_bf16 v[88:91], a[108:111], a[128:131], v[88:91]\n"
    "ds_read_b64_tr_b16 a[70:71], v137 offset:34816\n"
    "s_waitcnt lgkmcnt(10)\n"
    "buffer_load_dwordx4 v128, s[40:43], s63 offen offset:0 lds\n"
    "v_mfma_f32_16x16x32_bf16 v[96:99], a[112:115], a[128:131], v[96:99]\n"
    "ds_read_b64_tr_b16 a[72:73], v138 offset:32768\n"
    "v_mfma_f32_16x16x32_bf16 v[100:103], a[112:115], a[132:135], v[100:103]\n"
    "ds_read_b64_tr_b16 a[74:75], v138 offset:34816\n"
    "s_mov_b32 s47, 0x20000\n"
    "v_mfma_f32_16x16x32_bf16 v[108:111], a[116:119], a[132:135], v[108:111]\n"
    "ds_read_b64_tr_b16 a[76:77], v139 offset:32768\n"
    "buffer_load_dwordx4 v129, s[40:43], s63 offen offset:1024 lds\n"
    "v_mfma_f32_16x16x32_bf16 v[104:107], a[116:119], a[128:131], v[104:107]\n"
    "ds_read_b64_tr_b16 a[78:79], v139 offset:34816\n"
    "s_waitcnt lgkmcnt(10)\n"
    "buffer_load_dwordx4 v130, s[40:43], s63 offen offset:2048 lds\n"
    "v_mfma_f32_16x16x32_bf16 v[112:115], a[120:123], a[128:131], v[112:115]\n"
    "ds_read_b64_tr_b16 a[80:81], v140 offset:32768\n"
    "v_mfma_f32_16x16x32_bf16 v[116:119], a[120:123], a[132:135], v[116:119]\n"
    "ds_read_b64_tr_b16 a[82:83], v140 offset:34816\n"
    "v_mfma_f32_16x16x32_bf16 v[124:127], a[124:127], a[132:135], v[124:127]\n"
    "ds_read_b64_tr_b16 a[84:85], v141 offset:32768\n"
    "v_mfma_f32_16x16x32_bf16 v[120:123], a[124:127], a[128:131], v[120:123]\n"
    "ds_read_b64_tr_b16 a[86:87], v141 offset:34816\n"
    "s_waitcnt lgkmcnt(8)\n"
    "v_mfma_f32_16x16x32_bf16 v[0:3], a[64:67], a[136:139], v[0:3]\n"
    "ds_read_b64_tr_b16 a[88:89], v142 offset:32768\n"
    "buffer_load_dwordx4 v131, s[40:43], s63 offen offset:3072 lds\n"
    "v_mfma_f32_16x16x32_bf16 v[4:7], a[64:67], a[140:143], v[4:7]\n"
    "ds_read_b64_tr_b16 a[90:91], v142 offset:34816\n"
    "v_add_u32 v144, 0x10000, v144\n"
    "v_mfma_f32_16x16x32_bf16 v[12:15], a[68:71], a[140:143], v[12:15]\n"
    "ds_read_b64_tr_b16 a[92:93], v143 offset:32768\n"
    "s_add_u32 s63, s63, 128\n"
    "v_mfma_f32_16x16x32_bf16 v[8:11], a[68:71], a[136:139], v[8:11]\n"
    "ds_read_b64_tr_b16 a[94:95], v143 offset:34816\n"
    "s_waitcnt lgkmcnt(8)\n"
    "v_mfma_f32_16x16x32_bf16 v[16:19], a[72:75], a[136:139], v[16:19]\n"
    "ds_read_b64_tr_b16 a[96:97], v136 offset:33024\n"
    "s_add_u32 m0, s79, 0x20000\n"
    "v_mfma_f32_16x16x32_bf16 v[20:23], a[72:75], a[140:143], v[20:23]\n"
    "ds_read_b64_tr_b16 a[98:99], v136 offset:35072\n"
    "v_mfma_f32_16x16x32_bf16 v[28:31], a[76:79], a[140:143], v[28:31]\n"
    "ds_read_b64_tr_b16 a[100:101], v137 offset:33024\n"
    "v_add_u32 v136, 0x10000, v136\n"
    "v_mfma_f32_16x16x32_bf16 v[24:27], a[76:79], a[136:139], v[24:27]\n"
    "ds_read_b64_tr_b16 a[102:103], v137 offset:35072\n"
    "s_waitcnt lgkmcnt(8)\n"
    "buffer_load_dwordx4 v132, s[84:87], s78 offen offset:0 lds\n"
    "v_mfma_f32_16x16x32_bf16 v[32:35], a[80:83], a[136:139], v[32:35]\n"
    "ds_read_b64_tr_b16 a[104:105], v138 offset:33024\n"
    "v_add_u32 v137, 0x10000, v137\n"
    "v_mfma_f32_16x16x32_bf16 v[36:39], a[80:83], a[140:143], v[36:39]\n"
    "ds_read_b64_tr_b16 a[106:107], v138 offset:35072\n"
    "buffer_load_dwordx4 v133, s[84:87], s78 offen offset:1024 lds\n"
    "v_mfma_f32_16x16x32_bf16 v[44:47], a[84:87], a[140:143], v[44:47]\n"
    "ds_read_b64_tr_b16 a[108:109], v139 offset:33024\n"
    "v_add_u32 v138, 0x10000, v138\n"
    "v_mfma_f32_16x16x32_bf16 v[40:43], a[84:87], a[136:139], v[40:43]\n"
    "ds_read_b64_tr_b16 a[110:111], v139 offset:35072\n"
    "s_waitcnt lgkmcnt(8)\n"
    "buffer_load_dwordx4 v134, s[84:87], s78 offen offset:2048 lds\n"
    "v_mfma_f32_16x16x32_bf16 v[48:51], a[88:91], a[136:139], v[48:51]\n"
    "ds_read_b64_tr_b16 a[112:113], v140 offset:33024\n"
    "v_add_u32 v139, 0x10000, v139\n"
    "v_mfma_f32_16x16x32_bf16 v[52:55], a[88:91], a[140:143], v[52:55]\n"
    "ds_read_b64_tr_b16 a[114:115], v140 offset:35072\n"
    "buffer_load_dwordx4 v135, s[84:87], s78 offen offset:3072 lds\n"
    "v_mfma_f32_16x16x32_bf16 v[60:63], a[92:95], a[140:143], v[60:63]\n"
    "ds_read_b64_tr_b16 a[116:117], v141 offset:33024\n"
    "v_add_u32 v140, 0x10000, v140\n"
    "s_add_u32 s78, s78, 0x40000\n"
    "v_mfma_f32_16x16x32_bf16 v[56:59], a[92:95], a[136:139], v[56:59]\n"
    "ds_read_b64_tr_b16 a[118:119], v141 offset:35072\n"
    "s_waitcnt lgkmcnt(8)\n"
    "v_mfma_f32_16x16x32_bf16 v[64:67], a[96:99], a[136:139], v[64:67]\n"
    "ds_read_b64_tr_b16 a[120:121], v142 offset:33024\n"
    "v_add_u32 v141, 0x10000, v141\n"
    "v_mfma_f32_16x16x32_bf16 v[68:71], a[96:99], a[140:143], v[68:71]\n"
    "ds_read_b64_tr_b16 a[122:123], v142 offset:35072\n"
    "v_mfma_f32_16x16x32_bf16 v[76:79], a[100:103], a[140:143], v[76:79]\n"
    "ds_read_b64_tr_b16 a[124:125], v143 offset:33024\n"
    "s_waitcnt vmcnt(14) lgkmcnt(7)\n"
    "v_mfma_f32_16x16x32_bf16 v[72:75], a[100:103], a[136:139], v[72:75]\n"
    "ds_read_b64_tr_b16 a[126:127], v143 offset:35072\n"
    "s_waitcnt vmcnt(12)\n"
    "s_barrier\n"
    "v_mfma_f32_16x16x32_bf16 v[80:83], a[104:107], a[136:139], v[80:83]\n"
    "ds_read_b64_tr_b16 a[64:65], v136 offset:0\n"
    "ds_read_b128 a[128:131], v144 offset:0\n"
    "v_mfma_f32_16x16x32_bf16 v[84:87], a[104:107], a[140:143], v[84:87]\n"
    "ds_read_b64_tr_b16 a[66:67], v136 offset:2048\n"
    "ds_read_b128 a[132:135], v144 offset:2048\n"
    "v_mfma_f32_16x16x32_bf16 v[92:95], a[108:111], a[140:143], v[92:95]\n"
    "ds_read_b64_tr_b16 a[68:69], v137 offset:0\n"
    "v_add_u32 v142, 0x10000, v142\n"
    "s_lshr_b32 s97, s26, 4\n"
    "v_mfma_f32_16x16x32_bf16 v[88:91], a[108:111], a[136:139], v[88:91]\n"
    "ds_read_b64_tr_b16 a[70:71], v137 offset:2048\n"
    "s_waitcnt lgkmcnt(10)\n"
    "v_mov_b32 v218, s34\n"
    "v_mfma_f32_16x16x32_bf16 v[96:99], a[112:115], a[136:139], v[96:99]\n"
    "ds_read_b64_tr_b16 a[72:73], v138 offset:0\n"
    "v_add_u32 v143, 0x10000, v143\n"
    "v_mov_b32 v219, s35\n"
    "v_mfma_f32_16x16x32_bf16 v[100:103], a[112:115], a[140:143], v[100:103]\n"
    "ds_read_b64_tr_b16 a[74:75], v138 offset:2048\n"
    "v_lshlrev_b32 v216, 21, v210\n"
    "s_lshl_b32 s98, s56, 25\n"
    "v_mfma_f32_16x16x32_bf16 v[108:111], a[116:119], a[140:143], v[108:111]\n"
    "ds_read_b64_tr_b16 a[76:77], v139 offset:0\n"
    "s_add_u32 s98, s98, s80\n"
    "s_add_u32 m0, s79, 0x0\n"
    "v_mfma_f32_16x16x32_bf16 v[104:107], a[116:119], a[136:139], v[104:107]\n"
    "ds_read_b64_tr_b16 a[78:79], v139 offset:2048\n"
    "s_waitcnt lgkmcnt(10)\n"
    "s_lshl3_add_u32 s98, s97, s98\n"
    "v_mfma_f32_16x16x32_bf16 v[112:115], a[120:123], a[136:139], v[112:115]\n"
    "ds_read_b64_tr_b16 a[80:81], v140 offset:0\n"
    "buffer_store_dwordx2 v[218:219], v216, s[52:55], s98 offen offset:0 sc1\n"
    "v_mfma_f32_16x16x32_bf16 v[116:119], a[120:123], a[140:143], v[116:119]\n"
    "ds_read_b64_tr_b16 a[82:83], v140 offset:2048\n"
    "buffer_load_dwordx4 v132, s[84:87], s78 offen offset:0 lds\n"
    "v_mfma_f32_16x16x32_bf16 v[124:127], a[124:127], a[140:143], v[124:127]\n"
    "ds_read_b64_tr_b16 a[84:85], v141 offset:0\n"
    "v_mfma_f32_16x16x32_bf16 v[120:123], a[124:127], a[136:139], v[120:123]\n"
    "ds_read_b64_tr_b16 a[86:87], v141 offset:2048\n"
    "s_waitcnt lgkmcnt(8)\n"
    "v_mfma_f32_16x16x32_bf16 v[0:3], a[64:67], a[128:131], v[0:3]\n"
    "ds_read_b64_tr_b16 a[88:89], v142 offset:0\n"
    "buffer_load_dwordx4 v133, s[84:87], s78 offen offset:1024 lds\n"
    "v_mfma_f32_16x16x32_bf16 v[4:7], a[64:67], a[132:135], v[4:7]\n"
    "ds_read_b64_tr_b16 a[90:91], v142 offset:2048\n"
    "v_add_u32 v145, 0x10000, v145\n"
    "v_mfma_f32_16x16x32_bf16 v[12:15], a[68:71], a[132:135], v[12:15]\n"
    "ds_read_b64_tr_b16 a[92:93], v143 offset:0\n"
    "v_mfma_f32_16x16x32_bf16 v[8:11], a[68:71], a[128:131], v[8:11]\n"
    "ds_read_b64_tr_b16 a[94:95], v143 offset:2048\n"
    "s_waitcnt lgkmcnt(8)\n"
    "v_mfma_f32_16x16x32_bf16 v[16:19], a[72:75], a[128:131], v[16:19]\n"
    "ds_read_b64_tr_b16 a[96:97], v136 offset:256\n"
    "buffer_load_dwordx4 v134, s[84:87], s78 offen offset:2048 lds\n"
    "v_mfma_f32_16x16x32_bf16 v[20:23], a[72:75], a[132:135], v[20:23]\n"
    "ds_read_b64_tr_b16 a[98:99], v136 offset:2304\n"
    "v_mfma_f32_16x16x32_bf16 v[28:31], a[76:79], a[132:135], v[28:31]\n"
    "ds_read_b64_tr_b16 a[100:101], v137 offset:256\n"
    "v_mfma_f32_16x16x32_bf16 v[24:27], a[76:79], a[128:131], v[24:27]\n"
    "ds_read_b64_tr_b16 a[102:103], v137 offset:2304\n"
    "s_waitcnt lgkmcnt(8)\n"
    "v_mfma_f32_16x16x32_bf16 v[32:35], a[80:83], a[128:131], v[32:35]\n"
    "ds_read_b64_tr_b16 a[104:105], v138 offset:256\n"
    "buffer_load_dwordx4 v135, s[84:87], s78 offen offset:3072 lds\n"
    "v_mfma_f32_16x16x32_bf16 v[36:39], a[80:83], a[132:135], v[36:39]\n"
    "ds_read_b64_tr_b16 a[106:107], v138 offset:2304\n"
    "v_mfma_f32_16x16x32_bf16 v[44:47], a[84:87], a[132:135], v[44:47]\n"
    "ds_read_b64_tr_b16 a[108:109], v139 offset:256\n"
    "v_mfma_f32_16x16x32_bf16 v[40:43], a[84:87], a[128:131], v[40:43]\n"
    "ds_read_b64_tr_b16 a[110:111], v139 offset:2304\n"
    "s_waitcnt lgkmcnt(8)\n"
    "v_mfma_f32_16x16x32_bf16 v[48:51], a[88:91], a[128:131], v[48:51]\n"
    "ds_read_b64_tr_b16 a[112:113], v140 offset:256\n"
    "s_add_u32 s78, s78, 0x40000\n"
    "v_mfma_f32_16x16x32_bf16 v[52:55], a[88:91], a[132:135], v[52:55]\n"
    "ds_read_b64_tr_b16 a[114:115], v140 offset:2304\n"
    "v_mfma_f32_16x16x32_bf16 v[60:63], a[92:95], a[132:135], v[60:63]\n"
    "ds_read_b64_tr_b16 a[116:117], v141 offset:256\n"
    "v_mfma_f32_16x16x32_bf16 v[56:59], a[92:95], a[128:131], v[56:59]\n"
    "ds_read_b64_tr_b16 a[118:119], v141 offset:2304\n"
    "s_waitcnt lgkmcnt(8)\n"
    "v_mfma_f32_16x16x32_bf16 v[64:67], a[96:99], a[128:131], v[64:67]\n"
    "ds_read_b64_tr_b16 a[120:121], v142 offset:256\n"
    "v_mfma_f32_16x16x32_bf16 v[68:71], a[96:99], a[132:135], v[68:71]\n"
    "ds_read_b64_tr_b16 a[122:123], v142 offset:2304\n"
    "v_mfma_f32_16x16x32_bf16 v[76:79], a[100:103], a[132:135], v[76:79]\n"
    "ds_read_b64_tr_b16 a[124:125], v143 offset:256\n"
    "s_waitcnt vmcnt(13) lgkmcnt(7)\n"
    "v_mfma_f32_16x16x32_bf16 v[72:75], a[100:103], a[128:131], v[72:75]\n"
    "ds_read_b64_tr_b16 a[126:127], v143 offset:2304\n"
    "s_barrier\n"
    "v_mfma_f32_16x16x32_bf16 v[80:83], a[104:107], a[128:131], v[80:83]\n"
    "ds_read_b64_tr_b16 a[64:65], v136 offset:32768\n"
    "ds_read_b128 a[136:139], v145 offset:0\n"
    "v_mfma_f32_16x16x32_bf16 v[84:87], a[104:107], a[132:135], v[84:87]\n"
    "ds_read_b64_tr_b16 a[66:67], v136 offset:34816\n"
    "ds_read_b128 a[140:143], v145 offset:2048\n"
    "v_mfma_f32_16x16x32_bf16 v[92:95], a[108:111], a[132:135], v[92:95]\n"
    "ds_read_b64_tr_b16 a[68:69], v137 offset:32768\n"
    "s_add_u32 m0, s79, 0xc000\n"
    "v_mfma_f32_16x16x32_bf16 v[88:91], a[108:111], a[128:131], v[88:91]\n"
    "ds_read_b64_tr_b16 a[70:71], v137 offset:34816\n"
    "s_waitcnt lgkmcnt(10)\n"
    "buffer_load_dwordx4 v128, s[40:43], s63 offen offset:0 lds\n"
    "v_mfma_f32_16x16x32_bf16 v[96:99], a[112:115], a[128:131], v[96:99]\n"
    "ds_read_b64_tr_b16 a[72:73], v138 offset:32768\n"
    "v_mfma_f32_16x16x32_bf16 v[100:103], a[112:115], a[132:135], v[100:103]\n"
    "ds_read_b64_tr_b16 a[74:75], v138 offset:34816\n"
    "buffer_load_dwordx4 v129, s[40:43], s63 offen offset:1024 lds\n"
    "v_mfma_f32_16x16x32_bf16 v[108:111], a[116:119], a[132:135], v[108:111]\n"
    "ds_read_b64_tr_b16 a[76:77], v139 offset:32768\n"
    "v_mfma_f32_16x16x32_bf16 v[104:107], a[116:119], a[128:131], v[104:107]\n"
    "ds_read_b64_tr_b16 a[78:79], v139 offset:34816\n"
    "s_waitcnt lgkmcnt(10)\n"
    "buffer_load_dwordx4 v130, s[40:43], s63 offen offset:2048 lds\n"
    "v_mfma_f32_16x16x32_bf16 v[112:115], a[120:123], a[128:131], v[112:115]\n"
    "ds_read_b64_tr_b16 a[80:81], v140 offset:32768\n"
    "v_mfma_f32_16x16x32_bf16 v[116:119], a[120:123], a[132:135], v[116:119]\n"
    "ds_read_b64_tr_b16 a[82:83], v140 offset:34816\n"
    "v_mfma_f32_16x16x32_bf16 v[124:127], a[124:127], a[132:135], v[124:127]\n"
    "ds_read_b64_tr_b16 a[84:85], v141 offset:32768\n"
    "v_mfma_f32_16x16x32_bf16 v[120:123], a[124:127], a[128:131], v[120:123]\n"
    "ds_read_b64_tr_b16 a[86:87], v141 offset:34816\n"
    "s_waitcnt lgkmcnt(8)\n"
    "v_mfma_f32_16x16x32_bf16 v[0:3], a[64:67], a[136:139], v[0:3]\n"
    "ds_read_b64_tr_b16 a[88:89], v142 offset:32768\n"
    "buffer_load_dwordx4 v131, s[40:43], s63 offen offset:3072 lds\n"
    "v_mfma_f32_16x16x32_bf16 v[4:7], a[64:67], a[140:143], v[4:7]\n"
    "ds_read_b64_tr_b16 a[90:91], v142 offset:34816\n"
    "v_add_u32 v144, 0x10000, v144\n"
    "v_mfma_f32_16x16x32_bf16 v[12:15], a[68:71], a[140:143], v[12:15]\n"
    "ds_read_b64_tr_b16 a[92:93], v143 offset:32768\n"
    "s_add_u32 s63, s63, 128\n"
    "v_mfma_f32_16x16x32_bf16 v[8:11], a[68:71], a[136:139], v[8:11]\n"
    "ds_read_b64_tr_b16 a[94:95], v143 offset:34816\n"
    "s_waitcnt lgkmcnt(8)\n"
    "v_mfma_f32_16x16x32_bf16 v[16:19], a[72:75], a[136:139], v[16:19]\n"
    "ds_read_b64_tr_b16 a[96:97], v136 offset:33024\n"
    "s_add_u32 m0, s79, 0x8000\n"
    "v_mfma_f32_16x16x32_bf16 v[20:23], a[72:75], a[140:143], v[20:23]\n"
    "ds_read_b64_tr_b16 a[98:99], v136 offset:35072\n"
    "v_mfma_f32_16x16x32_bf16 v[28:31], a[76:79], a[140:143], v[28:31]\n"
    "ds_read_b64_tr_b16 a[100:101], v137 offset:33024\n"
    "v_add_u32 v136, 0x10000, v136\n"
    "v_mfma_f32_16x16x32_bf16 v[24:27], a[76:79], a[136:139], v[24:27]\n"
    "ds_read_b64_tr_b16 a[102:103], v137 offset:35072\n"
    "s_waitcnt lgkmcnt(8)\n"
    "buffer_load_dwordx4 v132, s[84:87], s78 offen offset:0 lds\n"
    "v_mfma_f32_16x16x32_bf16 v[32:35], a[80:83], a[136:139], v[32:35]\n"
    "ds_read_b64_tr_b16 a[104:105], v138 offset:33024\n"
    "v_add_u32 v137, 0x10000, v137\n"
    "v_mfma_f32_16x16x32_bf16 v[36:39], a[80:83], a[140:143], v[36:39]\n"
    "ds_read_b64_tr_b16 a[106:107], v138 offset:35072\n"
    "buffer_load_dwordx4 v133, s[84:87], s78 offen offset:1024 lds\n"
    "v_mfma_f32_16x16x32_bf16 v[44:47], a[84:87], a[140:143], v[44:47]\n"
    "ds_read_b64_tr_b16 a[108:109], v139 offset:33024\n"
    "v_add_u32 v138, 0x10000, v138\n"
    "v_mfma_f32_16x16x32_bf16 v[40:43], a[84:87], a[136:139], v[40:43]\n"
    "ds_read_b64_tr_b16 a[110:111], v139 offset:35072\n"
    "s_waitcnt lgkmcnt(8)\n"
    "buffer_load_dwordx4 v134, s[84:87], s78 offen offset:2048 lds\n"
    "v_mfma_f32_16x16x32_bf16 v[48:51], a[88:91], a[136:139], v[48:51]\n"
    "ds_read_b64_tr_b16 a[112:113], v140 offset:33024\n"
    "v_add_u32 v139, 0x10000, v139\n"
    "v_mfma_f32_16x16x32_bf16 v[52:55], a[88:91], a[140:143], v[52:55]\n"
    "ds_read_b64_tr_b16 a[114:115], v140 offset:35072\n"
    "buffer_load_dwordx4 v135, s[84:87], s78 offen offset:3072 lds\n"
    "v_mfma_f32_16x16x32_bf16 v[60:63], a[92:95], a[140:143], v[60:63]\n"
    "ds_read_b64_tr_b16 a[116:117], v141 offset:33024\n"
    "v_add_u32 v140, 0x10000, v140\n"
    "s_add_u32 s78, s78, 0x40000\n"
    "v_mfma_f32_16x16x32_bf16 v[56:59], a[92:95], a[136:139], v[56:59]\n"
    "ds_read_b64_tr_b16 a[118:119], v141 offset:35072\n"
    "s_waitcnt lgkmcnt(8)\n"
    "v_mfma_f32_16x16x32_bf16 v[64:67], a[96:99], a[136:139], v[64:67]\n"
    "ds_read_b64_tr_b16 a[120:121], v142 offset:33024\n"
    "v_add_u32 v141, 0x10000, v141\n"
    "v_mfma_f32_16x16x32_bf16 v[68:71], a[96:99], a[140:143], v[68:71]\n"
    "ds_read_b64_tr_b16 a[122:123], v142 offset:35072\n"
    "v_mfma_f32_16x16x32_bf16 v[76:79], a[100:103], a[140:143], v[76:79]\n"
    "ds_read_b64_tr_b16 a[124:125], v143 offset:33024\n"
    "s_waitcnt vmcnt(13) lgkmcnt(7)\n"
    "v_mfma_f32_16x16x32_bf16 v[72:75], a[100:103], a[136:139], v[72:75]\n"
    "ds_read_b64_tr_b16 a[126:127], v143 offset:35072\n"
    "s_barrier\n"
    "v_mfma_f32_16x16x32_bf16 v[80:83], a[104:107], a[136:139], v[80:83]\n"
    "ds_read_b64_tr_b16 a[64:65], v136 offset:0\n"
    "ds_read_b128 a[128:131], v144 offset:0\n"
    "v_mfma_f32_16x16x32_bf16 v[84:87], a[104:107], a[140:143], v[84:87]\n"
    "ds_read_b64_tr_b16 a[66:67], v136 offset:2048\n"
    "ds_read_b128 a[132:135], v144 offset:2048\n"
    "v_mfma_f32_16x16x32_bf16 v[92:95], a[108:111], a[140:143], v[92:95]\n"
    "ds_read_b64_tr_b16 a[68:69], v137 offset:0\n"
    "v_add_u32 v142, 0x10000, v142\n"
    "s_add_u32 m0, s79, 0x10000\n"
    "v_mfma_f32_16x16x32_bf16 v[88:91], a[108:111], a[136:139], v[88:91]\n"
    "ds_read_b64_tr_b16 a[70:71], v137 offset:2048\n"
    "s_waitcnt lgkmcnt(10)\n"
    "v_mfma_f32_16x16x32_bf16 v[96:99], a[112:115], a[136:139], v[96:99]\n"
    "ds_read_b64_tr_b16 a[72:73], v138 offset:0\n"
    "v_add_u32 v143, 0x10000, v143\n"
    "v_mfma_f32_16x16x32_bf16 v[100:103], a[112:115], a[140:143], v[100:103]\n"
    "ds_read_b64_tr_b16 a[74:75], v138 offset:2048\n"
    "buffer_load_dwordx4 v132, s[84:87], s78 offen offset:0 lds\n"
    "v_mfma_f32_16x16x32_bf16 v[108:111], a[116:119], a[140:143], v[108:111]\n"
    "ds_read_b64_tr_b16 a[76:77], v139 offset:0\n"
    "v_mfma_f32_16x16x32_bf16 v[104:107], a[116:119], a[136:139], v[104:107]\n"
    "ds_read_b64_tr_b16 a[78:79], v139 offset:2048\n"
    "s_waitcnt lgkmcnt(10)\n"
    "v_mfma_f32_16x16x32_bf16 v[112:115], a[120:123], a[136:139], v[112:115]\n"
    "ds_read_b64_tr_b16 a[80:81], v140 offset:0\n"
    "v_mfma_f32_16x16x32_bf16 v[116:119], a[120:123], a[140:143], v[116:119]\n"
    "ds_read_b64_tr_b16 a[82:83], v140 offset:2048\n"
    "v_mfma_f32_16x16x32_bf16 v[124:127], a[124:127], a[140:143], v[124:127]\n"
    "ds_read_b64_tr_b16 a[84:85], v141 offset:0\n"
    "v_mfma_f32_16x16x32_bf16 v[120:123], a[124:127], a[136:139], v[120:123]\n"
    "ds_read_b64_tr_b16 a[86:87], v141 offset:2048\n"
    "s_waitcnt lgkmcnt(8)\n"
    "v_mfma_f32_16x16x32_bf16 v[0:3], a[64:67], a[128:131], v[0:3]\n"
    "ds_read_b64_tr_b16 a[88:89], v142 offset:0\n"
    "buffer_load_dwordx4 v133, s[84:87], s78 offen offset:1024 lds\n"
    "v_mfma_f32_16x16x32_bf16 v[4:7], a[64:67], a[132:135], v[4:7]\n"
    "ds_read_b64_tr_b16 a[90:91], v142 offset:2048\n"
    "v_add_u32 v145, 0x10000, v145\n"
    "v_mfma_f32_16x16x32_bf16 v[12:15], a[68:71], a[132:135], v[12:15]\n"
    "ds_read_b64_tr_b16 a[92:93], v143 offset:0\n"
    "v_mfma_f32_16x16x32_bf16 v[8:11], a[68:71], a[128:131], v[8:11]\n"
    "ds_read_b64_tr_b16 a[94:95], v143 offset:2048\n"
    "s_waitcnt lgkmcnt(8)\n"
    "v_mfma_f32_16x16x32_bf16 v[16:19], a[72:75], a[128:131], v[16:19]\n"
    "ds_read_b64_tr_b16 a[96:97], v136 offset:256\n"
    "buffer_load_dwordx4 v134, s[84:87], s78 offen offset:2048 lds\n"
    "v_mfma_f32_16x16x32_bf16 v[20:23], a[72:75], a[132:135], v[20:23]\n"
    "ds_read_b64_tr_b16 a[98:99], v136 offset:2304\n"
    "v_mfma_f32_16x16x32_bf16 v[28:31], a[76:79], a[132:135], v[28:31]\n"
    "ds_read_b64_tr_b16 a[100:101], v137 offset:256\n"
    "v_add_u32 v136, 0xfffe0000, v136\n"
    "v_mfma_f32_16x16x32_bf16 v[24:27], a[76:79], a[128:131], v[24:27]\n"
    "ds_read_b64_tr_b16 a[102:103], v137 offset:2304\n"
    "s_waitcnt lgkmcnt(8)\n"
    "v_mfma_f32_16x16x32_bf16 v[32:35], a[80:83], a[128:131], v[32:35]\n"
    "ds_read_b64_tr_b16 a[104:105], v138 offset:256\n"
    "v_add_u32 v137, 0xfffe0000, v137\n"
    "v_mfma_f32_16x16x32_bf16 v[36:39], a[80:83], a[132:135], v[36:39]\n"
    "ds_read_b64_tr_b16 a[106:107], v138 offset:2304\n"
    "buffer_load_dwordx4 v135, s[84:87], s78 offen offset:3072 lds\n"
    "v_mfma_f32_16x16x32_bf16 v[44:47], a[84:87], a[132:135], v[44:47]\n"
    "ds_read_b64_tr_b16 a[108:109], v139 offset:256\n"
    "v_add_u32 v138, 0xfffe0000, v138\n"
    "v_mfma_f32_16x16x32_bf16 v[40:43], a[84:87], a[128:131], v[40:43]\n"
    "ds_read_b64_tr_b16 a[110:111], v139 offset:2304\n"
    "s_waitcnt lgkmcnt(8)\n"
    "v_mfma_f32_16x16x32_bf16 v[48:51], a[88:91], a[128:131], v[48:51]\n"
    "ds_read_b64_tr_b16 a[112:113], v140 offset:256\n"
    "v_add_u32 v139, 0xfffe0000, v139\n"
    "s_add_u32 s78, s78, 0x40000\n"
    "v_mfma_f32_16x16x32_bf16 v[52:55], a[88:91], a[132:135], v[52:55]\n"
    "ds_read_b64_tr_b16 a[114:115], v140 offset:2304\n"
    "v_mfma_f32_16x16x32_bf16 v[60:63], a[92:95], a[132:135], v[60:63]\n"
    "ds_read_b64_tr_b16 a[116:117], v141 offset:256\n"
    "v_add_u32 v140, 0xfffe0000, v140\n"
    "v_mfma_f32_16x16x32_bf16 v[56:59], a[92:95], a[128:131], v[56:59]\n"
    "ds_read_b64_tr_b16 a[118:119], v141 offset:2304\n"
    "s_waitcnt lgkmcnt(8)\n"
    "v_mfma_f32_16x16x32_bf16 v[64:67], a[96:99], a[128:131], v[64:67]\n"
    "ds_read_b64_tr_b16 a[120:121], v142 offset:256\n"
    "v_add_u32 v141, 0xfffe0000, v141\n"
    "v_mfma_f32_16x16x32_bf16 v[68:71], a[96:99], a[132:135], v[68:71]\n"
    "ds_read_b64_tr_b16 a[122:123], v142 offset:2304\n"
    "v_mfma_f32_16x16x32_bf16 v[76:79], a[100:103], a[132:135], v[76:79]\n"
    "ds_read_b64_tr_b16 a[124:125], v143 offset:256\n"
    "s_waitcnt vmcnt(12) lgkmcnt(7)\n"
    "v_mfma_f32_16x16x32_bf16 v[72:75], a[100:103], a[128:131], v[72:75]\n"
    "ds_read_b64_tr_b16 a[126:127], v143 offset:2304\n"
    "s_barrier\n"
    "v_mfma_f32_16x16x32_bf16 v[80:83], a[104:107], a[128:131], v[80:83]\n"
    "ds_read_b64_tr_b16 a[64:65], v136 offset:0\n"
    "ds_read_b128 a[136:139], v145 offset:0\n"
    "v_mfma_f32_16x16x32_bf16 v[84:87], a[104:107], a[132:135], v[84:87]\n"
    "ds_read_b64_tr_b16 a[66:67], v136 offset:2048\n"
    "ds_read_b128 a[140:143], v145 offset:2048\n"
    "v_mfma_f32_16x16x32_bf16 v[92:95], a[108:111], a[132:135], v[92:95]\n"
    "ds_read_b64_tr_b16 a[68:69], v137 offset:0\n"
    "v_add_u32 v142, 0xfffe0000, v142\n"
    "s_add_u32 m0, s79, 0x1c000\n"
    "v_mfma_f32_16x16x32_bf16 v[88:91], a[108:111], a[128:131], v[88:91]\n"
    "ds_read_b64_tr_b16 a[70:71], v137 offset:2048\n"
    "s_waitcnt lgkmcnt(10)\n"
    "buffer_load_dwordx4 v128, s[40:43], s63 offen offset:0 lds\n"
    "v_mfma_f32_16x16x32_bf16 v[96:99], a[112:115], a[128:131], v[96:99]\n"
    "ds_read_b64_tr_b16 a[72:73], v138 offset:0\n"
    "v_add_u32 v143, 0xfffe0000, v143\n"
    "v_mfma_f32_16x16x32_bf16 v[100:103], a[112:115], a[132:135], v[100:103]\n"
    "ds_read_b64_tr_b16 a[74:75], v138 offset:2048\n"
    "buffer_load_dwordx4 v129, s[40:43], s63 offen offset:1024 lds\n"
    "v_mfma_f32_16x16x32_bf16 v[108:111], a[116:119], a[132:135], v[108:111]\n"
    "ds_read_b64_tr_b16 a[76:77], v139 offset:0\n"
    "v_mfma_f32_16x16x32_bf16 v[104:107], a[116:119], a[128:131], v[104:107]\n"
    "ds_read_b64_tr_b16 a[78:79], v139 offset:2048\n"
    "s_waitcnt lgkmcnt(10)\n"
    "buffer_load_dwordx4 v130, s[40:43], s63 offen offset:2048 lds\n"
    "v_mfma_f32_16x16x32_bf16 v[112:115], a[120:123], a[128:131], v[112:115]\n"
    "ds_read_b64_tr_b16 a[80:81], v140 offset:0\n"
    "v_mfma_f32_16x16x32_bf16 v[116:119], a[120:123], a[132:135], v[116:119]\n"
    "ds_read_b64_tr_b16 a[82:83], v140 offset:2048\n"
    "v_mfma_f32_16x16x32_bf16 v[124:127], a[124:127], a[132:135], v[124:127]\n"
    "ds_read_b64_tr_b16 a[84:85], v141 offset:0\n"
    "v_mfma_f32_16x16x32_bf16 v[120:123], a[124:127], a[128:131], v[120:123]\n"
    "ds_read_b64_tr_b16 a[86:87], v141 offset:2048\n"
    "s_waitcnt lgkmcnt(8)\n"
    "v_mfma_f32_16x16x32_bf16 v[0:3], a[64:67], a[136:139], v[0:3]\n"
    "ds_read_b64_tr_b16 a[88:89], v142 offset:0\n"
    "buffer_load_dwordx4 v131, s[40:43], s63 offen offset:3072 lds\n"
    "v_mfma_f32_16x16x32_bf16 v[4:7], a[64:67], a[140:143], v[4:7]\n"
    "ds_read_b64_tr_b16 a[90:91], v142 offset:2048\n"
    "v_add_u32 v144, 0xfffe8000, v144\n"
    "v_mfma_f32_16x16x32_bf16 v[12:15], a[68:71], a[140:143], v[12:15]\n"
    "ds_read_b64_tr_b16 a[92:93], v143 offset:0\n"
    "s_add_u32 s63, s63, 128\n"
    "v_mfma_f32_16x16x32_bf16 v[8:11], a[68:71], a[136:139], v[8:11]\n"
    "ds_read_b64_tr_b16 a[94:95], v143 offset:2048\n"
    "s_waitcnt lgkmcnt(8)\n"
    "v_mfma_f32_16x16x32_bf16 v[16:19], a[72:75], a[136:139], v[16:19]\n"
    "ds_read_b64_tr_b16 a[96:97], v136 offset:256\n"
    "s_add_u32 m0, s79, 0x18000\n"
    "v_mfma_f32_16x16x32_bf16 v[20:23], a[72:75], a[140:143], v[20:23]\n"
    "ds_read_b64_tr_b16 a[98:99], v136 offset:2304\n"
    "v_mfma_f32_16x16x32_bf16 v[28:31], a[76:79], a[140:143], v[28:31]\n"
    "ds_read_b64_tr_b16 a[100:101], v137 offset:256\n"
    "buffer_load_dwordx4 v132, s[84:87], s78 offen offset:0 lds\n"
    "v_mfma_f32_16x16x32_bf16 v[24:27], a[76:79], a[136:139], v[24:27]\n"
    "ds_read_b64_tr_b16 a[102:103], v137 offset:2304\n"
    "s_waitcnt lgkmcnt(8)\n"
    "v_mfma_f32_16x16x32_bf16 v[32:35], a[80:83], a[136:139], v[32:35]\n"
    "ds_read_b64_tr_b16 a[104:105], v138 offset:256\n"
    "buffer_load_dwordx4 v133, s[84:87], s78 offen offset:1024 lds\n"
    "v_mfma_f32_16x16x32_bf16 v[36:39], a[80:83], a[140:143], v[36:39]\n"
    "ds_read_b64_tr_b16 a[106:107], v138 offset:2304\n"
    "v_mfma_f32_16x16x32_bf16 v[44:47], a[84:87], a[140:143], v[44:47]\n"
    "ds_read_b64_tr_b16 a[108:109], v139 offset:256\n"
    "buffer_load_dwordx4 v134, s[84:87], s78 offen offset:2048 lds\n"
    "v_mfma_f32_16x16x32_bf16 v[40:43], a[84:87], a[136:139], v[40:43]\n"
    "ds_read_b64_tr_b16 a[110:111], v139 offset:2304\n"
    "s_waitcnt lgkmcnt(8)\n"
    "v_mfma_f32_16x16x32_bf16 v[48:51], a[88:91], a[136:139], v[48:51]\n"
    "ds_read_b64_tr_b16 a[112:113], v140 offset:256\n"
    "buffer_load_dwordx4 v135, s[84:87], s78 offen offset:3072 lds\n"
    "v_mfma_f32_16x16x32_bf16 v[52:55], a[88:91], a[140:143], v[52:55]\n"
    "ds_read_b64_tr_b16 a[114:115], v140 offset:2304\n"
    "v_mfma_f32_16x16x32_bf16 v[60:63], a[92:95], a[140:143], v[60:63]\n"
    "ds_read_b64_tr_b16 a[116:117], v141 offset:256\n"
    "s_add_u32 s78, s78, 0x40000\n"
    "v_mfma_f32_16x16x32_bf16 v[56:59], a[92:95], a[136:139], v[56:59]\n"
    "ds_read_b64_tr_b16 a[118:119], v141 offset:2304\n"
    "s_waitcnt lgkmcnt(8)\n"
    "v_mfma_f32_16x16x32_bf16 v[64:67], a[96:99], a[136:139], v[64:67]\n"
    "ds_read_b64_tr_b16 a[120:121], v142 offset:256\n"
    "v_mfma_f32_16x16x32_bf16 v[68:71], a[96:99], a[140:143], v[68:71]\n"
    "ds_read_b64_tr_b16 a[122:123], v142 offset:2304\n"
    "v_mfma_f32_16x16x32_bf16 v[76:79], a[100:103], a[140:143], v[76:79]\n"
    "ds_read_b64_tr_b16 a[124:125], v143 offset:256\n"
    "s_waitcnt vmcnt(12) lgkmcnt(7)\n"
    "v_mfma_f32_16x16x32_bf16 v[72:75], a[100:103], a[136:139], v[72:75]\n"
    "ds_read_b64_tr_b16 a[126:127], v143 offset:2304\n"
    "s_barrier\n"
    "v_mfma_f32_16x16x32_bf16 v[80:83], a[104:107], a[136:139], v[80:83]\n"
    "ds_read_b64_tr_b16 a[64:65], v136 offset:32768\n"
    "ds_read_b128 a[128:131], v144 offset:0\n"
    "v_mfma_f32_16x16x32_bf16 v[84:87], a[104:107], a[140:143], v[84:87]\n"
    "ds_read_b64_tr_b16 a[66:67], v136 offset:34816\n"
    "ds_read_b128 a[132:135], v144 offset:2048\n"
    "v_mfma_f32_16x16x32_bf16 v[92:95], a[108:111], a[140:143], v[92:95]\n"
    "ds_read_b64_tr_b16 a[68:69], v137 offset:32768\n"
    "v_bfe_u32 v216, v210, 1, 7\n"
    "s_add_u32 m0, s79, 0x20000\n"
    "v_mfma_f32_16x16x32_bf16 v[88:91], a[108:111], a[136:139], v[88:91]\n"
    "ds_read_b64_tr_b16 a[70:71], v137 offset:34816\n"
    "s_waitcnt lgkmcnt(10)\n"
    "v_mfma_f32_16x16x32_bf16 v[96:99], a[112:115], a[136:139], v[96:99]\n"
    "ds_read_b64_tr_b16 a[72:73], v138 offset:32768\n"
    "v_mfma_f32_16x16x32_bf16 v[100:103], a[112:115], a[140:143], v[100:103]\n"
    "ds_read_b64_tr_b16 a[74:75], v138 offset:34816\n"
    "buffer_load_dwordx2 v[238:239], v216, s[52:55], s80 offen sc1\n"
    "v_mfma_f32_16x16x32_bf16 v[108:111], a[116:119], a[140:143], v[108:111]\n"
    "ds_read_b64_tr_b16 a[76:77], v139 offset:32768\n"
    "buffer_load_dwordx4 v132, s[84:87], s78 offen offset:0 lds\n"
    "v_mfma_f32_16x16x32_bf16 v[104:107], a[116:119], a[136:139], v[104:107]\n"
    "ds_read_b64_tr_b16 a[78:79], v139 offset:34816\n"
    "s_waitcnt lgkmcnt(10)\n"
    "v_mfma_f32_16x16x32_bf16 v[112:115], a[120:123], a[136:139], v[112:115]\n"
    "ds_read_b64_tr_b16 a[80:81], v140 offset:32768\n"
    "v_mfma_f32_16x16x32_bf16 v[116:119], a[120:123], a[140:143], v[116:119]\n"
    "ds_read_b64_tr_b16 a[82:83], v140 offset:34816\n"
    "v_mfma_f32_16x16x32_bf16 v[124:127], a[124:127], a[140:143], v[124:127]\n"
    "ds_read_b64_tr_b16 a[84:85], v141 offset:32768\n"
    "v_mfma_f32_16x16x32_bf16 v[120:123], a[124:127], a[136:139], v[120:123]\n"
    "ds_read_b64_tr_b16 a[86:87], v141 offset:34816\n"
    "s_waitcnt lgkmcnt(8)\n"
    "v_mfma_f32_16x16x32_bf16 v[0:3], a[64:67], a[128:131], v[0:3]\n"
    "ds_read_b64_tr_b16 a[88:89], v142 offset:32768\n"
    "buffer_load_dwordx4 v133, s[84:87], s78 offen offset:1024 lds\n"
    "v_mfma_f32_16x16x32_bf16 v[4:7], a[64:67], a[132:135], v[4:7]\n"
    "ds_read_b64_tr_b16 a[90:91], v142 offset:34816\n"
    "v_add_u32 v145, 0xfffe8000, v145\n"
    "v_mfma_f32_16x16x32_bf16 v[12:15], a[68:71], a[132:135], v[12:15]\n"
    "ds_read_b64_tr_b16 a[92:93], v143 offset:32768\n"
    "v_mfma_f32_16x16x32_bf16 v[8:11], a[68:71], a[128:131], v[8:11]\n"
    "ds_read_b64_tr_b16 a[94:95], v143 offset:34816\n"
    "s_waitcnt lgkmcnt(8)\n"
    "v_mfma_f32_16x16x32_bf16 v[16:19], a[72:75], a[128:131], v[16:19]\n"
    "ds_read_b64_tr_b16 a[96:97], v136 offset:33024\n"
    "buffer_load_dwordx4 v134, s[84:87], s78 offen offset:2048 lds\n"
    "v_mfma_f32_16x16x32_bf16 v[20:23], a[72:75], a[132:135], v[20:23]\n"
    "ds_read_b64_tr_b16 a[98:99], v136 offset:35072\n"
    "v_mfma_f32_16x16x32_bf16 v[28:31], a[76:79], a[132:135], v[28:31]\n"
    "ds_read_b64_tr_b16 a[100:101], v137 offset:33024\n"
    "v_add_u32 v136, 0x10000, v136\n"
    "v_mfma_f32_16x16x32_bf16 v[24:27], a[76:79], a[128:131], v[24:27]\n"
    "ds_read_b64_tr_b16 a[102:103], v137 offset:35072\n"
    "s_waitcnt lgkmcnt(8)\n"
    "v_mfma_f32_16x16x32_bf16 v[32:35], a[80:83], a[128:131], v[32:35]\n"
    "ds_read_b64_tr_b16 a[104:105], v138 offset:33024\n"
    "v_add_u32 v137, 0x10000, v137\n"
    "v_mfma_f32_16x16x32_bf16 v[36:39], a[80:83], a[132:135], v[36:39]\n"
    "ds_read_b64_tr_b16 a[106:107], v138 offset:35072\n"
    "buffer_load_dwordx4 v135, s[84:87], s78 offen offset:3072 lds\n"
    "v_mfma_f32_16x16x32_bf16 v[44:47], a[84:87], a[132:135], v[44:47]\n"
    "ds_read_b64_tr_b16 a[108:109], v139 offset:33024\n"
    "v_add_u32 v138, 0x10000, v138\n"
    "v_mfma_f32_16x16x32_bf16 v[40:43], a[84:87], a[128:131], v[40:43]\n"
    "ds_read_b64_tr_b16 a[110:111], v139 offset:35072\n"
    "s_waitcnt lgkmcnt(8)\n"
    "v_mfma_f32_16x16x32_bf16 v[48:51], a[88:91], a[128:131], v[48:51]\n"
    "ds_read_b64_tr_b16 a[112:113], v140 offset:33024\n"
    "v_add_u32 v139, 0x10000, v139\n"
    "s_add_u32 s78, s78, 0x40000\n"
    "v_mfma_f32_16x16x32_bf16 v[52:55], a[88:91], a[132:135], v[52:55]\n"
    "ds_read_b64_tr_b16 a[114:115], v140 offset:35072\n"
    "v_mfma_f32_16x16x32_bf16 v[60:63], a[92:95], a[132:135], v[60:63]\n"
    "ds_read_b64_tr_b16 a[116:117], v141 offset:33024\n"
    "v_add_u32 v140, 0x10000, v140\n"
    "v_mfma_f32_16x16x32_bf16 v[56:59], a[92:95], a[128:131], v[56:59]\n"
    "ds_read_b64_tr_b16 a[118:119], v141 offset:35072\n"
    "s_waitcnt lgkmcnt(8)\n"
    "v_mfma_f32_16x16x32_bf16 v[64:67], a[96:99], a[128:131], v[64:67]\n"
    "ds_read_b64_tr_b16 a[120:121], v142 offset:33024\n"
    "v_add_u32 v141, 0x10000, v141\n"
    "v_mfma_f32_16x16x32_bf16 v[68:71], a[96:99], a[132:135], v[68:71]\n"
    "ds_read_b64_tr_b16 a[122:123], v142 offset:35072\n"
    "v_mfma_f32_16x16x32_bf16 v[76:79], a[100:103], a[132:135], v[76:79]\n"
    "ds_read_b64_tr_b16 a[124:125], v143 offset:33024\n"
    "s_waitcnt vmcnt(13) lgkmcnt(7)\n"
    "v_mfma_f32_16x16x32_bf16 v[72:75], a[100:103], a[128:131], v[72:75]\n"
    "ds_read_b64_tr_b16 a[126:127], v143 offset:35072\n"
    "s_barrier\n"
    "v_mfma_f32_16x16x32_bf16 v[80:83], a[104:107], a[128:131], v[80:83]\n"
    "ds_read_b64_tr_b16 a[64:65], v136 offset:0\n"
    "ds_read_b128 a[136:139], v145 offset:0\n"
    "v_mfma_f32_16x16x32_bf16 v[84:87], a[104:107], a[132:135], v[84:87]\n"
    "ds_read_b64_tr_b16 a[66:67], v136 offset:2048\n"
    "ds_read_b128 a[140:143], v145 offset:2048\n"
    "v_mfma_f32_16x16x32_bf16 v[92:95], a[108:111], a[132:135], v[92:95]\n"
    "ds_read_b64_tr_b16 a[68:69], v137 offset:0\n"
    "v_add_u32 v142, 0x10000, v142\n"
    "v_mfma_f32_16x16x32_bf16 v[88:91], a[108:111], a[128:131], v[88:91]\n"
    "ds_read_b64_tr_b16 a[70:71], v137 offset:2048\n"
    "s_waitcnt lgkmcnt(10)\n"
    "v_mfma_f32_16x16x32_bf16 v[96:99], a[112:115], a[128:131], v[96:99]\n"
    "ds_read_b64_tr_b16 a[72:73], v138 offset:0\n"
    "v_add_u32 v143, 0x10000, v143\n"
    "v_mfma_f32_16x16x32_bf16 v[100:103], a[112:115], a[132:135], v[100:103]\n"
    "ds_read_b64_tr_b16 a[74:75], v138 offset:2048\n"
    "v_mfma_f32_16x16x32_bf16 v[108:111], a[116:119], a[132:135], v[108:111]\n"
    "ds_read_b64_tr_b16 a[76:77], v139 offset:0\n"
    "s_waitcnt vmcnt(4)\n"
    "v_cmp_eq_u64 vcc, s[34:35], v[238:239]\n"
    "s_nop 0\n"
    "s_cmp_eq_u64 vcc, exec\n"
    "s_cbranch_scc0 POLLKV_%=\n"
    "GOTKV_%=:\n"
    "v_mfma_f32_16x16x32_bf16 v[104:107], a[116:119], a[128:131], v[104:107]\n"
    "ds_read_b64_tr_b16 a[78:79], v139 offset:2048\n"
    "s_waitcnt lgkmcnt(10)\n"
    "v_mfma_f32_16x16x32_bf16 v[112:115], a[120:123], a[128:131], v[112:115]\n"
    "ds_read_b64_tr_b16 a[80:81], v140 offset:0\n"
    "v_mfma_f32_16x16x32_bf16 v[116:119], a[120:123], a[132:135], v[116:119]\n"
    "ds_read_b64_tr_b16 a[82:83], v140 offset:2048\n"
    "v_mfma_f32_16x16x32_bf16 v[124:127], a[124:127], a[132:135], v[124:127]\n"
    "ds_read_b64_tr_b16 a[84:85], v141 offset:0\n"
    "v_mfma_f32_16x16x32_bf16 v[120:123], a[124:127], a[128:131], v[120:123]\n"
    "ds_read_b64_tr_b16 a[86:87], v141 offset:2048\n"
    "s_waitcnt lgkmcnt(8)\n"
    "v_mfma_f32_16x16x32_bf16 v[0:3], a[64:67], a[136:139], v[0:3]\n"
    "ds_read_b64_tr_b16 a[88:89], v142 offset:0\n"
    "v_mfma_f32_16x16x32_bf16 v[4:7], a[64:67], a[140:143], v[4:7]\n"
    "ds_read_b64_tr_b16 a[90:91], v142 offset:2048\n"
    "v_add_u32 v144, 0x10000, v144\n"
    "v_mov_b64 v[160:161], 0\n"
    "v_mfma_f32_16x16x32_bf16 v[12:15], a[68:71], a[140:143], v[12:15]\n"
    "ds_read_b64_tr_b16 a[92:93], v143 offset:0\n"
    "v_mfma_f32_16x16x32_bf16 v[8:11], a[68:71], a[136:139], v[8:11]\n"
    "ds_read_b64_tr_b16 a[94:95], v143 offset:2048\n"
    "s_waitcnt lgkmcnt(8)\n"
    "v_mfma_f32_16x16x32_bf16 v[16:19], a[72:75], a[136:139], v[16:19]\n"
    "ds_read_b64_tr_b16 a[96:97], v136 offset:256\n"
    "v_mov_b64 v[162:163], 0\n"
    "v_mfma_f32_16x16x32_bf16 v[20:23], a[72:75], a[140:143], v[20:23]\n"
    "ds_read_b64_tr_b16 a[98:99], v136 offset:2304\n"
    "v_mfma_f32_16x16x32_bf16 v[28:31], a[76:79], a[140:143], v[28:31]\n"
    "ds_read_b64_tr_b16 a[100:101], v137 offset:256\n"
    "v_mfma_f32_16x16x32_bf16 v[24:27], a[76:79], a[136:139], v[24:27]\n"
    "ds_read_b64_tr_b16 a[102:103], v137 offset:2304\n"
    "s_waitcnt lgkmcnt(8)\n"
    "v_mfma_f32_16x16x32_bf16 v[32:35], a[80:83], a[136:139], v[32:35]\n"
    "ds_read_b64_tr_b16 a[104:105], v138 offset:256\n"
    "v_mov_b64 v[164:165], 0\n"
    "v_mfma_f32_16x16x32_bf16 v[36:39], a[80:83], a[140:143], v[36:39]\n"
    "ds_read_b64_tr_b16 a[106:107], v138 offset:2304\n"
    "v_mfma_f32_16x16x32_bf16 v[44:47], a[84:87], a[140:143], v[44:47]\n"
    "ds_read_b64_tr_b16 a[108:109], v139 offset:256\n"
    "v_mfma_f32_16x16x32_bf16 v[40:43], a[84:87], a[136:139], v[40:43]\n"
    "ds_read_b64_tr_b16 a[110:111], v139 offset:2304\n"
    "s_waitcnt lgkmcnt(8)\n"
    "v_mov_b64 v[166:167], 0\n"
    "v_mfma_f32_16x16x32_bf16 v[48:51], a[88:91], a[136:139], v[48:51]\n"
    "ds_read_b64_tr_b16 a[112:113], v140 offset:256\n"
    "v_mfma_f32_16x16x32_bf16 v[52:55], a[88:91], a[140:143], v[52:55]\n"
    "ds_read_b64_tr_b16 a[114:115], v140 offset:2304\n"
    "v_mfma_f32_16x16x32_bf16 v[60:63], a[92:95], a[140:143], v[60:63]\n"
    "ds_read_b64_tr_b16 a[116:117], v141 offset:256\n"
    "v_mfma_f32_16x16x32_bf16 v[56:59], a[92:95], a[136:139], v[56:59]\n"
    "ds_read_b64_tr_b16 a[118:119], v141 offset:2304\n"
    "s_waitcnt lgkmcnt(8)\n"
    "v_mov_b64 v[168:169], 0\n"
    "v_mfma_f32_16x16x32_bf16 v[64:67], a[96:99], a[136:139], v[64:67]\n"
    "ds_read_b64_tr_b16 a[120:121], v142 offset:256\n"
    "v_mfma_f32_16x16x32_bf16 v[68:71], a[96:99], a[140:143], v[68:71]\n"
    "ds_read_b64_tr_b16 a[122:123], v142 offset:2304\n"
    "v_mfma_f32_16x16x32_bf16 v[76:79], a[100:103], a[140:143], v[76:79]\n"
    "ds_read_b64_tr_b16 a[124:125], v143 offset:256\n"
    "s_waitcnt vmcnt(5) lgkmcnt(7)\n"
    "v_mov_b64 v[170:171], 0\n"
    "v_mfma_f32_16x16x32_bf16 v[72:75], a[100:103], a[136:139], v[72:75]\n"
    "ds_read_b64_tr_b16 a[126:127], v143 offset:2304\n"
    "s_barrier\n"
    "v_mfma_f32_16x16x32_bf16 v[80:83], a[104:107], a[136:139], v[80:83]\n"
    "ds_read_b64_tr_b16 a[64:65], v136 offset:32768\n"
    "ds_read_b128 a[128:131], v144 offset:0\n"
    "v_mfma_f32_16x16x32_bf16 v[84:87], a[104:107], a[140:143], v[84:87]\n"
    "ds_read_b64_tr_b16 a[66:67], v136 offset:34816\n"
    "ds_read_b128 a[132:135], v144 offset:2048\n"
    "v_mfma_f32_16x16x32_bf16 v[92:95], a[108:111], a[140:143], v[92:95]\n"
    "ds_read_b64_tr_b16 a[68:69], v137 offset:32768\n"
    "s_sub_u32 s92, 68, s74\n"
    "s_cmp_eq_u32 s75, 3\n"
    "s_cselect_b32 s92, s92, s74\n"
    "v_mfma_f32_16x16x32_bf16 v[88:91], a[108:111], a[136:139], v[88:91]\n"
    "ds_read_b64_tr_b16 a[70:71], v137 offset:34816\n"
    "s_waitcnt lgkmcnt(10)\n"
    "s_lshl_b32 s38, s92, 15\n"
    "v_mfma_f32_16x16x32_bf16 v[96:99], a[112:115], a[136:139], v[96:99]\n"
    "ds_read_b64_tr_b16 a[72:73], v138 offset:32768\n"
    "s_lshl_b32 s57, s73, 15\n"
    "s_add_u32 s58, s57, 0x4000\n"
    "v_mfma_f32_16x16x32_bf16 v[100:103], a[112:115], a[140:143], v[100:103]\n"
    "ds_read_b64_tr_b16 a[74:75], v138 offset:34816\n"
    "s_lshl_b32 s59, s56, 12\n"
    "v_mov_b64 v[172:173], 0\n"
    "v_mfma_f32_16x16x32_bf16 v[108:111], a[116:119], a[140:143], v[108:111]\n"
    "ds_read_b64_tr_b16 a[76:77], v139 offset:32768\n"
    "s_mov_b32 m0, s59\n"
    "v_mov_b64 v[174:175], 0\n"
    "v_mfma_f32_16x16x32_bf16 v[104:107], a[116:119], a[136:139], v[104:107]\n"
    "ds_read_b64_tr_b16 a[78:79], v139 offset:34816\n"
    "s_waitcnt lgkmcnt(10)\n"
    "buffer_load_dwordx4 v208, s[36:39], s57 offen offset:0 sc1 lds\n"
    "v_mfma_f32_16x16x32_bf16 v[112:115], a[120:123], a[136:139], v[112:115]\n"
    "ds_read_b64_tr_b16 a[80:81], v140 offset:32768\n"
    "buffer_load_dwordx4 v208, s[36:39], s57 offen offset:1024 sc1 lds\n"
    "v_mfma_f32_16x16x32_bf16 v[116:119], a[120:123], a[140:143], v[116:119]\n"
    "ds_read_b64_tr_b16 a[82:83], v140 offset:34816\n"
    "buffer_load_dwordx4 v208, s[36:39], s57 offen offset:2048 sc1 lds\n"
    "v_mfma_f32_16x16x32_bf16 v[124:127], a[124:127], a[140:143], v[124:127]\n"
    "ds_read_b64_tr_b16 a[84:85], v141 offset:32768\n"
    "buffer_load_dwordx4 v208, s[36:39], s57 offen offset:3072 sc1 lds\n"
    "v_mfma_f32_16x16x32_bf16 v[120:123], a[124:127], a[136:139], v[120:123]\n"
    "ds_read_b64_tr_b16 a[86:87], v141 offset:34816\n"
    "s_waitcnt lgkmcnt(8)\n"
    "v_mfma_f32_16x16x32_bf16 v[0:3], a[64:67], a[128:131], v[0:3]\n"
    "ds_read_b64_tr_b16 a[88:89], v142 offset:32768\n"
    "s_xor_b32 m0, s59, 0x4000\n"
    "v_mfma_f32_16x16x32_bf16 v[4:7], a[64:67], a[132:135], v[4:7]\n"
    "ds_read_b64_tr_b16 a[90:91], v142 offset:34816\n"
    "v_add_u32 v145, 0x10000, v145\n"
    "v_mfma_f32_16x16x32_bf16 v[12:15], a[68:71], a[132:135], v[12:15]\n"
    "ds_read_b64_tr_b16 a[92:93], v143 offset:32768\n"
    "v_mfma_f32_16x16x32_bf16 v[8:11], a[68:71], a[128:131], v[8:11]\n"
    "ds_read_b64_tr_b16 a[94:95], v143 offset:34816\n"
    "s_waitcnt lgkmcnt(8)\n"
    "buffer_load_dwordx4 v208, s[36:39], s58 offen offset:0 sc1 lds\n"
    "v_mfma_f32_16x16x32_bf16 v[16:19], a[72:75], a[128:131], v[16:19]\n"
    "ds_read_b64_tr_b16 a[96:97], v136 offset:33024\n"
    "v_mfma_f32_16x16x32_bf16 v[20:23], a[72:75], a[132:135], v[20:23]\n"
    "ds_read_b64_tr_b16 a[98:99], v136 offset:35072\n"
    "v_mfma_f32_16x16x32_bf16 v[28:31], a[76:79], a[132:135], v[28:31]\n"
    "ds_read_b64_tr_b16 a[100:101], v137 offset:33024\n"
    "v_add_u32 v136, 0x10000, v136\n"
    "v_mfma_f32_16x16x32_bf16 v[24:27], a[76:79], a[128:131], v[24:27]\n"
    "ds_read_b64_tr_b16 a[102:103], v137 offset:35072\n"
    "s_waitcnt lgkmcnt(8)\n"
    "buffer_load_dwordx4 v208, s[36:39], s58 offen offset:1024 sc1 lds\n"
    "v_mfma_f32_16x16x32_bf16 v[32:35], a[80:83], a[128:131], v[32:35]\n"
    "ds_read_b64_tr_b16 a[104:105], v138 offset:33024\n"
    "v_add_u32 v137, 0x10000, v137\n"
    "v_mfma_f32_16x16x32_bf16 v[36:39], a[80:83], a[132:135], v[36:39]\n"
    "ds_read_b64_tr_b16 a[106:107], v138 offset:35072\n"
    "v_mfma_f32_16x16x32_bf16 v[44:47], a[84:87], a[132:135], v[44:47]\n"
    "ds_read_b64_tr_b16 a[108:109], v139 offset:33024\n"
    "v_add_u32 v138, 0x10000, v138\n"
    "v_mfma_f32_16x16x32_bf16 v[40:43], a[84:87], a[128:131], v[40:43]\n"
    "ds_read_b64_tr_b16 a[110:111], v139 offset:35072\n"
    "s_waitcnt lgkmcnt(8)\n"
    "buffer_load_dwordx4 v208, s[36:39], s58 offen offset:2048 sc1 lds\n"
    "v_mfma_f32_16x16x32_bf16 v[48:51], a[88:91], a[128:131], v[48:51]\n"
    "ds_read_b64_tr_b16 a[112:113], v140 offset:33024\n"
    "v_add_u32 v139, 0x10000, v139\n"
    "v_mfma_f32_16x16x32_bf16 v[52:55], a[88:91], a[132:135], v[52:55]\n"
    "ds_read_b64_tr_b16 a[114:115], v140 offset:35072\n"
    "buffer_load_dwordx4 v208, s[36:39], s58 offen offset:3072 sc1 lds\n"
    "v_mfma_f32_16x16x32_bf16 v[60:63], a[92:95], a[132:135], v[60:63]\n"
    "ds_read_b64_tr_b16 a[116:117], v141 offset:33024\n"
    "v_add_u32 v140, 0x10000, v140\n"
    "v_mfma_f32_16x16x32_bf16 v[56:59], a[92:95], a[128:131], v[56:59]\n"
    "ds_read_b64_tr_b16 a[118:119], v141 offset:35072\n"
    "s_waitcnt lgkmcnt(8)\n"
    "v_mfma_f32_16x16x32_bf16 v[64:67], a[96:99], a[128:131], v[64:67]\n"
    "ds_read_b64_tr_b16 a[120:121], v142 offset:33024\n"
    "v_add_u32 v141, 0x10000, v141\n"
    "v_mfma_f32_16x16x32_bf16 v[68:71], a[96:99], a[132:135], v[68:71]\n"
    "ds_read_b64_tr_b16 a[122:123], v142 offset:35072\n"
    "v_mfma_f32_16x16x32_bf16 v[76:79], a[100:103], a[132:135], v[76:79]\n"
    "ds_read_b64_tr_b16 a[124:125], v143 offset:33024\n"
    "s_waitcnt vmcnt(8) lgkmcnt(7)\n"
    "v_mfma_f32_16x16x32_bf16 v[72:75], a[100:103], a[128:131], v[72:75]\n"
    "ds_read_b64_tr_b16 a[126:127], v143 offset:35072\n"
    "s_barrier\n"
    "v_mfma_f32_16x16x32_bf16 v[80:83], a[104:107], a[128:131], v[80:83]\n"
    "ds_read_b64_tr_b16 a[64:65], v136 offset:0\n"
    "ds_read_b128 a[136:139], v145 offset:0\n"
    "v_mfma_f32_16x16x32_bf16 v[84:87], a[104:107], a[132:135], v[84:87]\n"
    "ds_read_b64_tr_b16 a[66:67], v136 offset:2048\n"
    "ds_read_b128 a[140:143], v145 offset:2048\n"
    "v_mfma_f32_16x16x32_bf16 v[92:95], a[108:111], a[132:135], v[92:95]\n"
    "ds_read_b64_tr_b16 a[68:69], v137 offset:0\n"
    "v_add_u32 v142, 0x10000, v142\n"
    "s_xor_b32 m0, s59, 0x8000\n"
    "v_mfma_f32_16x16x32_bf16 v[88:91], a[108:111], a[128:131], v[88:91]\n"
    "ds_read_b64_tr_b16 a[70:71], v137 offset:2048\n"
    "s_waitcnt lgkmcnt(10)\n"
    "buffer_load_dwordx4 v209, s[36:39], s57 offen offset:0 sc1 lds\n"
    "v_mfma_f32_16x16x32_bf16 v[96:99], a[112:115], a[128:131], v[96:99]\n"
    "ds_read_b64_tr_b16 a[72:73], v138 offset:0\n"
    "v_add_u32 v143, 0x10000, v143\n"
    "v_mfma_f32_16x16x32_bf16 v[100:103], a[112:115], a[132:135], v[100:103]\n"
    "ds_read_b64_tr_b16 a[74:75], v138 offset:2048\n"
    "buffer_load_dwordx4 v209, s[36:39], s57 offen offset:1024 sc1 lds\n"
    "v_mfma_f32_16x16x32_bf16 v[108:111], a[116:119], a[132:135], v[108:111]\n"
    "ds_read_b64_tr_b16 a[76:77], v139 offset:0\n"
    "v_mfma_f32_16x16x32_bf16 v[104:107], a[116:119], a[128:131], v[104:107]\n"
    "ds_read_b64_tr_b16 a[78:79], v139 offset:2048\n"
    "s_waitcnt lgkmcnt(10)\n"
    "buffer_load_dwordx4 v209, s[36:39], s57 offen offset:2048 sc1 lds\n"
    "v_mfma_f32_16x16x32_bf16 v[112:115], a[120:123], a[128:131], v[112:115]\n"
    "ds_read_b64_tr_b16 a[80:81], v140 offset:0\n"
    "v_mfma_f32_16x16x32_bf16 v[116:119], a[120:123], a[132:135], v[116:119]\n"
    "ds_read_b64_tr_b16 a[82:83], v140 offset:2048\n"
    "v_mfma_f32_16x16x32_bf16 v[124:127], a[124:127], a[132:135], v[124:127]\n"
    "ds_read_b64_tr_b16 a[84:85], v141 offset:0\n"
    "v_mfma_f32_16x16x32_bf16 v[120:123], a[124:127], a[128:131], v[120:123]\n"
    "ds_read_b64_tr_b16 a[86:87], v141 offset:2048\n"
    "s_waitcnt lgkmcnt(8)\n"
    "v_mfma_f32_16x16x32_bf16 v[0:3], a[64:67], a[136:139], v[0:3]\n"
    "ds_read_b64_tr_b16 a[88:89], v142 offset:0\n"
    "buffer_load_dwordx4 v209, s[36:39], s57 offen offset:3072 sc1 lds\n"
    "v_mfma_f32_16x16x32_bf16 v[4:7], a[64:67], a[140:143], v[4:7]\n"
    "ds_read_b64_tr_b16 a[90:91], v142 offset:2048\n"
    "v_add_u32 v206, 0x10000, v210\n"
    "v_mov_b32 v207, v210\n"
    "v_mfma_f32_16x16x32_bf16 v[12:15], a[68:71], a[140:143], v[12:15]\n"
    "ds_read_b64_tr_b16 a[92:93], v143 offset:0\n"
    "s_xor_b32 m0, s59, 0xc000\n"
    "s_lshr_b32 s60, s74, 1\n"
    "v_mfma_f32_16x16x32_bf16 v[8:11], a[68:71], a[136:139], v[8:11]\n"
    "ds_read_b64_tr_b16 a[94:95], v143 offset:2048\n"
    "s_waitcnt lgkmcnt(8)\n"
    "s_sub_u32 s60, s60, 1\n"
    "v_mfma_f32_16x16x32_bf16 v[16:19], a[72:75], a[136:139], v[16:19]\n"
    "ds_read_b64_tr_b16 a[96:97], v136 offset:256\n"
    "s_lshl_b32 s92, s72, 2\n"
    "s_add_u32 s92, s92, s56\n"
    "v_mfma_f32_16x16x32_bf16 v[20:23], a[72:75], a[140:143], v[20:23]\n"
    "ds_read_b64_tr_b16 a[98:99], v136 offset:2304\n"
    "buffer_load_dwordx4 v209, s[36:39], s58 offen offset:0 sc1 lds\n"
    "v_mfma_f32_16x16x32_bf16 v[28:31], a[76:79], a[140:143], v[28:31]\n"
    "ds_read_b64_tr_b16 a[100:101], v137 offset:256\n"
    "s_sub_i32 s92, s92, s73\n"
    "s_mov_b32 s62, 0\n"
    "v_mfma_f32_16x16x32_bf16 v[24:27], a[76:79], a[136:139], v[24:27]\n"
    "ds_read_b64_tr_b16 a[102:103], v137 offset:2304\n"
    "s_waitcnt lgkmcnt(8)\n"
    "s_sub_u32 s61, s92, 1\n"
    "v_mfma_f32_16x16x32_bf16 v[32:35], a[80:83], a[136:139], v[32:35]\n"
    "ds_read_b64_tr_b16 a[104:105], v138 offset:256\n"
    "buffer_load_dwordx4 v209, s[36:39], s58 offen offset:1024 sc1 lds\n"
    "v_mfma_f32_16x16x32_bf16 v[36:39], a[80:83], a[140:143], v[36:39]\n"
    "ds_read_b64_tr_b16 a[106:107], v138 offset:2304\n"
    "s_cmp_eq_u32 s56, 3\n"
    "s_cselect_b32 s63, 0x7fffffff, 0\n"
    "v_mfma_f32_16x16x32_bf16 v[44:47], a[84:87], a[140:143], v[44:47]\n"
    "ds_read_b64_tr_b16 a[108:109], v139 offset:256\n"
    "s_mov_b32 s64, 0xfff0ffff\n"
    "v_mfma_f32_16x16x32_bf16 v[40:43], a[84:87], a[136:139], v[40:43]\n"
    "ds_read_b64_tr_b16 a[110:111], v139 offset:2304\n"
    "s_waitcnt lgkmcnt(8)\n"
    "buffer_load_dwordx4 v209, s[36:39], s58 offen offset:2048 sc1 lds\n"
    "v_mfma_f32_16x16x32_bf16 v[48:51], a[88:91], a[136:139], v[48:51]\n"
    "ds_read_b64_tr_b16 a[112:113], v140 offset:256\n"
    "s_mov_b32 s65, 0xf000ff00\n"
    "s_mov_b32 s66, 0xffe0fffe\n"
    "v_mfma_f32_16x16x32_bf16 v[52:55], a[88:91], a[140:143], v[52:55]\n"
    "ds_read_b64_tr_b16 a[114:115], v140 offset:2304\n"
    "s_mov_b32 s67, 0xe000fe00\n"
    "v_mfma_f32_16x16x32_bf16 v[60:63], a[92:95], a[140:143], v[60:63]\n"
    "ds_read_b64_tr_b16 a[116:117], v141 offset:256\n"
    "buffer_load_dwordx4 v209, s[36:39], s58 offen offset:3072 sc1 lds\n"
    "v_mfma_f32_16x16x32_bf16 v[56:59], a[92:95], a[136:139], v[56:59]\n"
    "ds_read_b64_tr_b16 a[118:119], v141 offset:2304\n"
    "s_waitcnt lgkmcnt(8)\n"
    "s_mov_b32 s68, 0xffc0fffc\n"
    "v_mfma_f32_16x16x32_bf16 v[64:67], a[96:99], a[136:139], v[64:67]\n"
    "ds_read_b64_tr_b16 a[120:121], v142 offset:256\n"
    "s_mov_b32 s69, 0xc000fc00\n"
    "s_mov_b32 s70, 0xff80fff8\n"
    "v_mfma_f32_16x16x32_bf16 v[68:71], a[96:99], a[140:143], v[68:71]\n"
    "ds_read_b64_tr_b16 a[122:123], v142 offset:2304\n"
    "s_mov_b32 s71, 0x8000f800\n"
    "s_mov_b64 s[84:85], -1\n"
    "v_mfma_f32_16x16x32_bf16 v[76:79], a[100:103], a[140:143], v[76:79]\n"
    "ds_read_b64_tr_b16 a[124:125], v143 offset:256\n"
    "s_waitcnt vmcnt(12) lgkmcnt(7)\n"
    "s_sub_u32 s73, 40, s74\n"
    "v_mfma_f32_16x16x32_bf16 v[72:75], a[100:103], a[136:139], v[72:75]\n"
    "ds_read_b64_tr_b16 a[126:127], v143 offset:2304\n"
    "s_barrier\n"
    "s_lshl_b32 s73, s73, 15\n"
    "v_mfma_f32_16x16x32_bf16 v[80:83], a[104:107], a[136:139], v[80:83]\n"
    "ds_read_b128 a[64:67], v210 offset:0\n"
    "s_cmp_eq_u32 s75, 3\n"
    "s_cselect_b32 s73, s73, 0x10000\n"
    "v_mfma_f32_16x16x32_bf16 v[84:87], a[104:107], a[140:143], v[84:87]\n"
    "ds_read_b128 a[68:71], v210 offset:2048\n"
    "s_add_u32 s57, s57, 0x10000\n"
    "v_mfma_f32_16x16x32_bf16 v[92:95], a[108:111], a[140:143], v[92:95]\n"
    "ds_read_b128 a[72:75], v210 offset:4096\n"
    "s_xor_b32 m0, s59, 0x10000\n"
    "s_cmp_eq_u32 s60, 1\n"
    "s_cselect_b32 s79, s73, 0x10000\n"
    "v_mfma_f32_16x16x32_bf16 v[88:91], a[108:111], a[136:139], v[88:91]\n"
    "ds_read_b128 a[76:79], v210 offset:6144\n"
    "s_waitcnt lgkmcnt(8)\n"
    "buffer_load_dwordx4 v208, s[36:39], s57 offen offset:0 sc1 lds\n"
    "v_mov_b64 v[204:205], 0\n"
    "v_mfma_f32_16x16x32_bf16 v[96:99], a[112:115], a[136:139], v[96:99]\n"
    "ds_read_b128 a[80:83], v210 offset:8192\n"
    "buffer_load_dwordx4 v208, s[36:39], s57 offen offset:1024 sc1 lds\n"
    "v_mfma_f32_16x16x32_bf16 v[100:103], a[112:115], a[140:143], v[100:103]\n"
    "ds_read_b128 a[84:87], v210 offset:10240\n"
    "s_mov_b32 s78, 0x10000\n"
    "s_cmp_eq_u32 s75, 2\n"
    "s_cselect_b32 s62, 2, 0\n"
    "s_cselect_b32 s97, 3, s61\n"
    "v_mfma_f32_16x16x32_bf16 v[108:111], a[116:119], a[140:143], v[108:111]\n"
    "ds_read_b128 a[88:91], v210 offset:12288\n"
    "buffer_load_dwordx4 v208, s[36:39], s57 offen offset:2048 sc1 lds\n"
    "v_mov_b64 v[202:203], 0\n"
    "v_mfma_f32_16x16x32_bf16 v[104:107], a[116:119], a[136:139], v[104:107]\n"
    "ds_read_b128 a[92:95], v210 offset:14336\n"
    "s_waitcnt lgkmcnt(8)\n"
    "buffer_load_dwordx4 v208, s[36:39], s57 offen offset:3072 sc1 lds\n"
    "v_mfma_f32_16x16x32_bf16 v[112:115], a[120:123], a[136:139], v[112:115]\n"
    "ds_read_b128 a[96:99], v210 offset:1024\n"
    "s_xor_b32 s59, s59, 0x10000\n"
    "s_sub_u32 s80, s61, 4\n"
    "v_mfma_f32_16x16x32_bf16 v[116:119], a[120:123], a[140:143], v[116:119]\n"
    "ds_read_b128 a[100:103], v210 offset:3072\n"
    "s_mov_b32 s61, s97\n"
    "v_mfma_f32_16x16x32_bf16 v[124:127], a[124:127], a[140:143], v[124:127]\n"
    "v_mfma_f32_16x16x32_bf16 v[120:123], a[124:127], a[136:139], v[120:123]\n"
    "s_nop 1\n"
    "v_cvt_pk_bf16_f32 v240, v0, v1\n"
    "v_cvt_pk_bf16_f32 v241, v2, v3\n"
    "v_cvt_pk_bf16_f32 v242, v8, v9\n"
    "v_cvt_pk_bf16_f32 v243, v10, v11\n"
    "v_accvgpr_write_b32 a0, v240\n"
    "v_accvgpr_write_b32 a1, v241\n"
    "v_accvgpr_write_b32 a2, v242\n"
    "v_accvgpr_write_b32 a3, v243\n"
    "v_cvt_pk_bf16_f32 v244, v4, v5\n"
    "v_cvt_pk_bf16_f32 v245, v6, v7\n"
    "v_cvt_pk_bf16_f32 v246, v12, v13\n"
    "v_cvt_pk_bf16_f32 v247, v14, v15\n"
    "v_accvgpr_write_b32 a4, v244\n"
    "v_accvgpr_write_b32 a5, v245\n"
    "v_accvgpr_write_b32 a6, v246\n"
    "v_accvgpr_write_b32 a7, v247\n"
    "v_cvt_pk_bf16_f32 v248, v16, v17\n"
    "v_cvt_pk_bf16_f32 v249, v18, v19\n"
    "v_cvt_pk_bf16_f32 v250, v24, v25\n"
    "v_cvt_pk_bf16_f32 v251, v26, v27\n"
    "v_accvgpr_write_b32 a8, v248\n"
    "v_accvgpr_write_b32 a9, v249\n"
    "v_accvgpr_write_b32 a10, v250\n"
    "v_accvgpr_write_b32 a11, v251\n"
    "v_cvt_pk_bf16_f32 v252, v20, v21\n"
    "v_cvt_pk_bf16_f32 v253, v22, v23\n"
    "v_cvt_pk_bf16_f32 v254, v28, v29\n"
    "v_cvt_pk_bf16_f32 v255, v30, v31\n"
    "v_accvgpr_write_b32 a12, v252\n"
    "v_accvgpr_write_b32 a13, v253\n"
    "v_accvgpr_write_b32 a14, v254\n"
    "v_accvgpr_write_b32 a15, v255\n"
    "v_cvt_pk_bf16_f32 v240, v32, v33\n"
    "v_cvt_pk_bf16_f32 v241, v34, v35\n"
    "v_cvt_pk_bf16_f32 v242, v40, v41\n"
    "v_cvt_pk_bf16_f32 v243, v42, v43\n"
    "v_accvgpr_write_b32 a16, v240\n"
    "v_accvgpr_write_b32 a17, v241\n"
    "v_accvgpr_write_b32 a18, v242\n"
    "v_accvgpr_write_b32 a19, v243\n"
    "v_cvt_pk_bf16_f32 v244, v36, v37\n"
    "v_cvt_pk_bf16_f32 v245, v38, v39\n"
    "v_cvt_pk_bf16_f32 v246, v44, v45\n"
    "v_cvt_pk_bf16_f32 v247, v46, v47\n"
    "v_accvgpr_write_b32 a20, v244\n"
    "v_accvgpr_write_b32 a21, v245\n"
    "v_accvgpr_write_b32 a22, v246\n"
    "v_accvgpr_write_b32 a23, v247\n"
    "v_cvt_pk_bf16_f32 v248, v48, v49\n"
    "v_cvt_pk_bf16_f32 v249, v50, v51\n"
    "v_cvt_pk_bf16_f32 v250, v56, v57\n"
    "v_cvt_pk_bf16_f32 v251, v58, v59\n"
    "v_accvgpr_write_b32 a24, v248\n"
    "v_accvgpr_write_b32 a25, v249\n"
    "v_accvgpr_write_b32 a26, v250\n"
    "v_accvgpr_write_b32 a27, v251\n"
    "v_cvt_pk_bf16_f32 v252, v52, v53\n"
    "v_cvt_pk_bf16_f32 v253, v54, v55\n"
    "v_cvt_pk_bf16_f32 v254, v60, v61\n"
    "v_cvt_pk_bf16_f32 v255, v62, v63\n"
    "v_accvgpr_write_b32 a28, v252\n"
    "v_accvgpr_write_b32 a29, v253\n"
    "v_accvgpr_write_b32 a30, v254\n"
    "v_accvgpr_write_b32 a31, v255\n"
    "v_cvt_pk_bf16_f32 v240, v64, v65\n"
    "v_cvt_pk_bf16_f32 v241, v66, v67\n"
    "v_cvt_pk_bf16_f32 v242, v72, v73\n"
    "v_cvt_pk_bf16_f32 v243, v74, v75\n"
    "v_accvgpr_write_b32 a32, v240\n"
    "v_accvgpr_write_b32 a33, v241\n"
    "v_accvgpr_write_b32 a34, v242\n"
    "v_accvgpr_write_b32 a35, v243\n"
    "v_cvt_pk_bf16_f32 v244, v68, v69\n"
    "v_cvt_pk_bf16_f32 v245, v70, v71\n"
    "v_cvt_pk_bf16_f32 v246, v76, v77\n"
    "v_cvt_pk_bf16_f32 v247, v78, v79\n"
    "v_accvgpr_write_b32 a36, v244\n"
    "v_accvgpr_write_b32 a37, v245\n"
    "v_accvgpr_write_b32 a38, v246\n"
    "v_accvgpr_write_b32 a39, v247\n"
    "v_cvt_pk_bf16_f32 v248, v80, v81\n"
    "v_cvt_pk_bf16_f32 v249, v82, v83\n"
    "v_cvt_pk_bf16_f32 v250, v88, v89\n"
    "v_cvt_pk_bf16_f32 v251, v90, v91\n"
    "v_accvgpr_write_b32 a40, v248\n"
    "v_accvgpr_write_b32 a41, v249\n"
    "v_accvgpr_write_b32 a42, v250\n"
    "v_accvgpr_write_b32 a43, v251\n"
    "v_cvt_pk_bf16_f32 v252, v84, v85\n"
    "v_cvt_pk_bf16_f32 v253, v86, v87\n"
    "v_cvt_pk_bf16_f32 v254, v92, v93\n"
    "v_cvt_pk_bf16_f32 v255, v94, v95\n"
    "v_accvgpr_write_b32 a44, v252\n"
    "v_accvgpr_write_b32 a45, v253\n"
    "v_accvgpr_write_b32 a46, v254\n"
    "v_accvgpr_write_b32 a47, v255\n"
    "v_cvt_pk_bf16_f32 v240, v96, v97\n"
    "v_cvt_pk_bf16_f32 v241, v98, v99\n"
    "v_cvt_pk_bf16_f32 v242, v104, v105\n"
    "v_cvt_pk_bf16_f32 v243, v106, v107\n"
    "v_accvgpr_write_b32 a48, v240\n"
    "v_accvgpr_write_b32 a49, v241\n"
    "v_accvgpr_write_b32 a50, v242\n"
    "v_accvgpr_write_b32 a51, v243\n"
    "v_cvt_pk_bf16_f32 v244, v100, v101\n"
    "v_cvt_pk_bf16_f32 v245, v102, v103\n"
    "v_cvt_pk_bf16_f32 v246, v108, v109\n"
    "v_cvt_pk_bf16_f32 v247, v110, v111\n"
    "v_accvgpr_write_b32 a52, v244\n"
    "v_accvgpr_write_b32 a53, v245\n"
    "v_accvgpr_write_b32 a54, v246\n"
    "v_accvgpr_write_b32 a55, v247\n"
    "v_cvt_pk_bf16_f32 v248, v112, v113\n"
    "v_cvt_pk_bf16_f32 v249, v114, v115\n"
    "v_cvt_pk_bf16_f32 v250, v120, v121\n"
    "v_cvt_pk_bf16_f32 v251, v122, v123\n"
    "v_accvgpr_write_b32 a56, v248\n"
    "v_accvgpr_write_b32 a57, v249\n"
    "v_accvgpr_write_b32 a58, v250\n"
    "v_accvgpr_write_b32 a59, v251\n"
    "v_cvt_pk_bf16_f32 v252, v116, v117\n"
    "v_cvt_pk_bf16_f32 v253, v118, v119\n"
    "v_cvt_pk_bf16_f32 v254, v124, v125\n"
    "v_cvt_pk_bf16_f32 v255, v126, v127\n"
    "v_accvgpr_write_b32 a60, v252\n"
    "v_accvgpr_write_b32 a61, v253\n"
    "v_accvgpr_write_b32 a62, v254\n"
    "v_accvgpr_write_b32 a63, v255\n"
    "s_waitcnt lgkmcnt(0)\n"
    "FIRST_%=:\n"
    "s_cmp_gt_i32 s92, 0\n"
    "s_cbranch_scc0 M0_%=\n"
    "M0DONE_%=:\n"
    "v_mfma_f32_16x16x32_bf16 v[128:131], a[64:67], a[0:3], v[160:163]\n"
    "ds_read_b128 a[104:107], v207 offset:5120\n"
    "s_xor_b32 m0, s59, 0x8000\n"
    "v_mfma_f32_16x16x32_bf16 v[128:131], a[68:71], a[8:11], v[128:131]\n"
    "s_add_u32 s97, s81, 0x800000\n"
    "v_mfma_f32_16x16x32_bf16 v[128:131], a[72:75], a[16:19], v[128:131]\n"
    "ds_read_b128 a[108:111], v207 offset:7168\n"
    "v_mfma_f32_16x16x32_bf16 v[128:131], a[76:79], a[24:27], v[128:131]\n"
    "s_waitcnt lgkmcnt(4)\n"
    "v_mfma_f32_16x16x32_bf16 v[128:131], a[80:83], a[32:35], v[128:131]\n"
    "ds_read_b128 a[112:115], v207 offset:9216\n"
    "v_mfma_f32_16x16x32_bf16 v[128:131], a[84:87], a[40:43], v[128:131]\n"
    "buffer_store_dwordx4 a[0:3], v210, s[88:91], s97 offen offset:0 sc1\n"
    "v_mfma_f32_16x16x32_bf16 v[128:131], a[88:91], a[48:51], v[128:131]\n"
    "ds_read_b128 a[116:119], v207 offset:11264\n"
    "v_mfma_f32_16x16x32_bf16 v[128:131], a[92:95], a[56:59], v[128:131]\n"
    "buffer_load_dwordx4 v209, s[36:39], s57 offen offset:0 sc1 lds\n"
    "v_mfma_f32_16x16x32_bf16 v[132:135], a[64:67], a[4:7], v[164:167]\n"
    "ds_read_b128 a[120:123], v207 offset:13312\n"
    "v_mfma_f32_16x16x32_bf16 v[132:135], a[68:71], a[12:15], v[132:135]\n"
    "v_mfma_f32_16x16x32_bf16 v[132:135], a[72:75], a[20:23], v[132:135]\n"
    "ds_read_b128 a[124:127], v207 offset:15360\n"
    "v_mfma_f32_16x16x32_bf16 v[132:135], a[76:79], a[28:31], v[132:135]\n"
    "v_mfma_f32_16x16x32_bf16 v[132:135], a[80:83], a[36:39], v[132:135]\n"
    "buffer_store_dwordx4 a[4:7], v210, s[88:91], s97 offen offset:1024 sc1\n"
    "v_mfma_f32_16x16x32_bf16 v[132:135], a[84:87], a[44:47], v[132:135]\n"
    "v_mfma_f32_16x16x32_bf16 v[132:135], a[88:91], a[52:55], v[132:135]\n"
    "v_mfma_f32_16x16x32_bf16 v[132:135], a[92:95], a[60:63], v[132:135]\n"
    "s_waitcnt lgkmcnt(4)\n"
    "buffer_load_dwordx4 v209, s[36:39], s57 offen offset:1024 sc1 lds\n"
    "v_mfma_f32_16x16x32_bf16 v[136:139], a[96:99], a[0:3], v[168:171]\n"
    "v_mfma_f32_16x16x32_bf16 v[136:139], a[100:103], a[8:11], v[136:139]\n"
    "v_mfma_f32_16x16x32_bf16 v[136:139], a[104:107], a[16:19], v[136:139]\n"
    "v_mfma_f32_16x16x32_bf16 v[136:139], a[108:111], a[24:27], v[136:139]\n"
    "s_waitcnt lgkmcnt(0)\n"
    "buffer_load_dwordx4 v209, s[36:39], s57 offen offset:2048 sc1 lds\n"
    "v_mfma_f32_16x16x32_bf16 v[136:139], a[112:115], a[32:35], v[136:139]\n"
    "v_mfma_f32_16x16x32_bf16 v[136:139], a[116:119], a[40:43], v[136:139]\n"
    "v_mfma_f32_16x16x32_bf16 v[136:139], a[120:123], a[48:51], v[136:139]\n"
    "buffer_store_dwordx4 a[8:11], v210, s[88:91], s97 offen offset:2048 sc1\n"
    "v_mfma_f32_16x16x32_bf16 v[136:139], a[124:127], a[56:59], v[136:139]\n"
    "v_mfma_f32_16x16x32_bf16 v[140:143], a[96:99], a[4:7], v[172:175]\n"
    "v_mfma_f32_16x16x32_bf16 v[140:143], a[100:103], a[12:15], v[140:143]\n"
    "v_mfma_f32_16x16x32_bf16 v[140:143], a[104:107], a[20:23], v[140:143]\n"
    "v_mfma_f32_16x16x32_bf16 v[140:143], a[108:111], a[28:31], v[140:143]\n"
    "buffer_load_dwordx4 v209, s[36:39], s57 offen offset:3072 sc1 lds\n"
    "v_mfma_f32_16x16x32_bf16 v[140:143], a[112:115], a[36:39], v[140:143]\n"
    "v_mfma_f32_16x16x32_bf16 v[140:143], a[116:119], a[44:47], v[140:143]\n"
    "v_mfma_f32_16x16x32_bf16 v[140:143], a[120:123], a[52:55], v[140:143]\n"
    "buffer_store_dwordx4 a[12:15], v210, s[88:91], s97 offen offset:3072 sc1\n"
    "v_mfma_f32_16x16x32_bf16 v[140:143], a[124:127], a[60:63], v[140:143]\n"
    "s_waitcnt vmcnt(16)\n"
    "s_barrier\n"
    "v_xor_b32 v206, 0x10000, v206\n"
    "v_xor_b32 v207, 0x10000, v207\n"
    "s_add_u32 s58, s58, 0x10000\n"
    "s_xor_b32 m0, s59, 0x4000\n"
    "s_xor_b32 s59, s59, 0x10000\n"
    "ds_read_b128 a[64:67], v206 offset:32768\n"
    "ds_read_b128 a[68:71], v206 offset:34816\n"
    "ds_read_b128 a[72:75], v206 offset:36864\n"
    "ds_read_b128 a[76:79], v206 offset:38912\n"
    "v_max3_f32 v218, v128, v129, v130\n"
    "v_max3_f32 v220, v132, v133, v134\n"
    "v_max3_f32 v218, v218, v131, v136\n"
    "v_max3_f32 v220, v220, v135, v140\n"
    "ds_read_b128 a[80:83], v206 offset:40960\n"
    "v_max3_f32 v218, v218, v137, v138\n"
    "v_max3_f32 v220, v220, v141, v142\n"
    "v_max_f32 v218, v218, v139\n"
    "v_max_f32 v220, v220, v143\n"
    "ds_read_b128 a[84:87], v206 offset:43008\n"
    "v_mov_b32 v219, v218\n"
    "v_mov_b32 v221, v220\n"
    "s_nop 0\n"
    "v_permlane16_swap_b32 v218, v219\n"
    "s_add_u32 s97, s81, 0x801000\n"
    "v_permlane16_swap_b32 v220, v221\n"
    "v_max_f32 v218, v218, v219\n"
    "v_max_f32 v220, v220, v221\n"
    "v_mov_b32 v219, v218\n"
    "buffer_store_dwordx4 a[16:19], v210, s[88:91], s97 offen offset:0 sc1\n"
    "v_mov_b32 v221, v220\n"
    "s_nop 0\n"
    "v_permlane32_swap_b32 v218, v219\n"
    "v_permlane32_swap_b32 v220, v221\n"
    "ds_read_b128 a[88:91], v206 offset:45056\n"
    "v_max_f32 v218, v218, v219\n"
    "v_max_f32 v220, v220, v221\n"
    "v_mov_b32 v219, v218\n"
    "v_mov_b32 v221, v220\n"
    "ds_read_b128 a[92:95], v206 offset:47104\n"
    "v_pk_add_f32 v[128:129], v[128:129], v[218:219] neg_lo:[0,1] neg_hi:[0,1]\n"
    "v_pk_add_f32 v[130:131], v[130:131], v[218:219] neg_lo:[0,1] neg_hi:[0,1]\n"
    "v_pk_add_f32 v[132:133], v[132:133], v[220:221] neg_lo:[0,1] neg_hi:[0,1]\n"
    "v_pk_add_f32 v[134:135], v[134:135], v[220:221] neg_lo:[0,1] neg_hi:[0,1]\n"
    "buffer_load_dwordx4 v208, s[36:39], s58 offen offset:0 sc1 lds\n"
    "v_pk_add_f32 v[136:137], v[136:137], v[218:219] neg_lo:[0,1] neg_hi:[0,1]\n"
    "v_pk_add_f32 v[138:139], v[138:139], v[218:219] neg_lo:[0,1] neg_hi:[0,1]\n"
    "v_pk_add_f32 v[140:141], v[140:141], v[220:221] neg_lo:[0,1] neg_hi:[0,1]\n"
    "ds_read_b128 a[96:99], v206 offset:33792\n"
    "v_pk_add_f32 v[142:143], v[142:143], v[220:221] neg_lo:[0,1] neg_hi:[0,1]\n"
    "v_pk_add_f32 v[160:161], v[160:161], v[218:219] neg_lo:[0,1] neg_hi:[0,1]\n"
    "v_pk_add_f32 v[162:163], v[162:163], v[218:219] neg_lo:[0,1] neg_hi:[0,1]\n"
    "v_pk_add_f32 v[164:165], v[164:165], v[220:221] neg_lo:[0,1] neg_hi:[0,1]\n"
    "ds_read_b128 a[100:103], v206 offset:35840\n"
    "v_pk_add_f32 v[166:167], v[166:167], v[220:221] neg_lo:[0,1] neg_hi:[0,1]\n"
    "v_pk_add_f32 v[168:169], v[168:169], v[218:219] neg_lo:[0,1] neg_hi:[0,1]\n"
    "v_pk_add_f32 v[170:171], v[170:171], v[218:219] neg_lo:[0,1] neg_hi:[0,1]\n"
    "v_pk_add_f32 v[172:173], v[172:173], v[220:221] neg_lo:[0,1] neg_hi:[0,1]\n"
    "buffer_store_dwordx4 a[20:23], v210, s[88:91], s97 offen offset:1024 sc1\n"
    "v_pk_add_f32 v[174:175], v[174:175], v[220:221] neg_lo:[0,1] neg_hi:[0,1]\n"
    "v_sub_f32 v204, v204, v218\n"
    "v_sub_f32 v205, v205, v220\n"
    "v_exp_f32 v184, v128\n"
    "buffer_load_dwordx4 v208, s[36:39], s58 offen offset:1024 sc1 lds\n"
    "v_exp_f32 v185, v129\n"
    "v_exp_f32 v186, v130\n"
    "v_exp_f32 v187, v131\n"
    "v_exp_f32 v188, v132\n"
    "buffer_store_dwordx4 a[24:27], v210, s[88:91], s97 offen offset:2048 sc1\n"
    "v_exp_f32 v189, v133\n"
    "v_exp_f32 v190, v134\n"
    "v_exp_f32 v191, v135\n"
    "v_add_f32 v200, v202, v184\n"
    "buffer_load_dwordx4 v208, s[36:39], s58 offen offset:2048 sc1 lds\n"
    "v_add_f32 v201, v203, v188\n"
    "v_add_f32 v200, v200, v185\n"
    "v_add_f32 v201, v201, v189\n"
    "v_add_f32 v200, v200, v186\n"
    "buffer_load_dwordx4 v208, s[36:39], s58 offen offset:3072 sc1 lds\n"
    "v_add_f32 v201, v201, v190\n"
    "v_add_f32 v200, v200, v187\n"
    "v_add_f32 v201, v201, v191\n"
    "v_cvt_pk_bf16_f32 v176, v184, v185\n"
    "buffer_store_dwordx4 a[28:31], v210, s[88:91], s97 offen offset:3072 sc1\n"
    "v_cvt_pk_bf16_f32 v177, v186, v187\n"
    "v_cvt_pk_bf16_f32 v180, v188, v189\n"
    "v_cvt_pk_bf16_f32 v181, v190, v191\n"
    "s_sub_u32 s61, s61, 1\n"
    "s_cbranch_scc1 MASKF_%=\n"
    "MBACKF_%=:\n"
    "s_waitcnt lgkmcnt(6)\n"
    "v_mfma_f32_16x16x32_bf16 v[144:147], a[64:67], a[0:3], v[160:163]\n"
    "ds_read_b128 a[104:107], v206 offset:37888\n"
    "s_add_u32 s57, s57, s79\n"
    "v_mfma_f32_16x16x32_bf16 v[144:147], a[68:71], a[8:11], v[144:147]\n"
    "v_exp_f32 v184, v136\n"
    "v_mfma_f32_16x16x32_bf16 v[144:147], a[72:75], a[16:19], v[144:147]\n"
    "ds_read_b128 a[108:111], v206 offset:39936\n"
    "v_add_f32 v200, v200, v184\n"
    "v_mfma_f32_16x16x32_bf16 v[144:147], a[76:79], a[24:27], v[144:147]\n"
    "s_waitcnt lgkmcnt(4)\n"
    "s_add_u32 s97, s81, 0x802000\n"
    "v_mfma_f32_16x16x32_bf16 v[144:147], a[80:83], a[32:35], v[144:147]\n"
    "ds_read_b128 a[112:115], v206 offset:41984\n"
    "s_mov_b32 m0, s59\n"
    "v_mfma_f32_16x16x32_bf16 v[144:147], a[84:87], a[40:43], v[144:147]\n"
    "v_exp_f32 v185, v137\n"
    "v_mfma_f32_16x16x32_bf16 v[144:147], a[88:91], a[48:51], v[144:147]\n"
    "ds_read_b128 a[116:119], v206 offset:44032\n"
    "buffer_store_dwordx4 a[32:35], v210, s[88:91], s97 offen offset:0 sc1\n"
    "v_add_f32 v200, v200, v185\n"
    "v_mfma_f32_16x16x32_bf16 v[144:147], a[92:95], a[56:59], v[144:147]\n"
    "buffer_load_dwordx4 v208, s[36:39], s57 offen offset:0 sc1 lds\n"
    "v_mfma_f32_16x16x32_bf16 v[148:151], a[64:67], a[4:7], v[164:167]\n"
    "ds_read_b128 a[120:123], v206 offset:46080\n"
    "v_cvt_pk_bf16_f32 v178, v184, v185\n"
    "v_mfma_f32_16x16x32_bf16 v[148:151], a[68:71], a[12:15], v[148:151]\n"
    "v_exp_f32 v186, v138\n"
    "v_mfma_f32_16x16x32_bf16 v[148:151], a[72:75], a[20:23], v[148:151]\n"
    "ds_read_b128 a[124:127], v206 offset:48128\n"
    "v_add_f32 v200, v200, v186\n"
    "v_mfma_f32_16x16x32_bf16 v[148:151], a[76:79], a[28:31], v[148:151]\n"
    "v_exp_f32 v187, v139\n"
    "v_mfma_f32_16x16x32_bf16 v[148:151], a[80:83], a[36:39], v[148:151]\n"
    "ds_read_b128 a[64:67], v206 offset:16384\n"
    "buffer_store_dwordx4 a[36:39], v210, s[88:91], s97 offen offset:1024 sc1\n"
    "v_add_f32 v200, v200, v187\n"
    "v_mfma_f32_16x16x32_bf16 v[148:151], a[84:87], a[44:47], v[148:151]\n"
    "v_exp_f32 v188, v140\n"
    "v_mfma_f32_16x16x32_bf16 v[148:151], a[88:91], a[52:55], v[148:151]\n"
    "ds_read_b128 a[68:71], v206 offset:17408\n"
    "v_add_f32 v201, v201, v188\n"
    "v_mfma_f32_16x16x32_bf16 v[148:151], a[92:95], a[60:63], v[148:151]\n"
    "s_waitcnt lgkmcnt(6)\n"
    "buffer_load_dwordx4 v208, s[36:39], s57 offen offset:1024 sc1 lds\n"
    "v_mfma_f32_16x16x32_bf16 v[152:155], a[96:99], a[0:3], v[168:171]\n"
    "ds_read_b128 a[72:75], v206 offset:18432\n"
    "v_cvt_pk_bf16_f32 v179, v186, v187\n"
    "v_mfma_f32_16x16x32_bf16 v[152:155], a[100:103], a[8:11], v[152:155]\n"
    "v_exp_f32 v189, v141\n"
    "v_mfma_f32_16x16x32_bf16 v[152:155], a[104:107], a[16:19], v[152:155]\n"
    "ds_read_b128 a[76:79], v206 offset:19456\n"
    "v_add_f32 v201, v201, v189\n"
    "v_mfma_f32_16x16x32_bf16 v[152:155], a[108:111], a[24:27], v[152:155]\n"
    "s_waitcnt lgkmcnt(4)\n"
    "buffer_load_dwordx4 v208, s[36:39], s57 offen offset:2048 sc1 lds\n"
    "v_mfma_f32_16x16x32_bf16 v[152:155], a[112:115], a[32:35], v[152:155]\n"
    "ds_read_b128 a[80:83], v206 offset:20480\n"
    "buffer_store_dwordx4 a[40:43], v210, s[88:91], s97 offen offset:2048 sc1\n"
    "v_mfma_f32_16x16x32_bf16 v[152:155], a[116:119], a[40:43], v[152:155]\n"
    "v_exp_f32 v190, v142\n"
    "v_mfma_f32_16x16x32_bf16 v[152:155], a[120:123], a[48:51], v[152:155]\n"
    "ds_read_b128 a[84:87], v206 offset:21504\n"
    "v_add_f32 v201, v201, v190\n"
    "v_mfma_f32_16x16x32_bf16 v[152:155], a[124:127], a[56:59], v[152:155]\n"
    "v_exp_f32 v191, v143\n"
    "v_mfma_f32_16x16x32_bf16 v[156:159], a[96:99], a[4:7], v[172:175]\n"
    "ds_read_b128 a[88:91], v206 offset:22528\n"
    "v_add_f32 v201, v201, v191\n"
    "v_mfma_f32_16x16x32_bf16 v[156:159], a[100:103], a[12:15], v[156:159]\n"
    "v_cvt_pk_bf16_f32 v182, v188, v189\n"
    "v_cvt_pk_bf16_f32 v183, v190, v191\n"
    "v_mfma_f32_16x16x32_bf16 v[156:159], a[104:107], a[20:23], v[156:159]\n"
    "ds_read_b128 a[92:95], v206 offset:23552\n"
    "v_max_f32 v212, v200, v201\n"
    "v_mfma_f32_16x16x32_bf16 v[156:159], a[108:111], a[28:31], v[156:159]\n"
    "buffer_load_dwordx4 v208, s[36:39], s57 offen offset:3072 sc1 lds\n"
    "v_mfma_f32_16x16x32_bf16 v[156:159], a[112:115], a[36:39], v[156:159]\n"
    "ds_read_b128 a[96:99], v206 offset:24576\n"
    "v_cmp_nle_f32 vcc, v212, v213\n"
    "v_mfma_f32_16x16x32_bf16 v[156:159], a[116:119], a[44:47], v[156:159]\n"
    "buffer_store_dwordx4 a[44:47], v210, s[88:91], s97 offen offset:3072 sc1\n"
    "v_mfma_f32_16x16x32_bf16 v[156:159], a[120:123], a[52:55], v[156:159]\n"
    "ds_read_b128 a[100:103], v206 offset:25600\n"
    "s_waitcnt lgkmcnt(6)\n"
    "v_mfma_f32_16x16x32_bf16 v[156:159], a[124:127], a[60:63], v[156:159]\n"
    "s_cbranch_vccnz RARE0P_%=\n"
    "BACK0P_%=:\n"
    "v_mfma_f32_16x16x32_bf16 v[0:3], a[64:67], v[176:179], 0\n"
    "ds_read_b128 a[104:107], v206 offset:26624\n"
    "s_xor_b32 m0, s59, 0x1c000\n"
    "v_mfma_f32_16x16x32_bf16 v[4:7], a[64:67], v[180:183], 0\n"
    "v_exp_f32 v184, v144\n"
    "v_mfma_f32_16x16x32_bf16 v[12:15], a[68:71], v[180:183], 0\n"
    "ds_read_b128 a[108:111], v206 offset:27648\n"
    "v_add_f32 v202, v200, v184\n"
    "v_mfma_f32_16x16x32_bf16 v[8:11], a[68:71], v[176:179], 0\n"
    "v_exp_f32 v185, v145\n"
    "v_mfma_f32_16x16x32_bf16 v[16:19], a[72:75], v[176:179], 0\n"
    "ds_read_b128 a[112:115], v206 offset:28672\n"
    "v_add_f32 v202, v202, v185\n"
    "v_mfma_f32_16x16x32_bf16 v[20:23], a[72:75], v[180:183], 0\n"
    "v_cvt_pk_bf16_f32 v192, v184, v185\n"
    "s_add_u32 s97, s81, 0x803000\n"
    "v_mfma_f32_16x16x32_bf16 v[28:31], a[76:79], v[180:183], 0\n"
    "ds_read_b128 a[116:119], v206 offset:29696\n"
    "s_sub_u32 s61, s61, 1\n"
    "v_mfma_f32_16x16x32_bf16 v[24:27], a[76:79], v[176:179], 0\n"
    "s_waitcnt vmcnt(24) lgkmcnt(6)\n"
    "s_barrier\n"
    "v_mfma_f32_16x16x32_bf16 v[32:35], a[80:83], v[176:179], 0\n"
    "ds_read_b128 a[120:123], v206 offset:30720\n"
    "s_cbranch_scc1 MASK1P_%=\n"
    "MBACK1P_%=:\n"
    "v_mfma_f32_16x16x32_bf16 v[36:39], a[80:83], v[180:183], 0\n"
    "v_exp_f32 v186, v146\n"
    "v_mfma_f32_16x16x32_bf16 v[44:47], a[84:87], v[180:183], 0\n"
    "ds_read_b128 a[124:127], v206 offset:31744\n"
    "buffer_store_dwordx4 a[48:51], v210, s[88:91], s97 offen offset:0 sc1\n"
    "v_mfma_f32_16x16x32_bf16 v[40:43], a[84:87], v[176:179], 0\n"
    "buffer_load_dwordx4 v209, s[36:39], s58 offen offset:0 sc1 lds\n"
    "v_mfma_f32_16x16x32_bf16 v[48:51], a[88:91], v[176:179], 0\n"
    "ds_read_b128 a[64:67], v207 offset:0\n"
    "v_add_f32 v202, v202, v186\n"
    "v_mfma_f32_16x16x32_bf16 v[52:55], a[88:91], v[180:183], 0\n"
    "v_exp_f32 v187, v147\n"
    "v_mfma_f32_16x16x32_bf16 v[60:63], a[92:95], v[180:183], 0\n"
    "ds_read_b128 a[68:71], v207 offset:2048\n"
    "buffer_store_dwordx4 a[52:55], v210, s[88:91], s97 offen offset:1024 sc1\n"
    "v_add_f32 v202, v202, v187\n"
    "v_mfma_f32_16x16x32_bf16 v[56:59], a[92:95], v[176:179], 0\n"
    "s_waitcnt lgkmcnt(6)\n"
    "buffer_load_dwordx4 v209, s[36:39], s58 offen offset:1024 sc1 lds\n"
    "v_mfma_f32_16x16x32_bf16 v[64:67], a[96:99], v[176:179], 0\n"
    "ds_read_b128 a[72:75], v207 offset:4096\n"
    "v_cvt_pk_bf16_f32 v193, v186, v187\n"
    "v_mfma_f32_16x16x32_bf16 v[68:71], a[96:99], v[180:183], 0\n"
    "v_exp_f32 v188, v148\n"
    "v_mfma_f32_16x16x32_bf16 v[76:79], a[100:103], v[180:183], 0\n"
    "ds_read_b128 a[76:79], v207 offset:6144\n"
    "v_add_f32 v203, v201, v188\n"
    "v_mfma_f32_16x16x32_bf16 v[72:75], a[100:103], v[176:179], 0\n"
    "v_exp_f32 v189, v149\n"
    "v_mfma_f32_16x16x32_bf16 v[80:83], a[104:107], v[176:179], 0\n"
    "ds_read_b128 a[80:83], v207 offset:8192\n"
    "buffer_store_dwordx4 a[56:59], v210, s[88:91], s97 offen offset:2048 sc1\n"
    "v_add_f32 v203, v203, v189\n"
    "v_mfma_f32_16x16x32_bf16 v[84:87], a[104:107], v[180:183], 0\n"
    "v_exp_f32 v190, v150\n"
    "v_mfma_f32_16x16x32_bf16 v[92:95], a[108:111], v[180:183], 0\n"
    "ds_read_b128 a[84:87], v207 offset:10240\n"
    "v_add_f32 v203, v203, v190\n"
    "v_mfma_f32_16x16x32_bf16 v[88:91], a[108:111], v[176:179], 0\n"
    "s_waitcnt lgkmcnt(6)\n"
    "buffer_load_dwordx4 v209, s[36:39], s58 offen offset:2048 sc1 lds\n"
    "v_mfma_f32_16x16x32_bf16 v[96:99], a[112:115], v[176:179], 0\n"
    "ds_read_b128 a[88:91], v207 offset:12288\n"
    "buffer_store_dwordx4 a[60:63], v210, s[88:91], s97 offen offset:3072 sc1\n"
    "v_cvt_pk_bf16_f32 v196, v188, v189\n"
    "v_mfma_f32_16x16x32_bf16 v[100:103], a[112:115], v[180:183], 0\n"
    "v_exp_f32 v191, v151\n"
    "v_mfma_f32_16x16x32_bf16 v[108:111], a[116:119], v[180:183], 0\n"
    "ds_read_b128 a[92:95], v207 offset:14336\n"
    "v_add_f32 v203, v203, v191\n"
    "v_mfma_f32_16x16x32_bf16 v[104:107], a[116:119], v[176:179], 0\n"
    "buffer_load_dwordx4 v209, s[36:39], s58 offen offset:3072 sc1 lds\n"
    "v_mfma_f32_16x16x32_bf16 v[112:115], a[120:123], v[176:179], 0\n"
    "ds_read_b128 a[96:99], v207 offset:1024\n"
    "v_cvt_pk_bf16_f32 v197, v190, v191\n"
    "v_mfma_f32_16x16x32_bf16 v[116:119], a[120:123], v[180:183], 0\n"
    "s_sub_u32 s60, s60, 1\n"
    "v_mfma_f32_16x16x32_bf16 v[124:127], a[124:127], v[180:183], 0\n"
    "ds_read_b128 a[100:103], v207 offset:3072\n"
    "s_waitcnt lgkmcnt(6)\n"
    "v_mfma_f32_16x16x32_bf16 v[120:123], a[124:127], v[176:179], 0\n"
    "s_cbranch_scc0 MID_%=\n"
    "s_branch TAIL_%=\n"
    "LOOP_%=:\n"
    "v_mfma_f32_16x16x32_bf16 v[144:147], a[64:67], a[0:3], v[160:163]\n"
    "ds_read_b128 a[104:107], v206 offset:37888\n"
    "s_add_u32 s57, s57, s79\n"
    "v_mfma_f32_16x16x32_bf16 v[144:147], a[68:71], a[8:11], v[144:147]\n"
    "v_exp_f32 v184, v136\n"
    "v_mfma_f32_16x16x32_bf16 v[144:147], a[72:75], a[16:19], v[144:147]\n"
    "ds_read_b128 a[108:111], v206 offset:39936\n"
    "v_add_f32 v200, v200, v184\n"
    "v_mfma_f32_16x16x32_bf16 v[144:147], a[76:79], a[24:27], v[144:147]\n"
    "s_waitcnt lgkmcnt(4)\n"
    "v_mfma_f32_16x16x32_bf16 v[144:147], a[80:83], a[32:35], v[144:147]\n"
    "ds_read_b128 a[112:115], v206 offset:41984\n"
    "s_mov_b32 m0, s59\n"
    "v_mfma_f32_16x16x32_bf16 v[144:147], a[84:87], a[40:43], v[144:147]\n"
    "v_exp_f32 v185, v137\n"
    "v_mfma_f32_16x16x32_bf16 v[144:147], a[88:91], a[48:51], v[144:147]\n"
    "ds_read_b128 a[116:119], v206 offset:44032\n"
    "v_add_f32 v200, v200, v185\n"
    "v_mfma_f32_16x16x32_bf16 v[144:147], a[92:95], a[56:59], v[144:147]\n"
    "buffer_load_dwordx4 v208, s[36:39], s57 offen offset:0 sc1 lds\n"
    "v_mfma_f32_16x16x32_bf16 v[148:151], a[64:67], a[4:7], v[164:167]\n"
    "ds_read_b128 a[120:123], v206 offset:46080\n"
    "v_cvt_pk_bf16_f32 v178, v184, v185\n"
    "v_mfma_f32_16x16x32_bf16 v[148:151], a[68:71], a[12:15], v[148:151]\n"
    "v_exp_f32 v186, v138\n"
    "v_mfma_f32_16x16x32_bf16 v[148:151], a[72:75], a[20:23], v[148:151]\n"
    "ds_read_b128 a[124:127], v206 offset:48128\n"
    "v_add_f32 v200, v200, v186\n"
    "v_mfma_f32_16x16x32_bf16 v[148:151], a[76:79], a[28:31], v[148:151]\n"
    "v_exp_f32 v187, v139\n"
    "v_mfma_f32_16x16x32_bf16 v[148:151], a[80:83], a[36:39], v[148:151]\n"
    "ds_read_b128 a[64:67], v206 offset:16384\n"
    "v_add_f32 v200, v200, v187\n"
    "v_mfma_f32_16x16x32_bf16 v[148:151], a[84:87], a[44:47], v[148:151]\n"
    "v_exp_f32 v188, v140\n"
    "v_mfma_f32_16x16x32_bf16 v[148:151], a[88:91], a[52:55], v[148:151]\n"
    "ds_read_b128 a[68:71], v206 offset:17408\n"
    "v_add_f32 v201, v201, v188\n"
    "v_mfma_f32_16x16x32_bf16 v[148:151], a[92:95], a[60:63], v[148:151]\n"
    "s_waitcnt lgkmcnt(6)\n"
    "buffer_load_dwordx4 v208, s[36:39], s57 offen offset:1024 sc1 lds\n"
    "v_mfma_f32_16x16x32_bf16 v[152:155], a[96:99], a[0:3], v[168:171]\n"
    "ds_read_b128 a[72:75], v206 offset:18432\n"
    "v_cvt_pk_bf16_f32 v179, v186, v187\n"
    "v_mfma_f32_16x16x32_bf16 v[152:155], a[100:103], a[8:11], v[152:155]\n"
    "v_exp_f32 v189, v141\n"
    "v_mfma_f32_16x16x32_bf16 v[152:155], a[104:107], a[16:19], v[152:155]\n"
    "ds_read_b128 a[76:79], v206 offset:19456\n"
    "v_add_f32 v201, v201, v189\n"
    "v_mfma_f32_16x16x32_bf16 v[152:155], a[108:111], a[24:27], v[152:155]\n"
    "s_waitcnt lgkmcnt(4)\n"
    "buffer_load_dwordx4 v208, s[36:39], s57 offen offset:2048 sc1 lds\n"
    "v_mfma_f32_16x16x32_bf16 v[152:155], a[112:115], a[32:35], v[152:155]\n"
    "ds_read_b128 a[80:83], v206 offset:20480\n"
    "v_mfma_f32_16x16x32_bf16 v[152:155], a[116:119], a[40:43], v[152:155]\n"
    "v_exp_f32 v190, v142\n"
    "v_mfma_f32_16x16x32_bf16 v[152:155], a[120:123], a[48:51], v[152:155]\n"
    "ds_read_b128 a[84:87], v206 offset:21504\n"
    "v_add_f32 v201, v201, v190\n"
    "v_mfma_f32_16x16x32_bf16 v[152:155], a[124:127], a[56:59], v[152:155]\n"
    "v_exp_f32 v191, v143\n"
    "v_mfma_f32_16x16x32_bf16 v[156:159], a[96:99], a[4:7], v[172:175]\n"
    "ds_read_b128 a[88:91], v206 offset:22528\n"
    "v_add_f32 v201, v201, v191\n"
    "v_mfma_f32_16x16x32_bf16 v[156:159], a[100:103], a[12:15], v[156:159]\n"
    "v_cvt_pk_bf16_f32 v182, v188, v189\n"
    "v_cvt_pk_bf16_f32 v183, v190, v191\n"
    "v_mfma_f32_16x16x32_bf16 v[156:159], a[104:107], a[20:23], v[156:159]\n"
    "ds_read_b128 a[92:95], v206 offset:23552\n"
    "v_max_f32 v212, v200, v201\n"
    "v_mfma_f32_16x16x32_bf16 v[156:159], a[108:111], a[28:31], v[156:159]\n"
    "buffer_load_dwordx4 v208, s[36:39], s57 offen offset:3072 sc1 lds\n"
    "v_mfma_f32_16x16x32_bf16 v[156:159], a[112:115], a[36:39], v[156:159]\n"
    "ds_read_b128 a[96:99], v206 offset:24576\n"
    "v_cmp_nle_f32 vcc, v212, v213\n"
    "v_mfma_f32_16x16x32_bf16 v[156:159], a[116:119], a[44:47], v[156:159]\n"
    "v_mfma_f32_16x16x32_bf16 v[156:159], a[120:123], a[52:55], v[156:159]\n"
    "ds_read_b128 a[100:103], v206 offset:25600\n"
    "s_waitcnt lgkmcnt(6)\n"
    "v_mfma_f32_16x16x32_bf16 v[156:159], a[124:127], a[60:63], v[156:159]\n"
    "s_cbranch_vccnz RARE0_%=\n"
    "BACK0_%=:\n"
    "v_mfma_f32_16x16x32_bf16 v[0:3], a[64:67], v[176:179], v[0:3]\n"
    "ds_read_b128 a[104:107], v206 offset:26624\n"
    "s_xor_b32 m0, s59, 0x1c000\n"
    "v_mfma_f32_16x16x32_bf16 v[4:7], a[64:67], v[180:183], v[4:7]\n"
    "v_exp_f32 v184, v144\n"
    "v_mfma_f32_16x16x32_bf16 v[12:15], a[68:71], v[180:183], v[12:15]\n"
    "ds_read_b128 a[108:111], v206 offset:27648\n"
    "v_add_f32 v202, v200, v184\n"
    "v_mfma_f32_16x16x32_bf16 v[8:11], a[68:71], v[176:179], v[8:11]\n"
    "v_exp_f32 v185, v145\n"
    "v_mfma_f32_16x16x32_bf16 v[16:19], a[72:75], v[176:179], v[16:19]\n"
    "ds_read_b128 a[112:115], v206 offset:28672\n"
    "v_add_f32 v202, v202, v185\n"
    "v_mfma_f32_16x16x32_bf16 v[20:23], a[72:75], v[180:183], v[20:23]\n"
    "v_cvt_pk_bf16_f32 v192, v184, v185\n"
    "v_mfma_f32_16x16x32_bf16 v[28:31], a[76:79], v[180:183], v[28:31]\n"
    "ds_read_b128 a[116:119], v206 offset:29696\n"
    "s_sub_u32 s61, s61, 1\n"
    "v_mfma_f32_16x16x32_bf16 v[24:27], a[76:79], v[176:179], v[24:27]\n"
    "s_waitcnt vmcnt(12) lgkmcnt(6)\n"
    "s_barrier\n"
    "v_mfma_f32_16x16x32_bf16 v[32:35], a[80:83], v[176:179], v[32:35]\n"
    "ds_read_b128 a[120:123], v206 offset:30720\n"
    "s_cbranch_scc1 MASK1_%=\n"
    "MBACK1_%=:\n"
    "v_mfma_f32_16x16x32_bf16 v[36:39], a[80:83], v[180:183], v[36:39]\n"
    "v_exp_f32 v186, v146\n"
    "v_mfma_f32_16x16x32_bf16 v[44:47], a[84:87], v[180:183], v[44:47]\n"
    "ds_read_b128 a[124:127], v206 offset:31744\n"
    "v_mfma_f32_16x16x32_bf16 v[40:43], a[84:87], v[176:179], v[40:43]\n"
    "buffer_load_dwordx4 v209, s[36:39], s58 offen offset:0 sc1 lds\n"
    "v_mfma_f32_16x16x32_bf16 v[48:51], a[88:91], v[176:179], v[48:51]\n"
    "ds_read_b128 a[64:67], v207 offset:0\n"
    "v_add_f32 v202, v202, v186\n"
    "v_mfma_f32_16x16x32_bf16 v[52:55], a[88:91], v[180:183], v[52:55]\n"
    "v_exp_f32 v187, v147\n"
    "v_mfma_f32_16x16x32_bf16 v[60:63], a[92:95], v[180:183], v[60:63]\n"
    "ds_read_b128 a[68:71], v207 offset:2048\n"
    "v_add_f32 v202, v202, v187\n"
    "v_mfma_f32_16x16x32_bf16 v[56:59], a[92:95], v[176:179], v[56:59]\n"
    "s_waitcnt lgkmcnt(6)\n"
    "buffer_load_dwordx4 v209, s[36:39], s58 offen offset:1024 sc1 lds\n"
    "v_mfma_f32_16x16x32_bf16 v[64:67], a[96:99], v[176:179], v[64:67]\n"
    "ds_read_b128 a[72:75], v207 offset:4096\n"
    "v_cvt_pk_bf16_f32 v193, v186, v187\n"
    "v_mfma_f32_16x16x32_bf16 v[68:71], a[96:99], v[180:183], v[68:71]\n"
    "v_exp_f32 v188, v148\n"
    "v_mfma_f32_16x16x32_bf16 v[76:79], a[100:103], v[180:183], v[76:79]\n"
    "ds_read_b128 a[76:79], v207 offset:6144\n"
    "v_add_f32 v203, v201, v188\n"
    "v_mfma_f32_16x16x32_bf16 v[72:75], a[100:103], v[176:179], v[72:75]\n"
    "v_exp_f32 v189, v149\n"
    "v_mfma_f32_16x16x32_bf16 v[80:83], a[104:107], v[176:179], v[80:83]\n"
    "ds_read_b128 a[80:83], v207 offset:8192\n"
    "v_add_f32 v203, v203, v189\n"
    "v_mfma_f32_16x16x32_bf16 v[84:87], a[104:107], v[180:183], v[84:87]\n"
    "v_exp_f32 v190, v150\n"
    "v_mfma_f32_16x16x32_bf16 v[92:95], a[108:111], v[180:183], v[92:95]\n"
    "ds_read_b128 a[84:87], v207 offset:10240\n"
    "v_add_f32 v203, v203, v190\n"
    "v_mfma_f32_16x16x32_bf16 v[88:91], a[108:111], v[176:179], v[88:91]\n"
    "s_waitcnt lgkmcnt(6)\n"
    "buffer_load_dwordx4 v209, s[36:39], s58 offen offset:2048 sc1 lds\n"
    "v_mfma_f32_16x16x32_bf16 v[96:99], a[112:115], v[176:179], v[96:99]\n"
    "ds_read_b128 a[88:91], v207 offset:12288\n"
    "v_cvt_pk_bf16_f32 v196, v188, v189\n"
    "v_mfma_f32_16x16x32_bf16 v[100:103], a[112:115], v[180:183], v[100:103]\n"
    "v_exp_f32 v191, v151\n"
    "v_mfma_f32_16x16x32_bf16 v[108:111], a[116:119], v[180:183], v[108:111]\n"
    "ds_read_b128 a[92:95], v207 offset:14336\n"
    "v_add_f32 v203, v203, v191\n"
    "v_mfma_f32_16x16x32_bf16 v[104:107], a[116:119], v[176:179], v[104:107]\n"
    "buffer_load_dwordx4 v209, s[36:39], s58 offen offset:3072 sc1 lds\n"
    "v_mfma_f32_16x16x32_bf16 v[112:115], a[120:123], v[176:179], v[112:115]\n"
    "ds_read_b128 a[96:99], v207 offset:1024\n"
    "v_cvt_pk_bf16_f32 v197, v190, v191\n"
    "v_mfma_f32_16x16x32_bf16 v[116:119], a[120:123], v[180:183], v[116:119]\n"
    "s_sub_u32 s60, s60, 1\n"
    "v_mfma_f32_16x16x32_bf16 v[124:127], a[124:127], v[180:183], v[124:127]\n"
    "ds_read_b128 a[100:103], v207 offset:3072\n"
    "s_waitcnt lgkmcnt(6)\n"
    "v_mfma_f32_16x16x32_bf16 v[120:123], a[124:127], v[176:179], v[120:123]\n"
    "s_cbranch_scc1 TAIL_%=\n"
    "MID_%=:\n"
    "v_mfma_f32_16x16x32_bf16 v[128:131], a[64:67], a[0:3], v[160:163]\n"
    "ds_read_b128 a[104:107], v207 offset:5120\n"
    "s_xor_b32 m0, s59, 0x8000\n"
    "v_mfma_f32_16x16x32_bf16 v[128:131], a[68:71], a[8:11], v[128:131]\n"
    "v_exp_f32 v184, v152\n"
    "v_mfma_f32_16x16x32_bf16 v[128:131], a[72:75], a[16:19], v[128:131]\n"
    "ds_read_b128 a[108:111], v207 offset:7168\n"
    "v_add_f32 v202, v202, v184\n"
    "v_mfma_f32_16x16x32_bf16 v[128:131], a[76:79], a[24:27], v[128:131]\n"
    "s_waitcnt lgkmcnt(4)\n"
    "s_cmp_eq_u32 s60, 1\n"
    "v_mfma_f32_16x16x32_bf16 v[128:131], a[80:83], a[32:35], v[128:131]\n"
    "ds_read_b128 a[112:115], v207 offset:9216\n"
    "s_cselect_b32 s79, s73, 0x10000\n"
    "v_mfma_f32_16x16x32_bf16 v[128:131], a[84:87], a[40:43], v[128:131]\n"
    "v_exp_f32 v185, v153\n"
    "v_mfma_f32_16x16x32_bf16 v[128:131], a[88:91], a[48:51], v[128:131]\n"
    "ds_read_b128 a[116:119], v207 offset:11264\n"
    "v_add_f32 v202, v202, v185\n"
    "v_mfma_f32_16x16x32_bf16 v[128:131], a[92:95], a[56:59], v[128:131]\n"
    "buffer_load_dwordx4 v209, s[36:39], s57 offen offset:0 sc1 lds\n"
    "v_mfma_f32_16x16x32_bf16 v[132:135], a[64:67], a[4:7], v[164:167]\n"
    "ds_read_b128 a[120:123], v207 offset:13312\n"
    "v_cvt_pk_bf16_f32 v194, v184, v185\n"
    "v_mfma_f32_16x16x32_bf16 v[132:135], a[68:71], a[12:15], v[132:135]\n"
    "v_exp_f32 v186, v154\n"
    "v_mfma_f32_16x16x32_bf16 v[132:135], a[72:75], a[20:23], v[132:135]\n"
    "ds_read_b128 a[124:127], v207 offset:15360\n"
    "v_add_f32 v202, v202, v186\n"
    "v_mfma_f32_16x16x32_bf16 v[132:135], a[76:79], a[28:31], v[132:135]\n"
    "v_exp_f32 v187, v155\n"
    "v_mfma_f32_16x16x32_bf16 v[132:135], a[80:83], a[36:39], v[132:135]\n"
    "ds_read_b128 a[64:67], v206 offset:49152\n"
    "v_add_f32 v202, v202, v187\n"
    "v_mfma_f32_16x16x32_bf16 v[132:135], a[84:87], a[44:47], v[132:135]\n"
    "v_exp_f32 v188, v156\n"
    "v_mfma_f32_16x16x32_bf16 v[132:135], a[88:91], a[52:55], v[132:135]\n"
    "ds_read_b128 a[68:71], v206 offset:50176\n"
    "v_add_f32 v203, v203, v188\n"
    "v_mfma_f32_16x16x32_bf16 v[132:135], a[92:95], a[60:63], v[132:135]\n"
    "s_waitcnt lgkmcnt(6)\n"
    "buffer_load_dwordx4 v209, s[36:39], s57 offen offset:1024 sc1 lds\n"
    "v_mfma_f32_16x16x32_bf16 v[136:139], a[96:99], a[0:3], v[168:171]\n"
    "ds_read_b128 a[72:75], v206 offset:51200\n"
    "v_cvt_pk_bf16_f32 v195, v186, v187\n"
    "v_mfma_f32_16x16x32_bf16 v[136:139], a[100:103], a[8:11], v[136:139]\n"
    "v_exp_f32 v189, v157\n"
    "v_mfma_f32_16x16x32_bf16 v[136:139], a[104:107], a[16:19], v[136:139]\n"
    "ds_read_b128 a[76:79], v206 offset:52224\n"
    "v_add_f32 v203, v203, v189\n"
    "v_mfma_f32_16x16x32_bf16 v[136:139], a[108:111], a[24:27], v[136:139]\n"
    "s_waitcnt lgkmcnt(4)\n"
    "buffer_load_dwordx4 v209, s[36:39], s57 offen offset:2048 sc1 lds\n"
    "v_mfma_f32_16x16x32_bf16 v[136:139], a[112:115], a[32:35], v[136:139]\n"
    "ds_read_b128 a[80:83], v206 offset:53248\n"
    "v_xor_b32 v207, 0x10000, v207\n"
    "v_mfma_f32_16x16x32_bf16 v[136:139], a[116:119], a[40:43], v[136:139]\n"
    "v_exp_f32 v190, v158\n"
    "v_mfma_f32_16x16x32_bf16 v[136:139], a[120:123], a[48:51], v[136:139]\n"
    "ds_read_b128 a[84:87], v206 offset:54272\n"
    "v_add_f32 v203, v203, v190\n"
    "v_mfma_f32_16x16x32_bf16 v[136:139], a[124:127], a[56:59], v[136:139]\n"
    "v_exp_f32 v191, v159\n"
    "v_mfma_f32_16x16x32_bf16 v[140:143], a[96:99], a[4:7], v[172:175]\n"
    "ds_read_b128 a[88:91], v206 offset:55296\n"
    "v_add_f32 v203, v203, v191\n"
    "v_mfma_f32_16x16x32_bf16 v[140:143], a[100:103], a[12:15], v[140:143]\n"
    "v_cvt_pk_bf16_f32 v198, v188, v189\n"
    "v_cvt_pk_bf16_f32 v199, v190, v191\n"
    "v_mfma_f32_16x16x32_bf16 v[140:143], a[104:107], a[20:23], v[140:143]\n"
    "ds_read_b128 a[92:95], v206 offset:56320\n"
    "v_max_f32 v212, v202, v203\n"
    "v_mfma_f32_16x16x32_bf16 v[140:143], a[108:111], a[28:31], v[140:143]\n"
    "buffer_load_dwordx4 v209, s[36:39], s57 offen offset:3072 sc1 lds\n"
    "v_mfma_f32_16x16x32_bf16 v[140:143], a[112:115], a[36:39], v[140:143]\n"
    "ds_read_b128 a[96:99], v206 offset:57344\n"
    "v_cmp_nle_f32 vcc, v212, v213\n"
    "v_mfma_f32_16x16x32_bf16 v[140:143], a[116:119], a[44:47], v[140:143]\n"
    "s_cmp_eq_u32 s60, 0\n"
    "s_cselect_b32 s78, s73, 0x10000\n"
    "v_mfma_f32_16x16x32_bf16 v[140:143], a[120:123], a[52:55], v[140:143]\n"
    "ds_read_b128 a[100:103], v206 offset:58368\n"
    "s_waitcnt lgkmcnt(6)\n"
    "v_mfma_f32_16x16x32_bf16 v[140:143], a[124:127], a[60:63], v[140:143]\n"
    "s_cbranch_vccnz RARE1_%=\n"
    "BACK1_%=:\n"
    "v_mfma_f32_16x16x32_bf16 v[0:3], a[64:67], v[192:195], v[0:3]\n"
    "ds_read_b128 a[104:107], v206 offset:59392\n"
    "s_add_u32 s58, s58, s78\n"
    "v_mfma_f32_16x16x32_bf16 v[4:7], a[64:67], v[196:199], v[4:7]\n"
    "v_exp_f32 v184, v128\n"
    "v_mfma_f32_16x16x32_bf16 v[12:15], a[68:71], v[196:199], v[12:15]\n"
    "ds_read_b128 a[108:111], v206 offset:60416\n"
    "v_add_f32 v200, v202, v184\n"
    "v_mfma_f32_16x16x32_bf16 v[8:11], a[68:71], v[192:195], v[8:11]\n"
    "v_exp_f32 v185, v129\n"
    "v_mfma_f32_16x16x32_bf16 v[16:19], a[72:75], v[192:195], v[16:19]\n"
    "ds_read_b128 a[112:115], v206 offset:61440\n"
    "v_add_f32 v200, v200, v185\n"
    "v_mfma_f32_16x16x32_bf16 v[20:23], a[72:75], v[196:199], v[20:23]\n"
    "v_cvt_pk_bf16_f32 v176, v184, v185\n"
    "s_xor_b32 m0, s59, 0x4000\n"
    "v_mfma_f32_16x16x32_bf16 v[28:31], a[76:79], v[196:199], v[28:31]\n"
    "ds_read_b128 a[116:119], v206 offset:62464\n"
    "s_sub_u32 s61, s61, 1\n"
    "v_mfma_f32_16x16x32_bf16 v[24:27], a[76:79], v[192:195], v[24:27]\n"
    "s_waitcnt vmcnt(12) lgkmcnt(6)\n"
    "s_barrier\n"
    "v_mfma_f32_16x16x32_bf16 v[32:35], a[80:83], v[192:195], v[32:35]\n"
    "ds_read_b128 a[120:123], v206 offset:63488\n"
    "s_cbranch_scc1 MASK0_%=\n"
    "MBACK0_%=:\n"
    "v_mfma_f32_16x16x32_bf16 v[36:39], a[80:83], v[196:199], v[36:39]\n"
    "v_exp_f32 v186, v130\n"
    "v_mfma_f32_16x16x32_bf16 v[44:47], a[84:87], v[196:199], v[44:47]\n"
    "ds_read_b128 a[124:127], v206 offset:64512\n"
    "v_xor_b32 v206, 0x10000, v206\n"
    "v_mfma_f32_16x16x32_bf16 v[40:43], a[84:87], v[192:195], v[40:43]\n"
    "buffer_load_dwordx4 v208, s[36:39], s58 offen offset:0 sc1 lds\n"
    "v_mfma_f32_16x16x32_bf16 v[48:51], a[88:91], v[192:195], v[48:51]\n"
    "ds_read_b128 a[64:67], v206 offset:32768\n"
    "v_add_f32 v200, v200, v186\n"
    "v_mfma_f32_16x16x32_bf16 v[52:55], a[88:91], v[196:199], v[52:55]\n"
    "v_exp_f32 v187, v131\n"
    "v_mfma_f32_16x16x32_bf16 v[60:63], a[92:95], v[196:199], v[60:63]\n"
    "ds_read_b128 a[68:71], v206 offset:34816\n"
    "v_add_f32 v200, v200, v187\n"
    "v_mfma_f32_16x16x32_bf16 v[56:59], a[92:95], v[192:195], v[56:59]\n"
    "s_waitcnt lgkmcnt(6)\n"
    "buffer_load_dwordx4 v208, s[36:39], s58 offen offset:1024 sc1 lds\n"
    "v_mfma_f32_16x16x32_bf16 v[64:67], a[96:99], v[192:195], v[64:67]\n"
    "ds_read_b128 a[72:75], v206 offset:36864\n"
    "v_cvt_pk_bf16_f32 v177, v186, v187\n"
    "v_mfma_f32_16x16x32_bf16 v[68:71], a[96:99], v[196:199], v[68:71]\n"
    "v_exp_f32 v188, v132\n"
    "v_mfma_f32_16x16x32_bf16 v[76:79], a[100:103], v[196:199], v[76:79]\n"
    "ds_read_b128 a[76:79], v206 offset:38912\n"
    "v_add_f32 v201, v203, v188\n"
    "v_mfma_f32_16x16x32_bf16 v[72:75], a[100:103], v[192:195], v[72:75]\n"
    "v_exp_f32 v189, v133\n"
    "v_mfma_f32_16x16x32_bf16 v[80:83], a[104:107], v[192:195], v[80:83]\n"
    "ds_read_b128 a[80:83], v206 offset:40960\n"
    "v_add_f32 v201, v201, v189\n"
    "v_mfma_f32_16x16x32_bf16 v[84:87], a[104:107], v[196:199], v[84:87]\n"
    "v_exp_f32 v190, v134\n"
    "v_mfma_f32_16x16x32_bf16 v[92:95], a[108:111], v[196:199], v[92:95]\n"
    "ds_read_b128 a[84:87], v206 offset:43008\n"
    "v_add_f32 v201, v201, v190\n"
    "v_mfma_f32_16x16x32_bf16 v[88:91], a[108:111], v[192:195], v[88:91]\n"
    "s_waitcnt lgkmcnt(6)\n"
    "buffer_load_dwordx4 v208, s[36:39], s58 offen offset:2048 sc1 lds\n"
    "v_mfma_f32_16x16x32_bf16 v[96:99], a[112:115], v[192:195], v[96:99]\n"
    "ds_read_b128 a[88:91], v206 offset:45056\n"
    "v_cvt_pk_bf16_f32 v180, v188, v189\n"
    "v_mfma_f32_16x16x32_bf16 v[100:103], a[112:115], v[196:199], v[100:103]\n"
    "v_exp_f32 v191, v135\n"
    "v_mfma_f32_16x16x32_bf16 v[108:111], a[116:119], v[196:199], v[108:111]\n"
    "ds_read_b128 a[92:95], v206 offset:47104\n"
    "v_add_f32 v201, v201, v191\n"
    "v_mfma_f32_16x16x32_bf16 v[104:107], a[116:119], v[192:195], v[104:107]\n"
    "buffer_load_dwordx4 v208, s[36:39], s58 offen offset:3072 sc1 lds\n"
    "v_mfma_f32_16x16x32_bf16 v[112:115], a[120:123], v[192:195], v[112:115]\n"
    "ds_read_b128 a[96:99], v206 offset:33792\n"
    "v_cvt_pk_bf16_f32 v181, v190, v191\n"
    "v_mfma_f32_16x16x32_bf16 v[116:119], a[120:123], v[196:199], v[116:119]\n"
    "s_xor_b32 s59, s59, 0x10000\n"
    "v_mfma_f32_16x16x32_bf16 v[124:127], a[124:127], v[196:199], v[124:127]\n"
    "ds_read_b128 a[100:103], v206 offset:35840\n"
    "s_waitcnt lgkmcnt(6)\n"
    "v_mfma_f32_16x16x32_bf16 v[120:123], a[124:127], v[192:195], v[120:123]\n"
    "s_branch LOOP_%=\n"
    "TAIL_%=:\n"
    "s_cmp_eq_u32 s75, 2\n"
    "s_cbranch_scc1 TAIL2_%=\n"
    "s_cmp_eq_u32 s75, 0\n"
    "s_cbranch_scc1 TAIL0_%=\n"
    "s_cmp_eq_u32 s75, 1\n"
    "s_cbranch_scc1 TAIL1_%=\n"
    "s_add_u32 s83, s83, 0x4000\n"
    "buffer_load_dwordx2 v[238:239], v222, s[52:55], s83 offen sc1\n"
    "v_exp_f32 v184, v152\n"
    "v_exp_f32 v185, v153\n"
    "v_exp_f32 v186, v154\n"
    "v_exp_f32 v187, v155\n"
    "v_exp_f32 v188, v156\n"
    "v_exp_f32 v189, v157\n"
    "s_waitcnt lgkmcnt(0)\n"
    "v_exp_f32 v190, v158\n"
    "v_exp_f32 v191, v159\n"
    "ds_read_b128 a[192:195], v206 offset:49152\n"
    "v_add_f32 v202, v202, v184\n"
    "ds_read_b128 a[196:199], v206 offset:50176\n"
    "v_add_f32 v203, v203, v188\n"
    "ds_read_b128 a[200:203], v206 offset:51200\n"
    "v_add_f32 v202, v202, v185\n"
    "ds_read_b128 a[204:207], v206 offset:52224\n"
    "v_add_f32 v203, v203, v189\n"
    "ds_read_b128 a[208:211], v206 offset:53248\n"
    "v_add_f32 v202, v202, v186\n"
    "ds_read_b128 a[212:215], v206 offset:54272\n"
    "v_add_f32 v203, v203, v190\n"
    "ds_read_b128 a[216:219], v206 offset:55296\n"
    "v_add_f32 v202, v202, v187\n"
    "ds_read_b128 a[220:223], v206 offset:56320\n"
    "v_add_f32 v203, v203, v191\n"
    "ds_read_b128 a[224:227], v206 offset:57344\n"
    "v_cvt_pk_bf16_f32 v194, v184, v185\n"
    "ds_read_b128 a[228:231], v206 offset:58368\n"
    "v_cvt_pk_bf16_f32 v195, v186, v187\n"
    "ds_read_b128 a[232:235], v206 offset:59392\n"
    "v_cvt_pk_bf16_f32 v198, v188, v189\n"
    "ds_read_b128 a[236:239], v206 offset:60416\n"
    "v_cvt_pk_bf16_f32 v199, v190, v191\n"
    "v_max_f32 v212, v202, v203\n"
    "v_cmp_nle_f32 vcc, v212, v213\n"
    "s_cbranch_vccnz RARET3_%=\n"
    "BACKT3_%=:\n"
    "s_waitcnt vmcnt(0)\n"
    "v_cmp_eq_u64 vcc, s[34:35], v[238:239]\n"
    "s_nop 0\n"
    "s_cmp_eq_u64 vcc, exec\n"
    "s_cbranch_scc0 POLL3_%=\n"
    "GOT3_%=:\n"
    "s_waitcnt lgkmcnt(8)\n"
    "v_mfma_f32_16x16x32_bf16 v[0:3], a[192:195], v[192:195], v[0:3]\n"
    "ds_read_b128 a[240:243], v206 offset:61440\n"
    "s_add_u32 s97, s81, 0x800000\n"
    "v_mfma_f32_16x16x32_bf16 v[4:7], a[192:195], v[196:199], v[4:7]\n"
    "buffer_load_dwordx4 a[0:3], v210, s[52:55], s97 offen offset:0 sc1\n"
    "v_mov_b32 v218, v202\n"
    "v_mfma_f32_16x16x32_bf16 v[12:15], a[196:199], v[196:199], v[12:15]\n"
    "ds_read_b128 a[244:247], v206 offset:62464\n"
    "v_mov_b32 v220, v203\n"
    "v_mfma_f32_16x16x32_bf16 v[8:11], a[196:199], v[192:195], v[8:11]\n"
    "buffer_load_dwordx4 a[4:7], v210, s[52:55], s97 offen offset:1024 sc1\n"
    "s_nop 0\n"
    "v_mfma_f32_16x16x32_bf16 v[16:19], a[200:203], v[192:195], v[16:19]\n"
    "ds_read_b128 a[248:251], v206 offset:63488\n"
    "v_permlane16_swap_b32 v202, v218\n"
    "v_mfma_f32_16x16x32_bf16 v[20:23], a[200:203], v[196:199], v[20:23]\n"
    "buffer_load_dwordx4 a[8:11], v210, s[52:55], s97 offen offset:2048 sc1\n"
    "v_mfma_f32_16x16x32_bf16 v[28:31], a[204:207], v[196:199], v[28:31]\n"
    "ds_read_b128 a[252:255], v206 offset:64512\n"
    "v_permlane16_swap_b32 v203, v220\n"
    "v_mfma_f32_16x16x32_bf16 v[24:27], a[204:207], v[192:195], v[24:27]\n"
    "s_waitcnt lgkmcnt(8)\n"
    "buffer_load_dwordx4 a[12:15], v210, s[52:55], s97 offen offset:3072 sc1\n"
    "v_add_f32 v202, v202, v218\n"
    "v_mfma_f32_16x16x32_bf16 v[32:35], a[208:211], v[192:195], v[32:35]\n"
    "s_add_u32 s97, s81, 0x801000\n"
    "v_add_f32 v203, v203, v220\n"
    "v_mfma_f32_16x16x32_bf16 v[36:39], a[208:211], v[196:199], v[36:39]\n"
    "buffer_load_dwordx4 a[16:19], v210, s[52:55], s97 offen offset:0 sc1\n"
    "v_mfma_f32_16x16x32_bf16 v[44:47], a[212:215], v[196:199], v[44:47]\n"
    "buffer_load_dwordx4 a[20:23], v210, s[52:55], s97 offen offset:1024 sc1\n"
    "v_mov_b32 v218, v202\n"
    "v_mfma_f32_16x16x32_bf16 v[40:43], a[212:215], v[192:195], v[40:43]\n"
    "v_mov_b32 v220, v203\n"
    "v_mfma_f32_16x16x32_bf16 v[48:51], a[216:219], v[192:195], v[48:51]\n"
    "buffer_load_dwordx4 a[24:27], v210, s[52:55], s97 offen offset:2048 sc1\n"
    "s_nop 0\n"
    "v_mfma_f32_16x16x32_bf16 v[52:55], a[216:219], v[196:199], v[52:55]\n"
    "buffer_load_dwordx4 a[28:31], v210, s[52:55], s97 offen offset:3072 sc1\n"
    "v_mfma_f32_16x16x32_bf16 v[60:63], a[220:223], v[196:199], v[60:63]\n"
    "v_permlane32_swap_b32 v202, v218\n"
    "v_mfma_f32_16x16x32_bf16 v[56:59], a[220:223], v[192:195], v[56:59]\n"
    "s_waitcnt lgkmcnt(4)\n"
    "s_add_u32 s97, s81, 0x802000\n"
    "v_permlane32_swap_b32 v203, v220\n"
    "v_mfma_f32_16x16x32_bf16 v[64:67], a[224:227], v[192:195], v[64:67]\n"
    "buffer_load_dwordx4 a[32:35], v210, s[52:55], s97 offen offset:0 sc1\n"
    "v_add_f32 v202, v202, v218\n"
    "v_mfma_f32_16x16x32_bf16 v[68:71], a[224:227], v[196:199], v[68:71]\n"
    "v_mfma_f32_16x16x32_bf16 v[76:79], a[228:231], v[196:199], v[76:79]\n"
    "buffer_load_dwordx4 a[36:39], v210, s[52:55], s97 offen offset:1024 sc1\n"
    "v_add_f32 v203, v203, v220\n"
    "v_mfma_f32_16x16x32_bf16 v[72:75], a[228:231], v[192:195], v[72:75]\n"
    "buffer_load_dwordx4 a[40:43], v210, s[52:55], s97 offen offset:2048 sc1\n"
    "v_mfma_f32_16x16x32_bf16 v[80:83], a[232:235], v[192:195], v[80:83]\n"
    "v_rcp_f32 v232, v202\n"
    "v_mfma_f32_16x16x32_bf16 v[84:87], a[232:235], v[196:199], v[84:87]\n"
    "buffer_load_dwordx4 a[44:47], v210, s[52:55], s97 offen offset:3072 sc1\n"
    "v_mfma_f32_16x16x32_bf16 v[92:95], a[236:239], v[196:199], v[92:95]\n"
    "s_add_u32 s97, s81, 0x803000\n"
    "v_log_f32 v214, v202\n"
    "v_mfma_f32_16x16x32_bf16 v[88:91], a[236:239], v[192:195], v[88:91]\n"
    "s_waitcnt lgkmcnt(0)\n"
    "v_rcp_f32 v234, v203\n"
    "v_mfma_f32_16x16x32_bf16 v[96:99], a[240:243], v[192:195], v[96:99]\n"
    "buffer_load_dwordx4 a[48:51], v210, s[52:55], s97 offen offset:0 sc1\n"
    "v_mfma_f32_16x16x32_bf16 v[100:103], a[240:243], v[196:199], v[100:103]\n"
    "buffer_load_dwordx4 a[52:55], v210, s[52:55], s97 offen offset:1024 sc1\n"
    "v_mfma_f32_16x16x32_bf16 v[108:111], a[244:247], v[196:199], v[108:111]\n"
    "v_log_f32 v215, v203\n"
    "v_mov_b32 v233, v232\n"
    "v_mfma_f32_16x16x32_bf16 v[104:107], a[244:247], v[192:195], v[104:107]\n"
    "buffer_load_dwordx4 a[56:59], v210, s[52:55], s97 offen offset:2048 sc1\n"
    "v_sub_f32 v214, v214, v204\n"
    "v_mfma_f32_16x16x32_bf16 v[112:115], a[248:251], v[192:195], v[112:115]\n"
    "buffer_load_dwordx4 a[60:63], v210, s[52:55], s97 offen offset:3072 sc1\n"
    "v_mov_b32 v235, v234\n"
    "v_mfma_f32_16x16x32_bf16 v[116:119], a[248:251], v[196:199], v[116:119]\n"
    "v_sub_f32 v215, v215, v205\n"
    "v_mfma_f32_16x16x32_bf16 v[124:127], a[252:255], v[196:199], v[124:127]\n"
    "v_mfma_f32_16x16x32_bf16 v[120:123], a[252:255], v[192:195], v[120:123]\n"
    "s_sub_u32 s83, s83, 0x4000\n"
    "s_sub_u32 s72, 15, s77\n"
    "s_mov_b32 s73, 38\n"
    "s_lshl_b32 s74, s77, 2\n"
    "s_sub_u32 s74, 26, s74\n"
    "s_mov_b32 s75, 1\n"
    "v_add_u32 v206, 0x10000, v210\n"
    "v_mov_b32 v207, v210\n"
    "s_lshr_b32 s60, s74, 1\n"
    "s_sub_u32 s60, s60, 1\n"
    "s_lshl_b32 s92, s72, 2\n"
    "s_add_u32 s92, s92, s56\n"
    "s_sub_i32 s92, s92, s73\n"
    "s_mov_b32 s62, 0\n"
    "s_sub_u32 s61, s92, 1\n"
    "s_cmp_eq_u32 s56, 3\n"
    "s_cselect_b32 s63, 0x7fffffff, 0\n"
    "s_mov_b32 s64, 0xfff0ffff\n"
    "s_mov_b32 s65, 0xf000ff00\n"
    "s_mov_b32 s66, 0xffe0fffe\n"
    "s_mov_b32 s67, 0xe000fe00\n"
    "s_mov_b32 s68, 0xffc0fffc\n"
    "s_mov_b32 s69, 0xc000fc00\n"
    "s_mov_b32 s70, 0xff80fff8\n"
    "s_mov_b32 s71, 0x8000f800\n"
    "s_mov_b64 s[84:85], -1\n"
    "s_mov_b32 s73, 0x10000\n"
    "s_cmp_eq_u32 s60, 1\n"
    "s_cselect_b32 s79, s73, 0x10000\n"
    "s_mov_b32 s78, 0x10000\n"
    "v_pk_mul_f32 v[240:241], v[0:1], v[232:233]\n"
    "v_pk_mul_f32 v[242:243], v[2:3], v[232:233]\n"
    "v_pk_mul_f32 v[244:245], v[8:9], v[232:233]\n"
    "v_pk_mul_f32 v[246:247], v[10:11], v[232:233]\n"
    "v_cvt_pk_bf16_f32 v128, v240, v241\n"
    "v_cvt_pk_bf16_f32 v129, v242, v243\n"
    "v_cvt_pk_bf16_f32 v130, v244, v245\n"
    "v_cvt_pk_bf16_f32 v131, v246, v247\n"
    "v_accvgpr_write_b32 a128, v128\n"
    "v_accvgpr_write_b32 a129, v129\n"
    "v_accvgpr_write_b32 a130, v130\n"
    "v_accvgpr_write_b32 a131, v131\n"
    "v_pk_mul_f32 v[248:249], v[4:5], v[234:235]\n"
    "v_pk_mul_f32 v[250:251], v[6:7], v[234:235]\n"
    "v_pk_mul_f32 v[252:253], v[12:13], v[234:235]\n"
    "v_pk_mul_f32 v[254:255], v[14:15], v[234:235]\n"
    "v_cvt_pk_bf16_f32 v132, v248, v249\n"
    "v_cvt_pk_bf16_f32 v133, v250, v251\n"
    "v_cvt_pk_bf16_f32 v134, v252, v253\n"
    "v_cvt_pk_bf16_f32 v135, v254, v255\n"
    "v_accvgpr_write_b32 a132, v132\n"
    "v_accvgpr_write_b32 a133, v133\n"
    "v_accvgpr_write_b32 a134, v134\n"
    "v_accvgpr_write_b32 a135, v135\n"
    "v_pk_mul_f32 v[240:241], v[16:17], v[232:233]\n"
    "v_pk_mul_f32 v[242:243], v[18:19], v[232:233]\n"
    "v_pk_mul_f32 v[244:245], v[24:25], v[232:233]\n"
    "v_pk_mul_f32 v[246:247], v[26:27], v[232:233]\n"
    "v_cvt_pk_bf16_f32 v136, v240, v241\n"
    "v_cvt_pk_bf16_f32 v137, v242, v243\n"
    "v_cvt_pk_bf16_f32 v138, v244, v245\n"
    "v_cvt_pk_bf16_f32 v139, v246, v247\n"
    "v_accvgpr_write_b32 a136, v136\n"
    "v_accvgpr_write_b32 a137, v137\n"
    "v_accvgpr_write_b32 a138, v138\n"
    "v_accvgpr_write_b32 a139, v139\n"
    "v_pk_mul_f32 v[248:249], v[20:21], v[234:235]\n"
    "v_pk_mul_f32 v[250:251], v[22:23], v[234:235]\n"
    "v_pk_mul_f32 v[252:253], v[28:29], v[234:235]\n"
    "v_pk_mul_f32 v[254:255], v[30:31], v[234:235]\n"
    "v_cvt_pk_bf16_f32 v140, v248, v249\n"
    "v_cvt_pk_bf16_f32 v141, v250, v251\n"
    "v_cvt_pk_bf16_f32 v142, v252, v253\n"
    "v_cvt_pk_bf16_f32 v143, v254, v255\n"
    "v_accvgpr_write_b32 a140, v140\n"
    "v_accvgpr_write_b32 a141, v141\n"
    "v_accvgpr_write_b32 a142, v142\n"
    "v_accvgpr_write_b32 a143, v143\n"
    "v_pk_mul_f32 v[240:241], v[32:33], v[232:233]\n"
    "v_pk_mul_f32 v[242:243], v[34:35], v[232:233]\n"
    "v_pk_mul_f32 v[244:245], v[40:41], v[232:233]\n"
    "v_pk_mul_f32 v[246:247], v[42:43], v[232:233]\n"
    "v_cvt_pk_bf16_f32 v144, v240, v241\n"
    "v_cvt_pk_bf16_f32 v145, v242, v243\n"
    "v_cvt_pk_bf16_f32 v146, v244, v245\n"
    "v_cvt_pk_bf16_f32 v147, v246, v247\n"
    "v_accvgpr_write_b32 a144, v144\n"
    "v_accvgpr_write_b32 a145, v145\n"
    "v_accvgpr_write_b32 a146, v146\n"
    "v_accvgpr_write_b32 a147, v147\n"
    "v_pk_mul_f32 v[248:249], v[36:37], v[234:235]\n"
    "v_pk_mul_f32 v[250:251], v[38:39], v[234:235]\n"
    "v_pk_mul_f32 v[252:253], v[44:45], v[234:235]\n"
    "v_pk_mul_f32 v[254:255], v[46:47], v[234:235]\n"
    "v_cvt_pk_bf16_f32 v148, v248, v249\n"
    "v_cvt_pk_bf16_f32 v149, v250, v251\n"
    "v_cvt_pk_bf16_f32 v150, v252, v253\n"
    "v_cvt_pk_bf16_f32 v151, v254, v255\n"
    "v_accvgpr_write_b32 a148, v148\n"
    "v_accvgpr_write_b32 a149, v149\n"
    "v_accvgpr_write_b32 a150, v150\n"
    "v_accvgpr_write_b32 a151, v151\n"
    "v_pk_mul_f32 v[240:241], v[48:49], v[232:233]\n"
    "v_pk_mul_f32 v[242:243], v[50:51], v[232:233]\n"
    "v_pk_mul_f32 v[244:245], v[56:57], v[232:233]\n"
    "v_pk_mul_f32 v[246:247], v[58:59], v[232:233]\n"
    "v_cvt_pk_bf16_f32 v152, v240, v241\n"
    "v_cvt_pk_bf16_f32 v153, v242, v243\n"
    "v_cvt_pk_bf16_f32 v154, v244, v245\n"
    "v_cvt_pk_bf16_f32 v155, v246, v247\n"
    "v_accvgpr_write_b32 a152, v152\n"
    "v_accvgpr_write_b32 a153, v153\n"
    "v_accvgpr_write_b32 a154, v154\n"
    "v_accvgpr_write_b32 a155, v155\n"
    "v_pk_mul_f32 v[248:249], v[52:53], v[234:235]\n"
    "v_pk_mul_f32 v[250:251], v[54:55], v[234:235]\n"
    "v_pk_mul_f32 v[252:253], v[60:61], v[234:235]\n"
    "v_pk_mul_f32 v[254:255], v[62:63], v[234:235]\n"
    "v_cvt_pk_bf16_f32 v156, v248, v249\n"
    "v_cvt_pk_bf16_f32 v157, v250, v251\n"
    "v_cvt_pk_bf16_f32 v158, v252, v253\n"
    "v_cvt_pk_bf16_f32 v159, v254, v255\n"
    "v_accvgpr_write_b32 a156, v156\n"
    "v_accvgpr_write_b32 a157, v157\n"
    "v_accvgpr_write_b32 a158, v158\n"
    "v_accvgpr_write_b32 a159, v159\n"
    "v_pk_mul_f32 v[240:241], v[64:65], v[232:233]\n"
    "v_pk_mul_f32 v[242:243], v[66:67], v[232:233]\n"
    "v_pk_mul_f32 v[244:245], v[72:73], v[232:233]\n"
    "v_pk_mul_f32 v[246:247], v[74:75], v[232:233]\n"
    "v_cvt_pk_bf16_f32 v160, v240, v241\n"
    "v_cvt_pk_bf16_f32 v161, v242, v243\n"
    "v_cvt_pk_bf16_f32 v162, v244, v245\n"
    "v_cvt_pk_bf16_f32 v163, v246, v247\n"
    "v_accvgpr_write_b32 a160, v160\n"
    "v_accvgpr_write_b32 a161, v161\n"
    "v_accvgpr_write_b32 a162, v162\n"
    "v_accvgpr_write_b32 a163, v163\n"
    "v_pk_mul_f32 v[248:249], v[68:69], v[234:235]\n"
    "v_pk_mul_f32 v[250:251], v[70:71], v[234:235]\n"
    "v_pk_mul_f32 v[252:253], v[76:77], v[234:235]\n"
    "v_pk_mul_f32 v[254:255], v[78:79], v[234:235]\n"
    "v_cvt_pk_bf16_f32 v164, v248, v249\n"
    "v_cvt_pk_bf16_f32 v165, v250, v251\n"
    "v_cvt_pk_bf16_f32 v166, v252, v253\n"
    "v_cvt_pk_bf16_f32 v167, v254, v255\n"
    "v_accvgpr_write_b32 a164, v164\n"
    "v_accvgpr_write_b32 a165, v165\n"
    "v_accvgpr_write_b32 a166, v166\n"
    "v_accvgpr_write_b32 a167, v167\n"
    "v_pk_mul_f32 v[240:241], v[80:81], v[232:233]\n"
    "v_pk_mul_f32 v[242:243], v[82:83], v[232:233]\n"
    "v_pk_mul_f32 v[244:245], v[88:89], v[232:233]\n"
    "v_pk_mul_f32 v[246:247], v[90:91], v[232:233]\n"
    "v_cvt_pk_bf16_f32 v168, v240, v241\n"
    "v_cvt_pk_bf16_f32 v169, v242, v243\n"
    "v_cvt_pk_bf16_f32 v170, v244, v245\n"
    "v_cvt_pk_bf16_f32 v171, v246, v247\n"
    "v_accvgpr_write_b32 a168, v168\n"
    "v_accvgpr_write_b32 a169, v169\n"
    "v_accvgpr_write_b32 a170, v170\n"
    "v_accvgpr_write_b32 a171, v171\n"
    "v_pk_mul_f32 v[248:249], v[84:85], v[234:235]\n"
    "v_pk_mul_f32 v[250:251], v[86:87], v[234:235]\n"
    "v_pk_mul_f32 v[252:253], v[92:93], v[234:235]\n"
    "v_pk_mul_f32 v[254:255], v[94:95], v[234:235]\n"
    "v_cvt_pk_bf16_f32 v172, v248, v249\n"
    "v_cvt_pk_bf16_f32 v173, v250, v251\n"
    "v_cvt_pk_bf16_f32 v174, v252, v253\n"
    "v_cvt_pk_bf16_f32 v175, v254, v255\n"
    "v_accvgpr_write_b32 a172, v172\n"
    "v_accvgpr_write_b32 a173, v173\n"
    "v_accvgpr_write_b32 a174, v174\n"
    "v_accvgpr_write_b32 a175, v175\n"
    "v_pk_mul_f32 v[240:241], v[96:97], v[232:233]\n"
    "v_pk_mul_f32 v[242:243], v[98:99], v[232:233]\n"
    "v_pk_mul_f32 v[244:245], v[104:105], v[232:233]\n"
    "v_pk_mul_f32 v[246:247], v[106:107], v[232:233]\n"
    "v_cvt_pk_bf16_f32 v176, v240, v241\n"
    "v_cvt_pk_bf16_f32 v177, v242, v243\n"
    "v_cvt_pk_bf16_f32 v178, v244, v245\n"
    "v_cvt_pk_bf16_f32 v179, v246, v247\n"
    "v_accvgpr_write_b32 a176, v176\n"
    "v_accvgpr_write_b32 a177, v177\n"
    "v_accvgpr_write_b32 a178, v178\n"
    "v_accvgpr_write_b32 a179, v179\n"
    "v_pk_mul_f32 v[248:249], v[100:101], v[234:235]\n"
    "v_pk_mul_f32 v[250:251], v[102:103], v[234:235]\n"
    "v_pk_mul_f32 v[252:253], v[108:109], v[234:235]\n"
    "v_pk_mul_f32 v[254:255], v[110:111], v[234:235]\n"
    "v_cvt_pk_bf16_f32 v180, v248, v249\n"
    "v_cvt_pk_bf16_f32 v181, v250, v251\n"
    "v_cvt_pk_bf16_f32 v182, v252, v253\n"
    "v_cvt_pk_bf16_f32 v183, v254, v255\n"
    "v_accvgpr_write_b32 a180, v180\n"
    "v_accvgpr_write_b32 a181, v181\n"
    "v_accvgpr_write_b32 a182, v182\n"
    "v_accvgpr_write_b32 a183, v183\n"
    "v_pk_mul_f32 v[240:241], v[112:113], v[232:233]\n"
    "v_pk_mul_f32 v[242:243], v[114:115], v[232:233]\n"
    "v_pk_mul_f32 v[244:245], v[120:121], v[232:233]\n"
    "v_pk_mul_f32 v[246:247], v[122:123], v[232:233]\n"
    "v_cvt_pk_bf16_f32 v184, v240, v241\n"
    "v_cvt_pk_bf16_f32 v185, v242, v243\n"
    "v_cvt_pk_bf16_f32 v186, v244, v245\n"
    "v_cvt_pk_bf16_f32 v187, v246, v247\n"
    "v_accvgpr_write_b32 a184, v184\n"
    "v_accvgpr_write_b32 a185, v185\n"
    "v_accvgpr_write_b32 a186, v186\n"
    "v_accvgpr_write_b32 a187, v187\n"
    "v_pk_mul_f32 v[248:249], v[116:117], v[234:235]\n"
    "v_pk_mul_f32 v[250:251], v[118:119], v[234:235]\n"
    "v_pk_mul_f32 v[252:253], v[124:125], v[234:235]\n"
    "v_pk_mul_f32 v[254:255], v[126:127], v[234:235]\n"
    "v_cvt_pk_bf16_f32 v188, v248, v249\n"
    "v_cvt_pk_bf16_f32 v189, v250, v251\n"
    "v_cvt_pk_bf16_f32 v190, v252, v253\n"
    "v_cvt_pk_bf16_f32 v191, v254, v255\n"
    "v_accvgpr_write_b32 a188, v188\n"
    "v_accvgpr_write_b32 a189, v189\n"
    "v_accvgpr_write_b32 a190, v190\n"
    "v_accvgpr_write_b32 a191, v191\n"
    "v_mov_b64 v[160:161], 0\n"
    "v_mov_b64 v[162:163], 0\n"
    "v_mov_b64 v[164:165], 0\n"
    "v_mov_b64 v[166:167], 0\n"
    "v_mov_b64 v[168:169], 0\n"
    "v_mov_b64 v[170:171], 0\n"
    "v_mov_b64 v[172:173], 0\n"
    "v_mov_b64 v[174:175], 0\n"
    "v_mov_b64 v[204:205], 0\n"
    "v_mov_b64 v[202:203], 0\n"
    "s_waitcnt vmcnt(0)\n"
    "s_branch FIRST_%=\n"
    "TAIL1_%=:\n"
    "v_exp_f32 v184, v152\n"
    "v_exp_f32 v185, v153\n"
    "v_exp_f32 v186, v154\n"
    "v_exp_f32 v187, v155\n"
    "v_exp_f32 v188, v156\n"
    "v_exp_f32 v189, v157\n"
    "s_waitcnt lgkmcnt(0)\n"
    "v_exp_f32 v190, v158\n"
    "v_exp_f32 v191, v159\n"
    "ds_read_b128 a[64:67], v206 offset:49152\n"
    "v_add_f32 v202, v202, v184\n"
    "ds_read_b128 a[68:71], v206 offset:50176\n"
    "v_add_f32 v203, v203, v188\n"
    "ds_read_b128 a[72:75], v206 offset:51200\n"
    "v_add_f32 v202, v202, v185\n"
    "ds_read_b128 a[76:79], v206 offset:52224\n"
    "v_add_f32 v203, v203, v189\n"
    "ds_read_b128 a[80:83], v206 offset:53248\n"
    "v_add_f32 v202, v202, v186\n"
    "ds_read_b128 a[84:87], v206 offset:54272\n"
    "v_add_f32 v203, v203, v190\n"
    "ds_read_b128 a[88:91], v206 offset:55296\n"
    "v_add_f32 v202, v202, v187\n"
    "ds_read_b128 a[92:95], v206 offset:56320\n"
    "v_add_f32 v203, v203, v191\n"
    "ds_read_b128 a[96:99], v206 offset:57344\n"
    "v_cvt_pk_bf16_f32 v194, v184, v185\n"
    "ds_read_b128 a[100:103], v206 offset:58368\n"
    "v_cvt_pk_bf16_f32 v195, v186, v187\n"
    "ds_read_b128 a[104:107], v206 offset:59392\n"
    "v_cvt_pk_bf16_f32 v198, v188, v189\n"
    "ds_read_b128 a[108:111], v206 offset:60416\n"
    "v_cvt_pk_bf16_f32 v199, v190, v191\n"
    "v_max_f32 v212, v202, v203\n"
    "v_cmp_nle_f32 vcc, v212, v213\n"
    "s_cbranch_vccnz RARET1_%=\n"
    "BACKT1_%=:\n"
    "s_waitcnt vmcnt(0)\n"
    "s_lshl_b32 s95, s56, 13\n"
    "s_barrier\n"
    "s_waitcnt lgkmcnt(8)\n"
    "v_mfma_f32_16x16x32_bf16 v[0:3], a[64:67], v[192:195], v[0:3]\n"
    "ds_read_b128 a[112:115], v206 offset:61440\n"
    "s_add_u32 m0, s95, 0x20000\n"
    "v_mfma_f32_16x16x32_bf16 v[4:7], a[64:67], v[196:199], v[4:7]\n"
    "v_mov_b32 v218, v202\n"
    "v_mfma_f32_16x16x32_bf16 v[12:15], a[68:71], v[196:199], v[12:15]\n"
    "ds_read_b128 a[116:119], v206 offset:62464\n"
    "s_mov_b32 s96, 0\n"
    "v_mfma_f32_16x16x32_bf16 v[8:11], a[68:71], v[192:195], v[8:11]\n"
    "v_mov_b32 v220, v203\n"
    "s_nop 0\n"
    "v_mfma_f32_16x16x32_bf16 v[16:19], a[72:75], v[192:195], v[16:19]\n"
    "ds_read_b128 a[120:123], v206 offset:63488\n"
    "v_permlane16_swap_b32 v202, v218\n"
    "v_mfma_f32_16x16x32_bf16 v[20:23], a[72:75], v[196:199], v[20:23]\n"
    "buffer_load_dwordx4 v228, s[44:47], s96 offen offset:0 lds\n"
    "v_mfma_f32_16x16x32_bf16 v[28:31], a[76:79], v[196:199], v[28:31]\n"
    "ds_read_b128 a[124:127], v206 offset:64512\n"
    "v_permlane16_swap_b32 v203, v220\n"
    "v_mfma_f32_16x16x32_bf16 v[24:27], a[76:79], v[192:195], v[24:27]\n"
    "s_waitcnt lgkmcnt(8)\n"
    "buffer_load_dwordx4 v229, s[44:47], s96 offen offset:1024 lds\n"
    "v_mfma_f32_16x16x32_bf16 v[32:35], a[80:83], v[192:195], v[32:35]\n"
    "v_add_f32 v202, v202, v218\n"
    "v_add_f32 v203, v203, v220\n"
    "v_mfma_f32_16x16x32_bf16 v[36:39], a[80:83], v[196:199], v[36:39]\n"
    "v_mfma_f32_16x16x32_bf16 v[44:47], a[84:87], v[196:199], v[44:47]\n"
    "buffer_load_dwordx4 v230, s[44:47], s96 offen offset:2048 lds\n"
    "v_mfma_f32_16x16x32_bf16 v[40:43], a[84:87], v[192:195], v[40:43]\n"
    "v_mov_b32 v218, v202\n"
    "v_mov_b32 v220, v203\n"
    "v_mfma_f32_16x16x32_bf16 v[48:51], a[88:91], v[192:195], v[48:51]\n"
    "buffer_load_dwordx4 v231, s[44:47], s96 offen offset:3072 lds\n"
    "v_mfma_f32_16x16x32_bf16 v[52:55], a[88:91], v[196:199], v[52:55]\n"
    "s_nop 0\n"
    "v_mfma_f32_16x16x32_bf16 v[60:63], a[92:95], v[196:199], v[60:63]\n"
    "v_permlane32_swap_b32 v202, v218\n"
    "v_mfma_f32_16x16x32_bf16 v[56:59], a[92:95], v[192:195], v[56:59]\n"
    "s_waitcnt lgkmcnt(4)\n"
    "s_add_u32 m0, s95, 0x21000\n"
    "v_mfma_f32_16x16x32_bf16 v[64:67], a[96:99], v[192:195], v[64:67]\n"
    "v_permlane32_swap_b32 v203, v220\n"
    "v_add_f32 v202, v202, v218\n"
    "v_mfma_f32_16x16x32_bf16 v[68:71], a[96:99], v[196:199], v[68:71]\n"
    "s_mov_b32 s96, 28672\n"
    "v_mfma_f32_16x16x32_bf16 v[76:79], a[100:103], v[196:199], v[76:79]\n"
    "v_add_f32 v203, v203, v220\n"
    "v_mfma_f32_16x16x32_bf16 v[72:75], a[100:103], v[192:195], v[72:75]\n"
    "v_rcp_f32 v232, v202\n"
    "v_mfma_f32_16x16x32_bf16 v[80:83], a[104:107], v[192:195], v[80:83]\n"
    "buffer_load_dwordx4 v228, s[44:47], s96 offen offset:0 lds\n"
    "v_mfma_f32_16x16x32_bf16 v[84:87], a[104:107], v[196:199], v[84:87]\n"
    "v_log_f32 v214, v202\n"
    "v_mfma_f32_16x16x32_bf16 v[92:95], a[108:111], v[196:199], v[92:95]\n"
    "buffer_load_dwordx4 v229, s[44:47], s96 offen offset:1024 lds\n"
    "v_mfma_f32_16x16x32_bf16 v[88:91], a[108:111], v[192:195], v[88:91]\n"
    "s_waitcnt lgkmcnt(0)\n"
    "v_mfma_f32_16x16x32_bf16 v[96:99], a[112:115], v[192:195], v[96:99]\n"
    "v_rcp_f32 v234, v203\n"
    "v_mfma_f32_16x16x32_bf16 v[100:103], a[112:115], v[196:199], v[100:103]\n"
    "buffer_load_dwordx4 v230, s[44:47], s96 offen offset:2048 lds\n"
    "v_mfma_f32_16x16x32_bf16 v[108:111], a[116:119], v[196:199], v[108:111]\n"
    "v_log_f32 v215, v203\n"
    "v_mfma_f32_16x16x32_bf16 v[104:107], a[116:119], v[192:195], v[104:107]\n"
    "buffer_load_dwordx4 v231, s[44:47], s96 offen offset:3072 lds\n"
    "v_mfma_f32_16x16x32_bf16 v[112:115], a[120:123], v[192:195], v[112:115]\n"
    "v_mov_b32 v233, v232\n"
    "v_sub_f32 v214, v214, v204\n"
    "v_mfma_f32_16x16x32_bf16 v[116:119], a[120:123], v[196:199], v[116:119]\n"
    "v_mov_b32 v235, v234\n"
    "v_sub_f32 v215, v215, v205\n"
    "v_mfma_f32_16x16x32_bf16 v[124:127], a[124:127], v[196:199], v[124:127]\n"
    "v_mfma_f32_16x16x32_bf16 v[120:123], a[124:127], v[192:195], v[120:123]\n"
    "buffer_store_dword v214, v217, s[52:55], s82 offen offset:0 sc1\n"
    "buffer_store_dword v215, v217, s[52:55], s82 offen offset:256 sc1\n"
    "s_nop 1\n"
    "v_pk_mul_f32 v[240:241], v[0:1], v[232:233]\n"
    "v_pk_mul_f32 v[242:243], v[2:3], v[232:233]\n"
    "v_pk_mul_f32 v[244:245], v[8:9], v[232:233]\n"
    "v_pk_mul_f32 v[246:247], v[10:11], v[232:233]\n"
    "v_cvt_pk_bf16_f32 v128, v240, v241\n"
    "v_cvt_pk_bf16_f32 v129, v242, v243\n"
    "v_cvt_pk_bf16_f32 v130, v244, v245\n"
    "v_cvt_pk_bf16_f32 v131, v246, v247\n"
    "s_add_u32 s97, s81, 0x0\n"
    "buffer_store_dwordx4 v[128:131], v210, s[52:55], s97 offen offset:0 sc1\n"
    "v_pk_mul_f32 v[248:249], v[4:5], v[234:235]\n"
    "v_pk_mul_f32 v[250:251], v[6:7], v[234:235]\n"
    "v_pk_mul_f32 v[252:253], v[12:13], v[234:235]\n"
    "v_pk_mul_f32 v[254:255], v[14:15], v[234:235]\n"
    "v_cvt_pk_bf16_f32 v132, v248, v249\n"
    "v_cvt_pk_bf16_f32 v133, v250, v251\n"
    "v_cvt_pk_bf16_f32 v134, v252, v253\n"
    "v_cvt_pk_bf16_f32 v135, v254, v255\n"
    "buffer_store_dwordx4 v[132:135], v210, s[52:55], s97 offen offset:1024 sc1\n"
    "v_pk_mul_f32 v[240:241], v[16:17], v[232:233]\n"
    "v_pk_mul_f32 v[242:243], v[18:19], v[232:233]\n"
    "v_pk_mul_f32 v[244:245], v[24:25], v[232:233]\n"
    "v_pk_mul_f32 v[246:247], v[26:27], v[232:233]\n"
    "v_cvt_pk_bf16_f32 v136, v240, v241\n"
    "v_cvt_pk_bf16_f32 v137, v242, v243\n"
    "v_cvt_pk_bf16_f32 v138, v244, v245\n"
    "v_cvt_pk_bf16_f32 v139, v246, v247\n"
    "buffer_store_dwordx4 v[136:139], v210, s[52:55], s97 offen offset:2048 sc1\n"
    "v_pk_mul_f32 v[248:249], v[20:21], v[234:235]\n"
    "v_pk_mul_f32 v[250:251], v[22:23], v[234:235]\n"
    "v_pk_mul_f32 v[252:253], v[28:29], v[234:235]\n"
    "v_pk_mul_f32 v[254:255], v[30:31], v[234:235]\n"
    "v_cvt_pk_bf16_f32 v140, v248, v249\n"
    "v_cvt_pk_bf16_f32 v141, v250, v251\n"
    "v_cvt_pk_bf16_f32 v142, v252, v253\n"
    "v_cvt_pk_bf16_f32 v143, v254, v255\n"
    "buffer_store_dwordx4 v[140:143], v210, s[52:55], s97 offen offset:3072 sc1\n"
    "v_pk_mul_f32 v[240:241], v[32:33], v[232:233]\n"
    "v_pk_mul_f32 v[242:243], v[34:35], v[232:233]\n"
    "v_pk_mul_f32 v[244:245], v[40:41], v[232:233]\n"
    "v_pk_mul_f32 v[246:247], v[42:43], v[232:233]\n"
    "v_cvt_pk_bf16_f32 v144, v240, v241\n"
    "v_cvt_pk_bf16_f32 v145, v242, v243\n"
    "v_cvt_pk_bf16_f32 v146, v244, v245\n"
    "v_cvt_pk_bf16_f32 v147, v246, v247\n"
    "s_add_u32 s97, s81, 0x1000\n"
    "buffer_store_dwordx4 v[144:147], v210, s[52:55], s97 offen offset:0 sc1\n"
    "v_pk_mul_f32 v[248:249], v[36:37], v[234:235]\n"
    "v_pk_mul_f32 v[250:251], v[38:39], v[234:235]\n"
    "v_pk_mul_f32 v[252:253], v[44:45], v[234:235]\n"
    "v_pk_mul_f32 v[254:255], v[46:47], v[234:235]\n"
    "v_cvt_pk_bf16_f32 v148, v248, v249\n"
    "v_cvt_pk_bf16_f32 v149, v250, v251\n"
    "v_cvt_pk_bf16_f32 v150, v252, v253\n"
    "v_cvt_pk_bf16_f32 v151, v254, v255\n"
    "buffer_store_dwordx4 v[148:151], v210, s[52:55], s97 offen offset:1024 sc1\n"
    "v_pk_mul_f32 v[240:241], v[48:49], v[232:233]\n"
    "v_pk_mul_f32 v[242:243], v[50:51], v[232:233]\n"
    "v_pk_mul_f32 v[244:245], v[56:57], v[232:233]\n"
    "v_pk_mul_f32 v[246:247], v[58:59], v[232:233]\n"
    "v_cvt_pk_bf16_f32 v152, v240, v241\n"
    "v_cvt_pk_bf16_f32 v153, v242, v243\n"
    "v_cvt_pk_bf16_f32 v154, v244, v245\n"
    "v_cvt_pk_bf16_f32 v155, v246, v247\n"
    "buffer_store_dwordx4 v[152:155], v210, s[52:55], s97 offen offset:2048 sc1\n"
    "v_pk_mul_f32 v[248:249], v[52:53], v[234:235]\n"
    "v_pk_mul_f32 v[250:251], v[54:55], v[234:235]\n"
    "v_pk_mul_f32 v[252:253], v[60:61], v[234:235]\n"
    "v_pk_mul_f32 v[254:255], v[62:63], v[234:235]\n"
    "v_cvt_pk_bf16_f32 v156, v248, v249\n"
    "v_cvt_pk_bf16_f32 v157, v250, v251\n"
    "v_cvt_pk_bf16_f32 v158, v252, v253\n"
    "v_cvt_pk_bf16_f32 v159, v254, v255\n"
    "buffer_store_dwordx4 v[156:159], v210, s[52:55], s97 offen offset:3072 sc1\n"
    "v_pk_mul_f32 v[240:241], v[64:65], v[232:233]\n"
    "v_pk_mul_f32 v[242:243], v[66:67], v[232:233]\n"
    "v_pk_mul_f32 v[244:245], v[72:73], v[232:233]\n"
    "v_pk_mul_f32 v[246:247], v[74:75], v[232:233]\n"
    "v_cvt_pk_bf16_f32 v160, v240, v241\n"
    "v_cvt_pk_bf16_f32 v161, v242, v243\n"
    "v_cvt_pk_bf16_f32 v162, v244, v245\n"
    "v_cvt_pk_bf16_f32 v163, v246, v247\n"
    "s_add_u32 s97, s81, 0x2000\n"
    "buffer_store_dwordx4 v[160:163], v210, s[52:55], s97 offen offset:0 sc1\n"
    "v_pk_mul_f32 v[248:249], v[68:69], v[234:235]\n"
    "v_pk_mul_f32 v[250:251], v[70:71], v[234:235]\n"
    "v_pk_mul_f32 v[252:253], v[76:77], v[234:235]\n"
    "v_pk_mul_f32 v[254:255], v[78:79], v[234:235]\n"
    "v_cvt_pk_bf16_f32 v164, v248, v249\n"
    "v_cvt_pk_bf16_f32 v165, v250, v251\n"
    "v_cvt_pk_bf16_f32 v166, v252, v253\n"
    "v_cvt_pk_bf16_f32 v167, v254, v255\n"
    "buffer_store_dwordx4 v[164:167], v210, s[52:55], s97 offen offset:1024 sc1\n"
    "v_pk_mul_f32 v[240:241], v[80:81], v[232:233]\n"
    "v_pk_mul_f32 v[242:243], v[82:83], v[232:233]\n"
    "v_pk_mul_f32 v[244:245], v[88:89], v[232:233]\n"
    "v_pk_mul_f32 v[246:247], v[90:91], v[232:233]\n"
    "v_cvt_pk_bf16_f32 v168, v240, v241\n"
    "v_cvt_pk_bf16_f32 v169, v242, v243\n"
    "v_cvt_pk_bf16_f32 v170, v244, v245\n"
    "v_cvt_pk_bf16_f32 v171, v246, v247\n"
    "buffer_store_dwordx4 v[168:171], v210, s[52:55], s97 offen offset:2048 sc1\n"
    "v_pk_mul_f32 v[248:249], v[84:85], v[234:235]\n"
    "v_pk_mul_f32 v[250:251], v[86:87], v[234:235]\n"
    "v_pk_mul_f32 v[252:253], v[92:93], v[234:235]\n"
    "v_pk_mul_f32 v[254:255], v[94:95], v[234:235]\n"
    "v_cvt_pk_bf16_f32 v172, v248, v249\n"
    "v_cvt_pk_bf16_f32 v173, v250, v251\n"
    "v_cvt_pk_bf16_f32 v174, v252, v253\n"
    "v_cvt_pk_bf16_f32 v175, v254, v255\n"
    "buffer_store_dwordx4 v[172:175], v210, s[52:55], s97 offen offset:3072 sc1\n"
    "v_pk_mul_f32 v[240:241], v[96:97], v[232:233]\n"
    "v_pk_mul_f32 v[242:243], v[98:99], v[232:233]\n"
    "v_pk_mul_f32 v[244:245], v[104:105], v[232:233]\n"
    "v_pk_mul_f32 v[246:247], v[106:107], v[232:233]\n"
    "v_cvt_pk_bf16_f32 v176, v240, v241\n"
    "v_cvt_pk_bf16_f32 v177, v242, v243\n"
    "v_cvt_pk_bf16_f32 v178, v244, v245\n"
    "v_cvt_pk_bf16_f32 v179, v246, v247\n"
    "s_add_u32 s97, s81, 0x3000\n"
    "buffer_store_dwordx4 v[176:179], v210, s[52:55], s97 offen offset:0 sc1\n"
    "v_pk_mul_f32 v[248:249], v[100:101], v[234:235]\n"
    "v_pk_mul_f32 v[250:251], v[102:103], v[234:235]\n"
    "v_pk_mul_f32 v[252:253], v[108:109], v[234:235]\n"
    "v_pk_mul_f32 v[254:255], v[110:111], v[234:235]\n"
    "v_cvt_pk_bf16_f32 v180, v248, v249\n"
    "v_cvt_pk_bf16_f32 v181, v250, v251\n"
    "v_cvt_pk_bf16_f32 v182, v252, v253\n"
    "v_cvt_pk_bf16_f32 v183, v254, v255\n"
    "buffer_store_dwordx4 v[180:183], v210, s[52:55], s97 offen offset:1024 sc1\n"
    "v_pk_mul_f32 v[240:241], v[112:113], v[232:233]\n"
    "v_pk_mul_f32 v[242:243], v[114:115], v[232:233]\n"
    "v_pk_mul_f32 v[244:245], v[120:121], v[232:233]\n"
    "v_pk_mul_f32 v[246:247], v[122:123], v[232:233]\n"
    "v_cvt_pk_bf16_f32 v184, v240, v241\n"
    "v_cvt_pk_bf16_f32 v185, v242, v243\n"
    "v_cvt_pk_bf16_f32 v186, v244, v245\n"
    "v_cvt_pk_bf16_f32 v187, v246, v247\n"
    "buffer_store_dwordx4 v[184:187], v210, s[52:55], s97 offen offset:2048 sc1\n"
    "v_pk_mul_f32 v[248:249], v[116:117], v[234:235]\n"
    "v_pk_mul_f32 v[250:251], v[118:119], v[234:235]\n"
    "v_pk_mul_f32 v[252:253], v[124:125], v[234:235]\n"
    "v_pk_mul_f32 v[254:255], v[126:127], v[234:235]\n"
    "v_cvt_pk_bf16_f32 v188, v248, v249\n"
    "v_cvt_pk_bf16_f32 v189, v250, v251\n"
    "v_cvt_pk_bf16_f32 v190, v252, v253\n"
    "v_cvt_pk_bf16_f32 v191, v254, v255\n"
    "buffer_store_dwordx4 v[188:191], v210, s[52:55], s97 offen offset:3072 sc1\n"
    "v_accvgpr_read_b32 v128, a128\n"
    "v_accvgpr_read_b32 v129, a129\n"
    "v_accvgpr_read_b32 v130, a130\n"
    "v_accvgpr_read_b32 v131, a131\n"
    "v_accvgpr_read_b32 v132, a132\n"
    "s_add_u32 m0, s95, 0x0\n"
    "v_accvgpr_read_b32 v133, a133\n"
    "v_accvgpr_read_b32 v134, a134\n"
    "v_accvgpr_read_b32 v135, a135\n"
    "v_accvgpr_read_b32 v136, a136\n"
    "v_accvgpr_read_b32 v137, a137\n"
    "s_mov_b32 s96, 128\n"
    "v_accvgpr_read_b32 v138, a138\n"
    "v_accvgpr_read_b32 v139, a139\n"
    "v_accvgpr_read_b32 v140, a140\n"
    "v_accvgpr_read_b32 v141, a141\n"
    "v_accvgpr_read_b32 v142, a142\n"
    "buffer_load_dwordx4 v228, s[44:47], s96 offen offset:0 lds\n"
    "v_accvgpr_read_b32 v143, a143\n"
    "v_accvgpr_read_b32 v144, a144\n"
    "v_accvgpr_read_b32 v145, a145\n"
    "v_accvgpr_read_b32 v146, a146\n"
    "v_accvgpr_read_b32 v147, a147\n"
    "buffer_load_dwordx4 v229, s[44:47], s96 offen offset:1024 lds\n"
    "v_accvgpr_read_b32 v148, a148\n"
    "v_accvgpr_read_b32 v149, a149\n"
    "v_accvgpr_read_b32 v150, a150\n"
    "v_accvgpr_read_b32 v151, a151\n"
    "v_accvgpr_read_b32 v152, a152\n"
    "buffer_load_dwordx4 v230, s[44:47], s96 offen offset:2048 lds\n"
    "v_accvgpr_read_b32 v153, a153\n"
    "v_accvgpr_read_b32 v154, a154\n"
    "v_accvgpr_read_b32 v155, a155\n"
    "v_accvgpr_read_b32 v156, a156\n"
    "v_accvgpr_read_b32 v157, a157\n"
    "buffer_load_dwordx4 v231, s[44:47], s96 offen offset:3072 lds\n"
    "v_accvgpr_read_b32 v158, a158\n"
    "v_accvgpr_read_b32 v159, a159\n"
    "v_accvgpr_read_b32 v160, a160\n"
    "v_accvgpr_read_b32 v161, a161\n"
    "v_accvgpr_read_b32 v162, a162\n"
    "s_add_u32 m0, s95, 0x1000\n"
    "v_accvgpr_read_b32 v163, a163\n"
    "v_accvgpr_read_b32 v164, a164\n"
    "v_accvgpr_read_b32 v165, a165\n"
    "v_accvgpr_read_b32 v166, a166\n"
    "v_accvgpr_read_b32 v167, a167\n"
    "s_mov_b32 s96, 28800\n"
    "v_accvgpr_read_b32 v168, a168\n"
    "v_accvgpr_read_b32 v169, a169\n"
    "v_accvgpr_read_b32 v170, a170\n"
    "v_accvgpr_read_b32 v171, a171\n"
    "v_accvgpr_read_b32 v172, a172\n"
    "buffer_load_dwordx4 v228, s[44:47], s96 offen offset:0 lds\n"
    "v_accvgpr_read_b32 v173, a173\n"
    "v_accvgpr_read_b32 v174, a174\n"
    "v_accvgpr_read_b32 v175, a175\n"
    "v_accvgpr_read_b32 v176, a176\n"
    "v_accvgpr_read_b32 v177, a177\n"
    "buffer_load_dwordx4 v229, s[44:47], s96 offen offset:1024 lds\n"
    "v_accvgpr_read_b32 v178, a178\n"
    "v_accvgpr_read_b32 v179, a179\n"
    "v_accvgpr_read_b32 v180, a180\n"
    "v_accvgpr_read_b32 v181, a181\n"
    "v_accvgpr_read_b32 v182, a182\n"
    "buffer_load_dwordx4 v230, s[44:47], s96 offen offset:2048 lds\n"
    "v_accvgpr_read_b32 v183, a183\n"
    "v_accvgpr_read_b32 v184, a184\n"
    "v_accvgpr_read_b32 v185, a185\n"
    "v_accvgpr_read_b32 v186, a186\n"
    "v_accvgpr_read_b32 v187, a187\n"
    "buffer_load_dwordx4 v231, s[44:47], s96 offen offset:3072 lds\n"
    "v_accvgpr_read_b32 v188, a188\n"
    "v_accvgpr_read_b32 v189, a189\n"
    "v_accvgpr_read_b32 v190, a190\n"
    "v_accvgpr_read_b32 v191, a191\n"
    "s_waitcnt vmcnt(8)\n"
    "s_barrier\n"
    "v_mov_b32 v218, s34\n"
    "v_mov_b32 v219, s35\n"
    "v_lshlrev_b32 v216, 21, v210\n"
    "s_lshl_b32 s98, s56, 25\n"
    "s_add_u32 s98, s98, s83\n"
    "buffer_store_dwordx2 v[218:219], v216, s[52:55], s98 offen offset:0 sc1\n"
    "s_mov_b32 s72, s77\n"
    "s_branch OUTP_%=\n"
    "TAIL0_%=:\n"
    "v_exp_f32 v184, v152\n"
    "v_exp_f32 v185, v153\n"
    "v_exp_f32 v186, v154\n"
    "v_exp_f32 v187, v155\n"
    "v_exp_f32 v188, v156\n"
    "v_exp_f32 v189, v157\n"
    "s_waitcnt lgkmcnt(0)\n"
    "v_exp_f32 v190, v158\n"
    "v_exp_f32 v191, v159\n"
    "ds_read_b128 a[64:67], v206 offset:49152\n"
    "v_add_f32 v202, v202, v184\n"
    "ds_read_b128 a[68:71], v206 offset:50176\n"
    "v_add_f32 v203, v203, v188\n"
    "ds_read_b128 a[72:75], v206 offset:51200\n"
    "v_add_f32 v202, v202, v185\n"
    "ds_read_b128 a[76:79], v206 offset:52224\n"
    "v_add_f32 v203, v203, v189\n"
    "ds_read_b128 a[80:83], v206 offset:53248\n"
    "v_add_f32 v202, v202, v186\n"
    "ds_read_b128 a[84:87], v206 offset:54272\n"
    "v_add_f32 v203, v203, v190\n"
    "ds_read_b128 a[88:91], v206 offset:55296\n"
    "v_add_f32 v202, v202, v187\n"
    "ds_read_b128 a[92:95], v206 offset:56320\n"
    "v_add_f32 v203, v203, v191\n"
    "ds_read_b128 a[96:99], v206 offset:57344\n"
    "v_cvt_pk_bf16_f32 v194, v184, v185\n"
    "ds_read_b128 a[100:103], v206 offset:58368\n"
    "v_cvt_pk_bf16_f32 v195, v186, v187\n"
    "ds_read_b128 a[104:107], v206 offset:59392\n"
    "v_cvt_pk_bf16_f32 v198, v188, v189\n"
    "ds_read_b128 a[108:111], v206 offset:60416\n"
    "v_cvt_pk_bf16_f32 v199, v190, v191\n"
    "v_max_f32 v212, v202, v203\n"
    "v_cmp_nle_f32 vcc, v212, v213\n"
    "s_cbranch_vccnz RARET0_%=\n"
    "BACKT0_%=:\n"
    "s_waitcnt vmcnt(0)\n"
    "s_lshl_b32 s95, s56, 13\n"
    "s_barrier\n"
    "s_waitcnt lgkmcnt(8)\n"
    "v_mfma_f32_16x16x32_bf16 v[0:3], a[64:67], v[192:195], v[0:3]\n"
    "ds_read_b128 a[112:115], v206 offset:61440\n"
    "s_add_u32 m0, s95, 0x20000\n"
    "v_mfma_f32_16x16x32_bf16 v[4:7], a[64:67], v[196:199], v[4:7]\n"
    "v_mov_b32 v218, v202\n"
    "v_mfma_f32_16x16x32_bf16 v[12:15], a[68:71], v[196:199], v[12:15]\n"
    "ds_read_b128 a[116:119], v206 offset:62464\n"
    "s_mov_b32 s96, 0\n"
    "v_mfma_f32_16x16x32_bf16 v[8:11], a[68:71], v[192:195], v[8:11]\n"
    "v_mov_b32 v220, v203\n"
    "s_nop 0\n"
    "v_mfma_f32_16x16x32_bf16 v[16:19], a[72:75], v[192:195], v[16:19]\n"
    "ds_read_b128 a[120:123], v206 offset:63488\n"
    "v_permlane16_swap_b32 v202, v218\n"
    "v_mfma_f32_16x16x32_bf16 v[20:23], a[72:75], v[196:199], v[20:23]\n"
    "buffer_load_dwordx4 v228, s[44:47], s96 offen offset:0 lds\n"
    "v_mfma_f32_16x16x32_bf16 v[28:31], a[76:79], v[196:199], v[28:31]\n"
    "ds_read_b128 a[124:127], v206 offset:64512\n"
    "v_permlane16_swap_b32 v203, v220\n"
    "v_mfma_f32_16x16x32_bf16 v[24:27], a[76:79], v[192:195], v[24:27]\n"
    "s_waitcnt lgkmcnt(8)\n"
    "buffer_load_dwordx4 v229, s[44:47], s96 offen offset:1024 lds\n"
    "v_mfma_f32_16x16x32_bf16 v[32:35], a[80:83], v[192:195], v[32:35]\n"
    "v_add_f32 v202, v202, v218\n"
    "v_add_f32 v203, v203, v220\n"
    "v_mfma_f32_16x16x32_bf16 v[36:39], a[80:83], v[196:199], v[36:39]\n"
    "v_mfma_f32_16x16x32_bf16 v[44:47], a[84:87], v[196:199], v[44:47]\n"
    "buffer_load_dwordx4 v230, s[44:47], s96 offen offset:2048 lds\n"
    "v_mfma_f32_16x16x32_bf16 v[40:43], a[84:87], v[192:195], v[40:43]\n"
    "v_mov_b32 v218, v202\n"
    "v_mov_b32 v220, v203\n"
    "v_mfma_f32_16x16x32_bf16 v[48:51], a[88:91], v[192:195], v[48:51]\n"
    "buffer_load_dwordx4 v231, s[44:47], s96 offen offset:3072 lds\n"
    "v_mfma_f32_16x16x32_bf16 v[52:55], a[88:91], v[196:199], v[52:55]\n"
    "s_nop 0\n"
    "v_mfma_f32_16x16x32_bf16 v[60:63], a[92:95], v[196:199], v[60:63]\n"
    "v_permlane32_swap_b32 v202, v218\n"
    "v_mfma_f32_16x16x32_bf16 v[56:59], a[92:95], v[192:195], v[56:59]\n"
    "s_waitcnt lgkmcnt(4)\n"
    "s_add_u32 m0, s95, 0x21000\n"
    "v_mfma_f32_16x16x32_bf16 v[64:67], a[96:99], v[192:195], v[64:67]\n"
    "v_permlane32_swap_b32 v203, v220\n"
    "v_add_f32 v202, v202, v218\n"
    "v_mfma_f32_16x16x32_bf16 v[68:71], a[96:99], v[196:199], v[68:71]\n"
    "s_mov_b32 s96, 28672\n"
    "v_mfma_f32_16x16x32_bf16 v[76:79], a[100:103], v[196:199], v[76:79]\n"
    "v_add_f32 v203, v203, v220\n"
    "v_mfma_f32_16x16x32_bf16 v[72:75], a[100:103], v[192:195], v[72:75]\n"
    "v_rcp_f32 v232, v202\n"
    "v_mfma_f32_16x16x32_bf16 v[80:83], a[104:107], v[192:195], v[80:83]\n"
    "buffer_load_dwordx4 v228, s[44:47], s96 offen offset:0 lds\n"
    "v_mfma_f32_16x16x32_bf16 v[84:87], a[104:107], v[196:199], v[84:87]\n"
    "v_log_f32 v214, v202\n"
    "v_mfma_f32_16x16x32_bf16 v[92:95], a[108:111], v[196:199], v[92:95]\n"
    "buffer_load_dwordx4 v229, s[44:47], s96 offen offset:1024 lds\n"
    "v_mfma_f32_16x16x32_bf16 v[88:91], a[108:111], v[192:195], v[88:91]\n"
    "s_waitcnt lgkmcnt(0)\n"
    "v_mfma_f32_16x16x32_bf16 v[96:99], a[112:115], v[192:195], v[96:99]\n"
    "v_rcp_f32 v234, v203\n"
    "v_mfma_f32_16x16x32_bf16 v[100:103], a[112:115], v[196:199], v[100:103]\n"
    "buffer_load_dwordx4 v230, s[44:47], s96 offen offset:2048 lds\n"
    "v_mfma_f32_16x16x32_bf16 v[108:111], a[116:119], v[196:199], v[108:111]\n"
    "v_log_f32 v215, v203\n"
    "v_mfma_f32_16x16x32_bf16 v[104:107], a[116:119], v[192:195], v[104:107]\n"
    "buffer_load_dwordx4 v231, s[44:47], s96 offen offset:3072 lds\n"
    "v_mfma_f32_16x16x32_bf16 v[112:115], a[120:123], v[192:195], v[112:115]\n"
    "v_mov_b32 v233, v232\n"
    "v_sub_f32 v214, v214, v204\n"
    "v_mfma_f32_16x16x32_bf16 v[116:119], a[120:123], v[196:199], v[116:119]\n"
    "v_mov_b32 v235, v234\n"
    "v_sub_f32 v215, v215, v205\n"
    "v_mfma_f32_16x16x32_bf16 v[124:127], a[124:127], v[196:199], v[124:127]\n"
    "v_mfma_f32_16x16x32_bf16 v[120:123], a[124:127], v[192:195], v[120:123]\n"
    "s_nop 3\n"
    "v_pk_mul_f32 v[240:241], v[0:1], v[232:233]\n"
    "v_pk_mul_f32 v[242:243], v[2:3], v[232:233]\n"
    "v_pk_mul_f32 v[244:245], v[8:9], v[232:233]\n"
    "v_pk_mul_f32 v[246:247], v[10:11], v[232:233]\n"
    "v_cvt_pk_bf16_f32 v128, v240, v241\n"
    "v_cvt_pk_bf16_f32 v129, v242, v243\n"
    "v_cvt_pk_bf16_f32 v130, v244, v245\n"
    "v_cvt_pk_bf16_f32 v131, v246, v247\n"
    "v_pk_mul_f32 v[248:249], v[4:5], v[234:235]\n"
    "v_pk_mul_f32 v[250:251], v[6:7], v[234:235]\n"
    "s_add_u32 m0, s95, 0x0\n"
    "v_pk_mul_f32 v[252:253], v[12:13], v[234:235]\n"
    "v_pk_mul_f32 v[254:255], v[14:15], v[234:235]\n"
    "v_cvt_pk_bf16_f32 v132, v248, v249\n"
    "v_cvt_pk_bf16_f32 v133, v250, v251\n"
    "v_cvt_pk_bf16_f32 v134, v252, v253\n"
    "v_cvt_pk_bf16_f32 v135, v254, v255\n"
    "v_pk_mul_f32 v[240:241], v[16:17], v[232:233]\n"
    "v_pk_mul_f32 v[242:243], v[18:19], v[232:233]\n"
    "v_pk_mul_f32 v[244:245], v[24:25], v[232:233]\n"
    "v_pk_mul_f32 v[246:247], v[26:27], v[232:233]\n"
    "s_mov_b32 s96, 128\n"
    "v_cvt_pk_bf16_f32 v136, v240, v241\n"
    "v_cvt_pk_bf16_f32 v137, v242, v243\n"
    "v_cvt_pk_bf16_f32 v138, v244, v245\n"
    "v_cvt_pk_bf16_f32 v139, v246, v247\n"
    "v_pk_mul_f32 v[248:249], v[20:21], v[234:235]\n"
    "v_pk_mul_f32 v[250:251], v[22:23], v[234:235]\n"
    "v_pk_mul_f32 v[252:253], v[28:29], v[234:235]\n"
    "v_pk_mul_f32 v[254:255], v[30:31], v[234:235]\n"
    "v_cvt_pk_bf16_f32 v140, v248, v249\n"
    "v_cvt_pk_bf16_f32 v141, v250, v251\n"
    "buffer_load_dwordx4 v228, s[44:47], s96 offen offset:0 lds\n"
    "v_cvt_pk_bf16_f32 v142, v252, v253\n"
    "v_cvt_pk_bf16_f32 v143, v254, v255\n"
    "v_pk_mul_f32 v[240:241], v[32:33], v[232:233]\n"
    "v_pk_mul_f32 v[242:243], v[34:35], v[232:233]\n"
    "v_pk_mul_f32 v[244:245], v[40:41], v[232:233]\n"
    "v_pk_mul_f32 v[246:247], v[42:43], v[232:233]\n"
    "v_cvt_pk_bf16_f32 v144, v240, v241\n"
    "v_cvt_pk_bf16_f32 v145, v242, v243\n"
    "v_cvt_pk_bf16_f32 v146, v244, v245\n"
    "v_cvt_pk_bf16_f32 v147, v246, v247\n"
    "buffer_load_dwordx4 v229, s[44:47], s96 offen offset:1024 lds\n"
    "v_pk_mul_f32 v[248:249], v[36:37], v[234:235]\n"
    "v_pk_mul_f32 v[250:251], v[38:39], v[234:235]\n"
    "v_pk_mul_f32 v[252:253], v[44:45], v[234:235]\n"
    "v_pk_mul_f32 v[254:255], v[46:47], v[234:235]\n"
    "v_cvt_pk_bf16_f32 v148, v248, v249\n"
    "v_cvt_pk_bf16_f32 v149, v250, v251\n"
    "v_cvt_pk_bf16_f32 v150, v252, v253\n"
    "v_cvt_pk_bf16_f32 v151, v254, v255\n"
    "v_pk_mul_f32 v[240:241], v[48:49], v[232:233]\n"
    "v_pk_mul_f32 v[242:243], v[50:51], v[232:233]\n"
    "buffer_load_dwordx4 v230, s[44:47], s96 offen offset:2048 lds\n"
    "v_pk_mul_f32 v[244:245], v[56:57], v[232:233]\n"
    "v_pk_mul_f32 v[246:247], v[58:59], v[232:233]\n"
    "v_cvt_pk_bf16_f32 v152, v240, v241\n"
    "v_cvt_pk_bf16_f32 v153, v242, v243\n"
    "v_cvt_pk_bf16_f32 v154, v244, v245\n"
    "v_cvt_pk_bf16_f32 v155, v246, v247\n"
    "v_pk_mul_f32 v[248:249], v[52:53], v[234:235]\n"
    "v_pk_mul_f32 v[250:251], v[54:55], v[234:235]\n"
    "v_pk_mul_f32 v[252:253], v[60:61], v[234:235]\n"
    "v_pk_mul_f32 v[254:255], v[62:63], v[234:235]\n"
    "buffer_load_dwordx4 v231, s[44:47], s96 offen offset:3072 lds\n"
    "v_cvt_pk_bf16_f32 v156, v248, v249\n"
    "v_cvt_pk_bf16_f32 v157, v250, v251\n"
    "v_cvt_pk_bf16_f32 v158, v252, v253\n"
    "v_cvt_pk_bf16_f32 v159, v254, v255\n"
    "v_pk_mul_f32 v[240:241], v[64:65], v[232:233]\n"
    "v_pk_mul_f32 v[242:243], v[66:67], v[232:233]\n"
    "v_pk_mul_f32 v[244:245], v[72:73], v[232:233]\n"
    "v_pk_mul_f32 v[246:247], v[74:75], v[232:233]\n"
    "v_cvt_pk_bf16_f32 v160, v240, v241\n"
    "s_add_u32 m0, s95, 0x1000\n"
    "v_cvt_pk_bf16_f32 v161, v242, v243\n"
    "v_cvt_pk_bf16_f32 v162, v244, v245\n"
    "v_cvt_pk_bf16_f32 v163, v246, v247\n"
    "v_pk_mul_f32 v[248:249], v[68:69], v[234:235]\n"
    "v_pk_mul_f32 v[250:251], v[70:71], v[234:235]\n"
    "v_pk_mul_f32 v[252:253], v[76:77], v[234:235]\n"
    "v_pk_mul_f32 v[254:255], v[78:79], v[234:235]\n"
    "v_cvt_pk_bf16_f32 v164, v248, v249\n"
    "v_cvt_pk_bf16_f32 v165, v250, v251\n"
    "v_cvt_pk_bf16_f32 v166, v252, v253\n"
    "s_mov_b32 s96, 28800\n"
    "v_cvt_pk_bf16_f32 v167, v254, v255\n"
    "v_pk_mul_f32 v[240:241], v[80:81], v[232:233]\n"
    "v_pk_mul_f32 v[242:243], v[82:83], v[232:233]\n"
    "v_pk_mul_f32 v[244:245], v[88:89], v[232:233]\n"
    "v_pk_mul_f32 v[246:247], v[90:91], v[232:233]\n"
    "v_cvt_pk_bf16_f32 v168, v240, v241\n"
    "v_cvt_pk_bf16_f32 v169, v242, v243\n"
    "v_cvt_pk_bf16_f32 v170, v244, v245\n"
    "v_cvt_pk_bf16_f32 v171, v246, v247\n"
    "v_pk_mul_f32 v[248:249], v[84:85], v[234:235]\n"
    "buffer_load_dwordx4 v228, s[44:47], s96 offen offset:0 lds\n"
    "v_pk_mul_f32 v[250:251], v[86:87], v[234:235]\n"
    "v_pk_mul_f32 v[252:253], v[92:93], v[234:235]\n"
    "v_pk_mul_f32 v[254:255], v[94:95], v[234:235]\n"
    "v_cvt_pk_bf16_f32 v172, v248, v249\n"
    "v_cvt_pk_bf16_f32 v173, v250, v251\n"
    "v_cvt_pk_bf16_f32 v174, v252, v253\n"
    "v_cvt_pk_bf16_f32 v175, v254, v255\n"
    "v_pk_mul_f32 v[240:241], v[96:97], v[232:233]\n"
    "v_pk_mul_f32 v[242:243], v[98:99], v[232:233]\n"
    "v_pk_mul_f32 v[244:245], v[104:105], v[232:233]\n"
    "buffer_load_dwordx4 v229, s[44:47], s96 offen offset:1024 lds\n"
    "v_pk_mul_f32 v[246:247], v[106:107], v[232:233]\n"
    "v_cvt_pk_bf16_f32 v176, v240, v241\n"
    "v_cvt_pk_bf16_f32 v177, v242, v243\n"
    "v_cvt_pk_bf16_f32 v178, v244, v245\n"
    "v_cvt_pk_bf16_f32 v179, v246, v247\n"
    "v_pk_mul_f32 v[248:249], v[100:101], v[234:235]\n"
    "v_pk_mul_f32 v[250:251], v[102:103], v[234:235]\n"
    "v_pk_mul_f32 v[252:253], v[108:109], v[234:235]\n"
    "v_pk_mul_f32 v[254:255], v[110:111], v[234:235]\n"
    "v_cvt_pk_bf16_f32 v180, v248, v249\n"
    "buffer_load_dwordx4 v230, s[44:47], s96 offen offset:2048 lds\n"
    "v_cvt_pk_bf16_f32 v181, v250, v251\n"
    "v_cvt_pk_bf16_f32 v182, v252, v253\n"
    "v_cvt_pk_bf16_f32 v183, v254, v255\n"
    "v_pk_mul_f32 v[240:241], v[112:113], v[232:233]\n"
    "v_pk_mul_f32 v[242:243], v[114:115], v[232:233]\n"
    "v_pk_mul_f32 v[244:245], v[120:121], v[232:233]\n"
    "v_pk_mul_f32 v[246:247], v[122:123], v[232:233]\n"
    "v_cvt_pk_bf16_f32 v184, v240, v241\n"
    "v_cvt_pk_bf16_f32 v185, v242, v243\n"
    "v_cvt_pk_bf16_f32 v186, v244, v245\n"
    "buffer_load_dwordx4 v231, s[44:47], s96 offen offset:3072 lds\n"
    "v_cvt_pk_bf16_f32 v187, v246, v247\n"
    "v_pk_mul_f32 v[248:249], v[116:117], v[234:235]\n"
    "v_pk_mul_f32 v[250:251], v[118:119], v[234:235]\n"
    "v_pk_mul_f32 v[252:253], v[124:125], v[234:235]\n"
    "v_pk_mul_f32 v[254:255], v[126:127], v[234:235]\n"
    "v_cvt_pk_bf16_f32 v188, v248, v249\n"
    "v_cvt_pk_bf16_f32 v189, v250, v251\n"
    "v_cvt_pk_bf16_f32 v190, v252, v253\n"
    "v_cvt_pk_bf16_f32 v191, v254, v255\n"
    "s_branch OUTP_%=\n"
    "TAIL2_%=:\n"
    "buffer_load_dwordx2 v[238:239], v222, s[52:55], s83 offen sc1\n"
    "v_exp_f32 v184, v152\n"
    "v_exp_f32 v185, v153\n"
    "v_exp_f32 v186, v154\n"
    "v_exp_f32 v187, v155\n"
    "v_exp_f32 v188, v156\n"
    "v_exp_f32 v189, v157\n"
    "s_waitcnt lgkmcnt(0)\n"
    "v_exp_f32 v190, v158\n"
    "v_exp_f32 v191, v159\n"
    "ds_read_b128 a[64:67], v206 offset:49152\n"
    "v_add_f32 v202, v202, v184\n"
    "ds_read_b128 a[68:71], v206 offset:50176\n"
    "v_add_f32 v203, v203, v188\n"
    "ds_read_b128 a[72:75], v206 offset:51200\n"
    "v_add_f32 v202, v202, v185\n"
    "ds_read_b128 a[76:79], v206 offset:52224\n"
    "v_add_f32 v203, v203, v189\n"
    "ds_read_b128 a[80:83], v206 offset:53248\n"
    "v_add_f32 v202, v202, v186\n"
    "ds_read_b128 a[84:87], v206 offset:54272\n"
    "v_add_f32 v203, v203, v190\n"
    "ds_read_b128 a[88:91], v206 offset:55296\n"
    "v_add_f32 v202, v202, v187\n"
    "ds_read_b128 a[92:95], v206 offset:56320\n"
    "v_add_f32 v203, v203, v191\n"
    "ds_read_b128 a[96:99], v206 offset:57344\n"
    "v_cvt_pk_bf16_f32 v194, v184, v185\n"
    "ds_read_b128 a[100:103], v206 offset:58368\n"
    "v_cvt_pk_bf16_f32 v195, v186, v187\n"
    "ds_read_b128 a[104:107], v206 offset:59392\n"
    "v_cvt_pk_bf16_f32 v198, v188, v189\n"
    "ds_read_b128 a[108:111], v206 offset:60416\n"
    "v_cvt_pk_bf16_f32 v199, v190, v191\n"
    "v_max_f32 v212, v202, v203\n"
    "v_cmp_nle_f32 vcc, v212, v213\n"
    "s_cbranch_vccnz RARET2_%=\n"
    "BACKT2_%=:\n"
    "s_waitcnt vmcnt(0)\n"
    "v_cmp_eq_u64 vcc, s[34:35], v[238:239]\n"
    "s_nop 0\n"
    "s_cmp_eq_u64 vcc, exec\n"
    "s_cbranch_scc0 POLL2_%=\n"
    "GOT2_%=:\n"
    "buffer_load_dword v236, v217, s[52:55], s82 offen offset:0 sc1\n"
    "buffer_load_dword v238, v217, s[52:55], s82 offen offset:256 sc1\n"
    "s_lshl_b32 s95, s56, 13\n"
    "s_barrier\n"
    "s_waitcnt lgkmcnt(8)\n"
    "v_mfma_f32_16x16x32_bf16 v[0:3], a[64:67], v[192:195], v[0:3]\n"
    "ds_read_b128 a[112:115], v206 offset:61440\n"
    "s_add_u32 s97, s81, 0x0\n"
    "v_mfma_f32_16x16x32_bf16 v[4:7], a[64:67], v[196:199], v[4:7]\n"
    "buffer_load_dwordx4 v[128:131], v210, s[52:55], s97 offen offset:0 sc1\n"
    "v_mov_b32 v218, v202\n"
    "v_mfma_f32_16x16x32_bf16 v[12:15], a[68:71], v[196:199], v[12:15]\n"
    "ds_read_b128 a[116:119], v206 offset:62464\n"
    "v_mov_b32 v220, v203\n"
    "v_mfma_f32_16x16x32_bf16 v[8:11], a[68:71], v[192:195], v[8:11]\n"
    "buffer_load_dwordx4 v[132:135], v210, s[52:55], s97 offen offset:1024 sc1\n"
    "s_nop 0\n"
    "v_mfma_f32_16x16x32_bf16 v[16:19], a[72:75], v[192:195], v[16:19]\n"
    "ds_read_b128 a[120:123], v206 offset:63488\n"
    "v_permlane16_swap_b32 v202, v218\n"
    "v_mfma_f32_16x16x32_bf16 v[20:23], a[72:75], v[196:199], v[20:23]\n"
    "buffer_load_dwordx4 v[136:139], v210, s[52:55], s97 offen offset:2048 sc1\n"
    "v_mfma_f32_16x16x32_bf16 v[28:31], a[76:79], v[196:199], v[28:31]\n"
    "ds_read_b128 a[124:127], v206 offset:64512\n"
    "v_permlane16_swap_b32 v203, v220\n"
    "v_mfma_f32_16x16x32_bf16 v[24:27], a[76:79], v[192:195], v[24:27]\n"
    "s_waitcnt lgkmcnt(8)\n"
    "buffer_load_dwordx4 v[140:143], v210, s[52:55], s97 offen offset:3072 sc1\n"
    "v_add_f32 v202, v202, v218\n"
    "v_mfma_f32_16x16x32_bf16 v[32:35], a[80:83], v[192:195], v[32:35]\n"
    "s_add_u32 s97, s81, 0x1000\n"
    "v_add_f32 v203, v203, v220\n"
    "v_mfma_f32_16x16x32_bf16 v[36:39], a[80:83], v[196:199], v[36:39]\n"
    "buffer_load_dwordx4 v[144:147], v210, s[52:55], s97 offen offset:0 sc1\n"
    "v_mfma_f32_16x16x32_bf16 v[44:47], a[84:87], v[196:199], v[44:47]\n"
    "buffer_load_dwordx4 v[148:151], v210, s[52:55], s97 offen offset:1024 sc1\n"
    "v_mov_b32 v218, v202\n"
    "v_mfma_f32_16x16x32_bf16 v[40:43], a[84:87], v[192:195], v[40:43]\n"
    "v_mov_b32 v220, v203\n"
    "v_mfma_f32_16x16x32_bf16 v[48:51], a[88:91], v[192:195], v[48:51]\n"
    "buffer_load_dwordx4 v[152:155], v210, s[52:55], s97 offen offset:2048 sc1\n"
    "s_nop 0\n"
    "v_mfma_f32_16x16x32_bf16 v[52:55], a[88:91], v[196:199], v[52:55]\n"
    "buffer_load_dwordx4 v[156:159], v210, s[52:55], s97 offen offset:3072 sc1\n"
    "v_mfma_f32_16x16x32_bf16 v[60:63], a[92:95], v[196:199], v[60:63]\n"
    "v_permlane32_swap_b32 v202, v218\n"
    "v_mfma_f32_16x16x32_bf16 v[56:59], a[92:95], v[192:195], v[56:59]\n"
    "s_waitcnt lgkmcnt(4)\n"
    "s_add_u32 s97, s81, 0x2000\n"
    "v_permlane32_swap_b32 v203, v220\n"
    "v_mfma_f32_16x16x32_bf16 v[64:67], a[96:99], v[192:195], v[64:67]\n"
    "buffer_load_dwordx4 v[160:163], v210, s[52:55], s97 offen offset:0 sc1\n"
    "v_add_f32 v202, v202, v218\n"
    "v_mfma_f32_16x16x32_bf16 v[68:71], a[96:99], v[196:199], v[68:71]\n"
    "v_mfma_f32_16x16x32_bf16 v[76:79], a[100:103], v[196:199], v[76:79]\n"
    "buffer_load_dwordx4 v[164:167], v210, s[52:55], s97 offen offset:1024 sc1\n"
    "v_add_f32 v203, v203, v220\n"
    "v_mfma_f32_16x16x32_bf16 v[72:75], a[100:103], v[192:195], v[72:75]\n"
    "buffer_load_dwordx4 v[168:171], v210, s[52:55], s97 offen offset:2048 sc1\n"
    "v_mfma_f32_16x16x32_bf16 v[80:83], a[104:107], v[192:195], v[80:83]\n"
    "v_rcp_f32 v232, v202\n"
    "v_mfma_f32_16x16x32_bf16 v[84:87], a[104:107], v[196:199], v[84:87]\n"
    "buffer_load_dwordx4 v[172:175], v210, s[52:55], s97 offen offset:3072 sc1\n"
    "v_mfma_f32_16x16x32_bf16 v[92:95], a[108:111], v[196:199], v[92:95]\n"
    "s_add_u32 s97, s81, 0x3000\n"
    "v_log_f32 v214, v202\n"
    "v_mfma_f32_16x16x32_bf16 v[88:91], a[108:111], v[192:195], v[88:91]\n"
    "s_waitcnt lgkmcnt(0)\n"
    "v_rcp_f32 v234, v203\n"
    "v_mfma_f32_16x16x32_bf16 v[96:99], a[112:115], v[192:195], v[96:99]\n"
    "buffer_load_dwordx4 v[176:179], v210, s[52:55], s97 offen offset:0 sc1\n"
    "v_mfma_f32_16x16x32_bf16 v[100:103], a[112:115], v[196:199], v[100:103]\n"
    "buffer_load_dwordx4 v[180:183], v210, s[52:55], s97 offen offset:1024 sc1\n"
    "v_mfma_f32_16x16x32_bf16 v[108:111], a[116:119], v[196:199], v[108:111]\n"
    "v_log_f32 v215, v203\n"
    "v_mov_b32 v233, v232\n"
    "v_mfma_f32_16x16x32_bf16 v[104:107], a[116:119], v[192:195], v[104:107]\n"
    "buffer_load_dwordx4 v[184:187], v210, s[52:55], s97 offen offset:2048 sc1\n"
    "v_sub_f32 v214, v214, v204\n"
    "v_mfma_f32_16x16x32_bf16 v[112:115], a[120:123], v[192:195], v[112:115]\n"
    "buffer_load_dwordx4 v[188:191], v210, s[52:55], s97 offen offset:3072 sc1\n"
    "v_mov_b32 v235, v234\n"
    "v_mfma_f32_16x16x32_bf16 v[116:119], a[120:123], v[196:199], v[116:119]\n"
    "v_sub_f32 v215, v215, v205\n"
    "v_mfma_f32_16x16x32_bf16 v[124:127], a[124:127], v[196:199], v[124:127]\n"
    "v_mfma_f32_16x16x32_bf16 v[120:123], a[124:127], v[192:195], v[120:123]\n"
    "s_waitcnt vmcnt(16)\n"
    "v_max_f32 v218, v214, v236\n"
    "v_sub_f32 v214, v214, v218\n"
    "v_sub_f32 v236, v236, v218\n"
    "v_exp_f32 v214, v214\n"
    "v_exp_f32 v236, v236\n"
    "v_max_f32 v219, v215, v238\n"
    "v_sub_f32 v215, v215, v219\n"
    "v_sub_f32 v238, v238, v219\n"
    "v_exp_f32 v215, v215\n"
    "v_exp_f32 v238, v238\n"
    "v_add_f32 v218, v214, v236\n"
    "v_add_f32 v219, v215, v238\n"
    "v_rcp_f32 v218, v218\n"
    "v_rcp_f32 v219, v219\n"
    "s_nop 0\n"
    "v_mul_f32 v214, v214, v218\n"
    "v_mul_f32 v236, v236, v218\n"
    "v_mul_f32 v232, v232, v214\n"
    "v_mov_b32 v237, v236\n"
    "v_mov_b32 v233, v232\n"
    "v_mul_f32 v215, v215, v219\n"
    "v_mul_f32 v238, v238, v219\n"
    "v_mul_f32 v234, v234, v215\n"
    "v_mov_b32 v239, v238\n"
    "v_mov_b32 v235, v234\n"
    "s_waitcnt vmcnt(15)\n"
    "v_lshlrev_b32 v240, 16, v128\n"
    "v_and_b32 v241, 0xffff0000, v128\n"
    "v_lshlrev_b32 v242, 16, v129\n"
    "v_and_b32 v243, 0xffff0000, v129\n"
    "v_lshlrev_b32 v244, 16, v130\n"
    "v_and_b32 v245, 0xffff0000, v130\n"
    "v_lshlrev_b32 v246, 16, v131\n"
    "v_and_b32 v247, 0xffff0000, v131\n"
    "v_pk_mul_f32 v[240:241], v[240:241], v[236:237]\n"
    "v_pk_mul_f32 v[242:243], v[242:243], v[236:237]\n"
    "v_pk_mul_f32 v[244:245], v[244:245], v[236:237]\n"
    "v_pk_mul_f32 v[246:247], v[246:247], v[236:237]\n"
    "v_pk_fma_f32 v[240:241], v[0:1], v[232:233], v[240:241]\n"
    "s_add_u32 m0, s95, 0x20000\n"
    "v_pk_fma_f32 v[242:243], v[2:3], v[232:233], v[242:243]\n"
    "v_pk_fma_f32 v[244:245], v[8:9], v[232:233], v[244:245]\n"
    "v_pk_fma_f32 v[246:247], v[10:11], v[232:233], v[246:247]\n"
    "v_cvt_pk_bf16_f32 v128, v240, v241\n"
    "v_cvt_pk_bf16_f32 v129, v242, v243\n"
    "v_cvt_pk_bf16_f32 v130, v244, v245\n"
    "v_cvt_pk_bf16_f32 v131, v246, v247\n"
    "s_waitcnt vmcnt(14)\n"
    "v_lshlrev_b32 v248, 16, v132\n"
    "v_and_b32 v249, 0xffff0000, v132\n"
    "v_lshlrev_b32 v250, 16, v133\n"
    "v_and_b32 v251, 0xffff0000, v133\n"
    "v_lshlrev_b32 v252, 16, v134\n"
    "s_mov_b32 s96, 0\n"
    "v_and_b32 v253, 0xffff0000, v134\n"
    "v_lshlrev_b32 v254, 16, v135\n"
    "v_and_b32 v255, 0xffff0000, v135\n"
    "v_pk_mul_f32 v[248:249], v[248:249], v[238:239]\n"
    "v_pk_mul_f32 v[250:251], v[250:251], v[238:239]\n"
    "v_pk_mul_f32 v[252:253], v[252:253], v[238:239]\n"
    "v_pk_mul_f32 v[254:255], v[254:255], v[238:239]\n"
    "v_pk_fma_f32 v[248:249], v[4:5], v[234:235], v[248:249]\n"
    "v_pk_fma_f32 v[250:251], v[6:7], v[234:235], v[250:251]\n"
    "v_pk_fma_f32 v[252:253], v[12:13], v[234:235], v[252:253]\n"
    "v_pk_fma_f32 v[254:255], v[14:15], v[234:235], v[254:255]\n"
    "v_cvt_pk_bf16_f32 v132, v248, v249\n"
    "v_cvt_pk_bf16_f32 v133, v250, v251\n"
    "v_cvt_pk_bf16_f32 v134, v252, v253\n"
    "buffer_load_dwordx4 v228, s[44:47], s96 offen offset:0 lds\n"
    "v_cvt_pk_bf16_f32 v135, v254, v255\n"
    "s_waitcnt vmcnt(14)\n"
    "v_lshlrev_b32 v240, 16, v136\n"
    "v_and_b32 v241, 0xffff0000, v136\n"
    "v_lshlrev_b32 v242, 16, v137\n"
    "v_and_b32 v243, 0xffff0000, v137\n"
    "v_lshlrev_b32 v244, 16, v138\n"
    "v_and_b32 v245, 0xffff0000, v138\n"
    "v_lshlrev_b32 v246, 16, v139\n"
    "v_and_b32 v247, 0xffff0000, v139\n"
    "v_pk_mul_f32 v[240:241], v[240:241], v[236:237]\n"
    "v_pk_mul_f32 v[242:243], v[242:243], v[236:237]\n"
    "v_pk_mul_f32 v[244:245], v[244:245], v[236:237]\n"
    "buffer_load_dwordx4 v229, s[44:47], s96 offen offset:1024 lds\n"
    "v_pk_mul_f32 v[246:247], v[246:247], v[236:237]\n"
    "v_pk_fma_f32 v[240:241], v[16:17], v[232:233], v[240:241]\n"
    "v_pk_fma_f32 v[242:243], v[18:19], v[232:233], v[242:243]\n"
    "v_pk_fma_f32 v[244:245], v[24:25], v[232:233], v[244:245]\n"
    "v_pk_fma_f32 v[246:247], v[26:27], v[232:233], v[246:247]\n"
    "v_cvt_pk_bf16_f32 v136, v240, v241\n"
    "v_cvt_pk_bf16_f32 v137, v242, v243\n"
    "v_cvt_pk_bf16_f32 v138, v244, v245\n"
    "v_cvt_pk_bf16_f32 v139, v246, v247\n"
    "s_waitcnt vmcnt(14)\n"
    "v_lshlrev_b32 v248, 16, v140\n"
    "v_and_b32 v249, 0xffff0000, v140\n"
    "v_lshlrev_b32 v250, 16, v141\n"
    "v_and_b32 v251, 0xffff0000, v141\n"
    "buffer_load_dwordx4 v230, s[44:47], s96 offen offset:2048 lds\n"
    "v_lshlrev_b32 v252, 16, v142\n"
    "v_and_b32 v253, 0xffff0000, v142\n"
    "v_lshlrev_b32 v254, 16, v143\n"
    "v_and_b32 v255, 0xffff0000, v143\n"
    "v_pk_mul_f32 v[248:249], v[248:249], v[238:239]\n"
    "v_pk_mul_f32 v[250:251], v[250:251], v[238:239]\n"
    "v_pk_mul_f32 v[252:253], v[252:253], v[238:239]\n"
    "v_pk_mul_f32 v[254:255], v[254:255], v[238:239]\n"
    "v_pk_fma_f32 v[248:249], v[20:21], v[234:235], v[248:249]\n"
    "v_pk_fma_f32 v[250:251], v[22:23], v[234:235], v[250:251]\n"
    "v_pk_fma_f32 v[252:253], v[28:29], v[234:235], v[252:253]\n"
    "v_pk_fma_f32 v[254:255], v[30:31], v[234:235], v[254:255]\n"
    "v_cvt_pk_bf16_f32 v140, v248, v249\n"
    "buffer_load_dwordx4 v231, s[44:47], s96 offen offset:3072 lds\n"
    "v_cvt_pk_bf16_f32 v141, v250, v251\n"
    "v_cvt_pk_bf16_f32 v142, v252, v253\n"
    "v_cvt_pk_bf16_f32 v143, v254, v255\n"
    "s_waitcnt vmcnt(15)\n"
    "v_lshlrev_b32 v240, 16, v144\n"
    "v_and_b32 v241, 0xffff0000, v144\n"
    "v_lshlrev_b32 v242, 16, v145\n"
    "v_and_b32 v243, 0xffff0000, v145\n"
    "v_lshlrev_b32 v244, 16, v146\n"
    "v_and_b32 v245, 0xffff0000, v146\n"
    "v_lshlrev_b32 v246, 16, v147\n"
    "v_and_b32 v247, 0xffff0000, v147\n"
    "v_pk_mul_f32 v[240:241], v[240:241], v[236:237]\n"
    "v_pk_mul_f32 v[242:243], v[242:243], v[236:237]\n"
    "s_add_u32 m0, s95, 0x21000\n"
    "v_pk_mul_f32 v[244:245], v[244:245], v[236:237]\n"
    "v_pk_mul_f32 v[246:247], v[246:247], v[236:237]\n"
    "v_pk_fma_f32 v[240:241], v[32:33], v[232:233], v[240:241]\n"
    "v_pk_fma_f32 v[242:243], v[34:35], v[232:233], v[242:243]\n"
    "v_pk_fma_f32 v[244:245], v[40:41], v[232:233], v[244:245]\n"
    "v_pk_fma_f32 v[246:247], v[42:43], v[232:233], v[246:247]\n"
    "v_cvt_pk_bf16_f32 v144, v240, v241\n"
    "v_cvt_pk_bf16_f32 v145, v242, v243\n"
    "v_cvt_pk_bf16_f32 v146, v244, v245\n"
    "v_cvt_pk_bf16_f32 v147, v246, v247\n"
    "s_waitcnt vmcnt(14)\n"
    "v_lshlrev_b32 v248, 16, v148\n"
    "v_and_b32 v249, 0xffff0000, v148\n"
    "s_mov_b32 s96, 28672\n"
    "v_lshlrev_b32 v250, 16, v149\n"
    "v_and_b32 v251, 0xffff0000, v149\n"
    "v_lshlrev_b32 v252, 16, v150\n"
    "v_and_b32 v253, 0xffff0000, v150\n"
    "v_lshlrev_b32 v254, 16, v151\n"
    "v_and_b32 v255, 0xffff0000, v151\n"
    "v_pk_mul_f32 v[248:249], v[248:249], v[238:239]\n"
    "v_pk_mul_f32 v[250:251], v[250:251], v[238:239]\n"
    "v_pk_mul_f32 v[252:253], v[252:253], v[238:239]\n"
    "v_pk_mul_f32 v[254:255], v[254:255], v[238:239]\n"
    "v_pk_fma_f32 v[248:249], v[36:37], v[234:235], v[248:249]\n"
    "v_pk_fma_f32 v[250:251], v[38:39], v[234:235], v[250:251]\n"
    "v_pk_fma_f32 v[252:253], v[44:45], v[234:235], v[252:253]\n"
    "buffer_load_dwordx4 v228, s[44:47], s96 offen offset:0 lds\n"
    "v_pk_fma_f32 v[254:255], v[46:47], v[234:235], v[254:255]\n"
    "v_cvt_pk_bf16_f32 v148, v248, v249\n"
    "v_cvt_pk_bf16_f32 v149, v250, v251\n"
    "v_cvt_pk_bf16_f32 v150, v252, v253\n"
    "v_cvt_pk_bf16_f32 v151, v254, v255\n"
    "s_waitcnt vmcnt(14)\n"
    "v_lshlrev_b32 v240, 16, v152\n"
    "v_and_b32 v241, 0xffff0000, v152\n"
    "v_lshlrev_b32 v242, 16, v153\n"
    "v_and_b32 v243, 0xffff0000, v153\n"
    "v_lshlrev_b32 v244, 16, v154\n"
    "v_and_b32 v245, 0xffff0000, v154\n"
    "v_lshlrev_b32 v246, 16, v155\n"
    "v_and_b32 v247, 0xffff0000, v155\n"
    "buffer_load_dwordx4 v229, s[44:47], s96 offen offset:1024 lds\n"
    "v_pk_mul_f32 v[240:241], v[240:241], v[236:237]\n"
    "v_pk_mul_f32 v[242:243], v[242:243], v[236:237]\n"
    "v_pk_mul_f32 v[244:245], v[244:245], v[236:237]\n"
    "v_pk_mul_f32 v[246:247], v[246:247], v[236:237]\n"
    "v_pk_fma_f32 v[240:241], v[48:49], v[232:233], v[240:241]\n"
    "v_pk_fma_f32 v[242:243], v[50:51], v[232:233], v[242:243]\n"
    "v_pk_fma_f32 v[244:245], v[56:57], v[232:233], v[244:245]\n"
    "v_pk_fma_f32 v[246:247], v[58:59], v[232:233], v[246:247]\n"
    "v_cvt_pk_bf16_f32 v152, v240, v241\n"
    "v_cvt_pk_bf16_f32 v153, v242, v243\n"
    "v_cvt_pk_bf16_f32 v154, v244, v245\n"
    "v_cvt_pk_bf16_f32 v155, v246, v247\n"
    "s_waitcnt vmcnt(14)\n"
    "buffer_load_dwordx4 v230, s[44:47], s96 offen offset:2048 lds\n"
    "v_lshlrev_b32 v248, 16, v156\n"
    "v_and_b32 v249, 0xffff0000, v156\n"
    "v_lshlrev_b32 v250, 16, v157\n"
    "v_and_b32 v251, 0xffff0000, v157\n"
    "v_lshlrev_b32 v252, 16, v158\n"
    "v_and_b32 v253, 0xffff0000, v158\n"
    "v_lshlrev_b32 v254, 16, v159\n"
    "v_and_b32 v255, 0xffff0000, v159\n"
    "v_pk_mul_f32 v[248:249], v[248:249], v[238:239]\n"
    "v_pk_mul_f32 v[250:251], v[250:251], v[238:239]\n"
    "v_pk_mul_f32 v[252:253], v[252:253], v[238:239]\n"
    "v_pk_mul_f32 v[254:255], v[254:255], v[238:239]\n"
    "v_pk_fma_f32 v[248:249], v[52:53], v[234:235], v[248:249]\n"
    "v_pk_fma_f32 v[250:251], v[54:55], v[234:235], v[250:251]\n"
    "buffer_load_dwordx4 v231, s[44:47], s96 offen offset:3072 lds\n"
    "v_pk_fma_f32 v[252:253], v[60:61], v[234:235], v[252:253]\n"
    "v_pk_fma_f32 v[254:255], v[62:63], v[234:235], v[254:255]\n"
    "v_cvt_pk_bf16_f32 v156, v248, v249\n"
    "v_cvt_pk_bf16_f32 v157, v250, v251\n"
    "v_cvt_pk_bf16_f32 v158, v252, v253\n"
    "v_cvt_pk_bf16_f32 v159, v254, v255\n"
    "s_waitcnt vmcnt(15)\n"
    "v_lshlrev_b32 v240, 16, v160\n"
    "v_and_b32 v241, 0xffff0000, v160\n"
    "v_lshlrev_b32 v242, 16, v161\n"
    "v_and_b32 v243, 0xffff0000, v161\n"
    "v_lshlrev_b32 v244, 16, v162\n"
    "v_and_b32 v245, 0xffff0000, v162\n"
    "s_add_u32 m0, s95, 0x0\n"
    "v_lshlrev_b32 v246, 16, v163\n"
    "v_and_b32 v247, 0xffff0000, v163\n"
    "v_pk_mul_f32 v[240:241], v[240:241], v[236:237]\n"
    "v_pk_mul_f32 v[242:243], v[242:243], v[236:237]\n"
    "v_pk_mul_f32 v[244:245], v[244:245], v[236:237]\n"
    "v_pk_mul_f32 v[246:247], v[246:247], v[236:237]\n"
    "v_pk_fma_f32 v[240:241], v[64:65], v[232:233], v[240:241]\n"
    "v_pk_fma_f32 v[242:243], v[66:67], v[232:233], v[242:243]\n"
    "v_pk_fma_f32 v[244:245], v[72:73], v[232:233], v[244:245]\n"
    "v_pk_fma_f32 v[246:247], v[74:75], v[232:233], v[246:247]\n"
    "v_cvt_pk_bf16_f32 v160, v240, v241\n"
    "v_cvt_pk_bf16_f32 v161, v242, v243\n"
    "v_cvt_pk_bf16_f32 v162, v244, v245\n"
    "v_cvt_pk_bf16_f32 v163, v246, v247\n"
    "s_mov_b32 s96, 128\n"
    "s_waitcnt vmcnt(14)\n"
    "v_lshlrev_b32 v248, 16, v164\n"
    "v_and_b32 v249, 0xffff0000, v164\n"
    "v_lshlrev_b32 v250, 16, v165\n"
    "v_and_b32 v251, 0xffff0000, v165\n"
    "v_lshlrev_b32 v252, 16, v166\n"
    "v_and_b32 v253, 0xffff0000, v166\n"
    "v_lshlrev_b32 v254, 16, v167\n"
    "v_and_b32 v255, 0xffff0000, v167\n"
    "v_pk_mul_f32 v[248:249], v[248:249], v[238:239]\n"
    "v_pk_mul_f32 v[250:251], v[250:251], v[238:239]\n"
    "v_pk_mul_f32 v[252:253], v[252:253], v[238:239]\n"
    "v_pk_mul_f32 v[254:255], v[254:255], v[238:239]\n"
    "buffer_load_dwordx4 v228, s[44:47], s96 offen offset:0 lds\n"
    "v_pk_fma_f32 v[248:249], v[68:69], v[234:235], v[248:249]\n"
    "v_pk_fma_f32 v[250:251], v[70:71], v[234:235], v[250:251]\n"
    "v_pk_fma_f32 v[252:253], v[76:77], v[234:235], v[252:253]\n"
    "v_pk_fma_f32 v[254:255], v[78:79], v[234:235], v[254:255]\n"
    "v_cvt_pk_bf16_f32 v164, v248, v249\n"
    "v_cvt_pk_bf16_f32 v165, v250, v251\n"
    "v_cvt_pk_bf16_f32 v166, v252, v253\n"
    "v_cvt_pk_bf16_f32 v167, v254, v255\n"
    "s_waitcnt vmcnt(14)\n"
    "v_lshlrev_b32 v240, 16, v168\n"
    "v_and_b32 v241, 0xffff0000, v168\n"
    "v_lshlrev_b32 v242, 16, v169\n"
    "v_and_b32 v243, 0xffff0000, v169\n"
    "v_lshlrev_b32 v244, 16, v170\n"
    "buffer_load_dwordx4 v229, s[44:47], s96 offen offset:1024 lds\n"
    "v_and_b32 v245, 0xffff0000, v170\n"
    "v_lshlrev_b32 v246, 16, v171\n"
    "v_and_b32 v247, 0xffff0000, v171\n"
    "v_pk_mul_f32 v[240:241], v[240:241], v[236:237]\n"
    "v_pk_mul_f32 v[242:243], v[242:243], v[236:237]\n"
    "v_pk_mul_f32 v[244:245], v[244:245], v[236:237]\n"
    "v_pk_mul_f32 v[246:247], v[246:247], v[236:237]\n"
    "v_pk_fma_f32 v[240:241], v[80:81], v[232:233], v[240:241]\n"
    "v_pk_fma_f32 v[242:243], v[82:83], v[232:233], v[242:243]\n"
    "v_pk_fma_f32 v[244:245], v[88:89], v[232:233], v[244:245]\n"
    "v_pk_fma_f32 v[246:247], v[90:91], v[232:233], v[246:247]\n"
    "v_cvt_pk_bf16_f32 v168, v240, v241\n"
    "v_cvt_pk_bf16_f32 v169, v242, v243\n"
    "buffer_load_dwordx4 v230, s[44:47], s96 offen offset:2048 lds\n"
    "v_cvt_pk_bf16_f32 v170, v244, v245\n"
    "v_cvt_pk_bf16_f32 v171, v246, v247\n"
    "s_waitcnt vmcnt(15)\n"
    "v_lshlrev_b32 v248, 16, v172\n"
    "v_and_b32 v249, 0xffff0000, v172\n"
    "v_lshlrev_b32 v250, 16, v173\n"
    "v_and_b32 v251, 0xffff0000, v173\n"
    "v_lshlrev_b32 v252, 16, v174\n"
    "v_and_b32 v253, 0xffff0000, v174\n"
    "v_lshlrev_b32 v254, 16, v175\n"
    "v_and_b32 v255, 0xffff0000, v175\n"
    "v_pk_mul_f32 v[248:249], v[248:249], v[238:239]\n"
    "v_pk_mul_f32 v[250:251], v[250:251], v[238:239]\n"
    "buffer_load_dwordx4 v231, s[44:47], s96 offen offset:3072 lds\n"
    "v_pk_mul_f32 v[252:253], v[252:253], v[238:239]\n"
    "v_pk_mul_f32 v[254:255], v[254:255], v[238:239]\n"
    "v_pk_fma_f32 v[248:249], v[84:85], v[234:235], v[248:249]\n"
    "v_pk_fma_f32 v[250:251], v[86:87], v[234:235], v[250:251]\n"
    "v_pk_fma_f32 v[252:253], v[92:93], v[234:235], v[252:253]\n"
    "v_pk_fma_f32 v[254:255], v[94:95], v[234:235], v[254:255]\n"
    "v_cvt_pk_bf16_f32 v172, v248, v249\n"
    "v_cvt_pk_bf16_f32 v173, v250, v251\n"
    "v_cvt_pk_bf16_f32 v174, v252, v253\n"
    "v_cvt_pk_bf16_f32 v175, v254, v255\n"
    "s_waitcnt vmcnt(15)\n"
    "v_lshlrev_b32 v240, 16, v176\n"
    "v_and_b32 v241, 0xffff0000, v176\n"
    "v_lshlrev_b32 v242, 16, v177\n"
    "s_add_u32 m0, s95, 0x1000\n"
    "v_and_b32 v243, 0xffff0000, v177\n"
    "v_lshlrev_b32 v244, 16, v178\n"
    "v_and_b32 v245, 0xffff0000, v178\n"
    "v_lshlrev_b32 v246, 16, v179\n"
    "v_and_b32 v247, 0xffff0000, v179\n"
    "v_pk_mul_f32 v[240:241], v[240:241], v[236:237]\n"
    "v_pk_mul_f32 v[242:243], v[242:243], v[236:237]\n"
    "v_pk_mul_f32 v[244:245], v[244:245], v[236:237]\n"
    "v_pk_mul_f32 v[246:247], v[246:247], v[236:237]\n"
    "v_pk_fma_f32 v[240:241], v[96:97], v[232:233], v[240:241]\n"
    "v_pk_fma_f32 v[242:243], v[98:99], v[232:233], v[242:243]\n"
    "v_pk_fma_f32 v[244:245], v[104:105], v[232:233], v[244:245]\n"
    "v_pk_fma_f32 v[246:247], v[106:107], v[232:233], v[246:247]\n"
    "s_mov_b32 s96, 28800\n"
    "v_cvt_pk_bf16_f32 v176, v240, v241\n"
    "v_cvt_pk_bf16_f32 v177, v242, v243\n"
    "v_cvt_pk_bf16_f32 v178, v244, v245\n"
    "v_cvt_pk_bf16_f32 v179, v246, v247\n"
    "s_waitcnt vmcnt(14)\n"
    "v_lshlrev_b32 v248, 16, v180\n"
    "v_and_b32 v249, 0xffff0000, v180\n"
    "v_lshlrev_b32 v250, 16, v181\n"
    "v_and_b32 v251, 0xffff0000, v181\n"
    "v_lshlrev_b32 v252, 16, v182\n"
    "v_and_b32 v253, 0xffff0000, v182\n"
    "v_lshlrev_b32 v254, 16, v183\n"
    "v_and_b32 v255, 0xffff0000, v183\n"
    "v_pk_mul_f32 v[248:249], v[248:249], v[238:239]\n"
    "buffer_load_dwordx4 v228, s[44:47], s96 offen offset:0 lds\n"
    "v_pk_mul_f32 v[250:251], v[250:251], v[238:239]\n"
    "v_pk_mul_f32 v[252:253], v[252:253], v[238:239]\n"
    "v_pk_mul_f32 v[254:255], v[254:255], v[238:239]\n"
    "v_pk_fma_f32 v[248:249], v[100:101], v[234:235], v[248:249]\n"
    "v_pk_fma_f32 v[250:251], v[102:103], v[234:235], v[250:251]\n"
    "v_pk_fma_f32 v[252:253], v[108:109], v[234:235], v[252:253]\n"
    "v_pk_fma_f32 v[254:255], v[110:111], v[234:235], v[254:255]\n"
    "v_cvt_pk_bf16_f32 v180, v248, v249\n"
    "v_cvt_pk_bf16_f32 v181, v250, v251\n"
    "v_cvt_pk_bf16_f32 v182, v252, v253\n"
    "v_cvt_pk_bf16_f32 v183, v254, v255\n"
    "s_waitcnt vmcnt(14)\n"
    "v_lshlrev_b32 v240, 16, v184\n"
    "buffer_load_dwordx4 v229, s[44:47], s96 offen offset:1024 lds\n"
    "v_and_b32 v241, 0xffff0000, v184\n"
    "v_lshlrev_b32 v242, 16, v185\n"
    "v_and_b32 v243, 0xffff0000, v185\n"
    "v_lshlrev_b32 v244, 16, v186\n"
    "v_and_b32 v245, 0xffff0000, v186\n"
    "v_lshlrev_b32 v246, 16, v187\n"
    "v_and_b32 v247, 0xffff0000, v187\n"
    "v_pk_mul_f32 v[240:241], v[240:241], v[236:237]\n"
    "v_pk_mul_f32 v[242:243], v[242:243], v[236:237]\n"
    "v_pk_mul_f32 v[244:245], v[244:245], v[236:237]\n"
    "v_pk_mul_f32 v[246:247], v[246:247], v[236:237]\n"
    "v_pk_fma_f32 v[240:241], v[112:113], v[232:233], v[240:241]\n"
    "v_pk_fma_f32 v[242:243], v[114:115], v[232:233], v[242:243]\n"
    "v_pk_fma_f32 v[244:245], v[120:121], v[232:233], v[244:245]\n"
    "buffer_load_dwordx4 v230, s[44:47], s96 offen offset:2048 lds\n"
    "v_pk_fma_f32 v[246:247], v[122:123], v[232:233], v[246:247]\n"
    "v_cvt_pk_bf16_f32 v184, v240, v241\n"
    "v_cvt_pk_bf16_f32 v185, v242, v243\n"
    "v_cvt_pk_bf16_f32 v186, v244, v245\n"
    "v_cvt_pk_bf16_f32 v187, v246, v247\n"
    "s_waitcnt vmcnt(15)\n"
    "v_lshlrev_b32 v248, 16, v188\n"
    "v_and_b32 v249, 0xffff0000, v188\n"
    "v_lshlrev_b32 v250, 16, v189\n"
    "v_and_b32 v251, 0xffff0000, v189\n"
    "v_lshlrev_b32 v252, 16, v190\n"
    "v_and_b32 v253, 0xffff0000, v190\n"
    "v_lshlrev_b32 v254, 16, v191\n"
    "buffer_load_dwordx4 v231, s[44:47], s96 offen offset:3072 lds\n"
    "v_and_b32 v255, 0xffff0000, v191\n"
    "v_pk_mul_f32 v[248:249], v[248:249], v[238:239]\n"
    "v_pk_mul_f32 v[250:251], v[250:251], v[238:239]\n"
    "v_pk_mul_f32 v[252:253], v[252:253], v[238:239]\n"
    "v_pk_mul_f32 v[254:255], v[254:255], v[238:239]\n"
    "v_pk_fma_f32 v[248:249], v[116:117], v[234:235], v[248:249]\n"
    "v_pk_fma_f32 v[250:251], v[118:119], v[234:235], v[250:251]\n"
    "v_pk_fma_f32 v[252:253], v[124:125], v[234:235], v[252:253]\n"
    "v_pk_fma_f32 v[254:255], v[126:127], v[234:235], v[254:255]\n"
    "v_cvt_pk_bf16_f32 v188, v248, v249\n"
    "v_cvt_pk_bf16_f32 v189, v250, v251\n"
    "v_cvt_pk_bf16_f32 v190, v252, v253\n"
    "v_cvt_pk_bf16_f32 v191, v254, v255\n"
    "OUTP_%=:\n"
    "s_lshl_b32 s92, s72, 3\n"
    "s_lshl_b32 s93, s56, 1\n"
    "s_add_u32 s92, s92, s93\n"
    "s_mul_i32 s92, s92, 229376\n"
    "s_add_u32 s93, s92, 229376\n"
    "s_waitcnt vmcnt(8)\n"
    "s_barrier\n"
    "ds_read_b64_tr_b16 a[64:65], v224 offset:0\n"
    "ds_read_b64_tr_b16 a[66:67], v224 offset:2048\n"
    "ds_read_b64_tr_b16 a[68:69], v224 offset:4096\n"
    "ds_read_b64_tr_b16 a[70:71], v224 offset:6144\n"
    "ds_read_b64_tr_b16 a[72:73], v224 offset:8192\n"
    "ds_read_b64_tr_b16 a[74:75], v224 offset:10240\n"
    "ds_read_b64_tr_b16 a[76:77], v224 offset:12288\n"
    "ds_read_b64_tr_b16 a[78:79], v224 offset:14336\n"
    "ds_read_b64_tr_b16 a[80:81], v224 offset:16384\n"
    "ds_read_b64_tr_b16 a[82:83], v224 offset:18432\n"
    "ds_read_b64_tr_b16 a[84:85], v224 offset:20480\n"
    "ds_read_b64_tr_b16 a[86:87], v224 offset:22528\n"
    "ds_read_b64_tr_b16 a[88:89], v224 offset:24576\n"
    "ds_read_b64_tr_b16 a[90:91], v224 offset:26624\n"
    "s_waitcnt lgkmcnt(10)\n"
    "ds_read_b64_tr_b16 a[92:93], v224 offset:28672\n"
    "ds_read_b64_tr_b16 a[94:95], v224 offset:30720\n"
    "ds_read_b64_tr_b16 a[96:97], v225 offset:0\n"
    "ds_read_b64_tr_b16 a[98:99], v225 offset:2048\n"
    "ds_read_b64_tr_b16 a[100:101], v225 offset:4096\n"
    "ds_read_b64_tr_b16 a[102:103], v225 offset:6144\n"
    "v_mfma_f32_16x16x32_bf16 v[0:3], a[64:67], v[128:131], 0\n"
    "ds_read_b64_tr_b16 a[104:105], v225 offset:8192\n"
    "v_mfma_f32_16x16x32_bf16 v[0:3], a[68:71], v[136:139], v[0:3]\n"
    "ds_read_b64_tr_b16 a[106:107], v225 offset:10240\n"
    "s_waitcnt lgkmcnt(14)\n"
    "s_add_u32 m0, s95, 0x10000\n"
    "v_mfma_f32_16x16x32_bf16 v[0:3], a[72:75], v[144:147], v[0:3]\n"
    "ds_read_b64_tr_b16 a[108:109], v225 offset:12288\n"
    "v_mfma_f32_16x16x32_bf16 v[0:3], a[76:79], v[152:155], v[0:3]\n"
    "ds_read_b64_tr_b16 a[110:111], v225 offset:14336\n"
    "s_waitcnt lgkmcnt(12)\n"
    "s_mov_b32 s96, 256\n"
    "v_mfma_f32_16x16x32_bf16 v[0:3], a[80:83], v[160:163], v[0:3]\n"
    "ds_read_b64_tr_b16 a[112:113], v225 offset:16384\n"
    "v_mfma_f32_16x16x32_bf16 v[0:3], a[84:87], v[168:171], v[0:3]\n"
    "ds_read_b64_tr_b16 a[114:115], v225 offset:18432\n"
    "s_waitcnt lgkmcnt(10)\n"
    "buffer_load_dwordx4 v228, s[44:47], s96 offen offset:0 lds\n"
    "v_mfma_f32_16x16x32_bf16 v[0:3], a[88:91], v[176:179], v[0:3]\n"
    "ds_read_b64_tr_b16 a[116:117], v225 offset:20480\n"
    "v_mfma_f32_16x16x32_bf16 v[0:3], a[92:95], v[184:187], v[0:3]\n"
    "ds_read_b64_tr_b16 a[118:119], v225 offset:22528\n"
    "v_mfma_f32_16x16x32_bf16 v[4:7], a[64:67], v[132:135], 0\n"
    "ds_read_b64_tr_b16 a[120:121], v225 offset:24576\n"
    "buffer_load_dwordx4 v229, s[44:47], s96 offen offset:1024 lds\n"
    "v_mfma_f32_16x16x32_bf16 v[4:7], a[68:71], v[140:143], v[4:7]\n"
    "ds_read_b64_tr_b16 a[122:123], v225 offset:26624\n"
    "v_mfma_f32_16x16x32_bf16 v[4:7], a[72:75], v[148:151], v[4:7]\n"
    "ds_read_b64_tr_b16 a[124:125], v225 offset:28672\n"
    "buffer_load_dwordx4 v230, s[44:47], s96 offen offset:2048 lds\n"
    "v_mfma_f32_16x16x32_bf16 v[4:7], a[76:79], v[156:159], v[4:7]\n"
    "ds_read_b64_tr_b16 a[126:127], v225 offset:30720\n"
    "v_mfma_f32_16x16x32_bf16 v[4:7], a[80:83], v[164:167], v[4:7]\n"
    "ds_read_b64_tr_b16 a[64:65], v226 offset:0\n"
    "v_mfma_f32_16x16x32_bf16 v[4:7], a[84:87], v[172:175], v[4:7]\n"
    "ds_read_b64_tr_b16 a[66:67], v226 offset:2048\n"
    "buffer_load_dwordx4 v231, s[44:47], s96 offen offset:3072 lds\n"
    "v_mfma_f32_16x16x32_bf16 v[4:7], a[88:91], v[180:183], v[4:7]\n"
    "ds_read_b64_tr_b16 a[68:69], v226 offset:4096\n"
    "v_mfma_f32_16x16x32_bf16 v[4:7], a[92:95], v[188:191], v[4:7]\n"
    "ds_read_b64_tr_b16 a[70:71], v226 offset:6144\n"
    "s_waitcnt lgkmcnt(15)\n"
    "s_add_u32 m0, s95, 0x11000\n"
    "v_mfma_f32_16x16x32_bf16 v[8:11], a[96:99], v[128:131], 0\n"
    "ds_read_b64_tr_b16 a[72:73], v226 offset:8192\n"
    "v_mfma_f32_16x16x32_bf16 v[8:11], a[100:103], v[136:139], v[8:11]\n"
    "ds_read_b64_tr_b16 a[74:75], v226 offset:10240\n"
    "s_waitcnt lgkmcnt(14)\n"
    "v_mfma_f32_16x16x32_bf16 v[8:11], a[104:107], v[144:147], v[8:11]\n"
    "ds_read_b64_tr_b16 a[76:77], v226 offset:12288\n"
    "s_mov_b32 s96, 28928\n"
    "v_mfma_f32_16x16x32_bf16 v[8:11], a[108:111], v[152:155], v[8:11]\n"
    "ds_read_b64_tr_b16 a[78:79], v226 offset:14336\n"
    "s_waitcnt lgkmcnt(12)\n"
    "v_mfma_f32_16x16x32_bf16 v[8:11], a[112:115], v[160:163], v[8:11]\n"
    "ds_read_b64_tr_b16 a[80:81], v226 offset:16384\n"
    "buffer_load_dwordx4 v228, s[44:47], s96 offen offset:0 lds\n"
    "v_mfma_f32_16x16x32_bf16 v[8:11], a[116:119], v[168:171], v[8:11]\n"
    "ds_read_b64_tr_b16 a[82:83], v226 offset:18432\n"
    "s_waitcnt lgkmcnt(10)\n"
    "v_mfma_f32_16x16x32_bf16 v[8:11], a[120:123], v[176:179], v[8:11]\n"
    "ds_read_b64_tr_b16 a[84:85], v226 offset:20480\n"
    "v_mfma_f32_16x16x32_bf16 v[8:11], a[124:127], v[184:187], v[8:11]\n"
    "ds_read_b64_tr_b16 a[86:87], v226 offset:22528\n"
    "buffer_load_dwordx4 v229, s[44:47], s96 offen offset:1024 lds\n"
    "v_mfma_f32_16x16x32_bf16 v[12:15], a[96:99], v[132:135], 0\n"
    "ds_read_b64_tr_b16 a[88:89], v226 offset:24576\n"
    "v_mfma_f32_16x16x32_bf16 v[12:15], a[100:103], v[140:143], v[12:15]\n"
    "ds_read_b64_tr_b16 a[90:91], v226 offset:26624\n"
    "buffer_load_dwordx4 v230, s[44:47], s96 offen offset:2048 lds\n"
    "v_mfma_f32_16x16x32_bf16 v[12:15], a[104:107], v[148:151], v[12:15]\n"
    "ds_read_b64_tr_b16 a[92:93], v226 offset:28672\n"
    "v_mfma_f32_16x16x32_bf16 v[12:15], a[108:111], v[156:159], v[12:15]\n"
    "ds_read_b64_tr_b16 a[94:95], v226 offset:30720\n"
    "v_mfma_f32_16x16x32_bf16 v[12:15], a[112:115], v[164:167], v[12:15]\n"
    "ds_read_b64_tr_b16 a[96:97], v227 offset:0\n"
    "buffer_load_dwordx4 v231, s[44:47], s96 offen offset:3072 lds\n"
    "v_mfma_f32_16x16x32_bf16 v[12:15], a[116:119], v[172:175], v[12:15]\n"
    "ds_read_b64_tr_b16 a[98:99], v227 offset:2048\n"
    "v_mfma_f32_16x16x32_bf16 v[12:15], a[120:123], v[180:183], v[12:15]\n"
    "ds_read_b64_tr_b16 a[100:101], v227 offset:4096\n"
    "s_add_u32 m0, s95, 0x18000\n"
    "v_mfma_f32_16x16x32_bf16 v[12:15], a[124:127], v[188:191], v[12:15]\n"
    "ds_read_b64_tr_b16 a[102:103], v227 offset:6144\n"
    "s_waitcnt lgkmcnt(15)\n"
    "v_mfma_f32_16x16x32_bf16 v[16:19], a[64:67], v[128:131], 0\n"
    "ds_read_b64_tr_b16 a[104:105], v227 offset:8192\n"
    "s_mov_b32 s96, 384\n"
    "v_mfma_f32_16x16x32_bf16 v[16:19], a[68:71], v[136:139], v[16:19]\n"
    "ds_read_b64_tr_b16 a[106:107], v227 offset:10240\n"
    "s_waitcnt lgkmcnt(14)\n"
    "v_mfma_f32_16x16x32_bf16 v[16:19], a[72:75], v[144:147], v[16:19]\n"
    "ds_read_b64_tr_b16 a[108:109], v227 offset:12288\n"
    "v_mfma_f32_16x16x32_bf16 v[16:19], a[76:79], v[152:155], v[16:19]\n"
    "ds_read_b64_tr_b16 a[110:111], v227 offset:14336\n"
    "s_waitcnt lgkmcnt(12)\n"
    "buffer_load_dwordx4 v228, s[44:47], s96 offen offset:0 lds\n"
    "v_mfma_f32_16x16x32_bf16 v[16:19], a[80:83], v[160:163], v[16:19]\n"
    "ds_read_b64_tr_b16 a[112:113], v227 offset:16384\n"
    "v_add_u32 v224, 0xfffe0000, v224\n"
    "v_mfma_f32_16x16x32_bf16 v[16:19], a[84:87], v[168:171], v[16:19]\n"
    "ds_read_b64_tr_b16 a[114:115], v227 offset:18432\n"
    "s_waitcnt lgkmcnt(10)\n"
    "buffer_load_dwordx4 v229, s[44:47], s96 offen offset:1024 lds\n"
    "v_mfma_f32_16x16x32_bf16 v[16:19], a[88:91], v[176:179], v[16:19]\n"
    "ds_read_b64_tr_b16 a[116:117], v227 offset:20480\n"
    "v_cvt_pk_bf16_f32 v64, v0, v1\n"
    "v_mfma_f32_16x16x32_bf16 v[16:19], a[92:95], v[184:187], v[16:19]\n"
    "ds_read_b64_tr_b16 a[118:119], v227 offset:22528\n"
    "v_cvt_pk_bf16_f32 v65, v2, v3\n"
    "v_mfma_f32_16x16x32_bf16 v[20:23], a[64:67], v[132:135], 0\n"
    "ds_read_b64_tr_b16 a[120:121], v227 offset:24576\n"
    "buffer_load_dwordx4 v230, s[44:47], s96 offen offset:2048 lds\n"
    "v_mfma_f32_16x16x32_bf16 v[20:23], a[68:71], v[140:143], v[20:23]\n"
    "ds_read_b64_tr_b16 a[122:123], v227 offset:26624\n"
    "v_cvt_pk_bf16_f32 v66, v8, v9\n"
    "v_mfma_f32_16x16x32_bf16 v[20:23], a[72:75], v[148:151], v[20:23]\n"
    "ds_read_b64_tr_b16 a[124:125], v227 offset:28672\n"
    "s_waitcnt vmcnt(11)\n"
    "buffer_load_dwordx4 v231, s[44:47], s96 offen offset:3072 lds\n"
    "v_mfma_f32_16x16x32_bf16 v[20:23], a[76:79], v[156:159], v[20:23]\n"
    "ds_read_b64_tr_b16 a[126:127], v227 offset:30720\n"
    "s_barrier\n"
    "v_cvt_pk_bf16_f32 v67, v10, v11\n"
    "v_mfma_f32_16x16x32_bf16 v[20:23], a[80:83], v[164:167], v[20:23]\n"
    "ds_read_b64_tr_b16 a[64:65], v224 offset:0\n"
    "v_mfma_f32_16x16x32_bf16 v[20:23], a[84:87], v[172:175], v[20:23]\n"
    "ds_read_b64_tr_b16 a[66:67], v224 offset:2048\n"
    "s_add_u32 m0, s95, 0x19000\n"
    "v_mfma_f32_16x16x32_bf16 v[20:23], a[88:91], v[180:183], v[20:23]\n"
    "ds_read_b64_tr_b16 a[68:69], v224 offset:4096\n"
    "buffer_store_dwordx4 v[64:67], v210, s[48:51], s92 offen offset:0 sc1\n"
    "v_mfma_f32_16x16x32_bf16 v[20:23], a[92:95], v[188:191], v[20:23]\n"
    "ds_read_b64_tr_b16 a[70:71], v224 offset:6144\n"
    "s_waitcnt lgkmcnt(15)\n"
    "s_mov_b32 s96, 29056\n"
    "v_mfma_f32_16x16x32_bf16 v[24:27], a[96:99], v[128:131], 0\n"
    "ds_read_b64_tr_b16 a[72:73], v224 offset:8192\n"
    "s_add_u32 s92, s92, 0x4000\n"
    "v_mfma_f32_16x16x32_bf16 v[24:27], a[100:103], v[136:139], v[24:27]\n"
    "ds_read_b64_tr_b16 a[74:75], v224 offset:10240\n"
    "s_waitcnt lgkmcnt(14)\n"
    "v_cvt_pk_bf16_f32 v68, v4, v5\n"
    "v_mfma_f32_16x16x32_bf16 v[24:27], a[104:107], v[144:147], v[24:27]\n"
    "ds_read_b64_tr_b16 a[76:77], v224 offset:12288\n"
    "buffer_load_dwordx4 v228, s[44:47], s96 offen offset:0 lds\n"
    "v_mfma_f32_16x16x32_bf16 v[24:27], a[108:111], v[152:155], v[24:27]\n"
    "ds_read_b64_tr_b16 a[78:79], v224 offset:14336\n"
    "s_waitcnt lgkmcnt(12)\n"
    "v_cvt_pk_bf16_f32 v69, v6, v7\n"
    "v_mfma_f32_16x16x32_bf16 v[24:27], a[112:115], v[160:163], v[24:27]\n"
    "ds_read_b64_tr_b16 a[80:81], v224 offset:16384\n"
    "v_add_u32 v225, 0xfffe0000, v225\n"
    "v_cvt_pk_bf16_f32 v70, v12, v13\n"
    "v_mfma_f32_16x16x32_bf16 v[24:27], a[116:119], v[168:171], v[24:27]\n"
    "ds_read_b64_tr_b16 a[82:83], v224 offset:18432\n"
    "s_waitcnt lgkmcnt(10)\n"
    "buffer_load_dwordx4 v229, s[44:47], s96 offen offset:1024 lds\n"
    "v_mfma_f32_16x16x32_bf16 v[24:27], a[120:123], v[176:179], v[24:27]\n"
    "ds_read_b64_tr_b16 a[84:85], v224 offset:20480\n"
    "v_cvt_pk_bf16_f32 v71, v14, v15\n"
    "v_mfma_f32_16x16x32_bf16 v[24:27], a[124:127], v[184:187], v[24:27]\n"
    "ds_read_b64_tr_b16 a[86:87], v224 offset:22528\n"
    "buffer_load_dwordx4 v230, s[44:47], s96 offen offset:2048 lds\n"
    "v_mfma_f32_16x16x32_bf16 v[28:31], a[96:99], v[132:135], 0\n"
    "ds_read_b64_tr_b16 a[88:89], v224 offset:24576\n"
    "buffer_store_dwordx4 v[68:71], v210, s[48:51], s93 offen offset:0 sc1\n"
    "v_mfma_f32_16x16x32_bf16 v[28:31], a[100:103], v[140:143], v[28:31]\n"
    "ds_read_b64_tr_b16 a[90:91], v224 offset:26624\n"
    "buffer_load_dwordx4 v231, s[44:47], s96 offen offset:3072 lds\n"
    "v_mfma_f32_16x16x32_bf16 v[28:31], a[104:107], v[148:151], v[28:31]\n"
    "ds_read_b64_tr_b16 a[92:93], v224 offset:28672\n"
    "s_add_u32 s93, s93, 0x4000\n"
    "v_mfma_f32_16x16x32_bf16 v[28:31], a[108:111], v[156:159], v[28:31]\n"
    "ds_read_b64_tr_b16 a[94:95], v224 offset:30720\n"
    "v_mfma_f32_16x16x32_bf16 v[28:31], a[112:115], v[164:167], v[28:31]\n"
    "ds_read_b64_tr_b16 a[96:97], v225 offset:0\n"
    "v_mfma_f32_16x16x32_bf16 v[28:31], a[116:119], v[172:175], v[28:31]\n"
    "ds_read_b64_tr_b16 a[98:99], v225 offset:2048\n"
    "v_mfma_f32_16x16x32_bf16 v[28:31], a[120:123], v[180:183], v[28:31]\n"
    "ds_read_b64_tr_b16 a[100:101], v225 offset:4096\n"
    "v_mfma_f32_16x16x32_bf16 v[28:31], a[124:127], v[188:191], v[28:31]\n"
    "ds_read_b64_tr_b16 a[102:103], v225 offset:6144\n"
    "s_waitcnt lgkmcnt(15)\n"
    "v_mfma_f32_16x16x32_bf16 v[32:35], a[64:67], v[128:131], 0\n"
    "ds_read_b64_tr_b16 a[104:105], v225 offset:8192\n"
    "v_mfma_f32_16x16x32_bf16 v[32:35], a[68:71], v[136:139], v[32:35]\n"
    "ds_read_b64_tr_b16 a[106:107], v225 offset:10240\n"
    "s_waitcnt lgkmcnt(14)\n"
    "s_add_u32 m0, s95, 0x8000\n"
    "v_mfma_f32_16x16x32_bf16 v[32:35], a[72:75], v[144:147], v[32:35]\n"
    "ds_read_b64_tr_b16 a[108:109], v225 offset:12288\n"
    "s_mov_b32 s40, s24\n"
    "v_mfma_f32_16x16x32_bf16 v[32:35], a[76:79], v[152:155], v[32:35]\n"
    "ds_read_b64_tr_b16 a[110:111], v225 offset:14336\n"
    "s_waitcnt lgkmcnt(12)\n"
    "v_mfma_f32_16x16x32_bf16 v[32:35], a[80:83], v[160:163], v[32:35]\n"
    "ds_read_b64_tr_b16 a[112:113], v225 offset:16384\n"
    "v_add_u32 v226, 0xfffe0000, v226\n"
    "s_mov_b32 s96, 512\n"
    "v_mfma_f32_16x16x32_bf16 v[32:35], a[84:87], v[168:171], v[32:35]\n"
    "ds_read_b64_tr_b16 a[114:115], v225 offset:18432\n"
    "s_waitcnt lgkmcnt(10)\n"
    "v_mfma_f32_16x16x32_bf16 v[32:35], a[88:91], v[176:179], v[32:35]\n"
    "ds_read_b64_tr_b16 a[116:117], v225 offset:20480\n"
    "v_cvt_pk_bf16_f32 v72, v16, v17\n"
    "v_mfma_f32_16x16x32_bf16 v[32:35], a[92:95], v[184:187], v[32:35]\n"
    "ds_read_b64_tr_b16 a[118:119], v225 offset:22528\n"
    "buffer_load_dwordx4 v228, s[44:47], s96 offen offset:0 lds\n"
    "v_mfma_f32_16x16x32_bf16 v[36:39], a[64:67], v[132:135], 0\n"
    "ds_read_b64_tr_b16 a[120:121], v225 offset:24576\n"
    "v_cvt_pk_bf16_f32 v73, v18, v19\n"
    "v_mfma_f32_16x16x32_bf16 v[36:39], a[68:71], v[140:143], v[36:39]\n"
    "ds_read_b64_tr_b16 a[122:123], v225 offset:26624\n"
    "s_and_b32 s41, s25, 0xffff\n"
    "v_mfma_f32_16x16x32_bf16 v[36:39], a[72:75], v[148:151], v[36:39]\n"
    "ds_read_b64_tr_b16 a[124:125], v225 offset:28672\n"
    "buffer_load_dwordx4 v229, s[44:47], s96 offen offset:1024 lds\n"
    "v_mfma_f32_16x16x32_bf16 v[36:39], a[76:79], v[156:159], v[36:39]\n"
    "ds_read_b64_tr_b16 a[126:127], v225 offset:30720\n"
    "v_cvt_pk_bf16_f32 v74, v24, v25\n"
    "v_mfma_f32_16x16x32_bf16 v[36:39], a[80:83], v[164:167], v[36:39]\n"
    "ds_read_b64_tr_b16 a[64:65], v226 offset:0\n"
    "v_cvt_pk_bf16_f32 v75, v26, v27\n"
    "v_mfma_f32_16x16x32_bf16 v[36:39], a[84:87], v[172:175], v[36:39]\n"
    "ds_read_b64_tr_b16 a[66:67], v226 offset:2048\n"
    "s_mov_b32 s42, 3670016\n"
    "v_mfma_f32_16x16x32_bf16 v[36:39], a[88:91], v[180:183], v[36:39]\n"
    "ds_read_b64_tr_b16 a[68:69], v226 offset:4096\n"
    "buffer_load_dwordx4 v230, s[44:47], s96 offen offset:2048 lds\n"
    "v_mfma_f32_16x16x32_bf16 v[36:39], a[92:95], v[188:191], v[36:39]\n"
    "ds_read_b64_tr_b16 a[70:71], v226 offset:6144\n"
    "s_waitcnt lgkmcnt(15)\n"
    "buffer_store_dwordx4 v[72:75], v210, s[48:51], s92 offen offset:0 sc1\n"
    "v_mfma_f32_16x16x32_bf16 v[40:43], a[96:99], v[128:131], 0\n"
    "ds_read_b64_tr_b16 a[72:73], v226 offset:8192\n"
    "s_add_u32 s92, s92, 0x4000\n"
    "v_mfma_f32_16x16x32_bf16 v[40:43], a[100:103], v[136:139], v[40:43]\n"
    "ds_read_b64_tr_b16 a[74:75], v226 offset:10240\n"
    "s_waitcnt lgkmcnt(14)\n"
    "buffer_load_dwordx4 v231, s[44:47], s96 offen offset:3072 lds\n"
    "v_mfma_f32_16x16x32_bf16 v[40:43], a[104:107], v[144:147], v[40:43]\n"
    "ds_read_b64_tr_b16 a[76:77], v226 offset:12288\n"
    "v_cvt_pk_bf16_f32 v76, v20, v21\n"
    "v_mfma_f32_16x16x32_bf16 v[40:43], a[108:111], v[152:155], v[40:43]\n"
    "ds_read_b64_tr_b16 a[78:79], v226 offset:14336\n"
    "s_waitcnt lgkmcnt(12)\n"
    "v_mfma_f32_16x16x32_bf16 v[40:43], a[112:115], v[160:163], v[40:43]\n"
    "ds_read_b64_tr_b16 a[80:81], v226 offset:16384\n"
    "v_add_u32 v227, 0xfffe0000, v227\n"
    "s_add_u32 m0, s95, 0x9000\n"
    "v_mfma_f32_16x16x32_bf16 v[40:43], a[116:119], v[168:171], v[40:43]\n"
    "ds_read_b64_tr_b16 a[82:83], v226 offset:18432\n"
    "s_waitcnt lgkmcnt(10)\n"
    "v_cvt_pk_bf16_f32 v77, v22, v23\n"
    "v_mfma_f32_16x16x32_bf16 v[40:43], a[120:123], v[176:179], v[40:43]\n"
    "ds_read_b64_tr_b16 a[84:85], v226 offset:20480\n"
    "v_cvt_pk_bf16_f32 v78, v28, v29\n"
    "v_mfma_f32_16x16x32_bf16 v[40:43], a[124:127], v[184:187], v[40:43]\n"
    "ds_read_b64_tr_b16 a[86:87], v226 offset:22528\n"
    "s_mov_b32 s96, 29184\n"
    "v_mfma_f32_16x16x32_bf16 v[44:47], a[96:99], v[132:135], 0\n"
    "ds_read_b64_tr_b16 a[88:89], v226 offset:24576\n"
    "v_cvt_pk_bf16_f32 v79, v30, v31\n"
    "v_mfma_f32_16x16x32_bf16 v[44:47], a[100:103], v[140:143], v[44:47]\n"
    "ds_read_b64_tr_b16 a[90:91], v226 offset:26624\n"
    "s_lshl_b32 s83, s72, 7\n"
    "v_mfma_f32_16x16x32_bf16 v[44:47], a[104:107], v[148:151], v[44:47]\n"
    "ds_read_b64_tr_b16 a[92:93], v226 offset:28672\n"
    "buffer_store_dwordx4 v[76:79], v210, s[48:51], s93 offen offset:0 sc1\n"
    "v_mfma_f32_16x16x32_bf16 v[44:47], a[108:111], v[156:159], v[44:47]\n"
    "ds_read_b64_tr_b16 a[94:95], v226 offset:30720\n"
    "buffer_load_dwordx4 v228, s[44:47], s96 offen offset:0 lds\n"
    "v_mfma_f32_16x16x32_bf16 v[44:47], a[112:115], v[164:167], v[44:47]\n"
    "ds_read_b64_tr_b16 a[96:97], v227 offset:0\n"
    "s_add_u32 s93, s93, 0x4000\n"
    "v_mfma_f32_16x16x32_bf16 v[44:47], a[116:119], v[172:175], v[44:47]\n"
    "ds_read_b64_tr_b16 a[98:99], v227 offset:2048\n"
    "s_add_u32 s83, s83, 0x1048000\n"
    "v_mfma_f32_16x16x32_bf16 v[44:47], a[120:123], v[180:183], v[44:47]\n"
    "ds_read_b64_tr_b16 a[100:101], v227 offset:4096\n"
    "buffer_load_dwordx4 v229, s[44:47], s96 offen offset:1024 lds\n"
    "v_mfma_f32_16x16x32_bf16 v[44:47], a[124:127], v[188:191], v[44:47]\n"
    "ds_read_b64_tr_b16 a[102:103], v227 offset:6144\n"
    "s_waitcnt lgkmcnt(15)\n"
    "v_mfma_f32_16x16x32_bf16 v[48:51], a[64:67], v[128:131], 0\n"
    "ds_read_b64_tr_b16 a[104:105], v227 offset:8192\n"
    "s_lshl_b32 s97, s72, 3\n"
    "v_mfma_f32_16x16x32_bf16 v[48:51], a[68:71], v[136:139], v[48:51]\n"
    "ds_read_b64_tr_b16 a[106:107], v227 offset:10240\n"
    "s_waitcnt lgkmcnt(14)\n"
    "buffer_load_dwordx4 v230, s[44:47], s96 offen offset:2048 lds\n"
    "v_mfma_f32_16x16x32_bf16 v[48:51], a[72:75], v[144:147], v[48:51]\n"
    "ds_read_b64_tr_b16 a[108:109], v227 offset:12288\n"
    "v_mfma_f32_16x16x32_bf16 v[48:51], a[76:79], v[152:155], v[48:51]\n"
    "ds_read_b64_tr_b16 a[110:111], v227 offset:14336\n"
    "s_waitcnt lgkmcnt(12)\n"
    "v_mfma_f32_16x16x32_bf16 v[48:51], a[80:83], v[160:163], v[48:51]\n"
    "ds_read_b64_tr_b16 a[112:113], v227 offset:16384\n"
    "v_add_u32 v224, 0x10000, v224\n"
    "v_mfma_f32_16x16x32_bf16 v[48:51], a[84:87], v[168:171], v[48:51]\n"
    "ds_read_b64_tr_b16 a[114:115], v227 offset:18432\n"
    "s_waitcnt lgkmcnt(10)\n"
    "buffer_load_dwordx4 v231, s[44:47], s96 offen offset:3072 lds\n"
    "v_mfma_f32_16x16x32_bf16 v[48:51], a[88:91], v[176:179], v[48:51]\n"
    "ds_read_b64_tr_b16 a[116:117], v227 offset:20480\n"
    "v_cvt_pk_bf16_f32 v64, v32, v33\n"
    "v_mfma_f32_16x16x32_bf16 v[48:51], a[92:95], v[184:187], v[48:51]\n"
    "ds_read_b64_tr_b16 a[118:119], v227 offset:22528\n"
    "v_cvt_pk_bf16_f32 v65, v34, v35\n"
    "v_mfma_f32_16x16x32_bf16 v[52:55], a[64:67], v[132:135], 0\n"
    "ds_read_b64_tr_b16 a[120:121], v227 offset:24576\n"
    "s_lshr_b32 s98, s76, 1\n"
    "v_mfma_f32_16x16x32_bf16 v[52:55], a[68:71], v[140:143], v[52:55]\n"
    "ds_read_b64_tr_b16 a[122:123], v227 offset:26624\n"
    "v_cvt_pk_bf16_f32 v66, v40, v41\n"
    "v_mfma_f32_16x16x32_bf16 v[52:55], a[72:75], v[148:151], v[52:55]\n"
    "ds_read_b64_tr_b16 a[124:125], v227 offset:28672\n"
    "s_waitcnt vmcnt(20)\n"
    "v_mfma_f32_16x16x32_bf16 v[52:55], a[76:79], v[156:159], v[52:55]\n"
    "ds_read_b64_tr_b16 a[126:127], v227 offset:30720\n"
    "s_barrier\n"
    "v_cvt_pk_bf16_f32 v67, v42, v43\n"
    "v_mfma_f32_16x16x32_bf16 v[52:55], a[80:83], v[164:167], v[52:55]\n"
    "ds_read_b64_tr_b16 a[64:65], v224 offset:0\n"
    "s_add_u32 s97, s97, s98\n"
    "v_mfma_f32_16x16x32_bf16 v[52:55], a[84:87], v[172:175], v[52:55]\n"
    "ds_read_b64_tr_b16 a[66:67], v224 offset:2048\n"
    "buffer_store_dwordx4 v[64:67], v210, s[48:51], s92 offen offset:0 sc1\n"
    "v_mfma_f32_16x16x32_bf16 v[52:55], a[88:91], v[180:183], v[52:55]\n"
    "ds_read_b64_tr_b16 a[68:69], v224 offset:4096\n"
    "v_mfma_f32_16x16x32_bf16 v[52:55], a[92:95], v[188:191], v[52:55]\n"
    "ds_read_b64_tr_b16 a[70:71], v224 offset:6144\n"
    "s_waitcnt lgkmcnt(15)\n"
    "s_add_u32 s92, s92, 0x4000\n"
    "v_mfma_f32_16x16x32_bf16 v[56:59], a[96:99], v[128:131], 0\n"
    "ds_read_b64_tr_b16 a[72:73], v224 offset:8192\n"
    "v_mfma_f32_16x16x32_bf16 v[56:59], a[100:103], v[136:139], v[56:59]\n"
    "ds_read_b64_tr_b16 a[74:75], v224 offset:10240\n"
    "s_waitcnt lgkmcnt(14)\n"
    "v_cvt_pk_bf16_f32 v68, v36, v37\n"
    "v_mfma_f32_16x16x32_bf16 v[56:59], a[104:107], v[144:147], v[56:59]\n"
    "ds_read_b64_tr_b16 a[76:77], v224 offset:12288\n"
    "v_cvt_pk_bf16_f32 v69, v38, v39\n"
    "v_mfma_f32_16x16x32_bf16 v[56:59], a[108:111], v[152:155], v[56:59]\n"
    "ds_read_b64_tr_b16 a[78:79], v224 offset:14336\n"
    "s_waitcnt lgkmcnt(12)\n"
    "v_mfma_f32_16x16x32_bf16 v[56:59], a[112:115], v[160:163], v[56:59]\n"
    "ds_read_b64_tr_b16 a[80:81], v224 offset:16384\n"
    "v_add_u32 v225, 0x10000, v225\n"
    "v_cvt_pk_bf16_f32 v70, v44, v45\n"
    "v_mfma_f32_16x16x32_bf16 v[56:59], a[116:119], v[168:171], v[56:59]\n"
    "ds_read_b64_tr_b16 a[82:83], v224 offset:18432\n"
    "s_waitcnt lgkmcnt(10)\n"
    "v_mfma_f32_16x16x32_bf16 v[56:59], a[120:123], v[176:179], v[56:59]\n"
    "ds_read_b64_tr_b16 a[84:85], v224 offset:20480\n"
    "v_cvt_pk_bf16_f32 v71, v46, v47\n"
    "v_mfma_f32_16x16x32_bf16 v[56:59], a[124:127], v[184:187], v[56:59]\n"
    "ds_read_b64_tr_b16 a[86:87], v224 offset:22528\n"
    "s_and_b32 s98, s76, 1\n"
    "v_mfma_f32_16x16x32_bf16 v[60:63], a[96:99], v[132:135], 0\n"
    "ds_read_b64_tr_b16 a[88:89], v224 offset:24576\n"
    "buffer_store_dwordx4 v[68:71], v210, s[48:51], s93 offen offset:0 sc1\n"
    "v_mfma_f32_16x16x32_bf16 v[60:63], a[100:103], v[140:143], v[60:63]\n"
    "ds_read_b64_tr_b16 a[90:91], v224 offset:26624\n"
    "s_lshl_b32 s62, s56, 1\n"
    "v_mfma_f32_16x16x32_bf16 v[60:63], a[104:107], v[148:151], v[60:63]\n"
    "ds_read_b64_tr_b16 a[92:93], v224 offset:28672\n"
    "s_add_u32 s93, s93, 0x4000\n"
    "v_mfma_f32_16x16x32_bf16 v[60:63], a[108:111], v[156:159], v[60:63]\n"
    "ds_read_b64_tr_b16 a[94:95], v224 offset:30720\n"
    "v_mfma_f32_16x16x32_bf16 v[60:63], a[112:115], v[164:167], v[60:63]\n"
    "ds_read_b64_tr_b16 a[96:97], v225 offset:0\n"
    "v_mfma_f32_16x16x32_bf16 v[60:63], a[116:119], v[172:175], v[60:63]\n"
    "ds_read_b64_tr_b16 a[98:99], v225 offset:2048\n"
    "v_mfma_f32_16x16x32_bf16 v[60:63], a[120:123], v[180:183], v[60:63]\n"
    "ds_read_b64_tr_b16 a[100:101], v225 offset:4096\n"
    "v_mfma_f32_16x16x32_bf16 v[60:63], a[124:127], v[188:191], v[60:63]\n"
    "ds_read_b64_tr_b16 a[102:103], v225 offset:6144\n"
    "s_waitcnt lgkmcnt(15)\n"
    "v_mfma_f32_16x16x32_bf16 v[0:3], a[64:67], v[128:131], 0\n"
    "ds_read_b64_tr_b16 a[104:105], v225 offset:8192\n"
    "v_mfma_f32_16x16x32_bf16 v[0:3], a[68:71], v[136:139], v[0:3]\n"
    "ds_read_b64_tr_b16 a[106:107], v225 offset:10240\n"
    "s_waitcnt lgkmcnt(14)\n"
    "s_add_u32 m0, s95, 0x20000\n"
    "v_mfma_f32_16x16x32_bf16 v[0:3], a[72:75], v[144:147], v[0:3]\n"
    "ds_read_b64_tr_b16 a[108:109], v225 offset:12288\n"
    "s_add_u32 s98, s98, s62\n"
    "v_mfma_f32_16x16x32_bf16 v[0:3], a[76:79], v[152:155], v[0:3]\n"
    "ds_read_b64_tr_b16 a[110:111], v225 offset:14336\n"
    "s_waitcnt lgkmcnt(12)\n"
    "v_mfma_f32_16x16x32_bf16 v[0:3], a[80:83], v[160:163], v[0:3]\n"
    "ds_read_b64_tr_b16 a[112:113], v225 offset:16384\n"
    "v_add_u32 v226, 0x10000, v226\n"
    "v_mfma_f32_16x16x32_bf16 v[0:3], a[84:87], v[168:171], v[0:3]\n"
    "ds_read_b64_tr_b16 a[114:115], v225 offset:18432\n"
    "s_waitcnt lgkmcnt(10)\n"
    "v_mfma_f32_16x16x32_bf16 v[0:3], a[88:91], v[176:179], v[0:3]\n"
    "ds_read_b64_tr_b16 a[116:117], v225 offset:20480\n"
    "v_cvt_pk_bf16_f32 v72, v48, v49\n"
    "v_mfma_f32_16x16x32_bf16 v[0:3], a[92:95], v[184:187], v[0:3]\n"
    "ds_read_b64_tr_b16 a[118:119], v225 offset:22528\n"
    "s_mov_b32 s96, 640\n"
    "v_mfma_f32_16x16x32_bf16 v[4:7], a[64:67], v[132:135], 0\n"
    "ds_read_b64_tr_b16 a[120:121], v225 offset:24576\n"
    "v_cvt_pk_bf16_f32 v73, v50, v51\n"
    "v_mfma_f32_16x16x32_bf16 v[4:7], a[68:71], v[140:143], v[4:7]\n"
    "ds_read_b64_tr_b16 a[122:123], v225 offset:26624\n"
    "s_mul_i32 s62, s97, 14\n"
    "v_mfma_f32_16x16x32_bf16 v[4:7], a[72:75], v[148:151], v[4:7]\n"
    "ds_read_b64_tr_b16 a[124:125], v225 offset:28672\n"
    "v_cvt_pk_bf16_f32 v74, v56, v57\n"
    "v_mfma_f32_16x16x32_bf16 v[4:7], a[76:79], v[156:159], v[4:7]\n"
    "ds_read_b64_tr_b16 a[126:127], v225 offset:30720\n"
    "v_mfma_f32_16x16x32_bf16 v[4:7], a[80:83], v[164:167], v[4:7]\n"
    "ds_read_b64_tr_b16 a[64:65], v226 offset:0\n"
    "v_cvt_pk_bf16_f32 v75, v58, v59\n"
    "v_mfma_f32_16x16x32_bf16 v[4:7], a[84:87], v[172:175], v[4:7]\n"
    "ds_read_b64_tr_b16 a[66:67], v226 offset:2048\n"
    "s_add_u32 s62, s62, s98\n"
    "v_mfma_f32_16x16x32_bf16 v[4:7], a[88:91], v[180:183], v[4:7]\n"
    "ds_read_b64_tr_b16 a[68:69], v226 offset:4096\n"
    "buffer_load_dwordx4 v228, s[44:47], s96 offen offset:0 lds\n"
    "v_mfma_f32_16x16x32_bf16 v[4:7], a[92:95], v[188:191], v[4:7]\n"
    "ds_read_b64_tr_b16 a[70:71], v226 offset:6144\n"
    "s_waitcnt lgkmcnt(15)\n"
    "buffer_store_dwordx4 v[72:75], v210, s[48:51], s92 offen offset:0 sc1\n"
    "v_mfma_f32_16x16x32_bf16 v[8:11], a[96:99], v[128:131], 0\n"
    "ds_read_b64_tr_b16 a[72:73], v226 offset:8192\n"
    "s_add_u32 s92, s92, 0x4000\n"
    "v_mfma_f32_16x16x32_bf16 v[8:11], a[100:103], v[136:139], v[8:11]\n"
    "ds_read_b64_tr_b16 a[74:75], v226 offset:10240\n"
    "s_waitcnt lgkmcnt(14)\n"
    "v_mfma_f32_16x16x32_bf16 v[8:11], a[104:107], v[144:147], v[8:11]\n"
    "ds_read_b64_tr_b16 a[76:77], v226 offset:12288\n"
    "v_cvt_pk_bf16_f32 v76, v52, v53\n"
    "v_mfma_f32_16x16x32_bf16 v[8:11], a[108:111], v[152:155], v[8:11]\n"
    "ds_read_b64_tr_b16 a[78:79], v226 offset:14336\n"
    "s_waitcnt lgkmcnt(12)\n"
    "v_mfma_f32_16x16x32_bf16 v[8:11], a[112:115], v[160:163], v[8:11]\n"
    "ds_read_b64_tr_b16 a[80:81], v226 offset:16384\n"
    "v_add_u32 v227, 0x10000, v227\n"
    "v_cvt_pk_bf16_f32 v77, v54, v55\n"
    "v_mfma_f32_16x16x32_bf16 v[8:11], a[116:119], v[168:171], v[8:11]\n"
    "ds_read_b64_tr_b16 a[82:83], v226 offset:18432\n"
    "s_waitcnt lgkmcnt(10)\n"
    "buffer_load_dwordx4 v229, s[44:47], s96 offen offset:1024 lds\n"
    "v_mfma_f32_16x16x32_bf16 v[8:11], a[120:123], v[176:179], v[8:11]\n"
    "ds_read_b64_tr_b16 a[84:85], v226 offset:20480\n"
    "v_cvt_pk_bf16_f32 v78, v60, v61\n"
    "v_mfma_f32_16x16x32_bf16 v[8:11], a[124:127], v[184:187], v[8:11]\n"
    "ds_read_b64_tr_b16 a[86:87], v226 offset:22528\n"
    "s_lshl_b32 s62, s62, 4\n"
    "v_mfma_f32_16x16x32_bf16 v[12:15], a[96:99], v[132:135], 0\n"
    "ds_read_b64_tr_b16 a[88:89], v226 offset:24576\n"
    "v_cvt_pk_bf16_f32 v79, v62, v63\n"
    "v_mfma_f32_16x16x32_bf16 v[12:15], a[100:103], v[140:143], v[12:15]\n"
    "ds_read_b64_tr_b16 a[90:91], v226 offset:26624\n"
    "s_sub_u32 s62, s62, s76\n"
    "v_mfma_f32_16x16x32_bf16 v[12:15], a[104:107], v[148:151], v[12:15]\n"
    "ds_read_b64_tr_b16 a[92:93], v226 offset:28672\n"
    "buffer_store_dwordx4 v[76:79], v210, s[48:51], s93 offen offset:0 sc1\n"
    "v_mfma_f32_16x16x32_bf16 v[12:15], a[108:111], v[156:159], v[12:15]\n"
    "ds_read_b64_tr_b16 a[94:95], v226 offset:30720\n"
    "buffer_load_dwordx4 v230, s[44:47], s96 offen offset:2048 lds\n"
    "v_mfma_f32_16x16x32_bf16 v[12:15], a[112:115], v[164:167], v[12:15]\n"
    "ds_read_b64_tr_b16 a[96:97], v227 offset:0\n"
    "s_add_u32 s93, s93, 0x4000\n"
    "v_mfma_f32_16x16x32_bf16 v[12:15], a[116:119], v[172:175], v[12:15]\n"
    "ds_read_b64_tr_b16 a[98:99], v227 offset:2048\n"
    "s_lshl_b32 s62, s62, 10\n"
    "v_mfma_f32_16x16x32_bf16 v[12:15], a[120:123], v[180:183], v[12:15]\n"
    "ds_read_b64_tr_b16 a[100:101], v227 offset:4096\n"
    "v_mfma_f32_16x16x32_bf16 v[12:15], a[124:127], v[188:191], v[12:15]\n"
    "ds_read_b64_tr_b16 a[102:103], v227 offset:6144\n"
    "s_waitcnt lgkmcnt(15)\n"
    "v_mfma_f32_16x16x32_bf16 v[16:19], a[64:67], v[128:131], 0\n"
    "ds_read_b64_tr_b16 a[104:105], v227 offset:8192\n"
    "v_mfma_f32_16x16x32_bf16 v[16:19], a[68:71], v[136:139], v[16:19]\n"
    "ds_read_b64_tr_b16 a[106:107], v227 offset:10240\n"
    "s_waitcnt lgkmcnt(14)\n"
    "buffer_load_dwordx4 v231, s[44:47], s96 offen offset:3072 lds\n"
    "v_mfma_f32_16x16x32_bf16 v[16:19], a[72:75], v[144:147], v[16:19]\n"
    "ds_read_b64_tr_b16 a[108:109], v227 offset:12288\n"
    "s_mul_i32 s80, s97, 28672\n"
    "v_mfma_f32_16x16x32_bf16 v[16:19], a[76:79], v[152:155], v[16:19]\n"
    "ds_read_b64_tr_b16 a[110:111], v227 offset:14336\n"
    "s_waitcnt lgkmcnt(12)\n"
    "v_mfma_f32_16x16x32_bf16 v[16:19], a[80:83], v[160:163], v[16:19]\n"
    "ds_read_b64_tr_b16 a[112:113], v227 offset:16384\n"
    "v_mfma_f32_16x16x32_bf16 v[16:19], a[84:87], v[168:171], v[16:19]\n"
    "ds_read_b64_tr_b16 a[114:115], v227 offset:18432\n"
    "s_waitcnt lgkmcnt(10)\n"
    "v_mfma_f32_16x16x32_bf16 v[16:19], a[88:91], v[176:179], v[16:19]\n"
    "ds_read_b64_tr_b16 a[116:117], v227 offset:20480\n"
    "v_cvt_pk_bf16_f32 v64, v0, v1\n"
    "v_mfma_f32_16x16x32_bf16 v[16:19], a[92:95], v[184:187], v[16:19]\n"
    "ds_read_b64_tr_b16 a[118:119], v227 offset:22528\n"
    "v_cvt_pk_bf16_f32 v65, v2, v3\n"
    "v_mfma_f32_16x16x32_bf16 v[20:23], a[64:67], v[132:135], 0\n"
    "ds_read_b64_tr_b16 a[120:121], v227 offset:24576\n"
    "s_lshl_b32 s98, s98, 6\n"
    "v_mfma_f32_16x16x32_bf16 v[20:23], a[68:71], v[140:143], v[20:23]\n"
    "ds_read_b64_tr_b16 a[122:123], v227 offset:26624\n"
    "v_cvt_pk_bf16_f32 v66, v8, v9\n"
    "v_mfma_f32_16x16x32_bf16 v[20:23], a[72:75], v[148:151], v[20:23]\n"
    "ds_read_b64_tr_b16 a[124:125], v227 offset:28672\n"
    "s_waitcnt vmcnt(18)\n"
    "v_mfma_f32_16x16x32_bf16 v[20:23], a[76:79], v[156:159], v[20:23]\n"
    "ds_read_b64_tr_b16 a[126:127], v227 offset:30720\n"
    "s_barrier\n"
    "v_cvt_pk_bf16_f32 v67, v10, v11\n"
    "v_mfma_f32_16x16x32_bf16 v[20:23], a[80:83], v[164:167], v[20:23]\n"
    "ds_read_b64_tr_b16 a[64:65], v224 offset:32768\n"
    "v_mfma_f32_16x16x32_bf16 v[20:23], a[84:87], v[172:175], v[20:23]\n"
    "ds_read_b64_tr_b16 a[66:67], v224 offset:34816\n"
    "buffer_store_dwordx4 v[64:67], v210, s[48:51], s92 offen offset:0 sc1\n"
    "v_mfma_f32_16x16x32_bf16 v[20:23], a[88:91], v[180:183], v[20:23]\n"
    "ds_read_b64_tr_b16 a[68:69], v224 offset:36864\n"
    "s_add_u32 s80, s80, s98\n"
    "v_mfma_f32_16x16x32_bf16 v[20:23], a[92:95], v[188:191], v[20:23]\n"
    "ds_read_b64_tr_b16 a[70:71], v224 offset:38912\n"
    "s_waitcnt lgkmcnt(15)\n"
    "s_add_u32 s92, s92, 0x4000\n"
    "v_mfma_f32_16x16x32_bf16 v[24:27], a[96:99], v[128:131], 0\n"
    "ds_read_b64_tr_b16 a[72:73], v224 offset:40960\n"
    "v_mfma_f32_16x16x32_bf16 v[24:27], a[100:103], v[136:139], v[24:27]\n"
    "ds_read_b64_tr_b16 a[74:75], v224 offset:43008\n"
    "s_waitcnt lgkmcnt(14)\n"
    "v_cvt_pk_bf16_f32 v68, v4, v5\n"
    "v_mfma_f32_16x16x32_bf16 v[24:27], a[104:107], v[144:147], v[24:27]\n"
    "ds_read_b64_tr_b16 a[76:77], v224 offset:45056\n"
    "v_cvt_pk_bf16_f32 v69, v6, v7\n"
    "v_mfma_f32_16x16x32_bf16 v[24:27], a[108:111], v[152:155], v[24:27]\n"
    "ds_read_b64_tr_b16 a[78:79], v224 offset:47104\n"
    "s_waitcnt lgkmcnt(12)\n"
    "v_mfma_f32_16x16x32_bf16 v[24:27], a[112:115], v[160:163], v[24:27]\n"
    "ds_read_b64_tr_b16 a[80:81], v224 offset:49152\n"
    "v_cvt_pk_bf16_f32 v70, v12, v13\n"
    "v_mfma_f32_16x16x32_bf16 v[24:27], a[116:119], v[168:171], v[24:27]\n"
    "ds_read_b64_tr_b16 a[82:83], v224 offset:51200\n"
    "s_waitcnt lgkmcnt(10)\n"
    "v_mfma_f32_16x16x32_bf16 v[24:27], a[120:123], v[176:179], v[24:27]\n"
    "ds_read_b64_tr_b16 a[84:85], v224 offset:53248\n"
    "v_cvt_pk_bf16_f32 v71, v14, v15\n"
    "v_mfma_f32_16x16x32_bf16 v[24:27], a[124:127], v[184:187], v[24:27]\n"
    "ds_read_b64_tr_b16 a[86:87], v224 offset:55296\n"
    "s_add_u32 s80, s80, 896\n"
    "v_mfma_f32_16x16x32_bf16 v[28:31], a[96:99], v[132:135], 0\n"
    "ds_read_b64_tr_b16 a[88:89], v224 offset:57344\n"
    "buffer_store_dwordx4 v[68:71], v210, s[48:51], s93 offen offset:0 sc1\n"
    "v_mfma_f32_16x16x32_bf16 v[28:31], a[100:103], v[140:143], v[28:31]\n"
    "ds_read_b64_tr_b16 a[90:91], v224 offset:59392\n"
    "s_mov_b64 s[36:37], s[48:49]\n"
    "v_mfma_f32_16x16x32_bf16 v[28:31], a[104:107], v[148:151], v[28:31]\n"
    "ds_read_b64_tr_b16 a[92:93], v224 offset:61440\n"
    "s_add_u32 s93, s93, 0x4000\n"
    "v_mfma_f32_16x16x32_bf16 v[28:31], a[108:111], v[156:159], v[28:31]\n"
    "ds_read_b64_tr_b16 a[94:95], v224 offset:63488\n"
    "v_mfma_f32_16x16x32_bf16 v[28:31], a[112:115], v[164:167], v[28:31]\n"
    "ds_read_b64_tr_b16 a[96:97], v225 offset:32768\n"
    "v_mfma_f32_16x16x32_bf16 v[28:31], a[116:119], v[172:175], v[28:31]\n"
    "ds_read_b64_tr_b16 a[98:99], v225 offset:34816\n"
    "v_mfma_f32_16x16x32_bf16 v[28:31], a[120:123], v[180:183], v[28:31]\n"
    "ds_read_b64_tr_b16 a[100:101], v225 offset:36864\n"
    "v_mfma_f32_16x16x32_bf16 v[28:31], a[124:127], v[188:191], v[28:31]\n"
    "ds_read_b64_tr_b16 a[102:103], v225 offset:38912\n"
    "s_waitcnt lgkmcnt(15)\n"
    "v_mfma_f32_16x16x32_bf16 v[32:35], a[64:67], v[128:131], 0\n"
    "ds_read_b64_tr_b16 a[104:105], v225 offset:40960\n"
    "v_mfma_f32_16x16x32_bf16 v[32:35], a[68:71], v[136:139], v[32:35]\n"
    "ds_read_b64_tr_b16 a[106:107], v225 offset:43008\n"
    "s_waitcnt lgkmcnt(14)\n"
    "s_add_u32 m0, s95, 0x21000\n"
    "v_mfma_f32_16x16x32_bf16 v[32:35], a[72:75], v[144:147], v[32:35]\n"
    "ds_read_b64_tr_b16 a[108:109], v225 offset:45056\n"
    "v_mfma_f32_16x16x32_bf16 v[32:35], a[76:79], v[152:155], v[32:35]\n"
    "ds_read_b64_tr_b16 a[110:111], v225 offset:47104\n"
    "s_waitcnt lgkmcnt(12)\n"
    "v_mfma_f32_16x16x32_bf16 v[32:35], a[80:83], v[160:163], v[32:35]\n"
    "ds_read_b64_tr_b16 a[112:113], v225 offset:49152\n"
    "s_mov_b32 s96, 29312\n"
    "v_mfma_f32_16x16x32_bf16 v[32:35], a[84:87], v[168:171], v[32:35]\n"
    "ds_read_b64_tr_b16 a[114:115], v225 offset:51200\n"
    "s_waitcnt lgkmcnt(10)\n"
    "v_mfma_f32_16x16x32_bf16 v[32:35], a[88:91], v[176:179], v[32:35]\n"
    "ds_read_b64_tr_b16 a[116:117], v225 offset:53248\n"
    "v_cvt_pk_bf16_f32 v72, v16, v17\n"
    "v_mfma_f32_16x16x32_bf16 v[32:35], a[92:95], v[184:187], v[32:35]\n"
    "ds_read_b64_tr_b16 a[118:119], v225 offset:55296\n"
    "buffer_load_dwordx4 v228, s[44:47], s96 offen offset:0 lds\n"
    "v_mfma_f32_16x16x32_bf16 v[36:39], a[64:67], v[132:135], 0\n"
    "ds_read_b64_tr_b16 a[120:121], v225 offset:57344\n"
    "v_cvt_pk_bf16_f32 v73, v18, v19\n"
    "v_mfma_f32_16x16x32_bf16 v[36:39], a[68:71], v[140:143], v[36:39]\n"
    "ds_read_b64_tr_b16 a[122:123], v225 offset:59392\n"
    "v_mfma_f32_16x16x32_bf16 v[36:39], a[72:75], v[148:151], v[36:39]\n"
    "ds_read_b64_tr_b16 a[124:125], v225 offset:61440\n"
    "buffer_load_dwordx4 v229, s[44:47], s96 offen offset:1024 lds\n"
    "v_mfma_f32_16x16x32_bf16 v[36:39], a[76:79], v[156:159], v[36:39]\n"
    "ds_read_b64_tr_b16 a[126:127], v225 offset:63488\n"
    "v_cvt_pk_bf16_f32 v74, v24, v25\n"
    "v_mfma_f32_16x16x32_bf16 v[36:39], a[80:83], v[164:167], v[36:39]\n"
    "ds_read_b64_tr_b16 a[64:65], v226 offset:32768\n"
    "v_cvt_pk_bf16_f32 v75, v26, v27\n"
    "v_mfma_f32_16x16x32_bf16 v[36:39], a[84:87], v[172:175], v[36:39]\n"
    "ds_read_b64_tr_b16 a[66:67], v226 offset:34816\n"
    "v_mfma_f32_16x16x32_bf16 v[36:39], a[88:91], v[180:183], v[36:39]\n"
    "ds_read_b64_tr_b16 a[68:69], v226 offset:36864\n"
    "buffer_load_dwordx4 v230, s[44:47], s96 offen offset:2048 lds\n"
    "v_mfma_f32_16x16x32_bf16 v[36:39], a[92:95], v[188:191], v[36:39]\n"
    "ds_read_b64_tr_b16 a[70:71], v226 offset:38912\n"
    "s_waitcnt lgkmcnt(15)\n"
    "buffer_store_dwordx4 v[72:75], v210, s[48:51], s92 offen offset:0 sc1\n"
    "v_mfma_f32_16x16x32_bf16 v[40:43], a[96:99], v[128:131], 0\n"
    "ds_read_b64_tr_b16 a[72:73], v226 offset:40960\n"
    "s_add_u32 s92, s92, 0x4000\n"
    "v_mfma_f32_16x16x32_bf16 v[40:43], a[100:103], v[136:139], v[40:43]\n"
    "ds_read_b64_tr_b16 a[74:75], v226 offset:43008\n"
    "s_waitcnt lgkmcnt(14)\n"
    "buffer_load_dwordx4 v231, s[44:47], s96 offen offset:3072 lds\n"
    "v_mfma_f32_16x16x32_bf16 v[40:43], a[104:107], v[144:147], v[40:43]\n"
    "ds_read_b64_tr_b16 a[76:77], v226 offset:45056\n"
    "v_cvt_pk_bf16_f32 v76, v20, v21\n"
    "v_mfma_f32_16x16x32_bf16 v[40:43], a[108:111], v[152:155], v[40:43]\n"
    "ds_read_b64_tr_b16 a[78:79], v226 offset:47104\n"
    "s_waitcnt lgkmcnt(12)\n"
    "v_mfma_f32_16x16x32_bf16 v[40:43], a[112:115], v[160:163], v[40:43]\n"
    "ds_read_b64_tr_b16 a[80:81], v226 offset:49152\n"
    "s_add_u32 m0, s95, 0x0\n"
    "v_cvt_pk_bf16_f32 v77, v22, v23\n"
    "v_mfma_f32_16x16x32_bf16 v[40:43], a[116:119], v[168:171], v[40:43]\n"
    "ds_read_b64_tr_b16 a[82:83], v226 offset:51200\n"
    "s_waitcnt lgkmcnt(10)\n"
    "v_mfma_f32_16x16x32_bf16 v[40:43], a[120:123], v[176:179], v[40:43]\n"
    "ds_read_b64_tr_b16 a[84:85], v226 offset:53248\n"
    "v_cvt_pk_bf16_f32 v78, v28, v29\n"
    "v_mfma_f32_16x16x32_bf16 v[40:43], a[124:127], v[184:187], v[40:43]\n"
    "ds_read_b64_tr_b16 a[86:87], v226 offset:55296\n"
    "s_mov_b32 s96, 768\n"
    "v_mfma_f32_16x16x32_bf16 v[44:47], a[96:99], v[132:135], 0\n"
    "ds_read_b64_tr_b16 a[88:89], v226 offset:57344\n"
    "v_cvt_pk_bf16_f32 v79, v30, v31\n"
    "v_mfma_f32_16x16x32_bf16 v[44:47], a[100:103], v[140:143], v[44:47]\n"
    "ds_read_b64_tr_b16 a[90:91], v226 offset:59392\n"
    "v_mfma_f32_16x16x32_bf16 v[44:47], a[104:107], v[148:151], v[44:47]\n"
    "ds_read_b64_tr_b16 a[92:93], v226 offset:61440\n"
    "buffer_store_dwordx4 v[76:79], v210, s[48:51], s93 offen offset:0 sc1\n"
    "v_mfma_f32_16x16x32_bf16 v[44:47], a[108:111], v[156:159], v[44:47]\n"
    "ds_read_b64_tr_b16 a[94:95], v226 offset:63488\n"
    "buffer_load_dwordx4 v228, s[44:47], s96 offen offset:0 lds\n"
    "v_mfma_f32_16x16x32_bf16 v[44:47], a[112:115], v[164:167], v[44:47]\n"
    "ds_read_b64_tr_b16 a[96:97], v227 offset:32768\n"
    "s_add_u32 s93, s93, 0x4000\n"
    "v_mfma_f32_16x16x32_bf16 v[44:47], a[116:119], v[172:175], v[44:47]\n"
    "ds_read_b64_tr_b16 a[98:99], v227 offset:34816\n"
    "v_mfma_f32_16x16x32_bf16 v[44:47], a[120:123], v[180:183], v[44:47]\n"
    "ds_read_b64_tr_b16 a[100:101], v227 offset:36864\n"
    "buffer_load_dwordx4 v229, s[44:47], s96 offen offset:1024 lds\n"
    "v_mfma_f32_16x16x32_bf16 v[44:47], a[124:127], v[188:191], v[44:47]\n"
    "ds_read_b64_tr_b16 a[102:103], v227 offset:38912\n"
    "s_waitcnt lgkmcnt(15)\n"
    "v_mfma_f32_16x16x32_bf16 v[48:51], a[64:67], v[128:131], 0\n"
    "ds_read_b64_tr_b16 a[104:105], v227 offset:40960\n"
    "v_mfma_f32_16x16x32_bf16 v[48:51], a[68:71], v[136:139], v[48:51]\n"
    "ds_read_b64_tr_b16 a[106:107], v227 offset:43008\n"
    "s_waitcnt lgkmcnt(14)\n"
    "buffer_load_dwordx4 v230, s[44:47], s96 offen offset:2048 lds\n"
    "v_mfma_f32_16x16x32_bf16 v[48:51], a[72:75], v[144:147], v[48:51]\n"
    "ds_read_b64_tr_b16 a[108:109], v227 offset:45056\n"
    "v_mfma_f32_16x16x32_bf16 v[48:51], a[76:79], v[152:155], v[48:51]\n"
    "ds_read_b64_tr_b16 a[110:111], v227 offset:47104\n"
    "s_waitcnt lgkmcnt(12)\n"
    "v_mfma_f32_16x16x32_bf16 v[48:51], a[80:83], v[160:163], v[48:51]\n"
    "ds_read_b64_tr_b16 a[112:113], v227 offset:49152\n"
    "v_add_u32 v224, 0xffff8000, v224\n"
    "v_mfma_f32_16x16x32_bf16 v[48:51], a[84:87], v[168:171], v[48:51]\n"
    "ds_read_b64_tr_b16 a[114:115], v227 offset:51200\n"
    "s_waitcnt lgkmcnt(10)\n"
    "buffer_load_dwordx4 v231, s[44:47], s96 offen offset:3072 lds\n"
    "v_mfma_f32_16x16x32_bf16 v[48:51], a[88:91], v[176:179], v[48:51]\n"
    "ds_read_b64_tr_b16 a[116:117], v227 offset:53248\n"
    "v_cvt_pk_bf16_f32 v64, v32, v33\n"
    "v_mfma_f32_16x16x32_bf16 v[48:51], a[92:95], v[184:187], v[48:51]\n"
    "ds_read_b64_tr_b16 a[118:119], v227 offset:55296\n"
    "v_cvt_pk_bf16_f32 v65, v34, v35\n"
    "v_mfma_f32_16x16x32_bf16 v[52:55], a[64:67], v[132:135], 0\n"
    "ds_read_b64_tr_b16 a[120:121], v227 offset:57344\n"
    "v_mfma_f32_16x16x32_bf16 v[52:55], a[68:71], v[140:143], v[52:55]\n"
    "ds_read_b64_tr_b16 a[122:123], v227 offset:59392\n"
    "v_cvt_pk_bf16_f32 v66, v40, v41\n"
    "v_mfma_f32_16x16x32_bf16 v[52:55], a[72:75], v[148:151], v[52:55]\n"
    "ds_read_b64_tr_b16 a[124:125], v227 offset:61440\n"
    "s_waitcnt vmcnt(20)\n"
    "v_mfma_f32_16x16x32_bf16 v[52:55], a[76:79], v[156:159], v[52:55]\n"
    "ds_read_b64_tr_b16 a[126:127], v227 offset:63488\n"
    "s_barrier\n"
    "v_cvt_pk_bf16_f32 v67, v42, v43\n"
    "v_mfma_f32_16x16x32_bf16 v[52:55], a[80:83], v[164:167], v[52:55]\n"
    "ds_read_b64_tr_b16 a[64:65], v224 offset:0\n"
    "v_mfma_f32_16x16x32_bf16 v[52:55], a[84:87], v[172:175], v[52:55]\n"
    "ds_read_b64_tr_b16 a[66:67], v224 offset:2048\n"
    "buffer_store_dwordx4 v[64:67], v210, s[48:51], s92 offen offset:0 sc1\n"
    "v_mfma_f32_16x16x32_bf16 v[52:55], a[88:91], v[180:183], v[52:55]\n"
    "ds_read_b64_tr_b16 a[68:69], v224 offset:4096\n"
    "v_mfma_f32_16x16x32_bf16 v[52:55], a[92:95], v[188:191], v[52:55]\n"
    "ds_read_b64_tr_b16 a[70:71], v224 offset:6144\n"
    "s_waitcnt lgkmcnt(15)\n"
    "s_add_u32 s92, s92, 0x4000\n"
    "v_mfma_f32_16x16x32_bf16 v[56:59], a[96:99], v[128:131], 0\n"
    "ds_read_b64_tr_b16 a[72:73], v224 offset:8192\n"
    "v_mfma_f32_16x16x32_bf16 v[56:59], a[100:103], v[136:139], v[56:59]\n"
    "ds_read_b64_tr_b16 a[74:75], v224 offset:10240\n"
    "s_waitcnt lgkmcnt(14)\n"
    "v_cvt_pk_bf16_f32 v68, v36, v37\n"
    "v_mfma_f32_16x16x32_bf16 v[56:59], a[104:107], v[144:147], v[56:59]\n"
    "ds_read_b64_tr_b16 a[76:77], v224 offset:12288\n"
    "v_cvt_pk_bf16_f32 v69, v38, v39\n"
    "v_mfma_f32_16x16x32_bf16 v[56:59], a[108:111], v[152:155], v[56:59]\n"
    "ds_read_b64_tr_b16 a[78:79], v224 offset:14336\n"
    "s_waitcnt lgkmcnt(12)\n"
    "v_mfma_f32_16x16x32_bf16 v[56:59], a[112:115], v[160:163], v[56:59]\n"
    "ds_read_b64_tr_b16 a[80:81], v224 offset:16384\n"
    "v_add_u32 v225, 0xffff8000, v225\n"
    "v_cvt_pk_bf16_f32 v70, v44, v45\n"
    "v_mfma_f32_16x16x32_bf16 v[56:59], a[116:119], v[168:171], v[56:59]\n"
    "ds_read_b64_tr_b16 a[82:83], v224 offset:18432\n"
    "s_waitcnt lgkmcnt(10)\n"
    "v_mfma_f32_16x16x32_bf16 v[56:59], a[120:123], v[176:179], v[56:59]\n"
    "ds_read_b64_tr_b16 a[84:85], v224 offset:20480\n"
    "v_cvt_pk_bf16_f32 v71, v46, v47\n"
    "v_mfma_f32_16x16x32_bf16 v[56:59], a[124:127], v[184:187], v[56:59]\n"
    "ds_read_b64_tr_b16 a[86:87], v224 offset:22528\n"
    "v_mfma_f32_16x16x32_bf16 v[60:63], a[96:99], v[132:135], 0\n"
    "ds_read_b64_tr_b16 a[88:89], v224 offset:24576\n"
    "buffer_store_dwordx4 v[68:71], v210, s[48:51], s93 offen offset:0 sc1\n"
    "v_mfma_f32_16x16x32_bf16 v[60:63], a[100:103], v[140:143], v[60:63]\n"
    "ds_read_b64_tr_b16 a[90:91], v224 offset:26624\n"
    "v_mfma_f32_16x16x32_bf16 v[60:63], a[104:107], v[148:151], v[60:63]\n"
    "ds_read_b64_tr_b16 a[92:93], v224 offset:28672\n"
    "s_add_u32 s93, s93, 0x4000\n"
    "v_mfma_f32_16x16x32_bf16 v[60:63], a[108:111], v[156:159], v[60:63]\n"
    "ds_read_b64_tr_b16 a[94:95], v224 offset:30720\n"
    "v_mfma_f32_16x16x32_bf16 v[60:63], a[112:115], v[164:167], v[60:63]\n"
    "ds_read_b64_tr_b16 a[96:97], v225 offset:0\n"
    "v_mfma_f32_16x16x32_bf16 v[60:63], a[116:119], v[172:175], v[60:63]\n"
    "ds_read_b64_tr_b16 a[98:99], v225 offset:2048\n"
    "v_mfma_f32_16x16x32_bf16 v[60:63], a[120:123], v[180:183], v[60:63]\n"
    "ds_read_b64_tr_b16 a[100:101], v225 offset:4096\n"
    "v_mfma_f32_16x16x32_bf16 v[60:63], a[124:127], v[188:191], v[60:63]\n"
    "ds_read_b64_tr_b16 a[102:103], v225 offset:6144\n"
    "s_waitcnt lgkmcnt(15)\n"
    "v_mfma_f32_16x16x32_bf16 v[0:3], a[64:67], v[128:131], 0\n"
    "ds_read_b64_tr_b16 a[104:105], v225 offset:8192\n"
    "v_mfma_f32_16x16x32_bf16 v[0:3], a[68:71], v[136:139], v[0:3]\n"
    "ds_read_b64_tr_b16 a[106:107], v225 offset:10240\n"
    "s_waitcnt lgkmcnt(14)\n"
    "s_add_u32 m0, s95, 0x1000\n"
    "v_mfma_f32_16x16x32_bf16 v[0:3], a[72:75], v[144:147], v[0:3]\n"
    "ds_read_b64_tr_b16 a[108:109], v225 offset:12288\n"
    "v_mfma_f32_16x16x32_bf16 v[0:3], a[76:79], v[152:155], v[0:3]\n"
    "ds_read_b64_tr_b16 a[110:111], v225 offset:14336\n"
    "s_waitcnt lgkmcnt(12)\n"
    "v_mfma_f32_16x16x32_bf16 v[0:3], a[80:83], v[160:163], v[0:3]\n"
    "ds_read_b64_tr_b16 a[112:113], v225 offset:16384\n"
    "v_add_u32 v226, 0xffff8000, v226\n"
    "v_mfma_f32_16x16x32_bf16 v[0:3], a[84:87], v[168:171], v[0:3]\n"
    "ds_read_b64_tr_b16 a[114:115], v225 offset:18432\n"
    "s_waitcnt lgkmcnt(10)\n"
    "v_mfma_f32_16x16x32_bf16 v[0:3], a[88:91], v[176:179], v[0:3]\n"
    "ds_read_b64_tr_b16 a[116:117], v225 offset:20480\n"
    "v_cvt_pk_bf16_f32 v72, v48, v49\n"
    "v_mfma_f32_16x16x32_bf16 v[0:3], a[92:95], v[184:187], v[0:3]\n"
    "ds_read_b64_tr_b16 a[118:119], v225 offset:22528\n"
    "s_mov_b32 s96, 29440\n"
    "v_mfma_f32_16x16x32_bf16 v[4:7], a[64:67], v[132:135], 0\n"
    "ds_read_b64_tr_b16 a[120:121], v225 offset:24576\n"
    "v_cvt_pk_bf16_f32 v73, v50, v51\n"
    "v_mfma_f32_16x16x32_bf16 v[4:7], a[68:71], v[140:143], v[4:7]\n"
    "ds_read_b64_tr_b16 a[122:123], v225 offset:26624\n"
    "v_mfma_f32_16x16x32_bf16 v[4:7], a[72:75], v[148:151], v[4:7]\n"
    "ds_read_b64_tr_b16 a[124:125], v225 offset:28672\n"
    "v_cvt_pk_bf16_f32 v74, v56, v57\n"
    "v_mfma_f32_16x16x32_bf16 v[4:7], a[76:79], v[156:159], v[4:7]\n"
    "ds_read_b64_tr_b16 a[126:127], v225 offset:30720\n"
    "v_mfma_f32_16x16x32_bf16 v[4:7], a[80:83], v[164:167], v[4:7]\n"
    "ds_read_b64_tr_b16 a[64:65], v226 offset:0\n"
    "v_cvt_pk_bf16_f32 v75, v58, v59\n"
    "v_mfma_f32_16x16x32_bf16 v[4:7], a[84:87], v[172:175], v[4:7]\n"
    "ds_read_b64_tr_b16 a[66:67], v226 offset:2048\n"
    "v_mfma_f32_16x16x32_bf16 v[4:7], a[88:91], v[180:183], v[4:7]\n"
    "ds_read_b64_tr_b16 a[68:69], v226 offset:4096\n"
    "buffer_load_dwordx4 v228, s[44:47], s96 offen offset:0 lds\n"
    "v_mfma_f32_16x16x32_bf16 v[4:7], a[92:95], v[188:191], v[4:7]\n"
    "ds_read_b64_tr_b16 a[70:71], v226 offset:6144\n"
    "s_waitcnt lgkmcnt(15)\n"
    "buffer_store_dwordx4 v[72:75], v210, s[48:51], s92 offen offset:0 sc1\n"
    "v_mfma_f32_16x16x32_bf16 v[8:11], a[96:99], v[128:131], 0\n"
    "ds_read_b64_tr_b16 a[72:73], v226 offset:8192\n"
    "s_add_u32 s92, s92, 0x4000\n"
    "v_mfma_f32_16x16x32_bf16 v[8:11], a[100:103], v[136:139], v[8:11]\n"
    "ds_read_b64_tr_b16 a[74:75], v226 offset:10240\n"
    "s_waitcnt lgkmcnt(14)\n"
    "v_mfma_f32_16x16x32_bf16 v[8:11], a[104:107], v[144:147], v[8:11]\n"
    "ds_read_b64_tr_b16 a[76:77], v226 offset:12288\n"
    "v_cvt_pk_bf16_f32 v76, v52, v53\n"
    "v_mfma_f32_16x16x32_bf16 v[8:11], a[108:111], v[152:155], v[8:11]\n"
    "ds_read_b64_tr_b16 a[78:79], v226 offset:14336\n"
    "s_waitcnt lgkmcnt(12)\n"
    "v_mfma_f32_16x16x32_bf16 v[8:11], a[112:115], v[160:163], v[8:11]\n"
    "ds_read_b64_tr_b16 a[80:81], v226 offset:16384\n"
    "v_add_u32 v227, 0xffff8000, v227\n"
    "v_cvt_pk_bf16_f32 v77, v54, v55\n"
    "v_mfma_f32_16x16x32_bf16 v[8:11], a[116:119], v[168:171], v[8:11]\n"
    "ds_read_b64_tr_b16 a[82:83], v226 offset:18432\n"
    "s_waitcnt lgkmcnt(10)\n"
    "buffer_load_dwordx4 v229, s[44:47], s96 offen offset:1024 lds\n"
    "v_mfma_f32_16x16x32_bf16 v[8:11], a[120:123], v[176:179], v[8:11]\n"
    "ds_read_b64_tr_b16 a[84:85], v226 offset:20480\n"
    "v_cvt_pk_bf16_f32 v78, v60, v61\n"
    "v_mfma_f32_16x16x32_bf16 v[8:11], a[124:127], v[184:187], v[8:11]\n"
    "ds_read_b64_tr_b16 a[86:87], v226 offset:22528\n"
    "v_mfma_f32_16x16x32_bf16 v[12:15], a[96:99], v[132:135], 0\n"
    "ds_read_b64_tr_b16 a[88:89], v226 offset:24576\n"
    "v_cvt_pk_bf16_f32 v79, v62, v63\n"
    "v_mfma_f32_16x16x32_bf16 v[12:15], a[100:103], v[140:143], v[12:15]\n"
    "ds_read_b64_tr_b16 a[90:91], v226 offset:26624\n"
    "v_mfma_f32_16x16x32_bf16 v[12:15], a[104:107], v[148:151], v[12:15]\n"
    "ds_read_b64_tr_b16 a[92:93], v226 offset:28672\n"
    "buffer_store_dwordx4 v[76:79], v210, s[48:51], s93 offen offset:0 sc1\n"
    "v_mfma_f32_16x16x32_bf16 v[12:15], a[108:111], v[156:159], v[12:15]\n"
    "ds_read_b64_tr_b16 a[94:95], v226 offset:30720\n"
    "buffer_load_dwordx4 v230, s[44:47], s96 offen offset:2048 lds\n"
    "v_mfma_f32_16x16x32_bf16 v[12:15], a[112:115], v[164:167], v[12:15]\n"
    "ds_read_b64_tr_b16 a[96:97], v227 offset:0\n"
    "s_add_u32 s93, s93, 0x4000\n"
    "v_mfma_f32_16x16x32_bf16 v[12:15], a[116:119], v[172:175], v[12:15]\n"
    "ds_read_b64_tr_b16 a[98:99], v227 offset:2048\n"
    "v_mfma_f32_16x16x32_bf16 v[12:15], a[120:123], v[180:183], v[12:15]\n"
    "ds_read_b64_tr_b16 a[100:101], v227 offset:4096\n"
    "v_mfma_f32_16x16x32_bf16 v[12:15], a[124:127], v[188:191], v[12:15]\n"
    "ds_read_b64_tr_b16 a[102:103], v227 offset:6144\n"
    "s_waitcnt lgkmcnt(15)\n"
    "v_mfma_f32_16x16x32_bf16 v[16:19], a[64:67], v[128:131], 0\n"
    "ds_read_b64_tr_b16 a[104:105], v227 offset:8192\n"
    "v_mfma_f32_16x16x32_bf16 v[16:19], a[68:71], v[136:139], v[16:19]\n"
    "ds_read_b64_tr_b16 a[106:107], v227 offset:10240\n"
    "s_waitcnt lgkmcnt(14)\n"
    "buffer_load_dwordx4 v231, s[44:47], s96 offen offset:3072 lds\n"
    "v_mfma_f32_16x16x32_bf16 v[16:19], a[72:75], v[144:147], v[16:19]\n"
    "ds_read_b64_tr_b16 a[108:109], v227 offset:12288\n"
    "v_mfma_f32_16x16x32_bf16 v[16:19], a[76:79], v[152:155], v[16:19]\n"
    "ds_read_b64_tr_b16 a[110:111], v227 offset:14336\n"
    "s_waitcnt lgkmcnt(12)\n"
    "v_mfma_f32_16x16x32_bf16 v[16:19], a[80:83], v[160:163], v[16:19]\n"
    "ds_read_b64_tr_b16 a[112:113], v227 offset:16384\n"
    "v_add_u32 v224, 0x18000, v224\n"
    "v_mfma_f32_16x16x32_bf16 v[16:19], a[84:87], v[168:171], v[16:19]\n"
    "ds_read_b64_tr_b16 a[114:115], v227 offset:18432\n"
    "s_waitcnt lgkmcnt(10)\n"
    "v_mfma_f32_16x16x32_bf16 v[16:19], a[88:91], v[176:179], v[16:19]\n"
    "ds_read_b64_tr_b16 a[116:117], v227 offset:20480\n"
    "v_cvt_pk_bf16_f32 v64, v0, v1\n"
    "v_mfma_f32_16x16x32_bf16 v[16:19], a[92:95], v[184:187], v[16:19]\n"
    "ds_read_b64_tr_b16 a[118:119], v227 offset:22528\n"
    "v_cvt_pk_bf16_f32 v65, v2, v3\n"
    "v_mfma_f32_16x16x32_bf16 v[20:23], a[64:67], v[132:135], 0\n"
    "ds_read_b64_tr_b16 a[120:121], v227 offset:24576\n"
    "v_mfma_f32_16x16x32_bf16 v[20:23], a[68:71], v[140:143], v[20:23]\n"
    "ds_read_b64_tr_b16 a[122:123], v227 offset:26624\n"
    "v_cvt_pk_bf16_f32 v66, v8, v9\n"
    "v_mfma_f32_16x16x32_bf16 v[20:23], a[72:75], v[148:151], v[20:23]\n"
    "ds_read_b64_tr_b16 a[124:125], v227 offset:28672\n"
    "s_waitcnt vmcnt(13)\n"
    "v_mfma_f32_16x16x32_bf16 v[20:23], a[76:79], v[156:159], v[20:23]\n"
    "ds_read_b64_tr_b16 a[126:127], v227 offset:30720\n"
    "s_barrier\n"
    "v_cvt_pk_bf16_f32 v67, v10, v11\n"
    "v_mfma_f32_16x16x32_bf16 v[20:23], a[80:83], v[164:167], v[20:23]\n"
    "ds_read_b64_tr_b16 a[64:65], v224 offset:0\n"
    "v_mfma_f32_16x16x32_bf16 v[20:23], a[84:87], v[172:175], v[20:23]\n"
    "ds_read_b64_tr_b16 a[66:67], v224 offset:2048\n"
    "buffer_store_dwordx4 v[64:67], v210, s[48:51], s92 offen offset:0 sc1\n"
    "v_mfma_f32_16x16x32_bf16 v[20:23], a[88:91], v[180:183], v[20:23]\n"
    "ds_read_b64_tr_b16 a[68:69], v224 offset:4096\n"
    "v_mfma_f32_16x16x32_bf16 v[20:23], a[92:95], v[188:191], v[20:23]\n"
    "ds_read_b64_tr_b16 a[70:71], v224 offset:6144\n"
    "s_waitcnt lgkmcnt(15)\n"
    "s_add_u32 s92, s92, 0x4000\n"
    "v_mfma_f32_16x16x32_bf16 v[24:27], a[96:99], v[128:131], 0\n"
    "ds_read_b64_tr_b16 a[72:73], v224 offset:8192\n"
    "s_mov_b32 s84, s22\n"
    "v_mfma_f32_16x16x32_bf16 v[24:27], a[100:103], v[136:139], v[24:27]\n"
    "ds_read_b64_tr_b16 a[74:75], v224 offset:10240\n"
    "s_waitcnt lgkmcnt(14)\n"
    "v_cvt_pk_bf16_f32 v68, v4, v5\n"
    "v_mfma_f32_16x16x32_bf16 v[24:27], a[104:107], v[144:147], v[24:27]\n"
    "ds_read_b64_tr_b16 a[76:77], v224 offset:12288\n"
    "s_and_b32 s85, s23, 0xffff\n"
    "v_cvt_pk_bf16_f32 v69, v6, v7\n"
    "v_mfma_f32_16x16x32_bf16 v[24:27], a[108:111], v[152:155], v[24:27]\n"
    "ds_read_b64_tr_b16 a[78:79], v224 offset:14336\n"
    "s_waitcnt lgkmcnt(12)\n"
    "v_mfma_f32_16x16x32_bf16 v[24:27], a[112:115], v[160:163], v[24:27]\n"
    "ds_read_b64_tr_b16 a[80:81], v224 offset:16384\n"
    "v_add_u32 v225, 0x18000, v225\n"
    "v_cvt_pk_bf16_f32 v70, v12, v13\n"
    "v_mfma_f32_16x16x32_bf16 v[24:27], a[116:119], v[168:171], v[24:27]\n"
    "ds_read_b64_tr_b16 a[82:83], v224 offset:18432\n"
    "s_waitcnt lgkmcnt(10)\n"
    "v_mfma_f32_16x16x32_bf16 v[24:27], a[120:123], v[176:179], v[24:27]\n"
    "ds_read_b64_tr_b16 a[84:85], v224 offset:20480\n"
    "s_cmp_eq_u32 s75, 1\n"
    "s_cselect_b32 s86, 401408, 0\n"
    "v_mfma_f32_16x16x32_bf16 v[24:27], a[124:127], v[184:187], v[24:27]\n"
    "ds_read_b64_tr_b16 a[86:87], v224 offset:22528\n"
    "v_cvt_pk_bf16_f32 v71, v14, v15\n"
    "v_mfma_f32_16x16x32_bf16 v[28:31], a[96:99], v[132:135], 0\n"
    "ds_read_b64_tr_b16 a[88:89], v224 offset:24576\n"
    "buffer_store_dwordx4 v[68:71], v210, s[48:51], s93 offen offset:0 sc1\n"
    "v_mfma_f32_16x16x32_bf16 v[28:31], a[100:103], v[140:143], v[28:31]\n"
    "ds_read_b64_tr_b16 a[90:91], v224 offset:26624\n"
    "s_lshl_b32 s57, s77, 7\n"
    "v_mfma_f32_16x16x32_bf16 v[28:31], a[104:107], v[148:151], v[28:31]\n"
    "ds_read_b64_tr_b16 a[92:93], v224 offset:28672\n"
    "s_add_u32 s93, s93, 0x4000\n"
    "v_mfma_f32_16x16x32_bf16 v[28:31], a[108:111], v[156:159], v[28:31]\n"
    "ds_read_b64_tr_b16 a[94:95], v224 offset:30720\n"
    "s_add_u32 s59, s95, 0x10000\n"
    "v_mfma_f32_16x16x32_bf16 v[28:31], a[112:115], v[164:167], v[28:31]\n"
    "ds_read_b64_tr_b16 a[96:97], v225 offset:0\n"
    "v_mfma_f32_16x16x32_bf16 v[28:31], a[116:119], v[172:175], v[28:31]\n"
    "ds_read_b64_tr_b16 a[98:99], v225 offset:2048\n"
    "v_mfma_f32_16x16x32_bf16 v[28:31], a[120:123], v[180:183], v[28:31]\n"
    "ds_read_b64_tr_b16 a[100:101], v225 offset:4096\n"
    "v_mfma_f32_16x16x32_bf16 v[28:31], a[124:127], v[188:191], v[28:31]\n"
    "ds_read_b64_tr_b16 a[102:103], v225 offset:6144\n"
    "s_waitcnt lgkmcnt(15)\n"
    "v_mfma_f32_16x16x32_bf16 v[32:35], a[64:67], v[128:131], 0\n"
    "ds_read_b64_tr_b16 a[104:105], v225 offset:8192\n"
    "v_mfma_f32_16x16x32_bf16 v[32:35], a[68:71], v[136:139], v[32:35]\n"
    "ds_read_b64_tr_b16 a[106:107], v225 offset:10240\n"
    "s_waitcnt lgkmcnt(14)\n"
    "s_add_u32 m0, s59, 0x0\n"
    "v_mfma_f32_16x16x32_bf16 v[32:35], a[72:75], v[144:147], v[32:35]\n"
    "ds_read_b64_tr_b16 a[108:109], v225 offset:12288\n"
    "v_mfma_f32_16x16x32_bf16 v[32:35], a[76:79], v[152:155], v[32:35]\n"
    "ds_read_b64_tr_b16 a[110:111], v225 offset:14336\n"
    "s_waitcnt lgkmcnt(12)\n"
    "s_add_u32 s96, s57, 0\n"
    "v_mfma_f32_16x16x32_bf16 v[32:35], a[80:83], v[160:163], v[32:35]\n"
    "ds_read_b64_tr_b16 a[112:113], v225 offset:16384\n"
    "v_add_u32 v226, 0x18000, v226\n"
    "v_mfma_f32_16x16x32_bf16 v[32:35], a[84:87], v[168:171], v[32:35]\n"
    "ds_read_b64_tr_b16 a[114:115], v225 offset:18432\n"
    "s_waitcnt lgkmcnt(10)\n"
    "v_mfma_f32_16x16x32_bf16 v[32:35], a[88:91], v[176:179], v[32:35]\n"
    "ds_read_b64_tr_b16 a[116:117], v225 offset:20480\n"
    "buffer_load_dwordx4 v228, s[84:87], s96 offen offset:0 lds\n"
    "v_mfma_f32_16x16x32_bf16 v[32:35], a[92:95], v[184:187], v[32:35]\n"
    "ds_read_b64_tr_b16 a[118:119], v225 offset:22528\n"
    "v_cvt_pk_bf16_f32 v72, v16, v17\n"
    "v_mfma_f32_16x16x32_bf16 v[36:39], a[64:67], v[132:135], 0\n"
    "ds_read_b64_tr_b16 a[120:121], v225 offset:24576\n"
    "buffer_load_dwordx4 v229, s[84:87], s96 offen offset:1024 lds\n"
    "v_mfma_f32_16x16x32_bf16 v[36:39], a[68:71], v[140:143], v[36:39]\n"
    "ds_read_b64_tr_b16 a[122:123], v225 offset:26624\n"
    "v_cvt_pk_bf16_f32 v73, v18, v19\n"
    "v_mfma_f32_16x16x32_bf16 v[36:39], a[72:75], v[148:151], v[36:39]\n"
    "ds_read_b64_tr_b16 a[124:125], v225 offset:28672\n"
    "v_cvt_pk_bf16_f32 v74, v24, v25\n"
    "v_mfma_f32_16x16x32_bf16 v[36:39], a[76:79], v[156:159], v[36:39]\n"
    "ds_read_b64_tr_b16 a[126:127], v225 offset:30720\n"
    "buffer_load_dwordx4 v230, s[84:87], s96 offen offset:2048 lds\n"
    "v_mfma_f32_16x16x32_bf16 v[36:39], a[80:83], v[164:167], v[36:39]\n"
    "ds_read_b64_tr_b16 a[64:65], v226 offset:0\n"
    "v_cvt_pk_bf16_f32 v75, v26, v27\n"
    "v_mfma_f32_16x16x32_bf16 v[36:39], a[84:87], v[172:175], v[36:39]\n"
    "ds_read_b64_tr_b16 a[66:67], v226 offset:2048\n"
    "buffer_load_dwordx4 v231, s[84:87], s96 offen offset:3072 lds\n"
    "v_mfma_f32_16x16x32_bf16 v[36:39], a[88:91], v[180:183], v[36:39]\n"
    "ds_read_b64_tr_b16 a[68:69], v226 offset:4096\n"
    "buffer_store_dwordx4 v[72:75], v210, s[48:51], s92 offen offset:0 sc1\n"
    "v_mfma_f32_16x16x32_bf16 v[36:39], a[92:95], v[188:191], v[36:39]\n"
    "ds_read_b64_tr_b16 a[70:71], v226 offset:6144\n"
    "s_waitcnt lgkmcnt(15)\n"
    "v_mfma_f32_16x16x32_bf16 v[40:43], a[96:99], v[128:131], 0\n"
    "ds_read_b64_tr_b16 a[72:73], v226 offset:8192\n"
    "s_add_u32 m0, s59, 0x1000\n"
    "s_add_u32 s92, s92, 0x4000\n"
    "v_mfma_f32_16x16x32_bf16 v[40:43], a[100:103], v[136:139], v[40:43]\n"
    "ds_read_b64_tr_b16 a[74:75], v226 offset:10240\n"
    "s_waitcnt lgkmcnt(14)\n"
    "v_mfma_f32_16x16x32_bf16 v[40:43], a[104:107], v[144:147], v[40:43]\n"
    "ds_read_b64_tr_b16 a[76:77], v226 offset:12288\n"
    "v_cvt_pk_bf16_f32 v76, v20, v21\n"
    "v_mfma_f32_16x16x32_bf16 v[40:43], a[108:111], v[152:155], v[40:43]\n"
    "ds_read_b64_tr_b16 a[78:79], v226 offset:14336\n"
    "s_waitcnt lgkmcnt(12)\n"
    "s_add_u32 s96, s57, 28672\n"
    "v_mfma_f32_16x16x32_bf16 v[40:43], a[112:115], v[160:163], v[40:43]\n"
    "ds_read_b64_tr_b16 a[80:81], v226 offset:16384\n"
    "v_add_u32 v227, 0x18000, v227\n"
    "v_cvt_pk_bf16_f32 v77, v22, v23\n"
    "v_mfma_f32_16x16x32_bf16 v[40:43], a[116:119], v[168:171], v[40:43]\n"
    "ds_read_b64_tr_b16 a[82:83], v226 offset:18432\n"
    "s_waitcnt lgkmcnt(10)\n"
    "buffer_load_dwordx4 v228, s[84:87], s96 offen offset:0 lds\n"
    "v_mfma_f32_16x16x32_bf16 v[40:43], a[120:123], v[176:179], v[40:43]\n"
    "ds_read_b64_tr_b16 a[84:85], v226 offset:20480\n"
    "v_cvt_pk_bf16_f32 v78, v28, v29\n"
    "v_mfma_f32_16x16x32_bf16 v[40:43], a[124:127], v[184:187], v[40:43]\n"
    "ds_read_b64_tr_b16 a[86:87], v226 offset:22528\n"
    "v_mfma_f32_16x16x32_bf16 v[44:47], a[96:99], v[132:135], 0\n"
    "ds_read_b64_tr_b16 a[88:89], v226 offset:24576\n"
    "buffer_load_dwordx4 v229, s[84:87], s96 offen offset:1024 lds\n"
    "v_mfma_f32_16x16x32_bf16 v[44:47], a[100:103], v[140:143], v[44:47]\n"
    "ds_read_b64_tr_b16 a[90:91], v226 offset:26624\n"
    "v_cvt_pk_bf16_f32 v79, v30, v31\n"
    "v_mfma_f32_16x16x32_bf16 v[44:47], a[104:107], v[148:151], v[44:47]\n"
    "ds_read_b64_tr_b16 a[92:93], v226 offset:28672\n"
    "buffer_load_dwordx4 v230, s[84:87], s96 offen offset:2048 lds\n"
    "v_mfma_f32_16x16x32_bf16 v[44:47], a[108:111], v[156:159], v[44:47]\n"
    "ds_read_b64_tr_b16 a[94:95], v226 offset:30720\n"
    "buffer_store_dwordx4 v[76:79], v210, s[48:51], s93 offen offset:0 sc1\n"
    "v_mfma_f32_16x16x32_bf16 v[44:47], a[112:115], v[164:167], v[44:47]\n"
    "ds_read_b64_tr_b16 a[96:97], v227 offset:0\n"
    "s_add_u32 s93, s93, 0x4000\n"
    "v_mfma_f32_16x16x32_bf16 v[44:47], a[116:119], v[172:175], v[44:47]\n"
    "ds_read_b64_tr_b16 a[98:99], v227 offset:2048\n"
    "buffer_load_dwordx4 v231, s[84:87], s96 offen offset:3072 lds\n"
    "v_mfma_f32_16x16x32_bf16 v[44:47], a[120:123], v[180:183], v[44:47]\n"
    "ds_read_b64_tr_b16 a[100:101], v227 offset:4096\n"
    "v_mfma_f32_16x16x32_bf16 v[44:47], a[124:127], v[188:191], v[44:47]\n"
    "ds_read_b64_tr_b16 a[102:103], v227 offset:6144\n"
    "s_waitcnt lgkmcnt(15)\n"
    "s_add_u32 m0, s59, 0x8000\n"
    "v_mfma_f32_16x16x32_bf16 v[48:51], a[64:67], v[128:131], 0\n"
    "ds_read_b64_tr_b16 a[104:105], v227 offset:8192\n"
    "v_mfma_f32_16x16x32_bf16 v[48:51], a[68:71], v[136:139], v[48:51]\n"
    "ds_read_b64_tr_b16 a[106:107], v227 offset:10240\n"
    "s_waitcnt lgkmcnt(14)\n"
    "v_mfma_f32_16x16x32_bf16 v[48:51], a[72:75], v[144:147], v[48:51]\n"
    "ds_read_b64_tr_b16 a[108:109], v227 offset:12288\n"
    "s_add_u32 s96, s57, 229376\n"
    "v_mfma_f32_16x16x32_bf16 v[48:51], a[76:79], v[152:155], v[48:51]\n"
    "ds_read_b64_tr_b16 a[110:111], v227 offset:14336\n"
    "s_waitcnt lgkmcnt(12)\n"
    "v_mfma_f32_16x16x32_bf16 v[48:51], a[80:83], v[160:163], v[48:51]\n"
    "ds_read_b64_tr_b16 a[112:113], v227 offset:16384\n"
    "v_add_u32 v224, 0xfffe0000, v224\n"
    "v_mfma_f32_16x16x32_bf16 v[48:51], a[84:87], v[168:171], v[48:51]\n"
    "ds_read_b64_tr_b16 a[114:115], v227 offset:18432\n"
    "s_waitcnt lgkmcnt(10)\n"
    "buffer_load_dwordx4 v228, s[84:87], s96 offen offset:0 lds\n"
    "v_mfma_f32_16x16x32_bf16 v[48:51], a[88:91], v[176:179], v[48:51]\n"
    "ds_read_b64_tr_b16 a[116:117], v227 offset:20480\n"
    "v_cvt_pk_bf16_f32 v64, v32, v33\n"
    "v_mfma_f32_16x16x32_bf16 v[48:51], a[92:95], v[184:187], v[48:51]\n"
    "ds_read_b64_tr_b16 a[118:119], v227 offset:22528\n"
    "buffer_load_dwordx4 v229, s[84:87], s96 offen offset:1024 lds\n"
    "v_mfma_f32_16x16x32_bf16 v[52:55], a[64:67], v[132:135], 0\n"
    "ds_read_b64_tr_b16 a[120:121], v227 offset:24576\n"
    "v_cvt_pk_bf16_f32 v65, v34, v35\n"
    "v_mfma_f32_16x16x32_bf16 v[52:55], a[68:71], v[140:143], v[52:55]\n"
    "ds_read_b64_tr_b16 a[122:123], v227 offset:26624\n"
    "s_waitcnt vmcnt(16)\n"
    "v_cvt_pk_bf16_f32 v66, v40, v41\n"
    "v_mfma_f32_16x16x32_bf16 v[52:55], a[72:75], v[148:151], v[52:55]\n"
    "ds_read_b64_tr_b16 a[124:125], v227 offset:28672\n"
    "s_waitcnt vmcnt(14)\n"
    "buffer_load_dwordx4 v230, s[84:87], s96 offen offset:2048 lds\n"
    "v_mfma_f32_16x16x32_bf16 v[52:55], a[76:79], v[156:159], v[52:55]\n"
    "ds_read_b64_tr_b16 a[126:127], v227 offset:30720\n"
    "s_barrier\n"
    "v_cvt_pk_bf16_f32 v67, v42, v43\n"
    "v_mfma_f32_16x16x32_bf16 v[52:55], a[80:83], v[164:167], v[52:55]\n"
    "ds_read_b64_tr_b16 a[64:65], v224 offset:0\n"
    "buffer_load_dwordx4 v231, s[84:87], s96 offen offset:3072 lds\n"
    "v_mfma_f32_16x16x32_bf16 v[52:55], a[84:87], v[172:175], v[52:55]\n"
    "ds_read_b64_tr_b16 a[66:67], v224 offset:2048\n"
    "v_mov_b32 v218, s34\n"
    "v_mfma_f32_16x16x32_bf16 v[52:55], a[88:91], v[180:183], v[52:55]\n"
    "ds_read_b64_tr_b16 a[68:69], v224 offset:4096\n"
    "v_mov_b32 v219, s35\n"
    "v_mfma_f32_16x16x32_bf16 v[52:55], a[92:95], v[188:191], v[52:55]\n"
    "ds_read_b64_tr_b16 a[70:71], v224 offset:6144\n"
    "s_waitcnt lgkmcnt(15)\n"
    "s_add_u32 m0, s59, 0x9000\n"
    "v_mfma_f32_16x16x32_bf16 v[56:59], a[96:99], v[128:131], 0\n"
    "ds_read_b64_tr_b16 a[72:73], v224 offset:8192\n"
    "buffer_store_dwordx4 v[64:67], v210, s[48:51], s92 offen offset:0 sc1\n"
    "v_mfma_f32_16x16x32_bf16 v[56:59], a[100:103], v[136:139], v[56:59]\n"
    "ds_read_b64_tr_b16 a[74:75], v224 offset:10240\n"
    "s_waitcnt lgkmcnt(14)\n"
    "v_lshlrev_b32 v216, 21, v210\n"
    "v_mfma_f32_16x16x32_bf16 v[56:59], a[104:107], v[144:147], v[56:59]\n"
    "ds_read_b64_tr_b16 a[76:77], v224 offset:12288\n"
    "s_add_u32 s92, s92, 0x4000\n"
    "v_cvt_pk_bf16_f32 v68, v36, v37\n"
    "v_mfma_f32_16x16x32_bf16 v[56:59], a[108:111], v[152:155], v[56:59]\n"
    "ds_read_b64_tr_b16 a[78:79], v224 offset:14336\n"
    "s_waitcnt lgkmcnt(12)\n"
    "s_lshl_b32 s98, s56, 25\n"
    "v_mfma_f32_16x16x32_bf16 v[56:59], a[112:115], v[160:163], v[56:59]\n"
    "ds_read_b64_tr_b16 a[80:81], v224 offset:16384\n"
    "v_add_u32 v225, 0xfffe0000, v225\n"
    "v_cvt_pk_bf16_f32 v69, v38, v39\n"
    "v_mfma_f32_16x16x32_bf16 v[56:59], a[116:119], v[168:171], v[56:59]\n"
    "ds_read_b64_tr_b16 a[82:83], v224 offset:18432\n"
    "s_waitcnt lgkmcnt(10)\n"
    "v_cvt_pk_bf16_f32 v70, v44, v45\n"
    "v_mfma_f32_16x16x32_bf16 v[56:59], a[120:123], v[176:179], v[56:59]\n"
    "ds_read_b64_tr_b16 a[84:85], v224 offset:20480\n"
    "s_add_u32 s98, s98, s83\n"
    "v_cvt_pk_bf16_f32 v71, v46, v47\n"
    "v_mfma_f32_16x16x32_bf16 v[56:59], a[124:127], v[184:187], v[56:59]\n"
    "ds_read_b64_tr_b16 a[86:87], v224 offset:22528\n"
    "v_mfma_f32_16x16x32_bf16 v[60:63], a[96:99], v[132:135], 0\n"
    "ds_read_b64_tr_b16 a[88:89], v224 offset:24576\n"
    "s_lshl3_add_u32 s98, s76, s98\n"
    "v_mfma_f32_16x16x32_bf16 v[60:63], a[100:103], v[140:143], v[60:63]\n"
    "ds_read_b64_tr_b16 a[90:91], v224 offset:26624\n"
    "buffer_store_dwordx4 v[68:71], v210, s[48:51], s93 offen offset:0 sc1\n"
    "v_mfma_f32_16x16x32_bf16 v[60:63], a[104:107], v[148:151], v[60:63]\n"
    "ds_read_b64_tr_b16 a[92:93], v224 offset:28672\n"
    "s_add_u32 s93, s93, 0x4000\n"
    "v_mfma_f32_16x16x32_bf16 v[60:63], a[108:111], v[156:159], v[60:63]\n"
    "ds_read_b64_tr_b16 a[94:95], v224 offset:30720\n"
    "buffer_store_dwordx2 v[218:219], v216, s[52:55], s98 offen offset:0 sc1\n"
    "v_mfma_f32_16x16x32_bf16 v[60:63], a[112:115], v[164:167], v[60:63]\n"
    "ds_read_b64_tr_b16 a[96:97], v225 offset:0\n"
    "v_mfma_f32_16x16x32_bf16 v[60:63], a[116:119], v[172:175], v[60:63]\n"
    "ds_read_b64_tr_b16 a[98:99], v225 offset:2048\n"
    "v_mfma_f32_16x16x32_bf16 v[60:63], a[120:123], v[180:183], v[60:63]\n"
    "ds_read_b64_tr_b16 a[100:101], v225 offset:4096\n"
    "v_mfma_f32_16x16x32_bf16 v[60:63], a[124:127], v[188:191], v[60:63]\n"
    "ds_read_b64_tr_b16 a[102:103], v225 offset:6144\n"
    "s_waitcnt lgkmcnt(15)\n"
    "v_mfma_f32_16x16x32_bf16 v[0:3], a[64:67], v[128:131], 0\n"
    "ds_read_b64_tr_b16 a[104:105], v225 offset:8192\n"
    "v_mfma_f32_16x16x32_bf16 v[0:3], a[68:71], v[136:139], v[0:3]\n"
    "ds_read_b64_tr_b16 a[106:107], v225 offset:10240\n"
    "s_waitcnt lgkmcnt(14)\n"
    "s_add_u32 s96, s57, 258048\n"
    "v_mfma_f32_16x16x32_bf16 v[0:3], a[72:75], v[144:147], v[0:3]\n"
    "ds_read_b64_tr_b16 a[108:109], v225 offset:12288\n"
    "v_mfma_f32_16x16x32_bf16 v[0:3], a[76:79], v[152:155], v[0:3]\n"
    "ds_read_b64_tr_b16 a[110:111], v225 offset:14336\n"
    "s_waitcnt lgkmcnt(12)\n"
    "v_mfma_f32_16x16x32_bf16 v[0:3], a[80:83], v[160:163], v[0:3]\n"
    "ds_read_b64_tr_b16 a[112:113], v225 offset:16384\n"
    "v_add_u32 v226, 0xfffe0000, v226\n"
    "v_mfma_f32_16x16x32_bf16 v[0:3], a[84:87], v[168:171], v[0:3]\n"
    "ds_read_b64_tr_b16 a[114:115], v225 offset:18432\n"
    "s_waitcnt lgkmcnt(10)\n"
    "buffer_load_dwordx4 v228, s[84:87], s96 offen offset:0 lds\n"
    "v_mfma_f32_16x16x32_bf16 v[0:3], a[88:91], v[176:179], v[0:3]\n"
    "ds_read_b64_tr_b16 a[116:117], v225 offset:20480\n"
    "v_cvt_pk_bf16_f32 v72, v48, v49\n"
    "v_mfma_f32_16x16x32_bf16 v[0:3], a[92:95], v[184:187], v[0:3]\n"
    "ds_read_b64_tr_b16 a[118:119], v225 offset:22528\n"
    "buffer_load_dwordx4 v229, s[84:87], s96 offen offset:1024 lds\n"
    "v_mfma_f32_16x16x32_bf16 v[4:7], a[64:67], v[132:135], 0\n"
    "ds_read_b64_tr_b16 a[120:121], v225 offset:24576\n"
    "v_cvt_pk_bf16_f32 v73, v50, v51\n"
    "v_mfma_f32_16x16x32_bf16 v[4:7], a[68:71], v[140:143], v[4:7]\n"
    "ds_read_b64_tr_b16 a[122:123], v225 offset:26624\n"
    "v_mfma_f32_16x16x32_bf16 v[4:7], a[72:75], v[148:151], v[4:7]\n"
    "ds_read_b64_tr_b16 a[124:125], v225 offset:28672\n"
    "buffer_load_dwordx4 v230, s[84:87], s96 offen offset:2048 lds\n"
    "v_mfma_f32_16x16x32_bf16 v[4:7], a[76:79], v[156:159], v[4:7]\n"
    "ds_read_b64_tr_b16 a[126:127], v225 offset:30720\n"
    "v_cvt_pk_bf16_f32 v74, v56, v57\n"
    "v_mfma_f32_16x16x32_bf16 v[4:7], a[80:83], v[164:167], v[4:7]\n"
    "ds_read_b64_tr_b16 a[64:65], v226 offset:0\n"
    "v_cvt_pk_bf16_f32 v75, v58, v59\n"
    "v_mfma_f32_16x16x32_bf16 v[4:7], a[84:87], v[172:175], v[4:7]\n"
    "ds_read_b64_tr_b16 a[66:67], v226 offset:2048\n"
    "buffer_load_dwordx4 v231, s[84:87], s96 offen offset:3072 lds\n"
    "v_mfma_f32_16x16x32_bf16 v[4:7], a[88:91], v[180:183], v[4:7]\n"
    "ds_read_b64_tr_b16 a[68:69], v226 offset:4096\n"
    "buffer_store_dwordx4 v[72:75], v210, s[48:51], s92 offen offset:0 sc1\n"
    "v_mfma_f32_16x16x32_bf16 v[4:7], a[92:95], v[188:191], v[4:7]\n"
    "ds_read_b64_tr_b16 a[70:71], v226 offset:6144\n"
    "s_waitcnt lgkmcnt(15)\n"
    "v_mfma_f32_16x16x32_bf16 v[8:11], a[96:99], v[128:131], 0\n"
    "ds_read_b64_tr_b16 a[72:73], v226 offset:8192\n"
    "s_xor_b32 s59, s59, 0x10000\n"
    "s_add_u32 s92, s92, 0x4000\n"
    "v_mfma_f32_16x16x32_bf16 v[8:11], a[100:103], v[136:139], v[8:11]\n"
    "ds_read_b64_tr_b16 a[74:75], v226 offset:10240\n"
    "s_waitcnt lgkmcnt(14)\n"
    "v_mfma_f32_16x16x32_bf16 v[8:11], a[104:107], v[144:147], v[8:11]\n"
    "ds_read_b64_tr_b16 a[76:77], v226 offset:12288\n"
    "v_cvt_pk_bf16_f32 v76, v52, v53\n"
    "v_mfma_f32_16x16x32_bf16 v[8:11], a[108:111], v[152:155], v[8:11]\n"
    "ds_read_b64_tr_b16 a[78:79], v226 offset:14336\n"
    "s_waitcnt lgkmcnt(12)\n"
    "v_mfma_f32_16x16x32_bf16 v[8:11], a[112:115], v[160:163], v[8:11]\n"
    "ds_read_b64_tr_b16 a[80:81], v226 offset:16384\n"
    "v_add_u32 v227, 0xfffe0000, v227\n"
    "v_cvt_pk_bf16_f32 v77, v54, v55\n"
    "v_mfma_f32_16x16x32_bf16 v[8:11], a[116:119], v[168:171], v[8:11]\n"
    "ds_read_b64_tr_b16 a[82:83], v226 offset:18432\n"
    "s_waitcnt lgkmcnt(10)\n"
    "v_mfma_f32_16x16x32_bf16 v[8:11], a[120:123], v[176:179], v[8:11]\n"
    "ds_read_b64_tr_b16 a[84:85], v226 offset:20480\n"
    "v_cvt_pk_bf16_f32 v78, v60, v61\n"
    "v_mfma_f32_16x16x32_bf16 v[8:11], a[124:127], v[184:187], v[8:11]\n"
    "ds_read_b64_tr_b16 a[86:87], v226 offset:22528\n"
    "v_mfma_f32_16x16x32_bf16 v[12:15], a[96:99], v[132:135], 0\n"
    "ds_read_b64_tr_b16 a[88:89], v226 offset:24576\n"
    "v_cvt_pk_bf16_f32 v79, v62, v63\n"
    "v_mfma_f32_16x16x32_bf16 v[12:15], a[100:103], v[140:143], v[12:15]\n"
    "ds_read_b64_tr_b16 a[90:91], v226 offset:26624\n"
    "v_mfma_f32_16x16x32_bf16 v[12:15], a[104:107], v[148:151], v[12:15]\n"
    "ds_read_b64_tr_b16 a[92:93], v226 offset:28672\n"
    "buffer_store_dwordx4 v[76:79], v210, s[48:51], s93 offen offset:0 sc1\n"
    "v_mfma_f32_16x16x32_bf16 v[12:15], a[108:111], v[156:159], v[12:15]\n"
    "ds_read_b64_tr_b16 a[94:95], v226 offset:30720\n"
    "v_mfma_f32_16x16x32_bf16 v[12:15], a[112:115], v[164:167], v[12:15]\n"
    "ds_read_b64_tr_b16 a[96:97], v227 offset:0\n"
    "s_add_u32 s93, s93, 0x4000\n"
    "v_mfma_f32_16x16x32_bf16 v[12:15], a[116:119], v[172:175], v[12:15]\n"
    "ds_read_b64_tr_b16 a[98:99], v227 offset:2048\n"
    "v_mfma_f32_16x16x32_bf16 v[12:15], a[120:123], v[180:183], v[12:15]\n"
    "ds_read_b64_tr_b16 a[100:101], v227 offset:4096\n"
    "v_mfma_f32_16x16x32_bf16 v[12:15], a[124:127], v[188:191], v[12:15]\n"
    "ds_read_b64_tr_b16 a[102:103], v227 offset:6144\n"
    "s_waitcnt lgkmcnt(15)\n"
    "v_mfma_f32_16x16x32_bf16 v[16:19], a[64:67], v[128:131], 0\n"
    "ds_read_b64_tr_b16 a[104:105], v227 offset:8192\n"
    "v_mfma_f32_16x16x32_bf16 v[16:19], a[68:71], v[136:139], v[16:19]\n"
    "ds_read_b64_tr_b16 a[106:107], v227 offset:10240\n"
    "s_waitcnt lgkmcnt(14)\n"
    "v_mfma_f32_16x16x32_bf16 v[16:19], a[72:75], v[144:147], v[16:19]\n"
    "ds_read_b64_tr_b16 a[108:109], v227 offset:12288\n"
    "v_mfma_f32_16x16x32_bf16 v[16:19], a[76:79], v[152:155], v[16:19]\n"
    "ds_read_b64_tr_b16 a[110:111], v227 offset:14336\n"
    "s_waitcnt lgkmcnt(12)\n"
    "v_mfma_f32_16x16x32_bf16 v[16:19], a[80:83], v[160:163], v[16:19]\n"
    "ds_read_b64_tr_b16 a[112:113], v227 offset:16384\n"
    "v_mfma_f32_16x16x32_bf16 v[16:19], a[84:87], v[168:171], v[16:19]\n"
    "ds_read_b64_tr_b16 a[114:115], v227 offset:18432\n"
    "s_waitcnt lgkmcnt(10)\n"
    "v_mfma_f32_16x16x32_bf16 v[16:19], a[88:91], v[176:179], v[16:19]\n"
    "ds_read_b64_tr_b16 a[116:117], v227 offset:20480\n"
    "v_cvt_pk_bf16_f32 v64, v0, v1\n"
    "v_mfma_f32_16x16x32_bf16 v[16:19], a[92:95], v[184:187], v[16:19]\n"
    "ds_read_b64_tr_b16 a[118:119], v227 offset:22528\n"
    "v_cvt_pk_bf16_f32 v65, v2, v3\n"
    "v_mfma_f32_16x16x32_bf16 v[20:23], a[64:67], v[132:135], 0\n"
    "ds_read_b64_tr_b16 a[120:121], v227 offset:24576\n"
    "v_cvt_pk_bf16_f32 v66, v8, v9\n"
    "v_mfma_f32_16x16x32_bf16 v[20:23], a[68:71], v[140:143], v[20:23]\n"
    "ds_read_b64_tr_b16 a[122:123], v227 offset:26624\n"
    "v_cvt_pk_bf16_f32 v67, v10, v11\n"
    "v_mfma_f32_16x16x32_bf16 v[20:23], a[72:75], v[148:151], v[20:23]\n"
    "ds_read_b64_tr_b16 a[124:125], v227 offset:28672\n"
    "v_mfma_f32_16x16x32_bf16 v[20:23], a[76:79], v[156:159], v[20:23]\n"
    "ds_read_b64_tr_b16 a[126:127], v227 offset:30720\n"
    "v_mfma_f32_16x16x32_bf16 v[20:23], a[80:83], v[164:167], v[20:23]\n"
    "buffer_store_dwordx4 v[64:67], v210, s[48:51], s92 offen offset:0 sc1\n"
    "v_mfma_f32_16x16x32_bf16 v[20:23], a[84:87], v[172:175], v[20:23]\n"
    "s_add_u32 s92, s92, 0x4000\n"
    "v_cvt_pk_bf16_f32 v68, v4, v5\n"
    "v_mfma_f32_16x16x32_bf16 v[20:23], a[88:91], v[180:183], v[20:23]\n"
    "v_cvt_pk_bf16_f32 v69, v6, v7\n"
    "v_mfma_f32_16x16x32_bf16 v[20:23], a[92:95], v[188:191], v[20:23]\n"
    "s_waitcnt lgkmcnt(12)\n"
    "v_cvt_pk_bf16_f32 v70, v12, v13\n"
    "v_mfma_f32_16x16x32_bf16 v[24:27], a[96:99], v[128:131], 0\n"
    "v_cvt_pk_bf16_f32 v71, v14, v15\n"
    "v_mfma_f32_16x16x32_bf16 v[24:27], a[100:103], v[136:139], v[24:27]\n"
    "s_waitcnt lgkmcnt(8)\n"
    "buffer_store_dwordx4 v[68:71], v210, s[48:51], s93 offen offset:0 sc1\n"
    "v_mfma_f32_16x16x32_bf16 v[24:27], a[104:107], v[144:147], v[24:27]\n"
    "s_add_u32 s93, s93, 0x4000\n"
    "v_mfma_f32_16x16x32_bf16 v[24:27], a[108:111], v[152:155], v[24:27]\n"
    "s_waitcnt lgkmcnt(4)\n"
    "v_mfma_f32_16x16x32_bf16 v[24:27], a[112:115], v[160:163], v[24:27]\n"
    "v_mfma_f32_16x16x32_bf16 v[24:27], a[116:119], v[168:171], v[24:27]\n"
    "s_waitcnt lgkmcnt(0)\n"
    "v_mfma_f32_16x16x32_bf16 v[24:27], a[120:123], v[176:179], v[24:27]\n"
    "v_bfe_u32 v216, v210, 1, 7\n"
    "v_mfma_f32_16x16x32_bf16 v[24:27], a[124:127], v[184:187], v[24:27]\n"
    "v_mfma_f32_16x16x32_bf16 v[28:31], a[96:99], v[132:135], 0\n"
    "buffer_load_dwordx2 v[238:239], v216, s[52:55], s83 offen sc1\n"
    "v_mfma_f32_16x16x32_bf16 v[28:31], a[100:103], v[140:143], v[28:31]\n"
    "v_mfma_f32_16x16x32_bf16 v[28:31], a[104:107], v[148:151], v[28:31]\n"
    "v_mfma_f32_16x16x32_bf16 v[28:31], a[108:111], v[156:159], v[28:31]\n"
    "v_mfma_f32_16x16x32_bf16 v[28:31], a[112:115], v[164:167], v[28:31]\n"
    "v_mfma_f32_16x16x32_bf16 v[28:31], a[116:119], v[172:175], v[28:31]\n"
    "v_mfma_f32_16x16x32_bf16 v[28:31], a[120:123], v[180:183], v[28:31]\n"
    "v_mfma_f32_16x16x32_bf16 v[28:31], a[124:127], v[188:191], v[28:31]\n"
    "s_nop 7\n"
    "v_cvt_pk_bf16_f32 v72, v16, v17\n"
    "v_cvt_pk_bf16_f32 v73, v18, v19\n"
    "v_cvt_pk_bf16_f32 v74, v24, v25\n"
    "v_cvt_pk_bf16_f32 v75, v26, v27\n"
    "buffer_store_dwordx4 v[72:75], v210, s[48:51], s92 offen offset:0 sc1\n"
    "s_add_u32 s92, s92, 0x4000\n"
    "v_cvt_pk_bf16_f32 v76, v20, v21\n"
    "v_cvt_pk_bf16_f32 v77, v22, v23\n"
    "v_cvt_pk_bf16_f32 v78, v28, v29\n"
    "v_cvt_pk_bf16_f32 v79, v30, v31\n"
    "buffer_store_dwordx4 v[76:79], v210, s[48:51], s93 offen offset:0 sc1\n"
    "s_add_u32 s93, s93, 0x4000\n"
    "s_waitcnt vmcnt(2)\n"
    "v_cmp_eq_u64 vcc, s[34:35], v[238:239]\n"
    "s_nop 0\n"
    "s_cmp_eq_u64 vcc, exec\n"
    "s_cselect_b32 s38, s50, 0\n"
    "v_lshrrev_b32 v80, 4, v210\n"
    "v_and_b32 v81, 15, v80\n"
    "v_lshrrev_b32 v82, 4, v80\n"
    "v_mul_u32_u24 v83, 1792, v81\n"
    "v_lshl_add_u32 v85, v82, 4, v83\n"
    "s_add_u32 s97, s62, 0\n"
    "v_lshrrev_b32 v86, 1, v82\n"
    "v_and_b32 v87, 1, v82\n"
    "v_lshl_add_u32 v86, v87, 1, v86\n"
    "v_lshl_add_u32 v84, v86, 4, v83\n"
    "v_bfe_u32 v86, v81, 2, 1\n"
    "buffer_load_dwordx4 a[0:3], v210, s[36:39], s97 offen offset:0 sc1\n"
    "v_lshlrev_b32 v86, 1, v86\n"
    "v_and_b32 v87, 1, v81\n"
    "v_lshlrev_b32 v87, 4, v87\n"
    "v_mov_b32 v88, 0x3f80\n"
    "buffer_load_dwordx4 a[4:7], v210, s[36:39], s97 offen offset:1024 sc1\n"
    "v_lshlrev_b32 v87, v87, v88\n"
    "v_bfe_u32 v88, v81, 1, 1\n"
    "v_lshrrev_b32 v89, 3, v81\n"
    "v_lshl_add_u32 v88, v89, 1, v88\n"
    "v_add_u32 v89, 0, v86\n"
    "buffer_load_dwordx4 a[8:11], v210, s[36:39], s97 offen offset:2048 sc1\n"
    "v_cmp_eq_u32 vcc, v89, v82\n"
    "s_nop 1\n"
    "v_cndmask_b32 v90, 0, v87, vcc\n"
    "v_cmp_eq_u32 vcc, 0, v88\n"
    "buffer_load_dwordx4 a[12:15], v210, s[36:39], s97 offen offset:3072 sc1\n"
    "s_nop 1\n"
    "v_cndmask_b32 v240, 0, v90, vcc\n"
    "v_cmp_eq_u32 vcc, 1, v88\n"
    "s_nop 1\n"
    "v_cndmask_b32 v241, 0, v90, vcc\n"
    "s_add_u32 s97, s62, 4096\n"
    "v_cmp_eq_u32 vcc, 2, v88\n"
    "s_nop 1\n"
    "v_cndmask_b32 v242, 0, v90, vcc\n"
    "v_cmp_eq_u32 vcc, 3, v88\n"
    "buffer_load_dwordx4 a[16:19], v210, s[36:39], s97 offen offset:0 sc1\n"
    "s_nop 1\n"
    "v_cndmask_b32 v243, 0, v90, vcc\n"
    "v_add_u32 v89, 1, v86\n"
    "v_cmp_eq_u32 vcc, v89, v82\n"
    "s_nop 1\n"
    "buffer_load_dwordx4 a[20:23], v210, s[36:39], s97 offen offset:1024 sc1\n"
    "v_cndmask_b32 v90, 0, v87, vcc\n"
    "v_cmp_eq_u32 vcc, 0, v88\n"
    "s_nop 1\n"
    "v_cndmask_b32 v244, 0, v90, vcc\n"
    "buffer_load_dwordx4 a[24:27], v210, s[36:39], s97 offen offset:2048 sc1\n"
    "v_cmp_eq_u32 vcc, 1, v88\n"
    "s_nop 1\n"
    "v_cndmask_b32 v245, 0, v90, vcc\n"
    "v_cmp_eq_u32 vcc, 2, v88\n"
    "s_nop 1\n"
    "buffer_load_dwordx4 a[28:31], v210, s[36:39], s97 offen offset:3072 sc1\n"
    "v_cndmask_b32 v246, 0, v90, vcc\n"
    "v_cmp_eq_u32 vcc, 3, v88\n"
    "s_nop 1\n"
    "v_cndmask_b32 v247, 0, v90, vcc\n"
    "s_waitcnt vmcnt(8)\n"
    "s_barrier\n"
    "v_mov_b32 v218, s34\n"
    "v_mov_b32 v219, s35\n"
    "v_lshlrev_b32 v216, 21, v210\n"
    "s_lshl_b32 s98, s56, 25\n"
    "s_add_u32 s98, s98, s83\n"
    "s_lshl3_add_u32 s98, s76, s98\n"
    "buffer_store_dwordx2 v[218:219], v216, s[52:55], s98 offen offset:2048 sc1\n"
    "s_add_u32 s97, s62, 8192\n"
    "buffer_load_dwordx4 a[32:35], v210, s[36:39], s97 offen offset:0 sc1\n"
    "buffer_load_dwordx4 a[36:39], v210, s[36:39], s97 offen offset:1024 sc1\n"
    "buffer_load_dwordx4 a[40:43], v210, s[36:39], s97 offen offset:2048 sc1\n"
    "buffer_load_dwordx4 a[44:47], v210, s[36:39], s97 offen offset:3072 sc1\n"
    "s_add_u32 s97, s62, 12288\n"
    "buffer_load_dwordx4 a[48:51], v210, s[36:39], s97 offen offset:0 sc1\n"
    "buffer_load_dwordx4 a[52:55], v210, s[36:39], s97 offen offset:1024 sc1\n"
    "buffer_load_dwordx4 a[56:59], v210, s[36:39], s97 offen offset:2048 sc1\n"
    "buffer_load_dwordx4 a[60:63], v210, s[36:39], s97 offen offset:3072 sc1\n"
    "s_cmp_eq_u32 s38, 0\n"
    "s_cbranch_scc1 POLLR1_%=\n"
    "RDY1_%=:\n"
    "s_cmp_eq_u32 s75, 1\n"
    "s_cbranch_scc0 NOAP_%=\n"
    "s_lshl_b32 s99, s76, 17\n"
    "s_lshl_b32 s94, s56, 15\n"
    "s_add_u32 s99, s99, s94\n"
    "s_add_u32 s99, s99, 0x4e00000\n"
    "s_add_u32 s36, s4, s99\n"
    "s_addc_u32 s37, s5, 0\n"
    "s_and_b32 s37, s37, 0xffff\n"
    "s_mov_b32 s38, 0x8000\n"
    "s_mov_b32 s94, 0\n"
    "s_mov_b32 s99, 16384\n"
    "buffer_load_dwordx4 a[128:131], v210, s[36:39], s94 offen offset:0 sc1\n"
    "buffer_load_dwordx4 a[132:135], v210, s[36:39], s99 offen offset:0 sc1\n"
    "buffer_load_dwordx4 a[136:139], v210, s[36:39], s94 offen offset:1024 sc1\n"
    "buffer_load_dwordx4 a[140:143], v210, s[36:39], s99 offen offset:1024 sc1\n"
    "buffer_load_dwordx4 a[144:147], v210, s[36:39], s94 offen offset:2048 sc1\n"
    "buffer_load_dwordx4 a[148:151], v210, s[36:39], s99 offen offset:2048 sc1\n"
    "buffer_load_dwordx4 a[152:155], v210, s[36:39], s94 offen offset:3072 sc1\n"
    "buffer_load_dwordx4 a[156:159], v210, s[36:39], s99 offen offset:3072 sc1\n"
    "s_mov_b32 s94, 4096\n"
    "s_mov_b32 s99, 20480\n"
    "buffer_load_dwordx4 a[160:163], v210, s[36:39], s94 offen offset:0 sc1\n"
    "buffer_load_dwordx4 a[164:167], v210, s[36:39], s99 offen offset:0 sc1\n"
    "buffer_load_dwordx4 a[168:171], v210, s[36:39], s94 offen offset:1024 sc1\n"
    "buffer_load_dwordx4 a[172:175], v210, s[36:39], s99 offen offset:1024 sc1\n"
    "buffer_load_dwordx4 a[176:179], v210, s[36:39], s94 offen offset:2048 sc1\n"
    "buffer_load_dwordx4 a[180:183], v210, s[36:39], s99 offen offset:2048 sc1\n"
    "buffer_load_dwordx4 a[184:187], v210, s[36:39], s94 offen offset:3072 sc1\n"
    "buffer_load_dwordx4 a[188:191], v210, s[36:39], s99 offen offset:3072 sc1\n"
    "s_mov_b32 s94, 8192\n"
    "s_mov_b32 s99, 24576\n"
    "buffer_load_dwordx4 a[192:195], v210, s[36:39], s94 offen offset:0 sc1\n"
    "buffer_load_dwordx4 a[196:199], v210, s[36:39], s99 offen offset:0 sc1\n"
    "buffer_load_dwordx4 a[200:203], v210, s[36:39], s94 offen offset:1024 sc1\n"
    "buffer_load_dwordx4 a[204:207], v210, s[36:39], s99 offen offset:1024 sc1\n"
    "buffer_load_dwordx4 a[208:211], v210, s[36:39], s94 offen offset:2048 sc1\n"
    "buffer_load_dwordx4 a[212:215], v210, s[36:39], s99 offen offset:2048 sc1\n"
    "buffer_load_dwordx4 a[216:219], v210, s[36:39], s94 offen offset:3072 sc1\n"
    "buffer_load_dwordx4 a[220:223], v210, s[36:39], s99 offen offset:3072 sc1\n"
    "s_mov_b32 s94, 12288\n"
    "s_mov_b32 s99, 28672\n"
    "buffer_load_dwordx4 a[224:227], v210, s[36:39], s94 offen offset:0 sc1\n"
    "buffer_load_dwordx4 a[228:231], v210, s[36:39], s99 offen offset:0 sc1\n"
    "buffer_load_dwordx4 a[232:235], v210, s[36:39], s94 offen offset:1024 sc1\n"
    "buffer_load_dwordx4 a[236:239], v210, s[36:39], s99 offen offset:1024 sc1\n"
    "buffer_load_dwordx4 a[240:243], v210, s[36:39], s94 offen offset:2048 sc1\n"
    "buffer_load_dwordx4 a[244:247], v210, s[36:39], s99 offen offset:2048 sc1\n"
    "buffer_load_dwordx4 a[248:251], v210, s[36:39], s94 offen offset:3072 sc1\n"
    "buffer_load_dwordx4 a[252:255], v210, s[36:39], s99 offen offset:3072 sc1\n"
    "s_lshl_b32 s58, s76, 2\n"
    "s_add_u32 s58, s58, s56\n"
    "s_mul_i32 s58, s58, 57344\n"
    "s_add_u32 s58, s58, s57\n"
    "s_add_u32 s61, s58, 28672\n"
    "v_and_b32 v224, 0x7fff, v224\n"
    "v_or_b32 v224, 0x10000, v224\n"
    "v_and_b32 v225, 0x7fff, v225\n"
    "v_or_b32 v225, 0x10000, v225\n"
    "v_and_b32 v226, 0x7fff, v226\n"
    "v_or_b32 v226, 0x10000, v226\n"
    "v_and_b32 v227, 0x7fff, v227\n"
    "v_or_b32 v227, 0x10000, v227\n"
    "ds_read_b64_tr_b16 a[64:65], v224 offset:0\n"
    "ds_read_b64_tr_b16 a[66:67], v224 offset:2048\n"
    "ds_read_b64_tr_b16 a[68:69], v225 offset:0\n"
    "ds_read_b64_tr_b16 a[70:71], v225 offset:2048\n"
    "ds_read_b64_tr_b16 a[72:73], v226 offset:0\n"
    "ds_read_b64_tr_b16 a[74:75], v226 offset:2048\n"
    "ds_read_b64_tr_b16 a[76:77], v227 offset:0\n"
    "ds_read_b64_tr_b16 a[78:79], v227 offset:2048\n"
    "ds_read_b64_tr_b16 a[80:81], v226 offset:3584\n"
    "ds_read_b64_tr_b16 a[82:83], v226 offset:5632\n"
    "ds_read_b64_tr_b16 a[84:85], v227 offset:3584\n"
    "ds_read_b64_tr_b16 a[86:87], v227 offset:5632\n"
    "s_waitcnt vmcnt(28) lgkmcnt(8)\n"
    "v_mfma_f32_16x16x32_bf16 v[0:3], a[64:67], a[128:131], 0\n"
    "ds_read_b64_tr_b16 a[88:89], v224 offset:3584\n"
    "v_mfma_f32_16x16x32_bf16 v[4:7], a[64:67], a[132:135], 0\n"
    "ds_read_b64_tr_b16 a[90:91], v224 offset:5632\n"
    "v_mfma_f32_16x16x32_bf16 v[12:15], a[68:71], a[132:135], 0\n"
    "ds_read_b64_tr_b16 a[92:93], v225 offset:3584\n"
    "v_mfma_f32_16x16x32_bf16 v[8:11], a[68:71], a[128:131], 0\n"
    "ds_read_b64_tr_b16 a[94:95], v225 offset:5632\n"
    "s_waitcnt lgkmcnt(8)\n"
    "v_mfma_f32_16x16x32_bf16 v[16:19], a[72:75], a[128:131], 0\n"
    "ds_read_b64_tr_b16 a[96:97], v224 offset:7168\n"
    "v_mfma_f32_16x16x32_bf16 v[20:23], a[72:75], a[132:135], 0\n"
    "ds_read_b64_tr_b16 a[98:99], v224 offset:9216\n"
    "v_mfma_f32_16x16x32_bf16 v[28:31], a[76:79], a[132:135], 0\n"
    "ds_read_b64_tr_b16 a[100:101], v225 offset:7168\n"
    "v_mfma_f32_16x16x32_bf16 v[24:27], a[76:79], a[128:131], 0\n"
    "ds_read_b64_tr_b16 a[102:103], v225 offset:9216\n"
    "s_waitcnt lgkmcnt(8)\n"
    "v_mfma_f32_16x16x32_bf16 v[0:3], a[80:83], a[136:139], v[0:3]\n"
    "ds_read_b64_tr_b16 a[104:105], v226 offset:7168\n"
    "v_mfma_f32_16x16x32_bf16 v[4:7], a[80:83], a[140:143], v[4:7]\n"
    "ds_read_b64_tr_b16 a[106:107], v226 offset:9216\n"
    "v_mfma_f32_16x16x32_bf16 v[12:15], a[84:87], a[140:143], v[12:15]\n"
    "ds_read_b64_tr_b16 a[108:109], v227 offset:7168\n"
    "v_mfma_f32_16x16x32_bf16 v[8:11], a[84:87], a[136:139], v[8:11]\n"
    "ds_read_b64_tr_b16 a[110:111], v227 offset:9216\n"
    "s_waitcnt lgkmcnt(8)\n"
    "v_mfma_f32_16x16x32_bf16 v[16:19], a[88:91], a[136:139], v[16:19]\n"
    "ds_read_b64_tr_b16 a[112:113], v226 offset:10752\n"
    "v_mfma_f32_16x16x32_bf16 v[20:23], a[88:91], a[140:143], v[20:23]\n"
    "ds_read_b64_tr_b16 a[114:115], v226 offset:12800\n"
    "v_mfma_f32_16x16x32_bf16 v[28:31], a[92:95], a[140:143], v[28:31]\n"
    "ds_read_b64_tr_b16 a[116:117], v227 offset:10752\n"
    "v_mfma_f32_16x16x32_bf16 v[24:27], a[92:95], a[136:139], v[24:27]\n"
    "ds_read_b64_tr_b16 a[118:119], v227 offset:12800\n"
    "s_waitcnt vmcnt(24) lgkmcnt(8)\n"
    "v_mfma_f32_16x16x32_bf16 v[0:3], a[96:99], a[144:147], v[0:3]\n"
    "ds_read_b64_tr_b16 a[120:121], v224 offset:10752\n"
    "v_mfma_f32_16x16x32_bf16 v[4:7], a[96:99], a[148:151], v[4:7]\n"
    "ds_read_b64_tr_b16 a[122:123], v224 offset:12800\n"
    "v_mfma_f32_16x16x32_bf16 v[12:15], a[100:103], a[148:151], v[12:15]\n"
    "ds_read_b64_tr_b16 a[124:125], v225 offset:10752\n"
    "v_mfma_f32_16x16x32_bf16 v[8:11], a[100:103], a[144:147], v[8:11]\n"
    "ds_read_b64_tr_b16 a[126:127], v225 offset:12800\n"
    "s_waitcnt lgkmcnt(8)\n"
    "v_mfma_f32_16x16x32_bf16 v[16:19], a[104:107], a[144:147], v[16:19]\n"
    "ds_read_b64_tr_b16 a[64:65], v224 offset:14336\n"
    "v_mfma_f32_16x16x32_bf16 v[20:23], a[104:107], a[148:151], v[20:23]\n"
    "ds_read_b64_tr_b16 a[66:67], v224 offset:16384\n"
    "v_mfma_f32_16x16x32_bf16 v[28:31], a[108:111], a[148:151], v[28:31]\n"
    "ds_read_b64_tr_b16 a[68:69], v225 offset:14336\n"
    "v_mfma_f32_16x16x32_bf16 v[24:27], a[108:111], a[144:147], v[24:27]\n"
    "ds_read_b64_tr_b16 a[70:71], v225 offset:16384\n"
    "s_waitcnt lgkmcnt(8)\n"
    "v_mfma_f32_16x16x32_bf16 v[0:3], a[112:115], a[152:155], v[0:3]\n"
    "ds_read_b64_tr_b16 a[72:73], v226 offset:14336\n"
    "v_mfma_f32_16x16x32_bf16 v[4:7], a[112:115], a[156:159], v[4:7]\n"
    "ds_read_b64_tr_b16 a[74:75], v226 offset:16384\n"
    "v_mfma_f32_16x16x32_bf16 v[12:15], a[116:119], a[156:159], v[12:15]\n"
    "ds_read_b64_tr_b16 a[76:77], v227 offset:14336\n"
    "v_mfma_f32_16x16x32_bf16 v[8:11], a[116:119], a[152:155], v[8:11]\n"
    "ds_read_b64_tr_b16 a[78:79], v227 offset:16384\n"
    "s_waitcnt lgkmcnt(8)\n"
    "v_mfma_f32_16x16x32_bf16 v[16:19], a[120:123], a[152:155], v[16:19]\n"
    "ds_read_b64_tr_b16 a[80:81], v226 offset:17920\n"
    "v_mfma_f32_16x16x32_bf16 v[20:23], a[120:123], a[156:159], v[20:23]\n"
    "ds_read_b64_tr_b16 a[82:83], v226 offset:19968\n"
    "v_mfma_f32_16x16x32_bf16 v[28:31], a[124:127], a[156:159], v[28:31]\n"
    "ds_read_b64_tr_b16 a[84:85], v227 offset:17920\n"
    "v_mfma_f32_16x16x32_bf16 v[24:27], a[124:127], a[152:155], v[24:27]\n"
    "ds_read_b64_tr_b16 a[86:87], v227 offset:19968\n"
    "s_waitcnt vmcnt(20) lgkmcnt(8)\n"
    "v_mfma_f32_16x16x32_bf16 v[0:3], a[64:67], a[160:163], v[0:3]\n"
    "ds_read_b64_tr_b16 a[88:89], v224 offset:17920\n"
    "v_mfma_f32_16x16x32_bf16 v[4:7], a[64:67], a[164:167], v[4:7]\n"
    "ds_read_b64_tr_b16 a[90:91], v224 offset:19968\n"
    "v_mfma_f32_16x16x32_bf16 v[12:15], a[68:71], a[164:167], v[12:15]\n"
    "ds_read_b64_tr_b16 a[92:93], v225 offset:17920\n"
    "v_mfma_f32_16x16x32_bf16 v[8:11], a[68:71], a[160:163], v[8:11]\n"
    "ds_read_b64_tr_b16 a[94:95], v225 offset:19968\n"
    "s_waitcnt lgkmcnt(8)\n"
    "v_mfma_f32_16x16x32_bf16 v[16:19], a[72:75], a[160:163], v[16:19]\n"
    "ds_read_b64_tr_b16 a[96:97], v224 offset:21504\n"
    "v_mfma_f32_16x16x32_bf16 v[20:23], a[72:75], a[164:167], v[20:23]\n"
    "ds_read_b64_tr_b16 a[98:99], v224 offset:23552\n"
    "v_mfma_f32_16x16x32_bf16 v[28:31], a[76:79], a[164:167], v[28:31]\n"
    "ds_read_b64_tr_b16 a[100:101], v225 offset:21504\n"
    "v_mfma_f32_16x16x32_bf16 v[24:27], a[76:79], a[160:163], v[24:27]\n"
    "ds_read_b64_tr_b16 a[102:103], v225 offset:23552\n"
    "s_waitcnt lgkmcnt(8)\n"
    "v_mfma_f32_16x16x32_bf16 v[0:3], a[80:83], a[168:171], v[0:3]\n"
    "ds_read_b64_tr_b16 a[104:105], v226 offset:21504\n"
    "v_mfma_f32_16x16x32_bf16 v[4:7], a[80:83], a[172:175], v[4:7]\n"
    "ds_read_b64_tr_b16 a[106:107], v226 offset:23552\n"
    "v_mfma_f32_16x16x32_bf16 v[12:15], a[84:87], a[172:175], v[12:15]\n"
    "ds_read_b64_tr_b16 a[108:109], v227 offset:21504\n"
    "v_mfma_f32_16x16x32_bf16 v[8:11], a[84:87], a[168:171], v[8:11]\n"
    "ds_read_b64_tr_b16 a[110:111], v227 offset:23552\n"
    "s_waitcnt lgkmcnt(8)\n"
    "v_mfma_f32_16x16x32_bf16 v[16:19], a[88:91], a[168:171], v[16:19]\n"
    "ds_read_b64_tr_b16 a[112:113], v226 offset:25088\n"
    "v_mfma_f32_16x16x32_bf16 v[20:23], a[88:91], a[172:175], v[20:23]\n"
    "ds_read_b64_tr_b16 a[114:115], v226 offset:27136\n"
    "v_mfma_f32_16x16x32_bf16 v[28:31], a[92:95], a[172:175], v[28:31]\n"
    "ds_read_b64_tr_b16 a[116:117], v227 offset:25088\n"
    "v_mfma_f32_16x16x32_bf16 v[24:27], a[92:95], a[168:171], v[24:27]\n"
    "ds_read_b64_tr_b16 a[118:119], v227 offset:27136\n"
    "s_waitcnt vmcnt(16) lgkmcnt(8)\n"
    "v_mfma_f32_16x16x32_bf16 v[0:3], a[96:99], a[176:179], v[0:3]\n"
    "ds_read_b64_tr_b16 a[120:121], v224 offset:25088\n"
    "v_mfma_f32_16x16x32_bf16 v[4:7], a[96:99], a[180:183], v[4:7]\n"
    "ds_read_b64_tr_b16 a[122:123], v224 offset:27136\n"
    "v_mfma_f32_16x16x32_bf16 v[12:15], a[100:103], a[180:183], v[12:15]\n"
    "ds_read_b64_tr_b16 a[124:125], v225 offset:25088\n"
    "v_mfma_f32_16x16x32_bf16 v[8:11], a[100:103], a[176:179], v[8:11]\n"
    "ds_read_b64_tr_b16 a[126:127], v225 offset:27136\n"
    "s_waitcnt lgkmcnt(8)\n"
    "v_mfma_f32_16x16x32_bf16 v[16:19], a[104:107], a[176:179], v[16:19]\n"
    "ds_read_b64_tr_b16 a[64:65], v224 offset:28672\n"
    "v_mfma_f32_16x16x32_bf16 v[20:23], a[104:107], a[180:183], v[20:23]\n"
    "ds_read_b64_tr_b16 a[66:67], v224 offset:30720\n"
    "v_mfma_f32_16x16x32_bf16 v[28:31], a[108:111], a[180:183], v[28:31]\n"
    "ds_read_b64_tr_b16 a[68:69], v225 offset:28672\n"
    "v_mfma_f32_16x16x32_bf16 v[24:27], a[108:111], a[176:179], v[24:27]\n"
    "ds_read_b64_tr_b16 a[70:71], v225 offset:30720\n"
    "s_waitcnt lgkmcnt(8)\n"
    "v_mfma_f32_16x16x32_bf16 v[0:3], a[112:115], a[184:187], v[0:3]\n"
    "ds_read_b64_tr_b16 a[72:73], v226 offset:28672\n"
    "v_mfma_f32_16x16x32_bf16 v[4:7], a[112:115], a[188:191], v[4:7]\n"
    "ds_read_b64_tr_b16 a[74:75], v226 offset:30720\n"
    "v_mfma_f32_16x16x32_bf16 v[12:15], a[116:119], a[188:191], v[12:15]\n"
    "ds_read_b64_tr_b16 a[76:77], v227 offset:28672\n"
    "v_mfma_f32_16x16x32_bf16 v[8:11], a[116:119], a[184:187], v[8:11]\n"
    "ds_read_b64_tr_b16 a[78:79], v227 offset:30720\n"
    "s_waitcnt lgkmcnt(8)\n"
    "v_mfma_f32_16x16x32_bf16 v[16:19], a[120:123], a[184:187], v[16:19]\n"
    "ds_read_b64_tr_b16 a[80:81], v226 offset:32256\n"
    "v_mfma_f32_16x16x32_bf16 v[20:23], a[120:123], a[188:191], v[20:23]\n"
    "ds_read_b64_tr_b16 a[82:83], v226 offset:34304\n"
    "v_mfma_f32_16x16x32_bf16 v[28:31], a[124:127], a[188:191], v[28:31]\n"
    "ds_read_b64_tr_b16 a[84:85], v227 offset:32256\n"
    "v_mfma_f32_16x16x32_bf16 v[24:27], a[124:127], a[184:187], v[24:27]\n"
    "ds_read_b64_tr_b16 a[86:87], v227 offset:34304\n"
    "s_waitcnt vmcnt(12) lgkmcnt(8)\n"
    "v_mfma_f32_16x16x32_bf16 v[0:3], a[64:67], a[192:195], v[0:3]\n"
    "ds_read_b64_tr_b16 a[88:89], v224 offset:32256\n"
    "v_mfma_f32_16x16x32_bf16 v[4:7], a[64:67], a[196:199], v[4:7]\n"
    "ds_read_b64_tr_b16 a[90:91], v224 offset:34304\n"
    "v_mfma_f32_16x16x32_bf16 v[12:15], a[68:71], a[196:199], v[12:15]\n"
    "ds_read_b64_tr_b16 a[92:93], v225 offset:32256\n"
    "v_mfma_f32_16x16x32_bf16 v[8:11], a[68:71], a[192:195], v[8:11]\n"
    "ds_read_b64_tr_b16 a[94:95], v225 offset:34304\n"
    "s_waitcnt lgkmcnt(8)\n"
    "v_mfma_f32_16x16x32_bf16 v[16:19], a[72:75], a[192:195], v[16:19]\n"
    "ds_read_b64_tr_b16 a[96:97], v224 offset:35840\n"
    "v_mfma_f32_16x16x32_bf16 v[20:23], a[72:75], a[196:199], v[20:23]\n"
    "ds_read_b64_tr_b16 a[98:99], v224 offset:37888\n"
    "v_mfma_f32_16x16x32_bf16 v[28:31], a[76:79], a[196:199], v[28:31]\n"
    "ds_read_b64_tr_b16 a[100:101], v225 offset:35840\n"
    "v_mfma_f32_16x16x32_bf16 v[24:27], a[76:79], a[192:195], v[24:27]\n"
    "ds_read_b64_tr_b16 a[102:103], v225 offset:37888\n"
    "s_waitcnt lgkmcnt(8)\n"
    "v_mfma_f32_16x16x32_bf16 v[0:3], a[80:83], a[200:203], v[0:3]\n"
    "ds_read_b64_tr_b16 a[104:105], v226 offset:35840\n"
    "v_mfma_f32_16x16x32_bf16 v[4:7], a[80:83], a[204:207], v[4:7]\n"
    "ds_read_b64_tr_b16 a[106:107], v226 offset:37888\n"
    "v_mfma_f32_16x16x32_bf16 v[12:15], a[84:87], a[204:207], v[12:15]\n"
    "ds_read_b64_tr_b16 a[108:109], v227 offset:35840\n"
    "v_mfma_f32_16x16x32_bf16 v[8:11], a[84:87], a[200:203], v[8:11]\n"
    "ds_read_b64_tr_b16 a[110:111], v227 offset:37888\n"
    "s_waitcnt lgkmcnt(8)\n"
    "v_mfma_f32_16x16x32_bf16 v[16:19], a[88:91], a[200:203], v[16:19]\n"
    "ds_read_b64_tr_b16 a[112:113], v226 offset:39424\n"
    "v_mfma_f32_16x16x32_bf16 v[20:23], a[88:91], a[204:207], v[20:23]\n"
    "ds_read_b64_tr_b16 a[114:115], v226 offset:41472\n"
    "v_mfma_f32_16x16x32_bf16 v[28:31], a[92:95], a[204:207], v[28:31]\n"
    "ds_read_b64_tr_b16 a[116:117], v227 offset:39424\n"
    "v_mfma_f32_16x16x32_bf16 v[24:27], a[92:95], a[200:203], v[24:27]\n"
    "ds_read_b64_tr_b16 a[118:119], v227 offset:41472\n"
    "s_waitcnt vmcnt(8) lgkmcnt(8)\n"
    "v_mfma_f32_16x16x32_bf16 v[0:3], a[96:99], a[208:211], v[0:3]\n"
    "ds_read_b64_tr_b16 a[120:121], v224 offset:39424\n"
    "v_mfma_f32_16x16x32_bf16 v[4:7], a[96:99], a[212:215], v[4:7]\n"
    "ds_read_b64_tr_b16 a[122:123], v224 offset:41472\n"
    "v_mfma_f32_16x16x32_bf16 v[12:15], a[100:103], a[212:215], v[12:15]\n"
    "ds_read_b64_tr_b16 a[124:125], v225 offset:39424\n"
    "v_mfma_f32_16x16x32_bf16 v[8:11], a[100:103], a[208:211], v[8:11]\n"
    "ds_read_b64_tr_b16 a[126:127], v225 offset:41472\n"
    "s_waitcnt lgkmcnt(8)\n"
    "v_mfma_f32_16x16x32_bf16 v[16:19], a[104:107], a[208:211], v[16:19]\n"
    "ds_read_b64_tr_b16 a[64:65], v224 offset:43008\n"
    "v_mfma_f32_16x16x32_bf16 v[20:23], a[104:107], a[212:215], v[20:23]\n"
    "ds_read_b64_tr_b16 a[66:67], v224 offset:45056\n"
    "v_mfma_f32_16x16x32_bf16 v[28:31], a[108:111], a[212:215], v[28:31]\n"
    "ds_read_b64_tr_b16 a[68:69], v225 offset:43008\n"
    "v_mfma_f32_16x16x32_bf16 v[24:27], a[108:111], a[208:211], v[24:27]\n"
    "ds_read_b64_tr_b16 a[70:71], v225 offset:45056\n"
    "s_waitcnt lgkmcnt(8)\n"
    "v_mfma_f32_16x16x32_bf16 v[0:3], a[112:115], a[216:219], v[0:3]\n"
    "ds_read_b64_tr_b16 a[72:73], v226 offset:43008\n"
    "v_mfma_f32_16x16x32_bf16 v[4:7], a[112:115], a[220:223], v[4:7]\n"
    "ds_read_b64_tr_b16 a[74:75], v226 offset:45056\n"
    "v_mfma_f32_16x16x32_bf16 v[12:15], a[116:119], a[220:223], v[12:15]\n"
    "ds_read_b64_tr_b16 a[76:77], v227 offset:43008\n"
    "v_mfma_f32_16x16x32_bf16 v[8:11], a[116:119], a[216:219], v[8:11]\n"
    "ds_read_b64_tr_b16 a[78:79], v227 offset:45056\n"
    "s_waitcnt lgkmcnt(8)\n"
    "v_mfma_f32_16x16x32_bf16 v[16:19], a[120:123], a[216:219], v[16:19]\n"
    "ds_read_b64_tr_b16 a[80:81], v226 offset:46592\n"
    "v_mfma_f32_16x16x32_bf16 v[20:23], a[120:123], a[220:223], v[20:23]\n"
    "ds_read_b64_tr_b16 a[82:83], v226 offset:48640\n"
    "v_mfma_f32_16x16x32_bf16 v[28:31], a[124:127], a[220:223], v[28:31]\n"
    "ds_read_b64_tr_b16 a[84:85], v227 offset:46592\n"
    "v_mfma_f32_16x16x32_bf16 v[24:27], a[124:127], a[216:219], v[24:27]\n"
    "ds_read_b64_tr_b16 a[86:87], v227 offset:48640\n"
    "s_waitcnt vmcnt(4) lgkmcnt(8)\n"
    "v_mfma_f32_16x16x32_bf16 v[0:3], a[64:67], a[224:227], v[0:3]\n"
    "ds_read_b64_tr_b16 a[88:89], v224 offset:46592\n"
    "v_mfma_f32_16x16x32_bf16 v[4:7], a[64:67], a[228:231], v[4:7]\n"
    "ds_read_b64_tr_b16 a[90:91], v224 offset:48640\n"
    "v_mfma_f32_16x16x32_bf16 v[12:15], a[68:71], a[228:231], v[12:15]\n"
    "ds_read_b64_tr_b16 a[92:93], v225 offset:46592\n"
    "v_mfma_f32_16x16x32_bf16 v[8:11], a[68:71], a[224:227], v[8:11]\n"
    "ds_read_b64_tr_b16 a[94:95], v225 offset:48640\n"
    "s_waitcnt lgkmcnt(8)\n"
    "v_mfma_f32_16x16x32_bf16 v[16:19], a[72:75], a[224:227], v[16:19]\n"
    "ds_read_b64_tr_b16 a[96:97], v224 offset:50176\n"
    "v_mfma_f32_16x16x32_bf16 v[20:23], a[72:75], a[228:231], v[20:23]\n"
    "ds_read_b64_tr_b16 a[98:99], v224 offset:52224\n"
    "v_mfma_f32_16x16x32_bf16 v[28:31], a[76:79], a[228:231], v[28:31]\n"
    "ds_read_b64_tr_b16 a[100:101], v225 offset:50176\n"
    "v_mfma_f32_16x16x32_bf16 v[24:27], a[76:79], a[224:227], v[24:27]\n"
    "ds_read_b64_tr_b16 a[102:103], v225 offset:52224\n"
    "s_waitcnt lgkmcnt(8)\n"
    "v_mfma_f32_16x16x32_bf16 v[0:3], a[80:83], a[232:235], v[0:3]\n"
    "ds_read_b64_tr_b16 a[104:105], v226 offset:50176\n"
    "v_mfma_f32_16x16x32_bf16 v[4:7], a[80:83], a[236:239], v[4:7]\n"
    "ds_read_b64_tr_b16 a[106:107], v226 offset:52224\n"
    "v_mfma_f32_16x16x32_bf16 v[12:15], a[84:87], a[236:239], v[12:15]\n"
    "ds_read_b64_tr_b16 a[108:109], v227 offset:50176\n"
    "v_mfma_f32_16x16x32_bf16 v[8:11], a[84:87], a[232:235], v[8:11]\n"
    "ds_read_b64_tr_b16 a[110:111], v227 offset:52224\n"
    "s_waitcnt lgkmcnt(8)\n"
    "v_mfma_f32_16x16x32_bf16 v[16:19], a[88:91], a[232:235], v[16:19]\n"
    "ds_read_b64_tr_b16 a[112:113], v226 offset:53760\n"
    "v_mfma_f32_16x16x32_bf16 v[20:23], a[88:91], a[236:239], v[20:23]\n"
    "ds_read_b64_tr_b16 a[114:115], v226 offset:55808\n"
    "v_mfma_f32_16x16x32_bf16 v[28:31], a[92:95], a[236:239], v[28:31]\n"
    "ds_read_b64_tr_b16 a[116:117], v227 offset:53760\n"
    "v_mfma_f32_16x16x32_bf16 v[24:27], a[92:95], a[232:235], v[24:27]\n"
    "ds_read_b64_tr_b16 a[118:119], v227 offset:55808\n"
    "s_waitcnt vmcnt(0) lgkmcnt(8)\n"
    "v_mfma_f32_16x16x32_bf16 v[0:3], a[96:99], a[240:243], v[0:3]\n"
    "ds_read_b64_tr_b16 a[120:121], v224 offset:53760\n"
    "v_mfma_f32_16x16x32_bf16 v[4:7], a[96:99], a[244:247], v[4:7]\n"
    "ds_read_b64_tr_b16 a[122:123], v224 offset:55808\n"
    "v_mfma_f32_16x16x32_bf16 v[12:15], a[100:103], a[244:247], v[12:15]\n"
    "ds_read_b64_tr_b16 a[124:125], v225 offset:53760\n"
    "v_mfma_f32_16x16x32_bf16 v[8:11], a[100:103], a[240:243], v[8:11]\n"
    "ds_read_b64_tr_b16 a[126:127], v225 offset:55808\n"
    "s_waitcnt lgkmcnt(8)\n"
    "v_mfma_f32_16x16x32_bf16 v[16:19], a[104:107], a[240:243], v[16:19]\n"
    "v_mfma_f32_16x16x32_bf16 v[20:23], a[104:107], a[244:247], v[20:23]\n"
    "v_mfma_f32_16x16x32_bf16 v[28:31], a[108:111], a[244:247], v[28:31]\n"
    "v_mfma_f32_16x16x32_bf16 v[24:27], a[108:111], a[240:243], v[24:27]\n"
    "s_waitcnt lgkmcnt(4)\n"
    "v_mfma_f32_16x16x32_bf16 v[0:3], a[112:115], a[248:251], v[0:3]\n"
    "v_mfma_f32_16x16x32_bf16 v[4:7], a[112:115], a[252:255], v[4:7]\n"
    "v_mfma_f32_16x16x32_bf16 v[12:15], a[116:119], a[252:255], v[12:15]\n"
    "v_mfma_f32_16x16x32_bf16 v[8:11], a[116:119], a[248:251], v[8:11]\n"
    "s_waitcnt lgkmcnt(0)\n"
    "v_mfma_f32_16x16x32_bf16 v[16:19], a[120:123], a[248:251], v[16:19]\n"
    "v_mfma_f32_16x16x32_bf16 v[20:23], a[120:123], a[252:255], v[20:23]\n"
    "v_mfma_f32_16x16x32_bf16 v[28:31], a[124:127], a[252:255], v[28:31]\n"
    "v_mfma_f32_16x16x32_bf16 v[24:27], a[124:127], a[248:251], v[24:27]\n"
    "s_nop 7\n"
    "v_cvt_pk_bf16_f32 v64, v0, v1\n"
    "v_cvt_pk_bf16_f32 v65, v2, v3\n"
    "v_cvt_pk_bf16_f32 v66, v8, v9\n"
    "v_cvt_pk_bf16_f32 v67, v10, v11\n"
    "s_nop 0\n"
    "v_permlane16_swap_b32 v64, v66\n"
    "v_permlane16_swap_b32 v65, v67\n"
    "v_cvt_pk_bf16_f32 v68, v16, v17\n"
    "v_cvt_pk_bf16_f32 v69, v18, v19\n"
    "v_cvt_pk_bf16_f32 v70, v24, v25\n"
    "v_cvt_pk_bf16_f32 v71, v26, v27\n"
    "s_nop 0\n"
    "v_permlane16_swap_b32 v68, v70\n"
    "v_permlane16_swap_b32 v69, v71\n"
    "v_cvt_pk_bf16_f32 v72, v4, v5\n"
    "v_cvt_pk_bf16_f32 v73, v6, v7\n"
    "v_cvt_pk_bf16_f32 v74, v12, v13\n"
    "v_cvt_pk_bf16_f32 v75, v14, v15\n"
    "s_nop 0\n"
    "v_permlane16_swap_b32 v72, v74\n"
    "v_permlane16_swap_b32 v73, v75\n"
    "v_cvt_pk_bf16_f32 v76, v20, v21\n"
    "v_cvt_pk_bf16_f32 v77, v22, v23\n"
    "v_cvt_pk_bf16_f32 v78, v28, v29\n"
    "v_cvt_pk_bf16_f32 v79, v30, v31\n"
    "s_nop 0\n"
    "v_permlane16_swap_b32 v76, v78\n"
    "v_permlane16_swap_b32 v77, v79\n"
    "NOAP_%=:\n"
    "s_add_u32 s83, s83, 2048\n"
    "s_waitcnt vmcnt(15)\n"
    "v_mfma_f32_16x16x32_bf16 v[96:99], v[240:243], a[0:3], 0\n"
    "v_mfma_f32_16x16x32_bf16 v[100:103], v[244:247], a[0:3], 0\n"
    "v_mfma_f32_16x16x32_bf16 v[96:99], v[240:243], a[4:7], v[96:99]\n"
    "v_mfma_f32_16x16x32_bf16 v[100:103], v[244:247], a[4:7], v[100:103]\n"
    "s_waitcnt vmcnt(13)\n"
    "v_mfma_f32_16x16x32_bf16 v[96:99], v[240:243], a[8:11], v[96:99]\n"
    "v_mfma_f32_16x16x32_bf16 v[100:103], v[244:247], a[8:11], v[100:103]\n"
    "v_mfma_f32_16x16x32_bf16 v[96:99], v[240:243], a[12:15], v[96:99]\n"
    "v_mfma_f32_16x16x32_bf16 v[100:103], v[244:247], a[12:15], v[100:103]\n"
    "s_waitcnt vmcnt(11)\n"
    "v_mfma_f32_16x16x32_bf16 v[96:99], v[240:243], a[16:19], v[96:99]\n"
    "v_mfma_f32_16x16x32_bf16 v[100:103], v[244:247], a[16:19], v[100:103]\n"
    "v_mfma_f32_16x16x32_bf16 v[96:99], v[240:243], a[20:23], v[96:99]\n"
    "v_mfma_f32_16x16x32_bf16 v[100:103], v[244:247], a[20:23], v[100:103]\n"
    "s_waitcnt vmcnt(9)\n"
    "v_mfma_f32_16x16x32_bf16 v[96:99], v[240:243], a[24:27], v[96:99]\n"
    "v_mfma_f32_16x16x32_bf16 v[100:103], v[244:247], a[24:27], v[100:103]\n"
    "v_mfma_f32_16x16x32_bf16 v[96:99], v[240:243], a[28:31], v[96:99]\n"
    "v_mfma_f32_16x16x32_bf16 v[100:103], v[244:247], a[28:31], v[100:103]\n"
    "s_waitcnt vmcnt(6)\n"
    "v_bfe_u32 v216, v210, 1, 7\n"
    "buffer_load_dwordx2 v[238:239], v216, s[52:55], s83 offen sc1\n"
    "v_mfma_f32_16x16x32_bf16 v[96:99], v[240:243], a[32:35], v[96:99]\n"
    "v_mfma_f32_16x16x32_bf16 v[100:103], v[244:247], a[32:35], v[100:103]\n"
    "v_mfma_f32_16x16x32_bf16 v[96:99], v[240:243], a[36:39], v[96:99]\n"
    "v_mfma_f32_16x16x32_bf16 v[100:103], v[244:247], a[36:39], v[100:103]\n"
    "s_waitcnt vmcnt(5)\n"
    "v_mfma_f32_16x16x32_bf16 v[96:99], v[240:243], a[40:43], v[96:99]\n"
    "v_mfma_f32_16x16x32_bf16 v[100:103], v[244:247], a[40:43], v[100:103]\n"
    "v_mfma_f32_16x16x32_bf16 v[96:99], v[240:243], a[44:47], v[96:99]\n"
    "v_mfma_f32_16x16x32_bf16 v[100:103], v[244:247], a[44:47], v[100:103]\n"
    "s_waitcnt vmcnt(3)\n"
    "v_mfma_f32_16x16x32_bf16 v[96:99], v[240:243], a[48:51], v[96:99]\n"
    "v_mfma_f32_16x16x32_bf16 v[100:103], v[244:247], a[48:51], v[100:103]\n"
    "v_mfma_f32_16x16x32_bf16 v[96:99], v[240:243], a[52:55], v[96:99]\n"
    "v_mfma_f32_16x16x32_bf16 v[100:103], v[244:247], a[52:55], v[100:103]\n"
    "s_waitcnt vmcnt(1)\n"
    "v_mfma_f32_16x16x32_bf16 v[96:99], v[240:243], a[56:59], v[96:99]\n"
    "v_mfma_f32_16x16x32_bf16 v[100:103], v[244:247], a[56:59], v[100:103]\n"
    "v_mfma_f32_16x16x32_bf16 v[96:99], v[240:243], a[60:63], v[96:99]\n"
    "v_mfma_f32_16x16x32_bf16 v[100:103], v[244:247], a[60:63], v[100:103]\n"
    "s_nop 7\n"
    "v_cvt_pk_bf16_f32 v112, v96, v97\n"
    "v_cvt_pk_bf16_f32 v113, v98, v99\n"
    "v_cvt_pk_bf16_f32 v114, v100, v101\n"
    "v_cvt_pk_bf16_f32 v115, v102, v103\n"
    "s_cmp_eq_u32 s56, 3\n"
    "s_cselect_b32 s98, 0x8000000, 0\n"
    "s_add_u32 s62, s62, s98\n"
    "s_add_u32 s62, s62, 0x20000\n"
    "s_add_u32 s94, s80, s98\n"
    "s_waitcnt vmcnt(0)\n"
    "v_cmp_eq_u64 vcc, s[34:35], v[238:239]\n"
    "s_nop 0\n"
    "s_cmp_eq_u64 vcc, exec\n"
    "s_cbranch_scc0 POLLR2_%=\n"
    "GOTR2_%=:\n"
    "s_add_u32 s97, s62, 0\n"
    "buffer_load_dwordx4 a[64:67], v210, s[48:51], s97 offen offset:0 sc1\n"
    "buffer_load_dwordx4 a[68:71], v210, s[48:51], s97 offen offset:1024 sc1\n"
    "buffer_load_dwordx4 a[72:75], v210, s[48:51], s97 offen offset:2048 sc1\n"
    "buffer_load_dwordx4 a[76:79], v210, s[48:51], s97 offen offset:3072 sc1\n"
    "s_add_u32 s97, s62, 4096\n"
    "buffer_load_dwordx4 a[80:83], v210, s[48:51], s97 offen offset:0 sc1\n"
    "buffer_load_dwordx4 a[84:87], v210, s[48:51], s97 offen offset:1024 sc1\n"
    "buffer_load_dwordx4 a[88:91], v210, s[48:51], s97 offen offset:2048 sc1\n"
    "buffer_load_dwordx4 a[92:95], v210, s[48:51], s97 offen offset:3072 sc1\n"
    "s_add_u32 s97, s62, 8192\n"
    "buffer_load_dwordx4 a[96:99], v210, s[48:51], s97 offen offset:0 sc1\n"
    "buffer_load_dwordx4 a[100:103], v210, s[48:51], s97 offen offset:1024 sc1\n"
    "buffer_load_dwordx4 a[104:107], v210, s[48:51], s97 offen offset:2048 sc1\n"
    "buffer_load_dwordx4 a[108:111], v210, s[48:51], s97 offen offset:3072 sc1\n"
    "s_add_u32 s97, s62, 12288\n"
    "buffer_load_dwordx4 a[112:115], v210, s[48:51], s97 offen offset:0 sc1\n"
    "buffer_load_dwordx4 a[116:119], v210, s[48:51], s97 offen offset:1024 sc1\n"
    "buffer_load_dwordx4 a[120:123], v210, s[48:51], s97 offen offset:2048 sc1\n"
    "buffer_load_dwordx4 a[124:127], v210, s[48:51], s97 offen offset:3072 sc1\n"
    "buffer_store_dwordx4 v[112:115], v85, s[40:43], s80 offen\n"
    "s_waitcnt vmcnt(15)\n"
    "v_mfma_f32_16x16x32_bf16 v[96:99], v[240:243], a[64:67], 0\n"
    "v_mfma_f32_16x16x32_bf16 v[100:103], v[244:247], a[64:67], 0\n"
    "v_mfma_f32_16x16x32_bf16 v[96:99], v[240:243], a[68:71], v[96:99]\n"
    "v_mfma_f32_16x16x32_bf16 v[100:103], v[244:247], a[68:71], v[100:103]\n"
    "s_waitcnt vmcnt(13)\n"
    "v_mfma_f32_16x16x32_bf16 v[96:99], v[240:243], a[72:75], v[96:99]\n"
    "v_mfma_f32_16x16x32_bf16 v[100:103], v[244:247], a[72:75], v[100:103]\n"
    "v_mfma_f32_16x16x32_bf16 v[96:99], v[240:243], a[76:79], v[96:99]\n"
    "v_mfma_f32_16x16x32_bf16 v[100:103], v[244:247], a[76:79], v[100:103]\n"
    "s_waitcnt vmcnt(11)\n"
    "v_mfma_f32_16x16x32_bf16 v[96:99], v[240:243], a[80:83], v[96:99]\n"
    "v_mfma_f32_16x16x32_bf16 v[100:103], v[244:247], a[80:83], v[100:103]\n"
    "v_mfma_f32_16x16x32_bf16 v[96:99], v[240:243], a[84:87], v[96:99]\n"
    "v_mfma_f32_16x16x32_bf16 v[100:103], v[244:247], a[84:87], v[100:103]\n"
    "s_waitcnt vmcnt(9)\n"
    "v_mfma_f32_16x16x32_bf16 v[96:99], v[240:243], a[88:91], v[96:99]\n"
    "v_mfma_f32_16x16x32_bf16 v[100:103], v[244:247], a[88:91], v[100:103]\n"
    "v_mfma_f32_16x16x32_bf16 v[96:99], v[240:243], a[92:95], v[96:99]\n"
    "v_mfma_f32_16x16x32_bf16 v[100:103], v[244:247], a[92:95], v[100:103]\n"
    "s_waitcnt vmcnt(7)\n"
    "v_mfma_f32_16x16x32_bf16 v[96:99], v[240:243], a[96:99], v[96:99]\n"
    "v_mfma_f32_16x16x32_bf16 v[100:103], v[244:247], a[96:99], v[100:103]\n"
    "v_mfma_f32_16x16x32_bf16 v[96:99], v[240:243], a[100:103], v[96:99]\n"
    "v_mfma_f32_16x16x32_bf16 v[100:103], v[244:247], a[100:103], v[100:103]\n"
    "s_waitcnt vmcnt(5)\n"
    "v_mfma_f32_16x16x32_bf16 v[96:99], v[240:243], a[104:107], v[96:99]\n"
    "v_mfma_f32_16x16x32_bf16 v[100:103], v[244:247], a[104:107], v[100:103]\n"
    "v_mfma_f32_16x16x32_bf16 v[96:99], v[240:243], a[108:111], v[96:99]\n"
    "v_mfma_f32_16x16x32_bf16 v[100:103], v[244:247], a[108:111], v[100:103]\n"
    "s_waitcnt vmcnt(3)\n"
    "v_mfma_f32_16x16x32_bf16 v[96:99], v[240:243], a[112:115], v[96:99]\n"
    "v_mfma_f32_16x16x32_bf16 v[100:103], v[244:247], a[112:115], v[100:103]\n"
    "v_mfma_f32_16x16x32_bf16 v[96:99], v[240:243], a[116:119], v[96:99]\n"
    "v_mfma_f32_16x16x32_bf16 v[100:103], v[244:247], a[116:119], v[100:103]\n"
    "s_waitcnt vmcnt(1)\n"
    "v_mfma_f32_16x16x32_bf16 v[96:99], v[240:243], a[120:123], v[96:99]\n"
    "v_mfma_f32_16x16x32_bf16 v[100:103], v[244:247], a[120:123], v[100:103]\n"
    "v_mfma_f32_16x16x32_bf16 v[96:99], v[240:243], a[124:127], v[96:99]\n"
    "v_mfma_f32_16x16x32_bf16 v[100:103], v[244:247], a[124:127], v[100:103]\n"
    "s_nop 7\n"
    "v_cvt_pk_bf16_f32 v116, v96, v97\n"
    "v_cvt_pk_bf16_f32 v117, v98, v99\n"
    "v_cvt_pk_bf16_f32 v118, v100, v101\n"
    "v_cvt_pk_bf16_f32 v119, v102, v103\n"
    "buffer_store_dwordx4 v[116:119], v85, s[40:43], s94 offen offset:512\n"
    "s_cmp_eq_u32 s75, 1\n"
    "s_cbranch_scc0 DONE_%=\n"
    "buffer_store_dwordx4 v[64:67], v84, s[40:43], s58 offen offset:0\n"
    "buffer_store_dwordx4 v[68:71], v84, s[40:43], s58 offen offset:64\n"
    "buffer_store_dwordx4 v[72:75], v84, s[40:43], s61 offen offset:0\n"
    "buffer_store_dwordx4 v[76:79], v84, s[40:43], s61 offen offset:64\n"
    "DONE_%=:\n"
    "s_cmp_eq_u32 s26, 0\n"
    "s_cbranch_scc0 NOEP_%=\n"
    "v_mov_b32 v218, s34\n"
    "v_mov_b32 v219, s35\n"
    "v_lshlrev_b32 v216, 21, v210\n"
    "s_lshl_b32 s98, s56, 25\n"
    "s_add_u32 s98, s98, 0x1049800\n"
    "buffer_store_dwordx2 v[218:219], v216, s[52:55], s98 offen offset:0 sc1\n"
    "NOEP_%=:\n"
    "s_branch END_%=\n"
    "RARE0_%=:\n"
    "s_nop 7\n"
    "v_max3_f32 v218, v128, v129, v130\n"
    "v_max3_f32 v220, v132, v133, v134\n"
    "v_max3_f32 v218, v218, v131, v136\n"
    "v_max3_f32 v220, v220, v135, v140\n"
    "v_max3_f32 v218, v218, v137, v138\n"
    "v_max3_f32 v220, v220, v141, v142\n"
    "v_max_f32 v218, v218, v139\n"
    "v_max_f32 v220, v220, v143\n"
    "v_mov_b32 v219, v218\n"
    "v_mov_b32 v221, v220\n"
    "s_nop 0\n"
    "v_permlane16_swap_b32 v218, v219\n"
    "v_permlane16_swap_b32 v220, v221\n"
    "v_max_f32 v218, v218, v219\n"
    "v_max_f32 v220, v220, v221\n"
    "v_mov_b32 v219, v218\n"
    "v_mov_b32 v221, v220\n"
    "s_nop 0\n"
    "v_permlane32_swap_b32 v218, v219\n"
    "v_permlane32_swap_b32 v220, v221\n"
    "v_max_f32 v218, v218, v219\n"
    "v_max_f32 v220, v220, v221\n"
    "v_max_f32 v218, 0, v218\n"
    "v_max_f32 v220, 0, v220\n"
    "v_exp_f32 v232, -v218\n"
    "v_exp_f32 v234, -v220\n"
    "v_mov_b32 v219, v218\n"
    "v_mov_b32 v221, v220\n"
    "v_pk_add_f32 v[128:129], v[128:129], v[218:219] neg_lo:[0,1] neg_hi:[0,1]\n"
    "v_pk_add_f32 v[130:131], v[130:131], v[218:219] neg_lo:[0,1] neg_hi:[0,1]\n"
    "v_pk_add_f32 v[132:133], v[132:133], v[220:221] neg_lo:[0,1] neg_hi:[0,1]\n"
    "v_pk_add_f32 v[134:135], v[134:135], v[220:221] neg_lo:[0,1] neg_hi:[0,1]\n"
    "v_pk_add_f32 v[136:137], v[136:137], v[218:219] neg_lo:[0,1] neg_hi:[0,1]\n"
    "v_pk_add_f32 v[138:139], v[138:139], v[218:219] neg_lo:[0,1] neg_hi:[0,1]\n"
    "v_pk_add_f32 v[140:141], v[140:141], v[220:221] neg_lo:[0,1] neg_hi:[0,1]\n"
    "v_pk_add_f32 v[142:143], v[142:143], v[220:221] neg_lo:[0,1] neg_hi:[0,1]\n"
    "v_pk_add_f32 v[144:145], v[144:145], v[218:219] neg_lo:[0,1] neg_hi:[0,1]\n"
    "v_pk_add_f32 v[146:147], v[146:147], v[218:219] neg_lo:[0,1] neg_hi:[0,1]\n"
    "v_pk_add_f32 v[148:149], v[148:149], v[220:221] neg_lo:[0,1] neg_hi:[0,1]\n"
    "v_pk_add_f32 v[150:151], v[150:151], v[220:221] neg_lo:[0,1] neg_hi:[0,1]\n"
    "v_pk_add_f32 v[152:153], v[152:153], v[218:219] neg_lo:[0,1] neg_hi:[0,1]\n"
    "v_pk_add_f32 v[154:155], v[154:155], v[218:219] neg_lo:[0,1] neg_hi:[0,1]\n"
    "v_pk_add_f32 v[156:157], v[156:157], v[220:221] neg_lo:[0,1] neg_hi:[0,1]\n"
    "v_pk_add_f32 v[158:159], v[158:159], v[220:221] neg_lo:[0,1] neg_hi:[0,1]\n"
    "v_pk_add_f32 v[160:161], v[160:161], v[218:219] neg_lo:[0,1] neg_hi:[0,1]\n"
    "v_pk_add_f32 v[162:163], v[162:163], v[218:219] neg_lo:[0,1] neg_hi:[0,1]\n"
    "v_pk_add_f32 v[164:165], v[164:165], v[220:221] neg_lo:[0,1] neg_hi:[0,1]\n"
    "v_pk_add_f32 v[166:167], v[166:167], v[220:221] neg_lo:[0,1] neg_hi:[0,1]\n"
    "v_pk_add_f32 v[168:169], v[168:169], v[218:219] neg_lo:[0,1] neg_hi:[0,1]\n"
    "v_pk_add_f32 v[170:171], v[170:171], v[218:219] neg_lo:[0,1] neg_hi:[0,1]\n"
    "v_pk_add_f32 v[172:173], v[172:173], v[220:221] neg_lo:[0,1] neg_hi:[0,1]\n"
    "v_pk_add_f32 v[174:175], v[174:175], v[220:221] neg_lo:[0,1] neg_hi:[0,1]\n"
    "v_sub_f32 v204, v204, v218\n"
    "v_sub_f32 v205, v205, v220\n"
    "v_mov_b32 v233, v232\n"
    "v_mov_b32 v235, v234\n"
    "v_mul_f32 v202, v202, v232\n"
    "v_mul_f32 v203, v203, v234\n"
    "v_pk_mul_f32 v[0:1], v[0:1], v[232:233]\n"
    "v_pk_mul_f32 v[2:3], v[2:3], v[232:233]\n"
    "v_pk_mul_f32 v[4:5], v[4:5], v[234:235]\n"
    "v_pk_mul_f32 v[6:7], v[6:7], v[234:235]\n"
    "v_pk_mul_f32 v[8:9], v[8:9], v[232:233]\n"
    "v_pk_mul_f32 v[10:11], v[10:11], v[232:233]\n"
    "v_pk_mul_f32 v[12:13], v[12:13], v[234:235]\n"
    "v_pk_mul_f32 v[14:15], v[14:15], v[234:235]\n"
    "v_pk_mul_f32 v[16:17], v[16:17], v[232:233]\n"
    "v_pk_mul_f32 v[18:19], v[18:19], v[232:233]\n"
    "v_pk_mul_f32 v[20:21], v[20:21], v[234:235]\n"
    "v_pk_mul_f32 v[22:23], v[22:23], v[234:235]\n"
    "v_pk_mul_f32 v[24:25], v[24:25], v[232:233]\n"
    "v_pk_mul_f32 v[26:27], v[26:27], v[232:233]\n"
    "v_pk_mul_f32 v[28:29], v[28:29], v[234:235]\n"
    "v_pk_mul_f32 v[30:31], v[30:31], v[234:235]\n"
    "v_pk_mul_f32 v[32:33], v[32:33], v[232:233]\n"
    "v_pk_mul_f32 v[34:35], v[34:35], v[232:233]\n"
    "v_pk_mul_f32 v[36:37], v[36:37], v[234:235]\n"
    "v_pk_mul_f32 v[38:39], v[38:39], v[234:235]\n"
    "v_pk_mul_f32 v[40:41], v[40:41], v[232:233]\n"
    "v_pk_mul_f32 v[42:43], v[42:43], v[232:233]\n"
    "v_pk_mul_f32 v[44:45], v[44:45], v[234:235]\n"
    "v_pk_mul_f32 v[46:47], v[46:47], v[234:235]\n"
    "v_pk_mul_f32 v[48:49], v[48:49], v[232:233]\n"
    "v_pk_mul_f32 v[50:51], v[50:51], v[232:233]\n"
    "v_pk_mul_f32 v[52:53], v[52:53], v[234:235]\n"
    "v_pk_mul_f32 v[54:55], v[54:55], v[234:235]\n"
    "v_pk_mul_f32 v[56:57], v[56:57], v[232:233]\n"
    "v_pk_mul_f32 v[58:59], v[58:59], v[232:233]\n"
    "v_pk_mul_f32 v[60:61], v[60:61], v[234:235]\n"
    "v_pk_mul_f32 v[62:63], v[62:63], v[234:235]\n"
    "v_pk_mul_f32 v[64:65], v[64:65], v[232:233]\n"
    "v_pk_mul_f32 v[66:67], v[66:67], v[232:233]\n"
    "v_pk_mul_f32 v[68:69], v[68:69], v[234:235]\n"
    "v_pk_mul_f32 v[70:71], v[70:71], v[234:235]\n"
    "v_pk_mul_f32 v[72:73], v[72:73], v[232:233]\n"
    "v_pk_mul_f32 v[74:75], v[74:75], v[232:233]\n"
    "v_pk_mul_f32 v[76:77], v[76:77], v[234:235]\n"
    "v_pk_mul_f32 v[78:79], v[78:79], v[234:235]\n"
    "v_pk_mul_f32 v[80:81], v[80:81], v[232:233]\n"
    "v_pk_mul_f32 v[82:83], v[82:83], v[232:233]\n"
    "v_pk_mul_f32 v[84:85], v[84:85], v[234:235]\n"
    "v_pk_mul_f32 v[86:87], v[86:87], v[234:235]\n"
    "v_pk_mul_f32 v[88:89], v[88:89], v[232:233]\n"
    "v_pk_mul_f32 v[90:91], v[90:91], v[232:233]\n"
    "v_pk_mul_f32 v[92:93], v[92:93], v[234:235]\n"
    "v_pk_mul_f32 v[94:95], v[94:95], v[234:235]\n"
    "v_pk_mul_f32 v[96:97], v[96:97], v[232:233]\n"
    "v_pk_mul_f32 v[98:99], v[98:99], v[232:233]\n"
    "v_pk_mul_f32 v[100:101], v[100:101], v[234:235]\n"
    "v_pk_mul_f32 v[102:103], v[102:103], v[234:235]\n"
    "v_pk_mul_f32 v[104:105], v[104:105], v[232:233]\n"
    "v_pk_mul_f32 v[106:107], v[106:107], v[232:233]\n"
    "v_pk_mul_f32 v[108:109], v[108:109], v[234:235]\n"
    "v_pk_mul_f32 v[110:111], v[110:111], v[234:235]\n"
    "v_pk_mul_f32 v[112:113], v[112:113], v[232:233]\n"
    "v_pk_mul_f32 v[114:115], v[114:115], v[232:233]\n"
    "v_pk_mul_f32 v[116:117], v[116:117], v[234:235]\n"
    "v_pk_mul_f32 v[118:119], v[118:119], v[234:235]\n"
    "v_pk_mul_f32 v[120:121], v[120:121], v[232:233]\n"
    "v_pk_mul_f32 v[122:123], v[122:123], v[232:233]\n"
    "v_pk_mul_f32 v[124:125], v[124:125], v[234:235]\n"
    "v_pk_mul_f32 v[126:127], v[126:127], v[234:235]\n"
    "v_exp_f32 v184, v128\n"
    "v_exp_f32 v185, v129\n"
    "v_exp_f32 v186, v130\n"
    "v_exp_f32 v187, v131\n"
    "v_exp_f32 v188, v132\n"
    "v_exp_f32 v189, v133\n"
    "v_exp_f32 v190, v134\n"
    "v_exp_f32 v191, v135\n"
    "v_add_f32 v200, v202, v184\n"
    "v_add_f32 v201, v203, v188\n"
    "v_add_f32 v200, v200, v185\n"
    "v_add_f32 v201, v201, v189\n"
    "v_add_f32 v200, v200, v186\n"
    "v_add_f32 v201, v201, v190\n"
    "v_add_f32 v200, v200, v187\n"
    "v_add_f32 v201, v201, v191\n"
    "v_cvt_pk_bf16_f32 v176, v184, v185\n"
    "v_cvt_pk_bf16_f32 v177, v186, v187\n"
    "v_cvt_pk_bf16_f32 v180, v188, v189\n"
    "v_cvt_pk_bf16_f32 v181, v190, v191\n"
    "v_exp_f32 v184, v136\n"
    "v_exp_f32 v185, v137\n"
    "v_exp_f32 v186, v138\n"
    "v_exp_f32 v187, v139\n"
    "v_exp_f32 v188, v140\n"
    "v_exp_f32 v189, v141\n"
    "v_exp_f32 v190, v142\n"
    "v_exp_f32 v191, v143\n"
    "v_add_f32 v200, v200, v184\n"
    "v_add_f32 v201, v201, v188\n"
    "v_add_f32 v200, v200, v185\n"
    "v_add_f32 v201, v201, v189\n"
    "v_add_f32 v200, v200, v186\n"
    "v_add_f32 v201, v201, v190\n"
    "v_add_f32 v200, v200, v187\n"
    "v_add_f32 v201, v201, v191\n"
    "v_cvt_pk_bf16_f32 v178, v184, v185\n"
    "v_cvt_pk_bf16_f32 v179, v186, v187\n"
    "v_cvt_pk_bf16_f32 v182, v188, v189\n"
    "v_cvt_pk_bf16_f32 v183, v190, v191\n"
    "s_nop 1\n"
    "s_branch BACK0_%=\n"
    "RARE1_%=:\n"
    "s_nop 7\n"
    "v_max3_f32 v218, v144, v145, v146\n"
    "v_max3_f32 v220, v148, v149, v150\n"
    "v_max3_f32 v218, v218, v147, v152\n"
    "v_max3_f32 v220, v220, v151, v156\n"
    "v_max3_f32 v218, v218, v153, v154\n"
    "v_max3_f32 v220, v220, v157, v158\n"
    "v_max_f32 v218, v218, v155\n"
    "v_max_f32 v220, v220, v159\n"
    "v_mov_b32 v219, v218\n"
    "v_mov_b32 v221, v220\n"
    "s_nop 0\n"
    "v_permlane16_swap_b32 v218, v219\n"
    "v_permlane16_swap_b32 v220, v221\n"
    "v_max_f32 v218, v218, v219\n"
    "v_max_f32 v220, v220, v221\n"
    "v_mov_b32 v219, v218\n"
    "v_mov_b32 v221, v220\n"
    "s_nop 0\n"
    "v_permlane32_swap_b32 v218, v219\n"
    "v_permlane32_swap_b32 v220, v221\n"
    "v_max_f32 v218, v218, v219\n"
    "v_max_f32 v220, v220, v221\n"
    "v_max_f32 v218, 0, v218\n"
    "v_max_f32 v220, 0, v220\n"
    "v_exp_f32 v232, -v218\n"
    "v_exp_f32 v234, -v220\n"
    "v_mov_b32 v219, v218\n"
    "v_mov_b32 v221, v220\n"
    "v_pk_add_f32 v[144:145], v[144:145], v[218:219] neg_lo:[0,1] neg_hi:[0,1]\n"
    "v_pk_add_f32 v[146:147], v[146:147], v[218:219] neg_lo:[0,1] neg_hi:[0,1]\n"
    "v_pk_add_f32 v[148:149], v[148:149], v[220:221] neg_lo:[0,1] neg_hi:[0,1]\n"
    "v_pk_add_f32 v[150:151], v[150:151], v[220:221] neg_lo:[0,1] neg_hi:[0,1]\n"
    "v_pk_add_f32 v[152:153], v[152:153], v[218:219] neg_lo:[0,1] neg_hi:[0,1]\n"
    "v_pk_add_f32 v[154:155], v[154:155], v[218:219] neg_lo:[0,1] neg_hi:[0,1]\n"
    "v_pk_add_f32 v[156:157], v[156:157], v[220:221] neg_lo:[0,1] neg_hi:[0,1]\n"
    "v_pk_add_f32 v[158:159], v[158:159], v[220:221] neg_lo:[0,1] neg_hi:[0,1]\n"
    "v_pk_add_f32 v[128:129], v[128:129], v[218:219] neg_lo:[0,1] neg_hi:[0,1]\n"
    "v_pk_add_f32 v[130:131], v[130:131], v[218:219] neg_lo:[0,1] neg_hi:[0,1]\n"
    "v_pk_add_f32 v[132:133], v[132:133], v[220:221] neg_lo:[0,1] neg_hi:[0,1]\n"
    "v_pk_add_f32 v[134:135], v[134:135], v[220:221] neg_lo:[0,1] neg_hi:[0,1]\n"
    "v_pk_add_f32 v[136:137], v[136:137], v[218:219] neg_lo:[0,1] neg_hi:[0,1]\n"
    "v_pk_add_f32 v[138:139], v[138:139], v[218:219] neg_lo:[0,1] neg_hi:[0,1]\n"
    "v_pk_add_f32 v[140:141], v[140:141], v[220:221] neg_lo:[0,1] neg_hi:[0,1]\n"
    "v_pk_add_f32 v[142:143], v[142:143], v[220:221] neg_lo:[0,1] neg_hi:[0,1]\n"
    "v_pk_add_f32 v[160:161], v[160:161], v[218:219] neg_lo:[0,1] neg_hi:[0,1]\n"
    "v_pk_add_f32 v[162:163], v[162:163], v[218:219] neg_lo:[0,1] neg_hi:[0,1]\n"
    "v_pk_add_f32 v[164:165], v[164:165], v[220:221] neg_lo:[0,1] neg_hi:[0,1]\n"
    "v_pk_add_f32 v[166:167], v[166:167], v[220:221] neg_lo:[0,1] neg_hi:[0,1]\n"
    "v_pk_add_f32 v[168:169], v[168:169], v[218:219] neg_lo:[0,1] neg_hi:[0,1]\n"
    "v_pk_add_f32 v[170:171], v[170:171], v[218:219] neg_lo:[0,1] neg_hi:[0,1]\n"
    "v_pk_add_f32 v[172:173], v[172:173], v[220:221] neg_lo:[0,1] neg_hi:[0,1]\n"
    "v_pk_add_f32 v[174:175], v[174:175], v[220:221] neg_lo:[0,1] neg_hi:[0,1]\n"
    "v_sub_f32 v204, v204, v218\n"
    "v_sub_f32 v205, v205, v220\n"
    "v_mov_b32 v233, v232\n"
    "v_mov_b32 v235, v234\n"
    "v_mul_f32 v200, v200, v232\n"
    "v_mul_f32 v201, v201, v234\n"
    "v_pk_mul_f32 v[0:1], v[0:1], v[232:233]\n"
    "v_pk_mul_f32 v[2:3], v[2:3], v[232:233]\n"
    "v_pk_mul_f32 v[4:5], v[4:5], v[234:235]\n"
    "v_pk_mul_f32 v[6:7], v[6:7], v[234:235]\n"
    "v_pk_mul_f32 v[8:9], v[8:9], v[232:233]\n"
    "v_pk_mul_f32 v[10:11], v[10:11], v[232:233]\n"
    "v_pk_mul_f32 v[12:13], v[12:13], v[234:235]\n"
    "v_pk_mul_f32 v[14:15], v[14:15], v[234:235]\n"
    "v_pk_mul_f32 v[16:17], v[16:17], v[232:233]\n"
    "v_pk_mul_f32 v[18:19], v[18:19], v[232:233]\n"
    "v_pk_mul_f32 v[20:21], v[20:21], v[234:235]\n"
    "v_pk_mul_f32 v[22:23], v[22:23], v[234:235]\n"
    "v_pk_mul_f32 v[24:25], v[24:25], v[232:233]\n"
    "v_pk_mul_f32 v[26:27], v[26:27], v[232:233]\n"
    "v_pk_mul_f32 v[28:29], v[28:29], v[234:235]\n"
    "v_pk_mul_f32 v[30:31], v[30:31], v[234:235]\n"
    "v_pk_mul_f32 v[32:33], v[32:33], v[232:233]\n"
    "v_pk_mul_f32 v[34:35], v[34:35], v[232:233]\n"
    "v_pk_mul_f32 v[36:37], v[36:37], v[234:235]\n"
    "v_pk_mul_f32 v[38:39], v[38:39], v[234:235]\n"
    "v_pk_mul_f32 v[40:41], v[40:41], v[232:233]\n"
    "v_pk_mul_f32 v[42:43], v[42:43], v[232:233]\n"
    "v_pk_mul_f32 v[44:45], v[44:45], v[234:235]\n"
    "v_pk_mul_f32 v[46:47], v[46:47], v[234:235]\n"
    "v_pk_mul_f32 v[48:49], v[48:49], v[232:233]\n"
    "v_pk_mul_f32 v[50:51], v[50:51], v[232:233]\n"
    "v_pk_mul_f32 v[52:53], v[52:53], v[234:235]\n"
    "v_pk_mul_f32 v[54:55], v[54:55], v[234:235]\n"
    "v_pk_mul_f32 v[56:57], v[56:57], v[232:233]\n"
    "v_pk_mul_f32 v[58:59], v[58:59], v[232:233]\n"
    "v_pk_mul_f32 v[60:61], v[60:61], v[234:235]\n"
    "v_pk_mul_f32 v[62:63], v[62:63], v[234:235]\n"
    "v_pk_mul_f32 v[64:65], v[64:65], v[232:233]\n"
    "v_pk_mul_f32 v[66:67], v[66:67], v[232:233]\n"
    "v_pk_mul_f32 v[68:69], v[68:69], v[234:235]\n"
    "v_pk_mul_f32 v[70:71], v[70:71], v[234:235]\n"
    "v_pk_mul_f32 v[72:73], v[72:73], v[232:233]\n"
    "v_pk_mul_f32 v[74:75], v[74:75], v[232:233]\n"
    "v_pk_mul_f32 v[76:77], v[76:77], v[234:235]\n"
    "v_pk_mul_f32 v[78:79], v[78:79], v[234:235]\n"
    "v_pk_mul_f32 v[80:81], v[80:81], v[232:233]\n"
    "v_pk_mul_f32 v[82:83], v[82:83], v[232:233]\n"
    "v_pk_mul_f32 v[84:85], v[84:85], v[234:235]\n"
    "v_pk_mul_f32 v[86:87], v[86:87], v[234:235]\n"
    "v_pk_mul_f32 v[88:89], v[88:89], v[232:233]\n"
    "v_pk_mul_f32 v[90:91], v[90:91], v[232:233]\n"
    "v_pk_mul_f32 v[92:93], v[92:93], v[234:235]\n"
    "v_pk_mul_f32 v[94:95], v[94:95], v[234:235]\n"
    "v_pk_mul_f32 v[96:97], v[96:97], v[232:233]\n"
    "v_pk_mul_f32 v[98:99], v[98:99], v[232:233]\n"
    "v_pk_mul_f32 v[100:101], v[100:101], v[234:235]\n"
    "v_pk_mul_f32 v[102:103], v[102:103], v[234:235]\n"
    "v_pk_mul_f32 v[104:105], v[104:105], v[232:233]\n"
    "v_pk_mul_f32 v[106:107], v[106:107], v[232:233]\n"
    "v_pk_mul_f32 v[108:109], v[108:109], v[234:235]\n"
    "v_pk_mul_f32 v[110:111], v[110:111], v[234:235]\n"
    "v_pk_mul_f32 v[112:113], v[112:113], v[232:233]\n"
    "v_pk_mul_f32 v[114:115], v[114:115], v[232:233]\n"
    "v_pk_mul_f32 v[116:117], v[116:117], v[234:235]\n"
    "v_pk_mul_f32 v[118:119], v[118:119], v[234:235]\n"
    "v_pk_mul_f32 v[120:121], v[120:121], v[232:233]\n"
    "v_pk_mul_f32 v[122:123], v[122:123], v[232:233]\n"
    "v_pk_mul_f32 v[124:125], v[124:125], v[234:235]\n"
    "v_pk_mul_f32 v[126:127], v[126:127], v[234:235]\n"
    "v_exp_f32 v184, v144\n"
    "v_exp_f32 v185, v145\n"
    "v_exp_f32 v186, v146\n"
    "v_exp_f32 v187, v147\n"
    "v_exp_f32 v188, v148\n"
    "v_exp_f32 v189, v149\n"
    "v_exp_f32 v190, v150\n"
    "v_exp_f32 v191, v151\n"
    "v_add_f32 v202, v200, v184\n"
    "v_add_f32 v203, v201, v188\n"
    "v_add_f32 v202, v202, v185\n"
    "v_add_f32 v203, v203, v189\n"
    "v_add_f32 v202, v202, v186\n"
    "v_add_f32 v203, v203, v190\n"
    "v_add_f32 v202, v202, v187\n"
    "v_add_f32 v203, v203, v191\n"
    "v_cvt_pk_bf16_f32 v192, v184, v185\n"
    "v_cvt_pk_bf16_f32 v193, v186, v187\n"
    "v_cvt_pk_bf16_f32 v196, v188, v189\n"
    "v_cvt_pk_bf16_f32 v197, v190, v191\n"
    "v_exp_f32 v184, v152\n"
    "v_exp_f32 v185, v153\n"
    "v_exp_f32 v186, v154\n"
    "v_exp_f32 v187, v155\n"
    "v_exp_f32 v188, v156\n"
    "v_exp_f32 v189, v157\n"
    "v_exp_f32 v190, v158\n"
    "v_exp_f32 v191, v159\n"
    "v_add_f32 v202, v202, v184\n"
    "v_add_f32 v203, v203, v188\n"
    "v_add_f32 v202, v202, v185\n"
    "v_add_f32 v203, v203, v189\n"
    "v_add_f32 v202, v202, v186\n"
    "v_add_f32 v203, v203, v190\n"
    "v_add_f32 v202, v202, v187\n"
    "v_add_f32 v203, v203, v191\n"
    "v_cvt_pk_bf16_f32 v194, v184, v185\n"
    "v_cvt_pk_bf16_f32 v195, v186, v187\n"
    "v_cvt_pk_bf16_f32 v198, v188, v189\n"
    "v_cvt_pk_bf16_f32 v199, v190, v191\n"
    "s_nop 1\n"
    "s_branch BACK1_%=\n"
    "RARE0P_%=:\n"
    "s_nop 7\n"
    "v_max3_f32 v218, v128, v129, v130\n"
    "v_max3_f32 v220, v132, v133, v134\n"
    "v_max3_f32 v218, v218, v131, v136\n"
    "v_max3_f32 v220, v220, v135, v140\n"
    "v_max3_f32 v218, v218, v137, v138\n"
    "v_max3_f32 v220, v220, v141, v142\n"
    "v_max_f32 v218, v218, v139\n"
    "v_max_f32 v220, v220, v143\n"
    "v_mov_b32 v219, v218\n"
    "v_mov_b32 v221, v220\n"
    "s_nop 0\n"
    "v_permlane16_swap_b32 v218, v219\n"
    "v_permlane16_swap_b32 v220, v221\n"
    "v_max_f32 v218, v218, v219\n"
    "v_max_f32 v220, v220, v221\n"
    "v_mov_b32 v219, v218\n"
    "v_mov_b32 v221, v220\n"
    "s_nop 0\n"
    "v_permlane32_swap_b32 v218, v219\n"
    "v_permlane32_swap_b32 v220, v221\n"
    "v_max_f32 v218, v218, v219\n"
    "v_max_f32 v220, v220, v221\n"
    "v_max_f32 v218, 0, v218\n"
    "v_max_f32 v220, 0, v220\n"
    "v_exp_f32 v232, -v218\n"
    "v_exp_f32 v234, -v220\n"
    "v_mov_b32 v219, v218\n"
    "v_mov_b32 v221, v220\n"
    "v_pk_add_f32 v[128:129], v[128:129], v[218:219] neg_lo:[0,1] neg_hi:[0,1]\n"
    "v_pk_add_f32 v[130:131], v[130:131], v[218:219] neg_lo:[0,1] neg_hi:[0,1]\n"
    "v_pk_add_f32 v[132:133], v[132:133], v[220:221] neg_lo:[0,1] neg_hi:[0,1]\n"
    "v_pk_add_f32 v[134:135], v[134:135], v[220:221] neg_lo:[0,1] neg_hi:[0,1]\n"
    "v_pk_add_f32 v[136:137], v[136:137], v[218:219] neg_lo:[0,1] neg_hi:[0,1]\n"
    "v_pk_add_f32 v[138:139], v[138:139], v[218:219] neg_lo:[0,1] neg_hi:[0,1]\n"
    "v_pk_add_f32 v[140:141], v[140:141], v[220:221] neg_lo:[0,1] neg_hi:[0,1]\n"
    "v_pk_add_f32 v[142:143], v[142:143], v[220:221] neg_lo:[0,1] neg_hi:[0,1]\n"
    "v_pk_add_f32 v[144:145], v[144:145], v[218:219] neg_lo:[0,1] neg_hi:[0,1]\n"
    "v_pk_add_f32 v[146:147], v[146:147], v[218:219] neg_lo:[0,1] neg_hi:[0,1]\n"
    "v_pk_add_f32 v[148:149], v[148:149], v[220:221] neg_lo:[0,1] neg_hi:[0,1]\n"
    "v_pk_add_f32 v[150:151], v[150:151], v[220:221] neg_lo:[0,1] neg_hi:[0,1]\n"
    "v_pk_add_f32 v[152:153], v[152:153], v[218:219] neg_lo:[0,1] neg_hi:[0,1]\n"
    "v_pk_add_f32 v[154:155], v[154:155], v[218:219] neg_lo:[0,1] neg_hi:[0,1]\n"
    "v_pk_add_f32 v[156:157], v[156:157], v[220:221] neg_lo:[0,1] neg_hi:[0,1]\n"
    "v_pk_add_f32 v[158:159], v[158:159], v[220:221] neg_lo:[0,1] neg_hi:[0,1]\n"
    "v_pk_add_f32 v[160:161], v[160:161], v[218:219] neg_lo:[0,1] neg_hi:[0,1]\n"
    "v_pk_add_f32 v[162:163], v[162:163], v[218:219] neg_lo:[0,1] neg_hi:[0,1]\n"
    "v_pk_add_f32 v[164:165], v[164:165], v[220:221] neg_lo:[0,1] neg_hi:[0,1]\n"
    "v_pk_add_f32 v[166:167], v[166:167], v[220:221] neg_lo:[0,1] neg_hi:[0,1]\n"
    "v_pk_add_f32 v[168:169], v[168:169], v[218:219] neg_lo:[0,1] neg_hi:[0,1]\n"
    "v_pk_add_f32 v[170:171], v[170:171], v[218:219] neg_lo:[0,1] neg_hi:[0,1]\n"
    "v_pk_add_f32 v[172:173], v[172:173], v[220:221] neg_lo:[0,1] neg_hi:[0,1]\n"
    "v_pk_add_f32 v[174:175], v[174:175], v[220:221] neg_lo:[0,1] neg_hi:[0,1]\n"
    "v_sub_f32 v204, v204, v218\n"
    "v_sub_f32 v205, v205, v220\n"
    "v_mov_b32 v233, v232\n"
    "v_mov_b32 v235, v234\n"
    "v_mul_f32 v202, v202, v232\n"
    "v_mul_f32 v203, v203, v234\n"
    "v_exp_f32 v184, v128\n"
    "v_exp_f32 v185, v129\n"
    "v_exp_f32 v186, v130\n"
    "v_exp_f32 v187, v131\n"
    "v_exp_f32 v188, v132\n"
    "v_exp_f32 v189, v133\n"
    "v_exp_f32 v190, v134\n"
    "v_exp_f32 v191, v135\n"
    "v_add_f32 v200, v202, v184\n"
    "v_add_f32 v201, v203, v188\n"
    "v_add_f32 v200, v200, v185\n"
    "v_add_f32 v201, v201, v189\n"
    "v_add_f32 v200, v200, v186\n"
    "v_add_f32 v201, v201, v190\n"
    "v_add_f32 v200, v200, v187\n"
    "v_add_f32 v201, v201, v191\n"
    "v_cvt_pk_bf16_f32 v176, v184, v185\n"
    "v_cvt_pk_bf16_f32 v177, v186, v187\n"
    "v_cvt_pk_bf16_f32 v180, v188, v189\n"
    "v_cvt_pk_bf16_f32 v181, v190, v191\n"
    "v_exp_f32 v184, v136\n"
    "v_exp_f32 v185, v137\n"
    "v_exp_f32 v186, v138\n"
    "v_exp_f32 v187, v139\n"
    "v_exp_f32 v188, v140\n"
    "v_exp_f32 v189, v141\n"
    "v_exp_f32 v190, v142\n"
    "v_exp_f32 v191, v143\n"
    "v_add_f32 v200, v200, v184\n"
    "v_add_f32 v201, v201, v188\n"
    "v_add_f32 v200, v200, v185\n"
    "v_add_f32 v201, v201, v189\n"
    "v_add_f32 v200, v200, v186\n"
    "v_add_f32 v201, v201, v190\n"
    "v_add_f32 v200, v200, v187\n"
    "v_add_f32 v201, v201, v191\n"
    "v_cvt_pk_bf16_f32 v178, v184, v185\n"
    "v_cvt_pk_bf16_f32 v179, v186, v187\n"
    "v_cvt_pk_bf16_f32 v182, v188, v189\n"
    "v_cvt_pk_bf16_f32 v183, v190, v191\n"
    "s_nop 1\n"
    "s_branch BACK0P_%=\n"
    "RARET0_%=:\n"
    "s_nop 7\n"
    "v_max3_f32 v218, v144, v145, v146\n"
    "v_max3_f32 v220, v148, v149, v150\n"
    "v_max3_f32 v218, v218, v147, v152\n"
    "v_max3_f32 v220, v220, v151, v156\n"
    "v_max3_f32 v218, v218, v153, v154\n"
    "v_max3_f32 v220, v220, v157, v158\n"
    "v_max_f32 v218, v218, v155\n"
    "v_max_f32 v220, v220, v159\n"
    "v_mov_b32 v219, v218\n"
    "v_mov_b32 v221, v220\n"
    "s_nop 0\n"
    "v_permlane16_swap_b32 v218, v219\n"
    "v_permlane16_swap_b32 v220, v221\n"
    "v_max_f32 v218, v218, v219\n"
    "v_max_f32 v220, v220, v221\n"
    "v_mov_b32 v219, v218\n"
    "v_mov_b32 v221, v220\n"
    "s_nop 0\n"
    "v_permlane32_swap_b32 v218, v219\n"
    "v_permlane32_swap_b32 v220, v221\n"
    "v_max_f32 v218, v218, v219\n"
    "v_max_f32 v220, v220, v221\n"
    "v_max_f32 v218, 0, v218\n"
    "v_max_f32 v220, 0, v220\n"
    "v_exp_f32 v232, -v218\n"
    "v_exp_f32 v234, -v220\n"
    "v_mov_b32 v219, v218\n"
    "v_mov_b32 v221, v220\n"
    "v_pk_add_f32 v[144:145], v[144:145], v[218:219] neg_lo:[0,1] neg_hi:[0,1]\n"
    "v_pk_add_f32 v[146:147], v[146:147], v[218:219] neg_lo:[0,1] neg_hi:[0,1]\n"
    "v_pk_add_f32 v[148:149], v[148:149], v[220:221] neg_lo:[0,1] neg_hi:[0,1]\n"
    "v_pk_add_f32 v[150:151], v[150:151], v[220:221] neg_lo:[0,1] neg_hi:[0,1]\n"
    "v_pk_add_f32 v[152:153], v[152:153], v[218:219] neg_lo:[0,1] neg_hi:[0,1]\n"
    "v_pk_add_f32 v[154:155], v[154:155], v[218:219] neg_lo:[0,1] neg_hi:[0,1]\n"
    "v_pk_add_f32 v[156:157], v[156:157], v[220:221] neg_lo:[0,1] neg_hi:[0,1]\n"
    "v_pk_add_f32 v[158:159], v[158:159], v[220:221] neg_lo:[0,1] neg_hi:[0,1]\n"
    "v_pk_add_f32 v[128:129], v[128:129], v[218:219] neg_lo:[0,1] neg_hi:[0,1]\n"
    "v_pk_add_f32 v[130:131], v[130:131], v[218:219] neg_lo:[0,1] neg_hi:[0,1]\n"
    "v_pk_add_f32 v[132:133], v[132:133], v[220:221] neg_lo:[0,1] neg_hi:[0,1]\n"
    "v_pk_add_f32 v[134:135], v[134:135], v[220:221] neg_lo:[0,1] neg_hi:[0,1]\n"
    "v_pk_add_f32 v[136:137], v[136:137], v[218:219] neg_lo:[0,1] neg_hi:[0,1]\n"
    "v_pk_add_f32 v[138:139], v[138:139], v[218:219] neg_lo:[0,1] neg_hi:[0,1]\n"
    "v_pk_add_f32 v[140:141], v[140:141], v[220:221] neg_lo:[0,1] neg_hi:[0,1]\n"
    "v_pk_add_f32 v[142:143], v[142:143], v[220:221] neg_lo:[0,1] neg_hi:[0,1]\n"
    "v_pk_add_f32 v[160:161], v[160:161], v[218:219] neg_lo:[0,1] neg_hi:[0,1]\n"
    "v_pk_add_f32 v[162:163], v[162:163], v[218:219] neg_lo:[0,1] neg_hi:[0,1]\n"
    "v_pk_add_f32 v[164:165], v[164:165], v[220:221] neg_lo:[0,1] neg_hi:[0,1]\n"
    "v_pk_add_f32 v[166:167], v[166:167], v[220:221] neg_lo:[0,1] neg_hi:[0,1]\n"
    "v_pk_add_f32 v[168:169], v[168:169], v[218:219] neg_lo:[0,1] neg_hi:[0,1]\n"
    "v_pk_add_f32 v[170:171], v[170:171], v[218:219] neg_lo:[0,1] neg_hi:[0,1]\n"
    "v_pk_add_f32 v[172:173], v[172:173], v[220:221] neg_lo:[0,1] neg_hi:[0,1]\n"
    "v_pk_add_f32 v[174:175], v[174:175], v[220:221] neg_lo:[0,1] neg_hi:[0,1]\n"
    "v_sub_f32 v204, v204, v218\n"
    "v_sub_f32 v205, v205, v220\n"
    "v_mov_b32 v233, v232\n"
    "v_mov_b32 v235, v234\n"
    "v_mul_f32 v200, v200, v232\n"
    "v_mul_f32 v201, v201, v234\n"
    "v_pk_mul_f32 v[0:1], v[0:1], v[232:233]\n"
    "v_pk_mul_f32 v[2:3], v[2:3], v[232:233]\n"
    "v_pk_mul_f32 v[4:5], v[4:5], v[234:235]\n"
    "v_pk_mul_f32 v[6:7], v[6:7], v[234:235]\n"
    "v_pk_mul_f32 v[8:9], v[8:9], v[232:233]\n"
    "v_pk_mul_f32 v[10:11], v[10:11], v[232:233]\n"
    "v_pk_mul_f32 v[12:13], v[12:13], v[234:235]\n"
    "v_pk_mul_f32 v[14:15], v[14:15], v[234:235]\n"
    "v_pk_mul_f32 v[16:17], v[16:17], v[232:233]\n"
    "v_pk_mul_f32 v[18:19], v[18:19], v[232:233]\n"
    "v_pk_mul_f32 v[20:21], v[20:21], v[234:235]\n"
    "v_pk_mul_f32 v[22:23], v[22:23], v[234:235]\n"
    "v_pk_mul_f32 v[24:25], v[24:25], v[232:233]\n"
    "v_pk_mul_f32 v[26:27], v[26:27], v[232:233]\n"
    "v_pk_mul_f32 v[28:29], v[28:29], v[234:235]\n"
    "v_pk_mul_f32 v[30:31], v[30:31], v[234:235]\n"
    "v_pk_mul_f32 v[32:33], v[32:33], v[232:233]\n"
    "v_pk_mul_f32 v[34:35], v[34:35], v[232:233]\n"
    "v_pk_mul_f32 v[36:37], v[36:37], v[234:235]\n"
    "v_pk_mul_f32 v[38:39], v[38:39], v[234:235]\n"
    "v_pk_mul_f32 v[40:41], v[40:41], v[232:233]\n"
    "v_pk_mul_f32 v[42:43], v[42:43], v[232:233]\n"
    "v_pk_mul_f32 v[44:45], v[44:45], v[234:235]\n"
    "v_pk_mul_f32 v[46:47], v[46:47], v[234:235]\n"
    "v_pk_mul_f32 v[48:49], v[48:49], v[232:233]\n"
    "v_pk_mul_f32 v[50:51], v[50:51], v[232:233]\n"
    "v_pk_mul_f32 v[52:53], v[52:53], v[234:235]\n"
    "v_pk_mul_f32 v[54:55], v[54:55], v[234:235]\n"
    "v_pk_mul_f32 v[56:57], v[56:57], v[232:233]\n"
    "v_pk_mul_f32 v[58:59], v[58:59], v[232:233]\n"
    "v_pk_mul_f32 v[60:61], v[60:61], v[234:235]\n"
    "v_pk_mul_f32 v[62:63], v[62:63], v[234:235]\n"
    "v_pk_mul_f32 v[64:65], v[64:65], v[232:233]\n"
    "v_pk_mul_f32 v[66:67], v[66:67], v[232:233]\n"
    "v_pk_mul_f32 v[68:69], v[68:69], v[234:235]\n"
    "v_pk_mul_f32 v[70:71], v[70:71], v[234:235]\n"
    "v_pk_mul_f32 v[72:73], v[72:73], v[232:233]\n"
    "v_pk_mul_f32 v[74:75], v[74:75], v[232:233]\n"
    "v_pk_mul_f32 v[76:77], v[76:77], v[234:235]\n"
    "v_pk_mul_f32 v[78:79], v[78:79], v[234:235]\n"
    "v_pk_mul_f32 v[80:81], v[80:81], v[232:233]\n"
    "v_pk_mul_f32 v[82:83], v[82:83], v[232:233]\n"
    "v_pk_mul_f32 v[84:85], v[84:85], v[234:235]\n"
    "v_pk_mul_f32 v[86:87], v[86:87], v[234:235]\n"
    "v_pk_mul_f32 v[88:89], v[88:89], v[232:233]\n"
    "v_pk_mul_f32 v[90:91], v[90:91], v[232:233]\n"
    "v_pk_mul_f32 v[92:93], v[92:93], v[234:235]\n"
    "v_pk_mul_f32 v[94:95], v[94:95], v[234:235]\n"
    "v_pk_mul_f32 v[96:97], v[96:97], v[232:233]\n"
    "v_pk_mul_f32 v[98:99], v[98:99], v[232:233]\n"
    "v_pk_mul_f32 v[100:101], v[100:101], v[234:235]\n"
    "v_pk_mul_f32 v[102:103], v[102:103], v[234:235]\n"
    "v_pk_mul_f32 v[104:105], v[104:105], v[232:233]\n"
    "v_pk_mul_f32 v[106:107], v[106:107], v[232:233]\n"
    "v_pk_mul_f32 v[108:109], v[108:109], v[234:235]\n"
    "v_pk_mul_f32 v[110:111], v[110:111], v[234:235]\n"
    "v_pk_mul_f32 v[112:113], v[112:113], v[232:233]\n"
    "v_pk_mul_f32 v[114:115], v[114:115], v[232:233]\n"
    "v_pk_mul_f32 v[116:117], v[116:117], v[234:235]\n"
    "v_pk_mul_f32 v[118:119], v[118:119], v[234:235]\n"
    "v_pk_mul_f32 v[120:121], v[120:121], v[232:233]\n"
    "v_pk_mul_f32 v[122:123], v[122:123], v[232:233]\n"
    "v_pk_mul_f32 v[124:125], v[124:125], v[234:235]\n"
    "v_pk_mul_f32 v[126:127], v[126:127], v[234:235]\n"
    "v_exp_f32 v184, v144\n"
    "v_exp_f32 v185, v145\n"
    "v_exp_f32 v186, v146\n"
    "v_exp_f32 v187, v147\n"
    "v_exp_f32 v188, v148\n"
    "v_exp_f32 v189, v149\n"
    "v_exp_f32 v190, v150\n"
    "v_exp_f32 v191, v151\n"
    "v_add_f32 v202, v200, v184\n"
    "v_add_f32 v203, v201, v188\n"
    "v_add_f32 v202, v202, v185\n"
    "v_add_f32 v203, v203, v189\n"
    "v_add_f32 v202, v202, v186\n"
    "v_add_f32 v203, v203, v190\n"
    "v_add_f32 v202, v202, v187\n"
    "v_add_f32 v203, v203, v191\n"
    "v_cvt_pk_bf16_f32 v192, v184, v185\n"
    "v_cvt_pk_bf16_f32 v193, v186, v187\n"
    "v_cvt_pk_bf16_f32 v196, v188, v189\n"
    "v_cvt_pk_bf16_f32 v197, v190, v191\n"
    "v_exp_f32 v184, v152\n"
    "v_exp_f32 v185, v153\n"
    "v_exp_f32 v186, v154\n"
    "v_exp_f32 v187, v155\n"
    "v_exp_f32 v188, v156\n"
    "v_exp_f32 v189, v157\n"
    "v_exp_f32 v190, v158\n"
    "v_exp_f32 v191, v159\n"
    "v_add_f32 v202, v202, v184\n"
    "v_add_f32 v203, v203, v188\n"
    "v_add_f32 v202, v202, v185\n"
    "v_add_f32 v203, v203, v189\n"
    "v_add_f32 v202, v202, v186\n"
    "v_add_f32 v203, v203, v190\n"
    "v_add_f32 v202, v202, v187\n"
    "v_add_f32 v203, v203, v191\n"
    "v_cvt_pk_bf16_f32 v194, v184, v185\n"
    "v_cvt_pk_bf16_f32 v195, v186, v187\n"
    "v_cvt_pk_bf16_f32 v198, v188, v189\n"
    "v_cvt_pk_bf16_f32 v199, v190, v191\n"
    "s_nop 1\n"
    "s_branch BACKT0_%=\n"
    "RARET1_%=:\n"
    "s_nop 7\n"
    "v_max3_f32 v218, v144, v145, v146\n"
    "v_max3_f32 v220, v148, v149, v150\n"
    "v_max3_f32 v218, v218, v147, v152\n"
    "v_max3_f32 v220, v220, v151, v156\n"
    "v_max3_f32 v218, v218, v153, v154\n"
    "v_max3_f32 v220, v220, v157, v158\n"
    "v_max_f32 v218, v218, v155\n"
    "v_max_f32 v220, v220, v159\n"
    "v_mov_b32 v219, v218\n"
    "v_mov_b32 v221, v220\n"
    "s_nop 0\n"
    "v_permlane16_swap_b32 v218, v219\n"
    "v_permlane16_swap_b32 v220, v221\n"
    "v_max_f32 v218, v218, v219\n"
    "v_max_f32 v220, v220, v221\n"
    "v_mov_b32 v219, v218\n"
    "v_mov_b32 v221, v220\n"
    "s_nop 0\n"
    "v_permlane32_swap_b32 v218, v219\n"
    "v_permlane32_swap_b32 v220, v221\n"
    "v_max_f32 v218, v218, v219\n"
    "v_max_f32 v220, v220, v221\n"
    "v_max_f32 v218, 0, v218\n"
    "v_max_f32 v220, 0, v220\n"
    "v_exp_f32 v232, -v218\n"
    "v_exp_f32 v234, -v220\n"
    "v_mov_b32 v219, v218\n"
    "v_mov_b32 v221, v220\n"
    "v_pk_add_f32 v[144:145], v[144:145], v[218:219] neg_lo:[0,1] neg_hi:[0,1]\n"
    "v_pk_add_f32 v[146:147], v[146:147], v[218:219] neg_lo:[0,1] neg_hi:[0,1]\n"
    "v_pk_add_f32 v[148:149], v[148:149], v[220:221] neg_lo:[0,1] neg_hi:[0,1]\n"
    "v_pk_add_f32 v[150:151], v[150:151], v[220:221] neg_lo:[0,1] neg_hi:[0,1]\n"
    "v_pk_add_f32 v[152:153], v[152:153], v[218:219] neg_lo:[0,1] neg_hi:[0,1]\n"
    "v_pk_add_f32 v[154:155], v[154:155], v[218:219] neg_lo:[0,1] neg_hi:[0,1]\n"
    "v_pk_add_f32 v[156:157], v[156:157], v[220:221] neg_lo:[0,1] neg_hi:[0,1]\n"
    "v_pk_add_f32 v[158:159], v[158:159], v[220:221] neg_lo:[0,1] neg_hi:[0,1]\n"
    "v_pk_add_f32 v[128:129], v[128:129], v[218:219] neg_lo:[0,1] neg_hi:[0,1]\n"
    "v_pk_add_f32 v[130:131], v[130:131], v[218:219] neg_lo:[0,1] neg_hi:[0,1]\n"
    "v_pk_add_f32 v[132:133], v[132:133], v[220:221] neg_lo:[0,1] neg_hi:[0,1]\n"
    "v_pk_add_f32 v[134:135], v[134:135], v[220:221] neg_lo:[0,1] neg_hi:[0,1]\n"
    "v_pk_add_f32 v[136:137], v[136:137], v[218:219] neg_lo:[0,1] neg_hi:[0,1]\n"
    "v_pk_add_f32 v[138:139], v[138:139], v[218:219] neg_lo:[0,1] neg_hi:[0,1]\n"
    "v_pk_add_f32 v[140:141], v[140:141], v[220:221] neg_lo:[0,1] neg_hi:[0,1]\n"
    "v_pk_add_f32 v[142:143], v[142:143], v[220:221] neg_lo:[0,1] neg_hi:[0,1]\n"
    "v_pk_add_f32 v[160:161], v[160:161], v[218:219] neg_lo:[0,1] neg_hi:[0,1]\n"
    "v_pk_add_f32 v[162:163], v[162:163], v[218:219] neg_lo:[0,1] neg_hi:[0,1]\n"
    "v_pk_add_f32 v[164:165], v[164:165], v[220:221] neg_lo:[0,1] neg_hi:[0,1]\n"
    "v_pk_add_f32 v[166:167], v[166:167], v[220:221] neg_lo:[0,1] neg_hi:[0,1]\n"
    "v_pk_add_f32 v[168:169], v[168:169], v[218:219] neg_lo:[0,1] neg_hi:[0,1]\n"
    "v_pk_add_f32 v[170:171], v[170:171], v[218:219] neg_lo:[0,1] neg_hi:[0,1]\n"
    "v_pk_add_f32 v[172:173], v[172:173], v[220:221] neg_lo:[0,1] neg_hi:[0,1]\n"
    "v_pk_add_f32 v[174:175], v[174:175], v[220:221] neg_lo:[0,1] neg_hi:[0,1]\n"
    "v_sub_f32 v204, v204, v218\n"
    "v_sub_f32 v205, v205, v220\n"
    "v_mov_b32 v233, v232\n"
    "v_mov_b32 v235, v234\n"
    "v_mul_f32 v200, v200, v232\n"
    "v_mul_f32 v201, v201, v234\n"
    "v_pk_mul_f32 v[0:1], v[0:1], v[232:233]\n"
    "v_pk_mul_f32 v[2:3], v[2:3], v[232:233]\n"
    "v_pk_mul_f32 v[4:5], v[4:5], v[234:235]\n"
    "v_pk_mul_f32 v[6:7], v[6:7], v[234:235]\n"
    "v_pk_mul_f32 v[8:9], v[8:9], v[232:233]\n"
    "v_pk_mul_f32 v[10:11], v[10:11], v[232:233]\n"
    "v_pk_mul_f32 v[12:13], v[12:13], v[234:235]\n"
    "v_pk_mul_f32 v[14:15], v[14:15], v[234:235]\n"
    "v_pk_mul_f32 v[16:17], v[16:17], v[232:233]\n"
    "v_pk_mul_f32 v[18:19], v[18:19], v[232:233]\n"
    "v_pk_mul_f32 v[20:21], v[20:21], v[234:235]\n"
    "v_pk_mul_f32 v[22:23], v[22:23], v[234:235]\n"
    "v_pk_mul_f32 v[24:25], v[24:25], v[232:233]\n"
    "v_pk_mul_f32 v[26:27], v[26:27], v[232:233]\n"
    "v_pk_mul_f32 v[28:29], v[28:29], v[234:235]\n"
    "v_pk_mul_f32 v[30:31], v[30:31], v[234:235]\n"
    "v_pk_mul_f32 v[32:33], v[32:33], v[232:233]\n"
    "v_pk_mul_f32 v[34:35], v[34:35], v[232:233]\n"
    "v_pk_mul_f32 v[36:37], v[36:37], v[234:235]\n"
    "v_pk_mul_f32 v[38:39], v[38:39], v[234:235]\n"
    "v_pk_mul_f32 v[40:41], v[40:41], v[232:233]\n"
    "v_pk_mul_f32 v[42:43], v[42:43], v[232:233]\n"
    "v_pk_mul_f32 v[44:45], v[44:45], v[234:235]\n"
    "v_pk_mul_f32 v[46:47], v[46:47], v[234:235]\n"
    "v_pk_mul_f32 v[48:49], v[48:49], v[232:233]\n"
    "v_pk_mul_f32 v[50:51], v[50:51], v[232:233]\n"
    "v_pk_mul_f32 v[52:53], v[52:53], v[234:235]\n"
    "v_pk_mul_f32 v[54:55], v[54:55], v[234:235]\n"
    "v_pk_mul_f32 v[56:57], v[56:57], v[232:233]\n"
    "v_pk_mul_f32 v[58:59], v[58:59], v[232:233]\n"
    "v_pk_mul_f32 v[60:61], v[60:61], v[234:235]\n"
    "v_pk_mul_f32 v[62:63], v[62:63], v[234:235]\n"
    "v_pk_mul_f32 v[64:65], v[64:65], v[232:233]\n"
    "v_pk_mul_f32 v[66:67], v[66:67], v[232:233]\n"
    "v_pk_mul_f32 v[68:69], v[68:69], v[234:235]\n"
    "v_pk_mul_f32 v[70:71], v[70:71], v[234:235]\n"
    "v_pk_mul_f32 v[72:73], v[72:73], v[232:233]\n"
    "v_pk_mul_f32 v[74:75], v[74:75], v[232:233]\n"
    "v_pk_mul_f32 v[76:77], v[76:77], v[234:235]\n"
    "v_pk_mul_f32 v[78:79], v[78:79], v[234:235]\n"
    "v_pk_mul_f32 v[80:81], v[80:81], v[232:233]\n"
    "v_pk_mul_f32 v[82:83], v[82:83], v[232:233]\n"
    "v_pk_mul_f32 v[84:85], v[84:85], v[234:235]\n"
    "v_pk_mul_f32 v[86:87], v[86:87], v[234:235]\n"
    "v_pk_mul_f32 v[88:89], v[88:89], v[232:233]\n"
    "v_pk_mul_f32 v[90:91], v[90:91], v[232:233]\n"
    "v_pk_mul_f32 v[92:93], v[92:93], v[234:235]\n"
    "v_pk_mul_f32 v[94:95], v[94:95], v[234:235]\n"
    "v_pk_mul_f32 v[96:97], v[96:97], v[232:233]\n"
    "v_pk_mul_f32 v[98:99], v[98:99], v[232:233]\n"
    "v_pk_mul_f32 v[100:101], v[100:101], v[234:235]\n"
    "v_pk_mul_f32 v[102:103], v[102:103], v[234:235]\n"
    "v_pk_mul_f32 v[104:105], v[104:105], v[232:233]\n"
    "v_pk_mul_f32 v[106:107], v[106:107], v[232:233]\n"
    "v_pk_mul_f32 v[108:109], v[108:109], v[234:235]\n"
    "v_pk_mul_f32 v[110:111], v[110:111], v[234:235]\n"
    "v_pk_mul_f32 v[112:113], v[112:113], v[232:233]\n"
    "v_pk_mul_f32 v[114:115], v[114:115], v[232:233]\n"
    "v_pk_mul_f32 v[116:117], v[116:117], v[234:235]\n"
    "v_pk_mul_f32 v[118:119], v[118:119], v[234:235]\n"
    "v_pk_mul_f32 v[120:121], v[120:121], v[232:233]\n"
    "v_pk_mul_f32 v[122:123], v[122:123], v[232:233]\n"
    "v_pk_mul_f32 v[124:125], v[124:125], v[234:235]\n"
    "v_pk_mul_f32 v[126:127], v[126:127], v[234:235]\n"
    "v_exp_f32 v184, v144\n"
    "v_exp_f32 v185, v145\n"
    "v_exp_f32 v186, v146\n"
    "v_exp_f32 v187, v147\n"
    "v_exp_f32 v188, v148\n"
    "v_exp_f32 v189, v149\n"
    "v_exp_f32 v190, v150\n"
    "v_exp_f32 v191, v151\n"
    "v_add_f32 v202, v200, v184\n"
    "v_add_f32 v203, v201, v188\n"
    "v_add_f32 v202, v202, v185\n"
    "v_add_f32 v203, v203, v189\n"
    "v_add_f32 v202, v202, v186\n"
    "v_add_f32 v203, v203, v190\n"
    "v_add_f32 v202, v202, v187\n"
    "v_add_f32 v203, v203, v191\n"
    "v_cvt_pk_bf16_f32 v192, v184, v185\n"
    "v_cvt_pk_bf16_f32 v193, v186, v187\n"
    "v_cvt_pk_bf16_f32 v196, v188, v189\n"
    "v_cvt_pk_bf16_f32 v197, v190, v191\n"
    "v_exp_f32 v184, v152\n"
    "v_exp_f32 v185, v153\n"
    "v_exp_f32 v186, v154\n"
    "v_exp_f32 v187, v155\n"
    "v_exp_f32 v188, v156\n"
    "v_exp_f32 v189, v157\n"
    "v_exp_f32 v190, v158\n"
    "v_exp_f32 v191, v159\n"
    "v_add_f32 v202, v202, v184\n"
    "v_add_f32 v203, v203, v188\n"
    "v_add_f32 v202, v202, v185\n"
    "v_add_f32 v203, v203, v189\n"
    "v_add_f32 v202, v202, v186\n"
    "v_add_f32 v203, v203, v190\n"
    "v_add_f32 v202, v202, v187\n"
    "v_add_f32 v203, v203, v191\n"
    "v_cvt_pk_bf16_f32 v194, v184, v185\n"
    "v_cvt_pk_bf16_f32 v195, v186, v187\n"
    "v_cvt_pk_bf16_f32 v198, v188, v189\n"
    "v_cvt_pk_bf16_f32 v199, v190, v191\n"
    "s_nop 1\n"
    "s_branch BACKT1_%=\n"
    "RARET2_%=:\n"
    "s_nop 7\n"
    "v_max3_f32 v218, v144, v145, v146\n"
    "v_max3_f32 v220, v148, v149, v150\n"
    "v_max3_f32 v218, v218, v147, v152\n"
    "v_max3_f32 v220, v220, v151, v156\n"
    "v_max3_f32 v218, v218, v153, v154\n"
    "v_max3_f32 v220, v220, v157, v158\n"
    "v_max_f32 v218, v218, v155\n"
    "v_max_f32 v220, v220, v159\n"
    "v_mov_b32 v219, v218\n"
    "v_mov_b32 v221, v220\n"
    "s_nop 0\n"
    "v_permlane16_swap_b32 v218, v219\n"
    "v_permlane16_swap_b32 v220, v221\n"
    "v_max_f32 v218, v218, v219\n"
    "v_max_f32 v220, v220, v221\n"
    "v_mov_b32 v219, v218\n"
    "v_mov_b32 v221, v220\n"
    "s_nop 0\n"
    "v_permlane32_swap_b32 v218, v219\n"
    "v_permlane32_swap_b32 v220, v221\n"
    "v_max_f32 v218, v218, v219\n"
    "v_max_f32 v220, v220, v221\n"
    "v_max_f32 v218, 0, v218\n"
    "v_max_f32 v220, 0, v220\n"
    "v_exp_f32 v232, -v218\n"
    "v_exp_f32 v234, -v220\n"
    "v_mov_b32 v219, v218\n"
    "v_mov_b32 v221, v220\n"
    "v_pk_add_f32 v[144:145], v[144:145], v[218:219] neg_lo:[0,1] neg_hi:[0,1]\n"
    "v_pk_add_f32 v[146:147], v[146:147], v[218:219] neg_lo:[0,1] neg_hi:[0,1]\n"
    "v_pk_add_f32 v[148:149], v[148:149], v[220:221] neg_lo:[0,1] neg_hi:[0,1]\n"
    "v_pk_add_f32 v[150:151], v[150:151], v[220:221] neg_lo:[0,1] neg_hi:[0,1]\n"
    "v_pk_add_f32 v[152:153], v[152:153], v[218:219] neg_lo:[0,1] neg_hi:[0,1]\n"
    "v_pk_add_f32 v[154:155], v[154:155], v[218:219] neg_lo:[0,1] neg_hi:[0,1]\n"
    "v_pk_add_f32 v[156:157], v[156:157], v[220:221] neg_lo:[0,1] neg_hi:[0,1]\n"
    "v_pk_add_f32 v[158:159], v[158:159], v[220:221] neg_lo:[0,1] neg_hi:[0,1]\n"
    "v_pk_add_f32 v[128:129], v[128:129], v[218:219] neg_lo:[0,1] neg_hi:[0,1]\n"
    "v_pk_add_f32 v[130:131], v[130:131], v[218:219] neg_lo:[0,1] neg_hi:[0,1]\n"
    "v_pk_add_f32 v[132:133], v[132:133], v[220:221] neg_lo:[0,1] neg_hi:[0,1]\n"
    "v_pk_add_f32 v[134:135], v[134:135], v[220:221] neg_lo:[0,1] neg_hi:[0,1]\n"
    "v_pk_add_f32 v[136:137], v[136:137], v[218:219] neg_lo:[0,1] neg_hi:[0,1]\n"
    "v_pk_add_f32 v[138:139], v[138:139], v[218:219] neg_lo:[0,1] neg_hi:[0,1]\n"
    "v_pk_add_f32 v[140:141], v[140:141], v[220:221] neg_lo:[0,1] neg_hi:[0,1]\n"
    "v_pk_add_f32 v[142:143], v[142:143], v[220:221] neg_lo:[0,1] neg_hi:[0,1]\n"
    "v_pk_add_f32 v[160:161], v[160:161], v[218:219] neg_lo:[0,1] neg_hi:[0,1]\n"
    "v_pk_add_f32 v[162:163], v[162:163], v[218:219] neg_lo:[0,1] neg_hi:[0,1]\n"
    "v_pk_add_f32 v[164:165], v[164:165], v[220:221] neg_lo:[0,1] neg_hi:[0,1]\n"
    "v_pk_add_f32 v[166:167], v[166:167], v[220:221] neg_lo:[0,1] neg_hi:[0,1]\n"
    "v_pk_add_f32 v[168:169], v[168:169], v[218:219] neg_lo:[0,1] neg_hi:[0,1]\n"
    "v_pk_add_f32 v[170:171], v[170:171], v[218:219] neg_lo:[0,1] neg_hi:[0,1]\n"
    "v_pk_add_f32 v[172:173], v[172:173], v[220:221] neg_lo:[0,1] neg_hi:[0,1]\n"
    "v_pk_add_f32 v[174:175], v[174:175], v[220:221] neg_lo:[0,1] neg_hi:[0,1]\n"
    "v_sub_f32 v204, v204, v218\n"
    "v_sub_f32 v205, v205, v220\n"
    "v_mov_b32 v233, v232\n"
    "v_mov_b32 v235, v234\n"
    "v_mul_f32 v200, v200, v232\n"
    "v_mul_f32 v201, v201, v234\n"
    "v_pk_mul_f32 v[0:1], v[0:1], v[232:233]\n"
    "v_pk_mul_f32 v[2:3], v[2:3], v[232:233]\n"
    "v_pk_mul_f32 v[4:5], v[4:5], v[234:235]\n"
    "v_pk_mul_f32 v[6:7], v[6:7], v[234:235]\n"
    "v_pk_mul_f32 v[8:9], v[8:9], v[232:233]\n"
    "v_pk_mul_f32 v[10:11], v[10:11], v[232:233]\n"
    "v_pk_mul_f32 v[12:13], v[12:13], v[234:235]\n"
    "v_pk_mul_f32 v[14:15], v[14:15], v[234:235]\n"
    "v_pk_mul_f32 v[16:17], v[16:17], v[232:233]\n"
    "v_pk_mul_f32 v[18:19], v[18:19], v[232:233]\n"
    "v_pk_mul_f32 v[20:21], v[20:21], v[234:235]\n"
    "v_pk_mul_f32 v[22:23], v[22:23], v[234:235]\n"
    "v_pk_mul_f32 v[24:25], v[24:25], v[232:233]\n"
    "v_pk_mul_f32 v[26:27], v[26:27], v[232:233]\n"
    "v_pk_mul_f32 v[28:29], v[28:29], v[234:235]\n"
    "v_pk_mul_f32 v[30:31], v[30:31], v[234:235]\n"
    "v_pk_mul_f32 v[32:33], v[32:33], v[232:233]\n"
    "v_pk_mul_f32 v[34:35], v[34:35], v[232:233]\n"
    "v_pk_mul_f32 v[36:37], v[36:37], v[234:235]\n"
    "v_pk_mul_f32 v[38:39], v[38:39], v[234:235]\n"
    "v_pk_mul_f32 v[40:41], v[40:41], v[232:233]\n"
    "v_pk_mul_f32 v[42:43], v[42:43], v[232:233]\n"
    "v_pk_mul_f32 v[44:45], v[44:45], v[234:235]\n"
    "v_pk_mul_f32 v[46:47], v[46:47], v[234:235]\n"
    "v_pk_mul_f32 v[48:49], v[48:49], v[232:233]\n"
    "v_pk_mul_f32 v[50:51], v[50:51], v[232:233]\n"
    "v_pk_mul_f32 v[52:53], v[52:53], v[234:235]\n"
    "v_pk_mul_f32 v[54:55], v[54:55], v[234:235]\n"
    "v_pk_mul_f32 v[56:57], v[56:57], v[232:233]\n"
    "v_pk_mul_f32 v[58:59], v[58:59], v[232:233]\n"
    "v_pk_mul_f32 v[60:61], v[60:61], v[234:235]\n"
    "v_pk_mul_f32 v[62:63], v[62:63], v[234:235]\n"
    "v_pk_mul_f32 v[64:65], v[64:65], v[232:233]\n"
    "v_pk_mul_f32 v[66:67], v[66:67], v[232:233]\n"
    "v_pk_mul_f32 v[68:69], v[68:69], v[234:235]\n"
    "v_pk_mul_f32 v[70:71], v[70:71], v[234:235]\n"
    "v_pk_mul_f32 v[72:73], v[72:73], v[232:233]\n"
    "v_pk_mul_f32 v[74:75], v[74:75], v[232:233]\n"
    "v_pk_mul_f32 v[76:77], v[76:77], v[234:235]\n"
    "v_pk_mul_f32 v[78:79], v[78:79], v[234:235]\n"
    "v_pk_mul_f32 v[80:81], v[80:81], v[232:233]\n"
    "v_pk_mul_f32 v[82:83], v[82:83], v[232:233]\n"
    "v_pk_mul_f32 v[84:85], v[84:85], v[234:235]\n"
    "v_pk_mul_f32 v[86:87], v[86:87], v[234:235]\n"
    "v_pk_mul_f32 v[88:89], v[88:89], v[232:233]\n"
    "v_pk_mul_f32 v[90:91], v[90:91], v[232:233]\n"
    "v_pk_mul_f32 v[92:93], v[92:93], v[234:235]\n"
    "v_pk_mul_f32 v[94:95], v[94:95], v[234:235]\n"
    "v_pk_mul_f32 v[96:97], v[96:97], v[232:233]\n"
    "v_pk_mul_f32 v[98:99], v[98:99], v[232:233]\n"
    "v_pk_mul_f32 v[100:101], v[100:101], v[234:235]\n"
    "v_pk_mul_f32 v[102:103], v[102:103], v[234:235]\n"
    "v_pk_mul_f32 v[104:105], v[104:105], v[232:233]\n"
    "v_pk_mul_f32 v[106:107], v[106:107], v[232:233]\n"
    "v_pk_mul_f32 v[108:109], v[108:109], v[234:235]\n"
    "v_pk_mul_f32 v[110:111], v[110:111], v[234:235]\n"
    "v_pk_mul_f32 v[112:113], v[112:113], v[232:233]\n"
    "v_pk_mul_f32 v[114:115], v[114:115], v[232:233]\n"
    "v_pk_mul_f32 v[116:117], v[116:117], v[234:235]\n"
    "v_pk_mul_f32 v[118:119], v[118:119], v[234:235]\n"
    "v_pk_mul_f32 v[120:121], v[120:121], v[232:233]\n"
    "v_pk_mul_f32 v[122:123], v[122:123], v[232:233]\n"
    "v_pk_mul_f32 v[124:125], v[124:125], v[234:235]\n"
    "v_pk_mul_f32 v[126:127], v[126:127], v[234:235]\n"
    "v_exp_f32 v184, v144\n"
    "v_exp_f32 v185, v145\n"
    "v_exp_f32 v186, v146\n"
    "v_exp_f32 v187, v147\n"
    "v_exp_f32 v188, v148\n"
    "v_exp_f32 v189, v149\n"
    "v_exp_f32 v190, v150\n"
    "v_exp_f32 v191, v151\n"
    "v_add_f32 v202, v200, v184\n"
    "v_add_f32 v203, v201, v188\n"
    "v_add_f32 v202, v202, v185\n"
    "v_add_f32 v203, v203, v189\n"
    "v_add_f32 v202, v202, v186\n"
    "v_add_f32 v203, v203, v190\n"
    "v_add_f32 v202, v202, v187\n"
    "v_add_f32 v203, v203, v191\n"
    "v_cvt_pk_bf16_f32 v192, v184, v185\n"
    "v_cvt_pk_bf16_f32 v193, v186, v187\n"
    "v_cvt_pk_bf16_f32 v196, v188, v189\n"
    "v_cvt_pk_bf16_f32 v197, v190, v191\n"
    "v_exp_f32 v184, v152\n"
    "v_exp_f32 v185, v153\n"
    "v_exp_f32 v186, v154\n"
    "v_exp_f32 v187, v155\n"
    "v_exp_f32 v188, v156\n"
    "v_exp_f32 v189, v157\n"
    "v_exp_f32 v190, v158\n"
    "v_exp_f32 v191, v159\n"
    "v_add_f32 v202, v202, v184\n"
    "v_add_f32 v203, v203, v188\n"
    "v_add_f32 v202, v202, v185\n"
    "v_add_f32 v203, v203, v189\n"
    "v_add_f32 v202, v202, v186\n"
    "v_add_f32 v203, v203, v190\n"
    "v_add_f32 v202, v202, v187\n"
    "v_add_f32 v203, v203, v191\n"
    "v_cvt_pk_bf16_f32 v194, v184, v185\n"
    "v_cvt_pk_bf16_f32 v195, v186, v187\n"
    "v_cvt_pk_bf16_f32 v198, v188, v189\n"
    "v_cvt_pk_bf16_f32 v199, v190, v191\n"
    "s_nop 1\n"
    "s_branch BACKT2_%=\n"
    "RARET3_%=:\n"
    "s_nop 7\n"
    "v_max3_f32 v218, v144, v145, v146\n"
    "v_max3_f32 v220, v148, v149, v150\n"
    "v_max3_f32 v218, v218, v147, v152\n"
    "v_max3_f32 v220, v220, v151, v156\n"
    "v_max3_f32 v218, v218, v153, v154\n"
    "v_max3_f32 v220, v220, v157, v158\n"
    "v_max_f32 v218, v218, v155\n"
    "v_max_f32 v220, v220, v159\n"
    "v_mov_b32 v219, v218\n"
    "v_mov_b32 v221, v220\n"
    "s_nop 0\n"
    "v_permlane16_swap_b32 v218, v219\n"
    "v_permlane16_swap_b32 v220, v221\n"
    "v_max_f32 v218, v218, v219\n"
    "v_max_f32 v220, v220, v221\n"
    "v_mov_b32 v219, v218\n"
    "v_mov_b32 v221, v220\n"
    "s_nop 0\n"
    "v_permlane32_swap_b32 v218, v219\n"
    "v_permlane32_swap_b32 v220, v221\n"
    "v_max_f32 v218, v218, v219\n"
    "v_max_f32 v220, v220, v221\n"
    "v_max_f32 v218, 0, v218\n"
    "v_max_f32 v220, 0, v220\n"
    "v_exp_f32 v232, -v218\n"
    "v_exp_f32 v234, -v220\n"
    "v_mov_b32 v219, v218\n"
    "v_mov_b32 v221, v220\n"
    "v_pk_add_f32 v[144:145], v[144:145], v[218:219] neg_lo:[0,1] neg_hi:[0,1]\n"
    "v_pk_add_f32 v[146:147], v[146:147], v[218:219] neg_lo:[0,1] neg_hi:[0,1]\n"
    "v_pk_add_f32 v[148:149], v[148:149], v[220:221] neg_lo:[0,1] neg_hi:[0,1]\n"
    "v_pk_add_f32 v[150:151], v[150:151], v[220:221] neg_lo:[0,1] neg_hi:[0,1]\n"
    "v_pk_add_f32 v[152:153], v[152:153], v[218:219] neg_lo:[0,1] neg_hi:[0,1]\n"
    "v_pk_add_f32 v[154:155], v[154:155], v[218:219] neg_lo:[0,1] neg_hi:[0,1]\n"
    "v_pk_add_f32 v[156:157], v[156:157], v[220:221] neg_lo:[0,1] neg_hi:[0,1]\n"
    "v_pk_add_f32 v[158:159], v[158:159], v[220:221] neg_lo:[0,1] neg_hi:[0,1]\n"
    "v_pk_add_f32 v[128:129], v[128:129], v[218:219] neg_lo:[0,1] neg_hi:[0,1]\n"
    "v_pk_add_f32 v[130:131], v[130:131], v[218:219] neg_lo:[0,1] neg_hi:[0,1]\n"
    "v_pk_add_f32 v[132:133], v[132:133], v[220:221] neg_lo:[0,1] neg_hi:[0,1]\n"
    "v_pk_add_f32 v[134:135], v[134:135], v[220:221] neg_lo:[0,1] neg_hi:[0,1]\n"
    "v_pk_add_f32 v[136:137], v[136:137], v[218:219] neg_lo:[0,1] neg_hi:[0,1]\n"
    "v_pk_add_f32 v[138:139], v[138:139], v[218:219] neg_lo:[0,1] neg_hi:[0,1]\n"
    "v_pk_add_f32 v[140:141], v[140:141], v[220:221] neg_lo:[0,1] neg_hi:[0,1]\n"
    "v_pk_add_f32 v[142:143], v[142:143], v[220:221] neg_lo:[0,1] neg_hi:[0,1]\n"
    "v_pk_add_f32 v[160:161], v[160:161], v[218:219] neg_lo:[0,1] neg_hi:[0,1]\n"
    "v_pk_add_f32 v[162:163], v[162:163], v[218:219] neg_lo:[0,1] neg_hi:[0,1]\n"
    "v_pk_add_f32 v[164:165], v[164:165], v[220:221] neg_lo:[0,1] neg_hi:[0,1]\n"
    "v_pk_add_f32 v[166:167], v[166:167], v[220:221] neg_lo:[0,1] neg_hi:[0,1]\n"
    "v_pk_add_f32 v[168:169], v[168:169], v[218:219] neg_lo:[0,1] neg_hi:[0,1]\n"
    "v_pk_add_f32 v[170:171], v[170:171], v[218:219] neg_lo:[0,1] neg_hi:[0,1]\n"
    "v_pk_add_f32 v[172:173], v[172:173], v[220:221] neg_lo:[0,1] neg_hi:[0,1]\n"
    "v_pk_add_f32 v[174:175], v[174:175], v[220:221] neg_lo:[0,1] neg_hi:[0,1]\n"
    "v_sub_f32 v204, v204, v218\n"
    "v_sub_f32 v205, v205, v220\n"
    "v_mov_b32 v233, v232\n"
    "v_mov_b32 v235, v234\n"
    "v_mul_f32 v200, v200, v232\n"
    "v_mul_f32 v201, v201, v234\n"
    "v_pk_mul_f32 v[0:1], v[0:1], v[232:233]\n"
    "v_pk_mul_f32 v[2:3], v[2:3], v[232:233]\n"
    "v_pk_mul_f32 v[4:5], v[4:5], v[234:235]\n"
    "v_pk_mul_f32 v[6:7], v[6:7], v[234:235]\n"
    "v_pk_mul_f32 v[8:9], v[8:9], v[232:233]\n"
    "v_pk_mul_f32 v[10:11], v[10:11], v[232:233]\n"
    "v_pk_mul_f32 v[12:13], v[12:13], v[234:235]\n"
    "v_pk_mul_f32 v[14:15], v[14:15], v[234:235]\n"
    "v_pk_mul_f32 v[16:17], v[16:17], v[232:233]\n"
    "v_pk_mul_f32 v[18:19], v[18:19], v[232:233]\n"
    "v_pk_mul_f32 v[20:21], v[20:21], v[234:235]\n"
    "v_pk_mul_f32 v[22:23], v[22:23], v[234:235]\n"
    "v_pk_mul_f32 v[24:25], v[24:25], v[232:233]\n"
    "v_pk_mul_f32 v[26:27], v[26:27], v[232:233]\n"
    "v_pk_mul_f32 v[28:29], v[28:29], v[234:235]\n"
    "v_pk_mul_f32 v[30:31], v[30:31], v[234:235]\n"
    "v_pk_mul_f32 v[32:33], v[32:33], v[232:233]\n"
    "v_pk_mul_f32 v[34:35], v[34:35], v[232:233]\n"
    "v_pk_mul_f32 v[36:37], v[36:37], v[234:235]\n"
    "v_pk_mul_f32 v[38:39], v[38:39], v[234:235]\n"
    "v_pk_mul_f32 v[40:41], v[40:41], v[232:233]\n"
    "v_pk_mul_f32 v[42:43], v[42:43], v[232:233]\n"
    "v_pk_mul_f32 v[44:45], v[44:45], v[234:235]\n"
    "v_pk_mul_f32 v[46:47], v[46:47], v[234:235]\n"
    "v_pk_mul_f32 v[48:49], v[48:49], v[232:233]\n"
    "v_pk_mul_f32 v[50:51], v[50:51], v[232:233]\n"
    "v_pk_mul_f32 v[52:53], v[52:53], v[234:235]\n"
    "v_pk_mul_f32 v[54:55], v[54:55], v[234:235]\n"
    "v_pk_mul_f32 v[56:57], v[56:57], v[232:233]\n"
    "v_pk_mul_f32 v[58:59], v[58:59], v[232:233]\n"
    "v_pk_mul_f32 v[60:61], v[60:61], v[234:235]\n"
    "v_pk_mul_f32 v[62:63], v[62:63], v[234:235]\n"
    "v_pk_mul_f32 v[64:65], v[64:65], v[232:233]\n"
    "v_pk_mul_f32 v[66:67], v[66:67], v[232:233]\n"
    "v_pk_mul_f32 v[68:69], v[68:69], v[234:235]\n"
    "v_pk_mul_f32 v[70:71], v[70:71], v[234:235]\n"
    "v_pk_mul_f32 v[72:73], v[72:73], v[232:233]\n"
    "v_pk_mul_f32 v[74:75], v[74:75], v[232:233]\n"
    "v_pk_mul_f32 v[76:77], v[76:77], v[234:235]\n"
    "v_pk_mul_f32 v[78:79], v[78:79], v[234:235]\n"
    "v_pk_mul_f32 v[80:81], v[80:81], v[232:233]\n"
    "v_pk_mul_f32 v[82:83], v[82:83], v[232:233]\n"
    "v_pk_mul_f32 v[84:85], v[84:85], v[234:235]\n"
    "v_pk_mul_f32 v[86:87], v[86:87], v[234:235]\n"
    "v_pk_mul_f32 v[88:89], v[88:89], v[232:233]\n"
    "v_pk_mul_f32 v[90:91], v[90:91], v[232:233]\n"
    "v_pk_mul_f32 v[92:93], v[92:93], v[234:235]\n"
    "v_pk_mul_f32 v[94:95], v[94:95], v[234:235]\n"
    "v_pk_mul_f32 v[96:97], v[96:97], v[232:233]\n"
    "v_pk_mul_f32 v[98:99], v[98:99], v[232:233]\n"
    "v_pk_mul_f32 v[100:101], v[100:101], v[234:235]\n"
    "v_pk_mul_f32 v[102:103], v[102:103], v[234:235]\n"
    "v_pk_mul_f32 v[104:105], v[104:105], v[232:233]\n"
    "v_pk_mul_f32 v[106:107], v[106:107], v[232:233]\n"
    "v_pk_mul_f32 v[108:109], v[108:109], v[234:235]\n"
    "v_pk_mul_f32 v[110:111], v[110:111], v[234:235]\n"
    "v_pk_mul_f32 v[112:113], v[112:113], v[232:233]\n"
    "v_pk_mul_f32 v[114:115], v[114:115], v[232:233]\n"
    "v_pk_mul_f32 v[116:117], v[116:117], v[234:235]\n"
    "v_pk_mul_f32 v[118:119], v[118:119], v[234:235]\n"
    "v_pk_mul_f32 v[120:121], v[120:121], v[232:233]\n"
    "v_pk_mul_f32 v[122:123], v[122:123], v[232:233]\n"
    "v_pk_mul_f32 v[124:125], v[124:125], v[234:235]\n"
    "v_pk_mul_f32 v[126:127], v[126:127], v[234:235]\n"
    "v_exp_f32 v184, v144\n"
    "v_exp_f32 v185, v145\n"
    "v_exp_f32 v186, v146\n"
    "v_exp_f32 v187, v147\n"
    "v_exp_f32 v188, v148\n"
    "v_exp_f32 v189, v149\n"
    "v_exp_f32 v190, v150\n"
    "v_exp_f32 v191, v151\n"
    "v_add_f32 v202, v200, v184\n"
    "v_add_f32 v203, v201, v188\n"
    "v_add_f32 v202, v202, v185\n"
    "v_add_f32 v203, v203, v189\n"
    "v_add_f32 v202, v202, v186\n"
    "v_add_f32 v203, v203, v190\n"
    "v_add_f32 v202, v202, v187\n"
    "v_add_f32 v203, v203, v191\n"
    "v_cvt_pk_bf16_f32 v192, v184, v185\n"
    "v_cvt_pk_bf16_f32 v193, v186, v187\n"
    "v_cvt_pk_bf16_f32 v196, v188, v189\n"
    "v_cvt_pk_bf16_f32 v197, v190, v191\n"
    "v_exp_f32 v184, v152\n"
    "v_exp_f32 v185, v153\n"
    "v_exp_f32 v186, v154\n"
    "v_exp_f32 v187, v155\n"
    "v_exp_f32 v188, v156\n"
    "v_exp_f32 v189, v157\n"
    "v_exp_f32 v190, v158\n"
    "v_exp_f32 v191, v159\n"
    "v_add_f32 v202, v202, v184\n"
    "v_add_f32 v203, v203, v188\n"
    "v_add_f32 v202, v202, v185\n"
    "v_add_f32 v203, v203, v189\n"
    "v_add_f32 v202, v202, v186\n"
    "v_add_f32 v203, v203, v190\n"
    "v_add_f32 v202, v202, v187\n"
    "v_add_f32 v203, v203, v191\n"
    "v_cvt_pk_bf16_f32 v194, v184, v185\n"
    "v_cvt_pk_bf16_f32 v195, v186, v187\n"
    "v_cvt_pk_bf16_f32 v198, v188, v189\n"
    "v_cvt_pk_bf16_f32 v199, v190, v191\n"
    "s_nop 1\n"
    "s_branch BACKT3_%=\n"
    "MASK0_%=:\n"
    "v_cndmask_b32 v160, v211, v204, s[64:65]\n"
    "v_cndmask_b32 v172, v211, v205, s[64:65]\n"
    "v_cndmask_b32 v164, v211, v205, s[84:85]\n"
    "v_mov_b32 v168, v211\n"
    "v_cndmask_b32 v161, v211, v204, s[66:67]\n"
    "v_cndmask_b32 v173, v211, v205, s[66:67]\n"
    "v_cndmask_b32 v165, v211, v205, s[84:85]\n"
    "v_mov_b32 v169, v211\n"
    "v_cndmask_b32 v162, v211, v204, s[68:69]\n"
    "v_cndmask_b32 v174, v211, v205, s[68:69]\n"
    "v_cndmask_b32 v166, v211, v205, s[84:85]\n"
    "v_mov_b32 v170, v211\n"
    "v_cndmask_b32 v163, v211, v204, s[70:71]\n"
    "v_cndmask_b32 v175, v211, v205, s[70:71]\n"
    "v_cndmask_b32 v167, v211, v205, s[84:85]\n"
    "v_mov_b32 v171, v211\n"
    "s_mov_b64 s[64:65], 0\n"
    "s_mov_b64 s[66:67], 0\n"
    "s_mov_b64 s[68:69], 0\n"
    "s_mov_b64 s[70:71], 0\n"
    "s_mov_b64 s[84:85], 0\n"
    "s_mov_b32 s61, s63\n"
    "s_mov_b32 s63, 0x7fffffff\n"
    "s_branch MBACK0_%=\n"
    "MASK1_%=:\n"
    "s_cmp_eq_u32 s62, 2\n"
    "s_cbranch_scc1 MPUB_%=\n"
    "v_cndmask_b32 v160, v211, v204, s[64:65]\n"
    "v_cndmask_b32 v172, v211, v205, s[64:65]\n"
    "v_cndmask_b32 v164, v211, v205, s[84:85]\n"
    "v_mov_b32 v168, v211\n"
    "v_cndmask_b32 v161, v211, v204, s[66:67]\n"
    "v_cndmask_b32 v173, v211, v205, s[66:67]\n"
    "v_cndmask_b32 v165, v211, v205, s[84:85]\n"
    "v_mov_b32 v169, v211\n"
    "v_cndmask_b32 v162, v211, v204, s[68:69]\n"
    "v_cndmask_b32 v174, v211, v205, s[68:69]\n"
    "v_cndmask_b32 v166, v211, v205, s[84:85]\n"
    "v_mov_b32 v170, v211\n"
    "v_cndmask_b32 v163, v211, v204, s[70:71]\n"
    "v_cndmask_b32 v175, v211, v205, s[70:71]\n"
    "v_cndmask_b32 v167, v211, v205, s[84:85]\n"
    "v_mov_b32 v171, v211\n"
    "s_mov_b64 s[64:65], 0\n"
    "s_mov_b64 s[66:67], 0\n"
    "s_mov_b64 s[68:69], 0\n"
    "s_mov_b64 s[70:71], 0\n"
    "s_mov_b64 s[84:85], 0\n"
    "s_mov_b32 s61, s63\n"
    "s_mov_b32 s63, 0x7fffffff\n"
    "s_branch MBACK1_%=\n"
    "MPUB_%=:\n"
    "s_mov_b32 s62, 0\n"
    "s_mov_b32 s61, s80\n"
    "v_mov_b32 v218, s34\n"
    "v_mov_b32 v219, s35\n"
    "v_lshlrev_b32 v216, 21, v210\n"
    "s_lshl_b32 s98, s56, 25\n"
    "s_add_u32 s98, s98, s83\n"
    "s_add_u32 s98, s98, 0x4000\n"
    "buffer_store_dwordx2 v[218:219], v216, s[52:55], s98 offen offset:0 sc1\n"
    "s_branch MBACK1_%=\n"
    "MASK1P_%=:\n"
    "v_cndmask_b32 v160, v211, v204, s[64:65]\n"
    "v_cndmask_b32 v172, v211, v205, s[64:65]\n"
    "v_cndmask_b32 v164, v211, v205, s[84:85]\n"
    "v_mov_b32 v168, v211\n"
    "v_cndmask_b32 v161, v211, v204, s[66:67]\n"
    "v_cndmask_b32 v173, v211, v205, s[66:67]\n"
    "v_cndmask_b32 v165, v211, v205, s[84:85]\n"
    "v_mov_b32 v169, v211\n"
    "v_cndmask_b32 v162, v211, v204, s[68:69]\n"
    "v_cndmask_b32 v174, v211, v205, s[68:69]\n"
    "v_cndmask_b32 v166, v211, v205, s[84:85]\n"
    "v_mov_b32 v170, v211\n"
    "v_cndmask_b32 v163, v211, v204, s[70:71]\n"
    "v_cndmask_b32 v175, v211, v205, s[70:71]\n"
    "v_cndmask_b32 v167, v211, v205, s[84:85]\n"
    "v_mov_b32 v171, v211\n"
    "s_mov_b64 s[64:65], 0\n"
    "s_mov_b64 s[66:67], 0\n"
    "s_mov_b64 s[68:69], 0\n"
    "s_mov_b64 s[70:71], 0\n"
    "s_mov_b64 s[84:85], 0\n"
    "s_mov_b32 s61, s63\n"
    "s_mov_b32 s63, 0x7fffffff\n"
    "s_branch MBACK1P_%=\n"
    "MASKF_%=:\n"
    "v_cndmask_b32 v160, v211, v204, s[64:65]\n"
    "v_cndmask_b32 v172, v211, v205, s[64:65]\n"
    "v_cndmask_b32 v164, v211, v205, s[84:85]\n"
    "v_mov_b32 v168, v211\n"
    "v_cndmask_b32 v161, v211, v204, s[66:67]\n"
    "v_cndmask_b32 v173, v211, v205, s[66:67]\n"
    "v_cndmask_b32 v165, v211, v205, s[84:85]\n"
    "v_mov_b32 v169, v211\n"
    "v_cndmask_b32 v162, v211, v204, s[68:69]\n"
    "v_cndmask_b32 v174, v211, v205, s[68:69]\n"
    "v_cndmask_b32 v166, v211, v205, s[84:85]\n"
    "v_mov_b32 v170, v211\n"
    "v_cndmask_b32 v163, v211, v204, s[70:71]\n"
    "v_cndmask_b32 v175, v211, v205, s[70:71]\n"
    "v_cndmask_b32 v167, v211, v205, s[84:85]\n"
    "v_mov_b32 v171, v211\n"
    "s_mov_b64 s[64:65], 0\n"
    "s_mov_b64 s[66:67], 0\n"
    "s_mov_b64 s[68:69], 0\n"
    "s_mov_b64 s[70:71], 0\n"
    "s_mov_b64 s[84:85], 0\n"
    "s_mov_b32 s61, s63\n"
    "s_mov_b32 s63, 0x7fffffff\n"
    "s_branch MBACKF_%=\n"
    "M0_%=:\n"
    "s_cmp_eq_u32 s92, 0\n"
    "s_cbranch_scc1 M0TRI_%=\n"
    "s_mov_b64 s[64:65], 0\n"
    "s_mov_b64 s[66:67], 0\n"
    "s_mov_b64 s[68:69], 0\n"
    "s_mov_b64 s[70:71], 0\n"
    "s_mov_b64 s[84:85], 0\n"
    "s_mov_b32 s63, 0x7fffffff\n"
    "M0TRI_%=:\n"
    "v_cndmask_b32 v160, v211, v204, s[64:65]\n"
    "v_cndmask_b32 v172, v211, v205, s[64:65]\n"
    "v_cndmask_b32 v164, v211, v205, s[84:85]\n"
    "v_mov_b32 v168, v211\n"
    "v_cndmask_b32 v161, v211, v204, s[66:67]\n"
    "v_cndmask_b32 v173, v211, v205, s[66:67]\n"
    "v_cndmask_b32 v165, v211, v205, s[84:85]\n"
    "v_mov_b32 v169, v211\n"
    "v_cndmask_b32 v162, v211, v204, s[68:69]\n"
    "v_cndmask_b32 v174, v211, v205, s[68:69]\n"
    "v_cndmask_b32 v166, v211, v205, s[84:85]\n"
    "v_mov_b32 v170, v211\n"
    "v_cndmask_b32 v163, v211, v204, s[70:71]\n"
    "v_cndmask_b32 v175, v211, v205, s[70:71]\n"
    "v_cndmask_b32 v167, v211, v205, s[84:85]\n"
    "v_mov_b32 v171, v211\n"
    "s_mov_b64 s[64:65], 0\n"
    "s_mov_b64 s[66:67], 0\n"
    "s_mov_b64 s[68:69], 0\n"
    "s_mov_b64 s[70:71], 0\n"
    "s_mov_b64 s[84:85], 0\n"
    "s_mov_b32 s61, s63\n"
    "s_mov_b32 s63, 0x7fffffff\n"
    "s_branch M0DONE_%=\n"
    "POLL2_%=:\n"
    "s_mov_b32 s99, 0x40000\n"
    "POLLL2_%=:\n"
    "s_sleep 1\n"
    "buffer_load_dwordx2 v[238:239], v222, s[52:55], s83 offen sc1\n"
    "s_waitcnt vmcnt(0)\n"
    "v_cmp_eq_u64 vcc, s[34:35], v[238:239]\n"
    "s_nop 0\n"
    "s_cmp_eq_u64 vcc, exec\n"
    "s_cbranch_scc1 GOT2_%=\n"
    "s_sub_u32 s99, s99, 1\n"
    "s_cbranch_scc0 POLLL2_%=\n"
    "s_branch GOT2_%=\n"
    "POLL3_%=:\n"
    "s_mov_b32 s99, 0x40000\n"
    "POLLL3_%=:\n"
    "s_sleep 1\n"
    "buffer_load_dwordx2 v[238:239], v222, s[52:55], s83 offen sc1\n"
    "s_waitcnt vmcnt(0)\n"
    "v_cmp_eq_u64 vcc, s[34:35], v[238:239]\n"
    "s_nop 0\n"
    "s_cmp_eq_u64 vcc, exec\n"
    "s_cbranch_scc1 GOT3_%=\n"
    "s_sub_u32 s99, s99, 1\n"
    "s_cbranch_scc0 POLLL3_%=\n"
    "s_branch GOT3_%=\n"
    "POLLR1_%=:\n"
    "s_mov_b32 s99, 0x40000\n"
    "POLLLR1_%=:\n"
    "s_sleep 1\n"
    "v_bfe_u32 v216, v210, 1, 7\n"
    "buffer_load_dwordx2 v[238:239], v216, s[52:55], s83 offen sc1\n"
    "s_waitcnt vmcnt(0)\n"
    "v_cmp_eq_u64 vcc, s[34:35], v[238:239]\n"
    "s_nop 0\n"
    "s_cmp_eq_u64 vcc, exec\n"
    "s_cbranch_scc1 GOTR1_%=\n"
    "s_sub_u32 s99, s99, 1\n"
    "s_cbranch_scc0 POLLLR1_%=\n"
    "s_branch GOTR1_%=\n"
    "GOTR1_%=:\n"
    "s_add_u32 s97, s62, 0\n"
    "buffer_load_dwordx4 a[0:3], v210, s[48:51], s97 offen offset:0 sc1\n"
    "buffer_load_dwordx4 a[4:7], v210, s[48:51], s97 offen offset:1024 sc1\n"
    "buffer_load_dwordx4 a[8:11], v210, s[48:51], s97 offen offset:2048 sc1\n"
    "buffer_load_dwordx4 a[12:15], v210, s[48:51], s97 offen offset:3072 sc1\n"
    "s_add_u32 s97, s62, 4096\n"
    "buffer_load_dwordx4 a[16:19], v210, s[48:51], s97 offen offset:0 sc1\n"
    "buffer_load_dwordx4 a[20:23], v210, s[48:51], s97 offen offset:1024 sc1\n"
    "buffer_load_dwordx4 a[24:27], v210, s[48:51], s97 offen offset:2048 sc1\n"
    "buffer_load_dwordx4 a[28:31], v210, s[48:51], s97 offen offset:3072 sc1\n"
    "s_add_u32 s97, s62, 8192\n"
    "buffer_load_dwordx4 a[32:35], v210, s[48:51], s97 offen offset:0 sc1\n"
    "buffer_load_dwordx4 a[36:39], v210, s[48:51], s97 offen offset:1024 sc1\n"
    "buffer_load_dwordx4 a[40:43], v210, s[48:51], s97 offen offset:2048 sc1\n"
    "buffer_load_dwordx4 a[44:47], v210, s[48:51], s97 offen offset:3072 sc1\n"
    "s_add_u32 s97, s62, 12288\n"
    "buffer_load_dwordx4 a[48:51], v210, s[48:51], s97 offen offset:0 sc1\n"
    "buffer_load_dwordx4 a[52:55], v210, s[48:51], s97 offen offset:1024 sc1\n"
    "buffer_load_dwordx4 a[56:59], v210, s[48:51], s97 offen offset:2048 sc1\n"
    "buffer_load_dwordx4 a[60:63], v210, s[48:51], s97 offen offset:3072 sc1\n"
    "s_waitcnt vmcnt(0)\n"
    "s_branch RDY1_%=\n"
    "POLLR2_%=:\n"
    "s_mov_b32 s99, 0x40000\n"
    "POLLLR2_%=:\n"
    "s_sleep 1\n"
    "v_bfe_u32 v216, v210, 1, 7\n"
    "buffer_load_dwordx2 v[238:239], v216, s[52:55], s83 offen sc1\n"
    "s_waitcnt vmcnt(0)\n"
    "v_cmp_eq_u64 vcc, s[34:35], v[238:239]\n"
    "s_nop 0\n"
    "s_cmp_eq_u64 vcc, exec\n"
    "s_cbranch_scc1 GOTR2_%=\n"
    "s_sub_u32 s99, s99, 1\n"
    "s_cbranch_scc0 POLLLR2_%=\n"
    "s_branch GOTR2_%=\n"
    "POLLKV_%=:\n"
    "s_mov_b32 s99, 0x40000\n"
    "POLLLKV_%=:\n"
    "s_sleep 1\n"
    "v_bfe_u32 v216, v210, 1, 7\n"
    "buffer_load_dwordx2 v[238:239], v216, s[52:55], s80 offen sc1\n"
    "s_waitcnt vmcnt(0)\n"
    "v_cmp_eq_u64 vcc, s[34:35], v[238:239]\n"
    "s_nop 0\n"
    "s_cmp_eq_u64 vcc, exec\n"
    "s_cbranch_scc1 GOTKV_%=\n"
    "s_sub_u32 s99, s99, 1\n"
    "s_cbranch_scc0 POLLLKV_%=\n"
    "s_branch GOTKV_%=\n"
    "END_%=:\n"
    :: "s"(__builtin_amdgcn_kernarg_segment_ptr()), "s"(blockIdx.x), "s"(__builtin_amdgcn_readfirstlane(threadIdx.x >> 6)), "s"((unsigned)(size_t)lds)
    : ALL_REGS);
}

extern "C" void kernel_launch(void* const* d_in, const int* in_sizes, int n_in, void* d_out, int out_size, void* d_ws, size_t ws_size, hipStream_t stream) {
  fused_k<<<256, 256, 0, stream>>>((char*)d_ws + 167772160L, d_in[3], d_in[4], d_in[5], d_in[6], d_in[7], d_in[0], d_in[1], d_in[8], d_in[2], d_out);
}
```
